```python
import torch

def _softmax(x, axis=-1):
    return torch.softmax(x, dim=axis)
def _gelu(x):
    return torch.nn.functional.gelu(x, approximate="tanh")
def _silu(x):
    return torch.nn.functional.silu(x)
def _sigmoid(x):
    return torch.sigmoid(x)
def _layernorm(x, axis=-1, eps=1e-5):
    m = x.mean(dim=axis, keepdim=True)
    v = x.var(dim=axis, keepdim=True, unbiased=False)
    return (x - m) / torch.sqrt(v + eps)
def _scatter_add(tgt, idx, src, axis=0):
    return tgt.clone().index_add_(axis, idx, src)
def _segment_sum(src, seg, n):
    out = torch.zeros((n,) + src.shape[1:], dtype=src.dtype)
    return out.index_add_(0, seg, src)
def _pad(x, pw):
    flat = tuple(v for pair in reversed(pw) for v in pair)
    return torch.nn.functional.pad(x, flat)

kernel_name = 'recurrent_scan__compressed_kv_cla__dense_activation_relu__prompt'

def setup_inputs():

    _w = (lambda a: (lambda t: t.to(torch.bfloat16) if t.is_floating_point() else t)(torch.as_tensor(a)))
    def _A_setup():
        B, H, Sq, Sk, D, M = 1, 32, 2048, 2048, 128, 512
        Dc = 4 * D
        return {
            "x": _w((lambda *s: torch.randn(*s, dtype=torch.bfloat16) * 0.05)(B, Sq, M)),
            "c_kv": _w((lambda *s: torch.randn(*s, dtype=torch.bfloat16) * 0.2)(B, Sk, Dc)),
            "w_q": _w((lambda *s: torch.randn(*s, dtype=torch.bfloat16) * 3.2)(M, H, D) * (M ** -0.5)),
            "w_uk": _w((lambda *s: torch.randn(*s, dtype=torch.bfloat16) * 3.2)(Dc, H, D) * (Dc ** -0.5)),
            "w_uv": _w((lambda *s: torch.randn(*s, dtype=torch.bfloat16) * 0.2)(Dc, H, D) * (Dc ** -0.5)),
            "w_o": _w((lambda *s: torch.randn(*s, dtype=torch.bfloat16) * 0.2)(H, D, M) * ((H * D) ** -0.5)),
        }
    _A_in = _A_setup()

    def _B_setup():
        B, S, D, Dff = 1, 2048, 512, 1376
        return {
            "x": _w((lambda *s: torch.randn(*s, dtype=torch.bfloat16) * 0.05)(B, S, D)),
            "w1": _w((lambda *s: torch.randn(*s, dtype=torch.bfloat16) * 0.1)(D, Dff)),
            "w2": _w((lambda *s: torch.randn(*s, dtype=torch.bfloat16) * 0.1)(Dff, D)),
        }
    _B_in = _B_setup()

    return {
        "A_x": _A_in["x"],
        "A_c_kv": _A_in["c_kv"],
        "A_w_q": _A_in["w_q"],
        "A_w_uk": _A_in["w_uk"],
        "A_w_uv": _A_in["w_uv"],
        "A_w_o": _A_in["w_o"],
        "B_x": _B_in["x"],
        "B_w1": _B_in["w1"],
        "B_w2": _B_in["w2"],
    }

def reference(A_x, A_c_kv, A_w_q, A_w_uk, A_w_uv, A_w_o, B_x, B_w1, B_w2):
    def _A_ref(x, c_kv, w_q, w_uk, w_uv, w_o):
        B, Sq, M = x.shape[0], x.shape[1], x.shape[2]
        Sk = c_kv.shape[1]
        H, D = w_q.shape[1], w_q.shape[2]
        q = torch.einsum("bqm,mhd->bhqd", x, w_q)
        k = torch.einsum("bkc,chd->bhkd", c_kv, w_uk)
        v = torch.einsum("bkc,chd->bhkd", c_kv, w_uv)
        scores = torch.einsum("bhqd,bhkd->bhqk", q, k) * (D ** -0.5)
        off = Sk - Sq
        qi = torch.arange(Sq)[:, None]
        kj = torch.arange(Sk)[None, :]
        cmask = kj <= (qi + off)
        scores = torch.where(cmask[None, None], scores, -1e9)
        p = _softmax(scores, axis=-1)
        ctx = torch.einsum("bhqk,bhkd->bhqd", p, v)
        return torch.einsum("bhqd,hdm->bqm", ctx, w_o)

    def _B_ref(x, w1, w2):
        h = torch.einsum("bsd,df->bsf", x, w1)
        zero = torch.zeros_like(h)
        h = torch.maximum(h, zero)
        return torch.einsum("bsf,fd->bsd", h, w2)

    x = A_x
    xn = _layernorm(x, axis=-1)
    s = _A_ref(xn, A_c_kv, A_w_q, A_w_uk, A_w_uv, A_w_o)
    g = _silu(_B_ref(xn, B_w1, B_w2))
    return x + s * g
```

```cpp
#include <hip/hip_runtime.h>
#include <stdint.h>
#include <utility>

typedef unsigned short bf16_t;
typedef short bf16x8 __attribute__((ext_vector_type(8)));
typedef short s16x4 __attribute__((ext_vector_type(4)));
typedef float f32x16 __attribute__((ext_vector_type(16)));
typedef float f32x4 __attribute__((ext_vector_type(4)));
typedef float f32x2 __attribute__((ext_vector_type(2)));
typedef unsigned u32x4 __attribute__((ext_vector_type(4)));
typedef unsigned u32x2 __attribute__((ext_vector_type(2)));
typedef __bf16 bf16x2v __attribute__((ext_vector_type(2)));
#define LAS __attribute__((address_space(3)))
#define DI __device__ __forceinline__
template <int... Is> using iseq = std::integer_sequence<int, Is...>;
template <int N> using make_iseq = std::make_integer_sequence<int, N>;

constexpr int SEQ = 2048, NH = 32, HD = 128, DM = 512, DFF = 1376;
constexpr size_t WS_K = 0, WS_V = 16u << 20, WS_XN = 32u << 20, WS_H = 34u << 20, WS_CTX = 40u << 20, WS_PART = 56u << 20, WS_FLAG = 72u << 20;

DI unsigned cvtpk(float lo, float hi) { f32x2 v = {lo, hi}; bf16x2v b = __builtin_convertvector(v, bf16x2v); return __builtin_bit_cast(unsigned, b); }
DI float bf2f(unsigned short b) { return __uint_as_float(((unsigned)b) << 16); }
DI float rbf(float x) { return __uint_as_float(cvtpk(x, 0.f) << 16); }
#define ACLOB_LO "a0","a1","a2","a3","a4","a5","a6","a7","a8","a9","a10","a11","a12","a13","a14","a15","a16","a17","a18","a19","a20","a21","a22","a23","a24","a25","a26","a27","a28","a29","a30","a31","a32","a33","a34","a35","a36","a37","a38","a39","a40","a41","a42","a43","a44","a45","a46","a47","a48","a49","a50","a51","a52","a53","a54","a55","a56","a57","a58","a59","a60","a61","a62","a63","a64","a65","a66","a67","a68","a69","a70","a71","a72","a73","a74","a75","a76","a77","a78","a79","a80","a81","a82","a83","a84","a85","a86","a87","a88","a89","a90","a91","a92","a93","a94","a95","a96","a97","a98","a99","a100","a101","a102","a103","a104","a105","a106","a107","a108","a109","a110","a111","a112","a113","a114","a115","a116","a117","a118","a119","a120","a121","a122","a123","a124","a125","a126","a127"
#define ACLOB_HI "a128","a129","a130","a131","a132","a133","a134","a135","a136","a137","a138","a139","a140","a141","a142","a143","a144","a145","a146","a147","a148","a149","a150","a151","a152","a153","a154","a155","a156","a157","a158","a159","a160","a161","a162","a163","a164","a165","a166","a167","a168","a169","a170","a171","a172","a173","a174","a175","a176","a177","a178","a179","a180","a181","a182","a183","a184","a185","a186","a187","a188","a189","a190","a191","a192","a193","a194","a195","a196","a197","a198","a199","a200","a201","a202","a203","a204","a205","a206","a207","a208","a209","a210","a211","a212","a213","a214","a215","a216","a217","a218","a219","a220","a221","a222","a223","a224","a225","a226","a227","a228","a229","a230","a231","a232","a233","a234","a235","a236","a237","a238","a239","a240","a241","a242","a243","a244","a245","a246","a247","a248","a249","a250","a251","a252","a253","a254","a255"
#define MFMA32(a, b, c) __builtin_amdgcn_mfma_f32_32x32x16_bf16((a), (b), (c), 0, 0, 0)
#define SBAR() __builtin_amdgcn_sched_barrier(0)
DI s16x4 trread(LAS const char* p) { return __builtin_amdgcn_ds_read_tr16_b64_v4i16((LAS s16x4*)p); }
typedef __amdgpu_buffer_rsrc_t srd_t;
DI srd_t make_srd(const void* p, unsigned bytes) { return __builtin_amdgcn_make_buffer_rsrc((void*)p, (short)0, (int)bytes, 0x00020000); }
DI void dma16(unsigned m0v, unsigned voff, srd_t srd, unsigned soff) {
  asm volatile("s_mov_b32 m0, %0\n\ts_nop 0\n\tbuffer_load_dwordx4 %1, %2, %3 offen lds" ::"s"(m0v), "v"(voff), "s"(srd), "s"(soff) : "m0", "memory");
}
#define STAMP(ws, i)
template <unsigned MI, unsigned SI> DI void dma16i(unsigned m0b, unsigned voff, srd_t srd, unsigned sob) {
  unsigned t;
  asm volatile("s_add_u32 m0, %1, %c5\n\ts_add_u32 %0, %4, %c6\n\tbuffer_load_dwordx4 %2, %3, %0 offen lds" : "=&s"(t) : "s"(m0b), "v"(voff), "s"(srd), "s"(sob), "i"(MI), "i"(SI) : "m0", "scc", "memory");
}
template <int N> DI void wait_vm() { asm volatile("s_waitcnt vmcnt(%0)" ::"i"(N) : "memory"); }
DI void raw_barrier() { asm volatile("s_waitcnt lgkmcnt(0)\n\ts_barrier" ::: "memory"); }

struct KVCtx {
  LAS char* lds; srd_t sa, sb; unsigned va, vb0, vb1, soa, sob, m0w;
  unsigned a_rd[2], b_rd[4];
  bf16x8 fa[2][4]; s16x4 bl[2][4], bh[2][4];
  f32x16 acc[4][4];
};
template <int S> DI void kv_dma(KVCtx& c) {
  const unsigned st = c.m0w + (S & 3) * 32768;
#pragma unroll
  for (int q = 0; q < 4; ++q) dma16(st + q * 1024, c.va, c.sa, c.soa + S * 64 + q * 16384);
#pragma unroll
  for (int q = 0; q < 4; ++q) dma16(st + 16384 + q * 1024, (q & 1) ? c.vb1 : c.vb0, c.sb, c.sob + (S * 32 + (q >> 1) * 4) * 8192);
}
template <int S, int KS, int R> DI void kv_read(KVCtx& c) {
  LAS const char* st = c.lds + (S & 3) * 32768;
  if constexpr (R < 4) c.fa[KS][R] = *(LAS const bf16x8*)(st + c.a_rd[KS] + R * 2048);
  else { constexpr int j = (R - 4) >> 1, t = (R - 4) & 1; (t ? c.bh[KS][j] : c.bl[KS][j]) = trread(st + c.b_rd[j] + KS * 8192 + t * 2048); }
}
template <int S, int KS, int... Rs> DI void kv_reads(KVCtx& c, iseq<Rs...>) { (kv_read<S, KS, Rs>(c), ...); }
template <int S, int Q> DI void kv_dma1(KVCtx& c) {
  constexpr unsigned st = (S & 3) * 32768;
  if constexpr (Q < 4) dma16i<st + Q * 1024, S * 64 + Q * 16384>(c.m0w, c.va, c.sa, c.soa);
  else { constexpr int q = Q - 4; dma16i<st + 16384 + q * 1024, (S * 32 + (q >> 1) * 4) * 8192>(c.m0w, (q & 1) ? c.vb1 : c.vb0, c.sb, c.sob); }
}
template <bool ISK, int S, int M> DI void kv_gap(KVCtx& c) {
  constexpr int NS = 16, ks = M >> 4, i = (M >> 2) & 3, j = M & 3, g = M & 15;
  const bf16x8 fb = __builtin_shufflevector(c.bl[ks][j], c.bh[ks][j], 0, 1, 2, 3, 4, 5, 6, 7);
  c.acc[i][j] = ISK ? MFMA32(fb, c.fa[ks][i], c.acc[i][j]) : MFMA32(c.fa[ks][i], fb, c.acc[i][j]);
  constexpr int ord[12] = {4, 5, 0, 6, 7, 8, 9, 10, 11, 1, 2, 3};
  if constexpr (g < 12) {
    if constexpr (ks == 0) kv_read<S, 1, ord[g]>(c);
    else if constexpr (S + 1 < NS) kv_read<S + 1, 0, ord[g]>(c);
  }
  if constexpr (S + 3 < NS && (M & 3) == 1) kv_dma1<S + 3, (M >> 2)>(c);
  SBAR();
}
template <bool ISK, int S, int... Ms> DI void kv_gaps(KVCtx& c, iseq<Ms...>) { (kv_gap<ISK, S, Ms>(c), ...); }
template <bool ISK, int S> DI void kv_step(KVCtx& c) {
  constexpr int NS = 16;
  if constexpr (S + 2 < NS) wait_vm<8>(); else wait_vm<0>();
  asm volatile("s_barrier" ::: "memory");
  SBAR();
  kv_gaps<ISK, S>(c, make_iseq<32>{});
}
template <bool ISK, int... Ss> DI void kv_steps(KVCtx& c, iseq<Ss...>) { (kv_step<ISK, Ss>(c), ...); }

template <bool ISK> DI void kv_tile(LAS char* lds, const bf16_t* ckv, const bf16_t* W, char* img, int kb, int cb  , char* ws) {
  const unsigned tid = threadIdx.x, lane = tid & 63, l32 = lane & 31, hi = lane >> 5;
  const unsigned wid = __builtin_amdgcn_readfirstlane(tid >> 6), wr = wid >> 1, wc = wid & 1;
  KVCtx c; c.lds = lds; c.m0w = (unsigned)(size_t)lds + wid * 4096;
  c.sa = make_srd(ckv, SEQ * DM * 2); c.sb = make_srd(W, DM * NH * HD * 2);
  c.va = (64 * wid + (lane >> 2)) * 1024 + 16 * ((lane & 3) ^ ((lane >> 4) & 3)); c.soa = kb * 256 * 1024;
  c.vb0 = (8 * wid + hi) * 8192 + 16 * ((lane & 31) ^ (hi << 2));
  c.vb1 = (8 * wid + hi + 2) * 8192 + 16 * ((lane & 31) ^ ((2 + hi) << 2));
  c.sob = cb * 512;
#pragma unroll
  for (int ks = 0; ks < 2; ++ks) c.a_rd[ks] = 64 * (128 * wr + l32) + 16 * ((2 * ks + hi) ^ ((l32 >> 2) & 3));
  { const unsigned blk = (lane >> 4) & 1, q = (lane & 15) >> 2, p = lane & 3;
#pragma unroll
    for (int j = 0; j < 4; ++j) c.b_rd[j] = 16384 + 512 * (8 * hi + q) + 256 * wc + ((j ^ q) << 6) + 32 * blk + 8 * p; }
#pragma unroll
  for (int i = 0; i < 4; ++i)
#pragma unroll
    for (int j = 0; j < 4; ++j) c.acc[i][j] = f32x16{};
  kv_dma<0>(c); kv_dma<1>(c); kv_dma<2>(c);
  wait_vm<16>(); raw_barrier();

  kv_reads<0, 0>(c, make_iseq<12>{});
  kv_steps<ISK>(c, make_iseq<16>{});

  raw_barrier();
  LAS char* stg = lds + wid * 32768;
#pragma unroll
  for (int i = 0; i < 4; ++i)
#pragma unroll
    for (int j = 0; j < 4; ++j)
#pragma unroll
      for (int s = 0; s < 2; ++s) {
        const f32x16& a = c.acc[i][j];
        u32x4 w = {cvtpk(a[8 * s + 0], a[8 * s + 1]), cvtpk(a[8 * s + 2], a[8 * s + 3]), cvtpk(a[8 * s + 4], a[8 * s + 5]), cvtpk(a[8 * s + 6], a[8 * s + 7])};
        unsigned off;
        if (ISK) { const unsigned r = 32 * (i & 1) + l32, ch = 2 * (2 * j + s) + hi; off = 256 * r + 16 * (ch ^ (r & 15)); }
        else     { const unsigned d = 32 * j + l32, ch = 2 * (2 * (i & 1) + s) + hi; off = 128 * d + 16 * (ch ^ ((d >> 1) & 7)); }
        *(LAS u32x4*)(stg + (i >> 1) * 16384 + off) = w;
      }
  asm volatile("s_waitcnt lgkmcnt(0)" ::: "memory");

  const int h = 2 * cb + wc;
  char* dst = img + ((size_t)h * 32 + kb * 4 + wr * 2) * 16384 + lane * 16;
#pragma unroll
  for (int p = 0; p < 32; ++p) { u32x4 v = *(LAS const u32x4*)(stg + p * 1024 + lane * 16); *(u32x4*)(dst + p * 1024) = v; }

  asm volatile("s_waitcnt vmcnt(0)" ::: "memory");

}

DI float wave_sum(float v) {
#pragma unroll
  for (int o = 32; o >= 1; o >>= 1) v += __shfl_xor(v, o);
  return v;
}
DI void ln_rows(const bf16_t* x, bf16_t* xn, int row0) {
  const unsigned tid = threadIdx.x, lane = tid & 63, wid = tid >> 6;
#pragma unroll
  for (int rr = 0; rr < 2; ++rr) {
    const int row = row0 + wid * 2 + rr;
    u32x4 v = *(const u32x4*)(x + (size_t)row * DM + lane * 8);
    float f[8];
#pragma unroll
    for (int e = 0; e < 4; ++e) { f[2 * e] = __uint_as_float(v[e] << 16); f[2 * e + 1] = __uint_as_float(v[e] & 0xffff0000u); }
    float s = 0.f;
#pragma unroll
    for (int e = 0; e < 8; ++e) s += f[e];
    const float mean = wave_sum(s) * (1.f / DM);
    float q = 0.f;
#pragma unroll
    for (int e = 0; e < 8; ++e) { const float d = f[e] - mean; q += d * d; }
    const float var = wave_sum(q) * (1.f / DM);
    const float mb = rbf(mean), sd = rbf(sqrtf(rbf(rbf(var) + 1e-5f)));
    u32x4 o;
#pragma unroll
    for (int e = 0; e < 4; ++e) o[e] = cvtpk(rbf(f[2 * e] - mb) / sd, rbf(f[2 * e + 1] - mb) / sd);
    *(u32x4*)(xn + (size_t)row * DM + lane * 8) = o;
  }
}

__global__ __launch_bounds__(256, 1) void k1_kv(const bf16_t* __restrict__ x, const bf16_t* __restrict__ ckv, const bf16_t* __restrict__ wuk, const bf16_t* __restrict__ wuv, char* __restrict__ ws) {
  extern __shared__ __attribute__((aligned(16))) char smem[];
  LAS char* lds = (LAS char*)smem;
  const int id = blockIdx.x, xc = id & 7, j = id >> 3, cb = 4 * xc + (j & 3), kb = j >> 2;

  if (id == 0 && threadIdx.x < 64) ((unsigned*)(ws + WS_FLAG))[threadIdx.x * 64] = 0u;
  ln_rows(x, (bf16_t*)(ws + WS_XN), id * 8);

  if (cb < 16) kv_tile<true>(lds, ckv, wuk, ws + WS_K, kb, cb, ws);
  else kv_tile<false>(lds, ckv, wuv, ws + WS_V, kb, cb - 16, ws);
}

constexpr int AO_(int db, int qb) { return 16 * (2 * db + qb); }
constexpr int AQ_(int qb, int d0) { return 128 + 32 * qb + 4 * d0; }
constexpr int AF_(int n) { return 192 + 4 * (n & 15); }
constexpr float THR = 8.0f;
constexpr unsigned L_KA = 0, L_VA = 32768, L_SB = 65536  , L_X = 131072  , L_G = 49152  ;
constexpr unsigned GST = 28672;

DI float max3f(float a, float b, float c) { float r; asm("v_max3_f32 %0, %1, %2, %3" : "=v"(r) : "v"(a), "v"(b), "v"(c)); return r; }
DI float max2f(float a, float b) { float r; asm("v_max_f32_e32 %0, %1, %2" : "=v"(r) : "v"(a), "v"(b)); return r; }
DI float fadd_s(float a, float b) { float r; asm("v_add_f32_e32 %0, %1, %2" : "=v"(r) : "v"(a), "v"(b)); return r; }
DI float fmul_s(float a, float b) { float r; asm("v_mul_f32_e32 %0, %1, %2" : "=v"(r) : "v"(a), "v"(b)); return r; }
DI float fsub_s(float a, float b) { float r; asm("v_sub_f32_e32 %0, %1, %2" : "=v"(r) : "v"(a), "v"(b)); return r; }
DI unsigned lane_id() { unsigned l; asm volatile("v_mbcnt_lo_u32_b32 %0, -1, 0\n\tv_mbcnt_hi_u32_b32 %0, -1, %0" : "=v"(l)); return l; }
DI void negm_bcast(f32x16& negm, float neg_mhat) {
  const float aone = lane_id() < 32u ? 1.f : 0.f;
  asm volatile("s_nop 1\n\tv_mfma_f32_32x32x2_f32 %0, %1, %2, 0" : "+v"(negm) : "v"(aone), "v"(neg_mhat));
}
template <int A> DI float acc_rd() { float r; asm volatile("v_accvgpr_read_b32 %0, a[%c1]" : "=v"(r) : "i"(A)); return r; }
template <int A> DI void acc_wr(unsigned v) { asm volatile("v_accvgpr_write_b32 a[%c1], %0" ::"v"(v), "i"(A) : ACLOB_LO, ACLOB_HI); }
template <int A> DI void acc_scale(float f) { float t; asm volatile("v_accvgpr_read_b32 %0, a[%c2]\n\tv_mul_f32 %0, %1, %0\n\tv_accvgpr_write_b32 a[%c2], %0" : "=&v"(t) : "v"(f), "i"(A) : ACLOB_LO); }
template <int... As> DI void acc_scale_all(float f0, float f1, iseq<As...>) { (acc_scale<As>(((As >> 4) & 1) ? f1 : f0), ...); }
template <int... As> DI void acc_zero_all(iseq<As...>) { (acc_wr<As>(0u), ...); }
template <int N> DI void wait_lgkm() { asm volatile("s_waitcnt lgkmcnt(%0)" ::"i"(N) : "memory"); }

struct AC {
  unsigned kb_[8], vb_[4], vlin;
  srd_t srdK, srdV;
  unsigned m0w, sofw, soA, soB;
  int n1, n2; bool helper, first, pend;
  int row0;
  f32x16 S[2][2][2];
  f32x16 NEGM[2];
  bf16x8 Pk[2][4];
  float mhat[2], lsum[2], fct[2];
  float rs0[2], rs1[2], pmx0[2], pmx1[2], rmax[2];
};

constexpr int FW = 6, GBAR = 24;
constexpr int frag_ig(int n) { return n < 16 ? 2 * n - 2 * FW : (2 * n - 2 * FW > GBAR + (n - 16) ? 2 * n - 2 * FW : GBAR + (n - 16)); }
constexpr int frag_wait(int n) { int c = 0; for (int m = n + 1; m < n + 20; ++m) { const int ig = m < 32 ? frag_ig(m) : frag_ig(m - 32) + 64; if (ig < 2 * n) ++c; } return c; }
constexpr int frag_at(int g) { for (int n = 0; n < 32; ++n) if (frag_ig(n) == g) return n; for (int n = 0; n < 16; ++n) if (frag_ig(n) < 0 && frag_ig(n) + 64 == g) return 32 + n; return -1; }

template <int N, int SLOT> DI void frag_read(AC& c) {
  if constexpr (N < 16) asm volatile("ds_read_b128 a[%c1:%c2], %0 offset:%c3" ::"v"(c.kb_[N >> 1]), "i"(AF_(N)), "i"(AF_(N) + 3), "i"(L_KA + 8192 * (N & 1) + 16384 * SLOT) : "memory", ACLOB_HI);
  else asm volatile("ds_read_b128 a[%c1:%c2], %0 offset:%c3" ::"v"(c.vb_[(N - 16) >> 2]), "i"(AF_(N)), "i"(AF_(N) + 3), "i"(L_VA + 4096 * (N & 3) + 16384 * SLOT) : "memory", ACLOB_HI);
}
template <int X, bool FIRST, int M> DI void qk_mfma(AC& c) {
  constexpr int d0 = M >> 2, kb = (M >> 1) & 1, qb = M & 1, ak = AF_(M >> 1), aq = AQ_(qb, d0);
  if constexpr (d0 == 0) {
    if constexpr (FIRST) asm volatile("v_mfma_f32_32x32x16_bf16 %0, a[%c1:%c2], a[%c3:%c4], 0" : "=&v"(c.S[X][kb][qb]) : "i"(ak), "i"(ak + 3), "i"(aq), "i"(aq + 3) : ACLOB_LO, ACLOB_HI);
    else asm volatile("v_mfma_f32_32x32x16_bf16 %0, a[%c2:%c3], a[%c4:%c5], %1" : "=&v"(c.S[X][kb][qb]) : "v"(c.NEGM[qb]), "i"(ak), "i"(ak + 3), "i"(aq), "i"(aq + 3) : ACLOB_LO, ACLOB_HI);
  } else asm volatile("v_mfma_f32_32x32x16_bf16 %0, a[%c1:%c2], a[%c3:%c4], %0" : "+v"(c.S[X][kb][qb]) : "i"(ak), "i"(ak + 3), "i"(aq), "i"(aq + 3) : ACLOB_LO, ACLOB_HI);
}
template <int M> DI void pv_mfma(AC& c) {
  constexpr int ks = M >> 3, db = (M >> 1) & 3, qb = M & 1, ao = AO_(db, qb), av = AF_(16 + (M >> 1));
  asm volatile("v_mfma_f32_32x32x16_bf16 a[%c1:%c2], a[%c3:%c4], %0, a[%c1:%c2]" ::"v"(c.Pk[qb][ks]), "i"(ao), "i"(ao + 15), "i"(av), "i"(av + 3) : ACLOB_LO, ACLOB_HI);
}
template <int X, int QB, int KS, int P> DI void pack1(AC& c) {
  u32x4 w = __builtin_bit_cast(u32x4, c.Pk[QB][KS]);
  w[P] = cvtpk(c.S[X][KS >> 1][QB][8 * (KS & 1) + 2 * P], c.S[X][KS >> 1][QB][8 * (KS & 1) + 2 * P + 1]);
  c.Pk[QB][KS] = __builtin_bit_cast(bf16x8, w);
}
template <int X, int J> DI void rowsum1(AC& c) {
  constexpr int qb = J >> 4, ks = (J >> 2) & 3, p = J & 3;
  c.rs0[qb] = fadd_s(c.rs0[qb], c.S[X][ks >> 1][qb][8 * (ks & 1) + 2 * p]); c.rs1[qb] = fadd_s(c.rs1[qb], c.S[X][ks >> 1][qb][8 * (ks & 1) + 2 * p + 1]);
}
template <int X, int P> DI void step_dma(AC& c, const int t) {
  constexpr int q = P & 3, grp = P >> 2;
  if constexpr (grp == 0) dma16i<L_VA + 16384 * X + 1024 * q, 1024 * q>(c.m0w, c.vlin, c.srdV, c.soA);
  if constexpr (grp == 1) { if (t + 2 <= 16) dma16i<L_KA + 16384 * X + 1024 * q, 32768 + 1024 * q>(c.m0w, c.vlin, c.srdK, c.soA); }
  if constexpr (grp == 2) { if (t >= c.n1) dma16i<L_SB + L_VA + 16384 * X + 1024 * q, 1024 * q>(c.m0w, c.vlin, c.srdV, c.soB); }
  if constexpr (grp == 3) { if (t + 2 >= c.n1 && t + 2 <= 16) dma16i<L_SB + L_KA + 16384 * X + 1024 * q, 32768 + 1024 * q>(c.m0w, c.vlin, c.srdK, c.soB); }
}
template <int X, bool FIRST, int G> DI void gap_mem(AC& c, const int t) {
  constexpr int f = frag_at(G);
  if constexpr (f >= 32) frag_read<f - 32, X ^ 1>(c);
  else if constexpr (f >= 16) { if constexpr (!FIRST) frag_read<f, X ^ 1>(c); }
  else if constexpr (f >= 0) frag_read<f, X>(c);
  if constexpr (G >= 26 && G < 58 && ((G - 26) & 1) == 0) step_dma<X, (G - 26) / 2>(c, t);
}
template <bool FIRST, int G> DI void gap_wait() {
  if constexpr ((G & 1) == 0) {
    constexpr int n = G >> 1;
    if constexpr (FIRST) { if constexpr (n < 16) wait_lgkm<(frag_wait(n) < 15 - n ? frag_wait(n) : 15 - n)>(); }
    else wait_lgkm<frag_wait(n)>();
  }
}

template <int X, bool FIRST, int M> DI void gapA(AC& c, const int t, const int nst) {
  constexpr int Xp = X ^ 1;
  if constexpr (M == GBAR) {
    if (nst == 0) asm volatile("s_waitcnt vmcnt(0) lgkmcnt(0)\n\ts_barrier" ::: "memory");
    else asm volatile("s_waitcnt vmcnt(16) lgkmcnt(0)\n\ts_barrier" ::: "memory");
  }
  gap_wait<FIRST, M>();
  qk_mfma<X, FIRST, M>(c);
  gap_mem<X, FIRST, M>(c, t);
  if constexpr (!FIRST && M == 8) asm volatile("" ::"v"(c.NEGM[0]), "v"(c.NEGM[1]));
  if constexpr (!FIRST) {
    if constexpr (M < 16) {
      constexpr int e0 = 2 * M, e1 = 2 * M + 1;
      c.S[Xp][1][e0 >> 4][e0 & 15] = __builtin_amdgcn_exp2f(c.S[Xp][1][e0 >> 4][e0 & 15]);
      c.S[Xp][1][e1 >> 4][e1 & 15] = __builtin_amdgcn_exp2f(c.S[Xp][1][e1 >> 4][e1 & 15]);
      pack1<Xp, (M >> 3), ((M >> 2) & 1), (M & 3)>(c);
      asm volatile("" : "+v"(c.Pk[M >> 3][(M >> 2) & 1]), "+v"(c.S[Xp][1][e0 >> 4]));
    } else {
      constexpr int cc = M - 16;
      pack1<Xp, (cc >> 3), 2 + ((cc >> 2) & 1), (cc & 3)>(c);
      rowsum1<Xp, 16 * (cc >> 3) + 8 + 4 * ((cc >> 2) & 1) + (cc & 3)>(c);
      asm volatile("" : "+v"(c.Pk[cc >> 3][2 + ((cc >> 2) & 1)]), "+v"(c.rs0[cc >> 3]), "+v"(c.rs1[cc >> 3]));
    }
  }
  SBAR();
}
DI void lsum_merge(AC& c) {
#pragma unroll
  for (int qb = 0; qb < 2; ++qb) {
    const float rs = fadd_s(c.rs0[qb], c.rs1[qb]);
    auto pr = __builtin_amdgcn_permlane32_swap(__float_as_uint(rs), __float_as_uint(rs), false, false);
    c.lsum[qb] = fadd_s(c.lsum[qb], fadd_s(__uint_as_float(pr[0]), __uint_as_float(pr[1])));
  }
}
template <int X, bool FIRST, int... Ms> DI void phaseA_(AC& c, const int t, const int nst, iseq<Ms...>) {
  c.soA = c.sofw + (unsigned)t * 16384u; c.soB = c.soA + (unsigned)c.n2 * 16384u;
  (gapA<X, FIRST, Ms>(c, t, nst), ...);
  if constexpr (!FIRST) lsum_merge(c);
  SBAR();
}
template <int X, bool FIRST> DI void phaseA(AC& c, const int t, const int nst) { phaseA_<X, FIRST>(c, t, nst, make_iseq<32>{}); }

template <int X> DI void apply_mask(AC& c, int key0) {
  asm volatile("s_nop 7\n\ts_nop 7" ::: "memory");
  const unsigned l = lane_id();
  const float u = (float)(c.row0 + (int)(l & 31u) - key0 - 4 * (int)(l >> 5)) * 0x1p100f;
#pragma unroll
  for (int kb = 0; kb < 2; ++kb)
#pragma unroll
    for (int qb = 0; qb < 2; ++qb)
#pragma unroll
      for (int r = 0; r < 16; ++r) { const int kk = 32 * kb + (r & 3) + 8 * (r >> 2) - 32 * qb;
        float t; asm("v_add_f32_e32 %0, %1, %2" : "=v"(t) : "i"(__builtin_bit_cast(int, (0.5f - (float)kk) * 0x1p100f)), "v"(u));
        asm("v_min_f32_e32 %0, %1, %2" : "=v"(c.S[X][kb][qb][r]) : "v"(c.S[X][kb][qb][r]), "v"(t)); }
}

template <int X, bool FIRST, int M> DI void gapB(AC& c, const int t) {
  if constexpr (!FIRST) { gap_wait<false, 32 + M>(); pv_mfma<M>(c); }
  gap_mem<X, FIRST, 32 + M>(c, t);
  if constexpr (M >= 8 && M < 16) {
    constexpr int i = M - 8, q = i >> 2, r = 4 * (i & 3);
    if constexpr ((i & 3) == 0) { c.pmx0[q] = max3f(c.S[X][0][q][0], c.S[X][0][q][1], c.S[X][1][q][0]); c.pmx1[q] = max3f(c.S[X][0][q][2], c.S[X][0][q][3], c.S[X][1][q][1]);
                                  c.pmx0[q] = max3f(c.pmx0[q], c.S[X][1][q][2], c.S[X][1][q][3]); }
    else                        { c.pmx0[q] = max3f(c.pmx0[q], c.S[X][0][q][r], c.S[X][0][q][r + 1]); c.pmx1[q] = max3f(c.pmx1[q], c.S[X][0][q][r + 2], c.S[X][0][q][r + 3]);
                                  c.pmx0[q] = max3f(c.pmx0[q], c.S[X][1][q][r], c.S[X][1][q][r + 1]); c.pmx1[q] = max3f(c.pmx1[q], c.S[X][1][q][r + 2], c.S[X][1][q][r + 3]); }
    asm volatile("" : "+v"(c.pmx0[q]), "+v"(c.pmx1[q]));
  }
  if constexpr (M == 16 || M == 17) {
    constexpr int q = M - 16; const float mm = max2f(c.pmx0[q], c.pmx1[q]);
    auto pr = __builtin_amdgcn_permlane32_swap(__float_as_uint(mm), __float_as_uint(mm), false, false);
    c.rmax[q] = max2f(__uint_as_float(pr[0]), __uint_as_float(pr[1]));
    asm volatile("" : "+v"(c.rmax[q]));
  }
  if constexpr (M == 18) {
    const bool fst = FIRST || c.first;
    const bool grew = (c.rmax[0] > THR) | (c.rmax[1] > THR);
    if (__builtin_expect(fst || __any((int)grew), 0)) {
#pragma unroll
      for (int q = 0; q < 2; ++q) {
        const float dl = fst ? c.rmax[q] : max2f(c.rmax[q], 0.f);
        c.mhat[q] = fadd_s(c.mhat[q], dl); c.fct[q] = __builtin_amdgcn_exp2f(-dl);
#pragma unroll
        for (int kb = 0; kb < 2; ++kb) { _Pragma("unroll") for (int e = 0; e < 16; ++e) c.S[X][kb][q][e] = fsub_s(c.S[X][kb][q][e], dl); }
        negm_bcast(c.NEGM[q], -c.mhat[q]);
      }
      c.pend = !fst; c.first = false;
    }
    c.rs0[0] = c.rs0[1] = c.rs1[0] = c.rs1[1] = 0.f;
  }
  if constexpr (M >= 19) {
    constexpr int g = M - 19, e_lo = (32 * g) / 13, e_hi = (32 * (g + 1)) / 13;
    {
      constexpr int p_lo = g ? (32 * (g - 1)) / 13 : 0, p_hi = g ? (32 * g) / 13 : 0;
      if constexpr (p_lo + 0 < p_hi) { constexpr int e = p_lo + 0; if constexpr (e & 1) c.rs1[e >> 4] = fadd_s(c.rs1[e >> 4], c.S[X][0][e >> 4][e & 15]); else c.rs0[e >> 4] = fadd_s(c.rs0[e >> 4], c.S[X][0][e >> 4][e & 15]); }
      if constexpr (p_lo + 1 < p_hi) { constexpr int e = p_lo + 1; if constexpr (e & 1) c.rs1[e >> 4] = fadd_s(c.rs1[e >> 4], c.S[X][0][e >> 4][e & 15]); else c.rs0[e >> 4] = fadd_s(c.rs0[e >> 4], c.S[X][0][e >> 4][e & 15]); }
      if constexpr (p_lo + 2 < p_hi) { constexpr int e = p_lo + 2; if constexpr (e & 1) c.rs1[e >> 4] = fadd_s(c.rs1[e >> 4], c.S[X][0][e >> 4][e & 15]); else c.rs0[e >> 4] = fadd_s(c.rs0[e >> 4], c.S[X][0][e >> 4][e & 15]); }
    }
    if constexpr (e_lo + 0 < e_hi) c.S[X][0][(e_lo + 0) >> 4][(e_lo + 0) & 15] = __builtin_amdgcn_exp2f(c.S[X][0][(e_lo + 0) >> 4][(e_lo + 0) & 15]);
    if constexpr (e_lo + 1 < e_hi) c.S[X][0][(e_lo + 1) >> 4][(e_lo + 1) & 15] = __builtin_amdgcn_exp2f(c.S[X][0][(e_lo + 1) >> 4][(e_lo + 1) & 15]);
    if constexpr (e_lo + 2 < e_hi) c.S[X][0][(e_lo + 2) >> 4][(e_lo + 2) & 15] = __builtin_amdgcn_exp2f(c.S[X][0][(e_lo + 2) >> 4][(e_lo + 2) & 15]);
    if constexpr ((e_lo >> 4) == ((e_hi - 1) >> 4)) asm volatile("" : "+v"(c.S[X][0][e_lo >> 4]), "+v"(c.rs0[0]), "+v"(c.rs0[1]), "+v"(c.rs1[0]), "+v"(c.rs1[1]));
    else asm volatile("" : "+v"(c.S[X][0][0]), "+v"(c.S[X][0][1]), "+v"(c.rs0[0]), "+v"(c.rs0[1]), "+v"(c.rs1[0]), "+v"(c.rs1[1]));
  }
  if constexpr (M == 31) asm volatile("" ::"v"(c.S[X][0][0]), "v"(c.S[X][0][1]), "v"(c.NEGM[0]), "v"(c.NEGM[1]), "v"(c.rs0[0]), "v"(c.rs0[1]), "v"(c.rs1[0]), "v"(c.rs1[1]));
  SBAR();
}
template <int X, bool FIRST, int... Ms> DI void phaseB_(AC& c, const int t, iseq<Ms...>) {
  { const int kt = (t >= c.n1 && !c.helper) ? t + c.n2 : t;
    if (__builtin_expect(64 * kt + 63 > c.row0, 0)) apply_mask<X>(c, 64 * kt); }
  SBAR();
  (gapB<X, FIRST, Ms>(c, t), ...);
  asm volatile("s_nop 0" : "+v"(c.S[X][0][1]));
#pragma unroll
  for (int e = (32 * 12) / 13; e < 32; ++e) { if (e & 1) c.rs1[e >> 4] = fadd_s(c.rs1[e >> 4], c.S[X][0][e >> 4][e & 15]); else c.rs0[e >> 4] = fadd_s(c.rs0[e >> 4], c.S[X][0][e >> 4][e & 15]); }
  if constexpr (!FIRST) {
    if (__builtin_expect(c.pend, 0)) {
      asm volatile("s_nop 7\n\ts_nop 7" ::: "memory");
      acc_scale_all(c.fct[0], c.fct[1], make_iseq<128>{});
      asm volatile("s_nop 1" ::: "memory");
      c.lsum[0] = fmul_s(c.lsum[0], c.fct[0]); c.lsum[1] = fmul_s(c.lsum[1], c.fct[1]); c.pend = false;
    }
  }
  SBAR();
}
template <int X, bool FIRST> DI void phaseB(AC& c, const int t) { phaseB_<X, FIRST>(c, t, make_iseq<32>{}); }

template <int X, int... Cs> DI void packs(AC& c, iseq<Cs...>) { (pack1<X, ((Cs >> 3) & 1), 2 * (Cs >> 4) + ((Cs >> 2) & 1), (Cs & 3)>(c), ...); }
template <int X, int... Js> DI void rowsums(AC& c, iseq<Js...>) { (rowsum1<X, Js>(c), ...); }
template <int SLOT, int... Ns> DI void v_reads(AC& c, iseq<Ns...>) { (frag_read<16 + Ns, SLOT>(c), ...); }
template <int SLOT, int... Ns> DI void k_reads(AC& c, iseq<Ns...>) { (frag_read<Ns, SLOT>(c), ...); }
template <int... Ms> DI void pv_all(AC& c, iseq<Ms...>) { (pv_mfma<Ms>(c), ...); }

template <int QB, int DB, int G> DI void o_stage1(LAS char* stg, unsigned l32, unsigned hi, float sc) {
  constexpr int a = AO_(DB, QB) + 4 * G;
  const float o0 = acc_rd<a>(), o1 = acc_rd<a + 1>(), o2 = acc_rd<a + 2>(), o3 = acc_rd<a + 3>();
  u32x2 w = {cvtpk(o0 * sc, o1 * sc), cvtpk(o2 * sc, o3 * sc)};
  *(LAS u32x2*)(stg + 256 * l32 + 16 * ((4 * DB + G) ^ (l32 & 15u)) + 8 * hi) = w;
}
template <int QB, int... Is> DI void o_stage(LAS char* stg, unsigned l32, unsigned hi, float sc, iseq<Is...>) { (o_stage1<QB, (Is >> 2), (Is & 3)>(stg, l32, hi, sc), ...); }
DI void o_store32(LAS const char* stg, char* dst_row0  , unsigned lane) {
  const unsigned rr = lane >> 4, ch = lane & 15;
#pragma unroll
  for (int j = 0; j < 8; ++j) { const unsigned r = 4 * j + rr; u32x4 v = *(LAS const u32x4*)(stg + 256 * r + 16 * (ch ^ (r & 15u))); *(u32x4*)(dst_row0 + (size_t)r * 8192 + 16 * ch) = v; }
}
template <int QB, int I> DI void o_xw(unsigned addr) { constexpr int a = AO_(I >> 2, QB) + 4 * (I & 3); asm volatile("ds_write_b128 %0, a[%c1:%c2] offset:%c3" ::"v"(addr), "i"(a), "i"(a + 3), "i"(I * 1024) : "memory"); }
template <int QB, int... Is> DI void o_xw_all(unsigned addr, iseq<Is...>) { (o_xw<QB, Is>(addr), ...); }
template <int QB, int I> DI void o_merge1(LAS const char* src, LAS char* stg, unsigned l32, unsigned hi, float f_own, float f_oth) {
  constexpr int DB = I >> 2, G = I & 3, a = AO_(DB, QB) + 4 * G;
  const f32x4 p = *(LAS const f32x4*)(src + I * 1024);
  const float o0 = acc_rd<a>(), o1 = acc_rd<a + 1>(), o2 = acc_rd<a + 2>(), o3 = acc_rd<a + 3>();
  u32x2 w = {cvtpk(o0 * f_own + p[0] * f_oth, o1 * f_own + p[1] * f_oth), cvtpk(o2 * f_own + p[2] * f_oth, o3 * f_own + p[3] * f_oth)};
  *(LAS u32x2*)(stg + 256 * l32 + 16 * ((4 * DB + G) ^ (l32 & 15u)) + 8 * hi) = w;
}
template <int QB, int... Is> DI void o_merge(LAS const char* src, LAS char* stg, unsigned l32, unsigned hi, float f_own, float f_oth, iseq<Is...>) { (o_merge1<QB, Is>(src, stg, l32, hi, f_own, f_oth), ...); }
template <int I> DI void q_ld(unsigned addr) { asm volatile("ds_read_b128 a[%c1:%c2], %0 offset:%c3" ::"v"(addr), "i"(128 + 4 * I), "i"(128 + 4 * I + 3), "i"(I * 1024) : "memory", ACLOB_HI); }
template <int... Is> DI void q_ld_all(unsigned addr, iseq<Is...>) { (q_ld<Is>(addr), ...); }
template <int I> DI void q_st(unsigned addr) { asm volatile("ds_write_b128 %0, a[%c1:%c2] offset:%c3" ::"v"(addr), "i"(128 + 4 * I), "i"(128 + 4 * I + 3), "i"(I * 1024) : "memory"); }
template <int... Is> DI void q_st_all(unsigned addr, iseq<Is...>) { (q_st<Is>(addr), ...); }

struct PC {
  LAS char* lds; srd_t sx, sq, s1; unsigned vx, vq, v1, sox, soq, so1, m0x, m0q, m01;
  unsigned x_rd[2], xs_rd[2], q_rd[4], f_rd;
  bf16x8 fx[2][2], fxs[2]; s16x4 ql[2][4], qh[2][4], fl[2][2], fh[2][2];
  f32x16 F[3];
};
template <int S, int Q> DI void pj_dma1(PC& p) {
  constexpr unsigned st = L_G + (S & 3) * GST;
  if constexpr (Q < 4) dma16i<st + Q * 1024, S * 64 + Q * 16384>(p.m0x, p.vx, p.sx, p.sox);
  else if constexpr (Q < 6) dma16i<st + (Q - 4) * 1024, (S * 32 + (Q - 4) * 4) * 8192>(p.m0q, p.vq, p.sq, p.soq);
  else dma16i<st, S * 32 * 2752>(p.m01, p.v1, p.s1, p.so1);
}
template <int S, int... Qs> DI void pj_dmas(PC& p, iseq<Qs...>) { (pj_dma1<S, Qs>(p), ...); }
template <int S, int KS, int R> DI void pj_read(PC& p) {
  LAS const char* st = p.lds + L_G + (S & 3) * GST;
  if constexpr (R < 2) p.fx[KS][R] = *(LAS const bf16x8*)(st + p.x_rd[KS] + R * 2048);
  else if constexpr (R == 2) p.fxs[KS] = *(LAS const bf16x8*)(st + p.xs_rd[KS]);
  else if constexpr (R < 11) { constexpr int db = (R - 3) >> 1, t = (R - 3) & 1; (t ? p.qh[KS][db] : p.ql[KS][db]) = trread(st + p.q_rd[db] + KS * 4096 + t * 1024); }
  else { constexpr int fb = (R - 11) >> 1, t = (R - 11) & 1; (t ? p.fh[KS][fb] : p.fl[KS][fb]) = trread(st + p.f_rd + fb * 2048 + KS * 1024 + t * 256); }
}
template <int S, int KS, int... Rs> DI void pj_reads(PC& p, iseq<Rs...>) { (pj_read<S, KS, Rs>(p), ...); }
template <int S, int M> DI void pj_gap(PC& p) {
  constexpr int NS = 16, ks = M / 11, m = M % 11;
  constexpr bool Z = (S == 0 && ks == 0);
  if constexpr (m < 8) {
    constexpr int db = m >> 1, qb = m & 1, ao = AO_(db, qb);
    const bf16x8 fa = __builtin_shufflevector(p.ql[ks][db], p.qh[ks][db], 0, 1, 2, 3, 4, 5, 6, 7);
    if constexpr (Z) asm volatile("s_nop 1\n\tv_mfma_f32_32x32x16_bf16 a[%c2:%c3], %0, %1, 0" ::"v"(fa), "v"(p.fx[ks][qb]), "i"(ao), "i"(ao + 15) : ACLOB_LO, ACLOB_HI);
    else asm volatile("s_nop 1\n\tv_mfma_f32_32x32x16_bf16 a[%c2:%c3], %0, %1, a[%c2:%c3]" ::"v"(fa), "v"(p.fx[ks][qb]), "i"(ao), "i"(ao + 15) : ACLOB_LO, ACLOB_HI);
  } else {
    constexpr int fi = m - 8, fb = fi == 2 ? 1 : 0;
    const bf16x8 fa = __builtin_shufflevector(p.fl[ks][fb], p.fh[ks][fb], 0, 1, 2, 3, 4, 5, 6, 7);
    const bf16x8 fbv = fi == 2 ? p.fxs[ks] : p.fx[ks][fi];
    if constexpr (Z) asm volatile("s_nop 1\n\tv_mfma_f32_32x32x16_bf16 %0, %1, %2, 0" : "=&v"(p.F[fi]) : "v"(fa), "v"(fbv) : ACLOB_LO, ACLOB_HI);
    else asm volatile("s_nop 1\n\tv_mfma_f32_32x32x16_bf16 %0, %1, %2, %0" : "+v"(p.F[fi]) : "v"(fa), "v"(fbv) : ACLOB_LO, ACLOB_HI);
  }
  constexpr int ord[15] = {3, 4, 0, 1, 5, 6, 7, 8, 9, 10, 11, 12, 2, 13, 14};
  constexpr int r0 = (15 * m) / 11, r1 = (15 * (m + 1)) / 11;
  if constexpr (ks == 0) { if constexpr (r0 < r1) pj_read<S, 1, ord[r0]>(p); if constexpr (r0 + 1 < r1) pj_read<S, 1, ord[r0 + 1]>(p); }
  else if constexpr (S + 1 < NS) { if constexpr (r0 < r1) pj_read<S + 1, 0, ord[r0]>(p); if constexpr (r0 + 1 < r1) pj_read<S + 1, 0, ord[r0 + 1]>(p); }
  if constexpr (S + 3 < NS && (M % 3) == 1) pj_dma1<S + 3, M / 3>(p);
  SBAR();
}
template <int S, int... Ms> DI void pj_gaps(PC& p, iseq<Ms...>) { (pj_gap<S, Ms>(p), ...); }
template <int S> DI void pj_step(PC& p) {
  constexpr int NS = 16;
  if constexpr (S + 2 < NS) wait_vm<7>(); else wait_vm<0>();
  asm volatile("s_barrier" ::: "memory");
  SBAR();
  pj_gaps<S>(p, make_iseq<22>{});
}
template <int... Ss> DI void pj_steps(PC& p, iseq<Ss...>) { (pj_step<Ss>(p), ...); }
template <int I> DI void q_pack1(float sc) {
  constexpr int qb = I >> 3, d0 = I & 7, a = AO_(d0 >> 1, qb) + 8 * (d0 & 1), aq = AQ_(qb, d0);
  const unsigned w0 = cvtpk(fmul_s(acc_rd<a>(), sc), fmul_s(acc_rd<a + 1>(), sc)), w1 = cvtpk(fmul_s(acc_rd<a + 2>(), sc), fmul_s(acc_rd<a + 3>(), sc));
  const unsigned w2 = cvtpk(fmul_s(acc_rd<a + 4>(), sc), fmul_s(acc_rd<a + 5>(), sc)), w3 = cvtpk(fmul_s(acc_rd<a + 6>(), sc), fmul_s(acc_rd<a + 7>(), sc));
  acc_wr<aq>(w0); acc_wr<aq + 1>(w1); acc_wr<aq + 2>(w2); acc_wr<aq + 3>(w3);
}
template <int... Is> DI void q_pack(float sc, iseq<Is...>) { (q_pack1<Is>(sc), ...); }
DI void h_store(const f32x16& F, char* rowp  ) {
#pragma unroll
  for (int g = 0; g < 4; ++g) {
    u32x2 w = {cvtpk(fmaxf(F[4 * g], 0.f), fmaxf(F[4 * g + 1], 0.f)), cvtpk(fmaxf(F[4 * g + 2], 0.f), fmaxf(F[4 * g + 3], 0.f))};
    *(u32x2*)(rowp + 16 * g) = w;
  }
}

__global__ __launch_bounds__(256, 1) void k2_attn(const bf16_t* __restrict__ wq, const bf16_t* __restrict__ w1, char* __restrict__ ws) {
  extern __shared__ __attribute__((aligned(16))) char smem[];
  LAS char* lds = (LAS char*)smem;
  const unsigned ldsb = (unsigned)(size_t)lds;
  const unsigned tid = threadIdx.x, lane = tid & 63u, l32 = lane & 31u, hi = lane >> 5;
  const unsigned wid = __builtin_amdgcn_readfirstlane(tid >> 6);
  const int id = blockIdx.x, h = (id & 7) + 8 * ((id >> 3) & 3), pi = id >> 5;
  const int rowL = 128 * pi, rowH = 128 * (15 - pi);

  AC c;
  c.n1 = 2 * (pi + 1); c.n2 = 15 - 2 * pi; c.helper = wid < 2;
  c.row0 = (wid < 2 ? rowL : rowH) + 64 * (int)(wid & 1);
  c.srdK = make_srd(ws + WS_K, 16u << 20); c.srdV = make_srd(ws + WS_V, 16u << 20);
  c.vlin = lane * 16; c.m0w = ldsb + 4096 * wid; c.sofw = (unsigned)h * 32u * 16384u + 4096 * wid;
#pragma unroll
  for (int q = 0; q < 4; ++q) dma16(c.m0w + L_KA + 1024 * q, c.vlin, c.srdK, c.sofw + 1024 * q);
#pragma unroll
  for (int q = 0; q < 4; ++q) dma16(c.m0w + L_KA + 16384 + 1024 * q, c.vlin, c.srdK, c.sofw + 16384 + 1024 * q);
  {
    PC p; p.lds = lds;
    p.sx = make_srd(ws + WS_XN, SEQ * DM * 2); p.sq = make_srd(wq, DM * NH * HD * 2); p.s1 = make_srd(w1, DM * DFF * 2);
    p.vx = (lane >> 2) * 1024 + 16 * ((lane & 3) ^ ((lane >> 4) & 3)); p.sox = (unsigned)c.row0 * 1024u; p.m0x = ldsb + 4096 * wid;
    p.vq = (8 * wid + (lane >> 4)) * 8192 + 16 * ((lane & 15) ^ ((lane >> 4) << 2)); p.soq = h * 256; p.m0q = ldsb + 16384 + 2048 * wid;
    const int fb1 = h < 22 ? 32 + (h >> 1) : h, qsel = h & 1;
    p.v1 = (16 * (wid & 1) + (lane >> 2)) * 2752 + 16 * (lane & 3); p.so1 = ((wid >> 1) ? fb1 : h) * 64; p.m01 = ldsb + 24576 + 1024 * wid;
#pragma unroll
    for (int ks = 0; ks < 2; ++ks) { p.x_rd[ks] = 64 * (64 * wid + l32) + 16 * ((2 * ks + hi) ^ ((l32 >> 2) & 3)); p.xs_rd[ks] = p.x_rd[ks] + 2048 * qsel; }
    { const unsigned blk = (lane >> 4) & 1, q4 = (lane & 15) >> 2, pp = lane & 3;
#pragma unroll
      for (int db = 0; db < 4; ++db) p.q_rd[db] = 16384 + 256 * (8 * hi + q4) + ((db ^ q4) << 6) + 32 * blk + 8 * pp;
      p.f_rd = 24576 + 64 * (8 * hi + q4) + 32 * blk + 8 * pp; }
    pj_dmas<0>(p, make_iseq<7>{}); pj_dmas<1>(p, make_iseq<7>{}); pj_dmas<2>(p, make_iseq<7>{});
    wait_vm<14>(); raw_barrier();

    pj_reads<0, 0>(p, make_iseq<15>{});
    pj_steps(p, make_iseq<16>{});
    asm volatile("s_nop 7\n\ts_nop 7" : "+v"(p.F[0]), "+v"(p.F[1]), "+v"(p.F[2])::"memory");

    q_pack(0.12751743082f, make_iseq<16>{});
    char* hp = ws + WS_H + (size_t)(c.row0 + l32) * 2752 + 8 * hi;
    h_store(p.F[0], hp + 64 * h); h_store(p.F[1], hp + 32 * 2752 + 64 * h);
    if (h < 22) h_store(p.F[2], hp + (size_t)qsel * 32 * 2752 + 64 * fb1);
  }
  raw_barrier();
  if (wid >= 2) { asm volatile("s_nop 1" ::: "memory"); q_st_all(ldsb + L_X + 16384 * (wid & 1) + lane * 16, make_iseq<16>{}); }
#pragma unroll
  for (int d0 = 0; d0 < 8; ++d0) c.kb_[d0] = ldsb + 256 * l32 + 16 * ((2 * d0 + hi) ^ (l32 & 15u));
#pragma unroll
  for (int ks = 0; ks < 4; ++ks) c.vb_[ks] = ldsb + 128 * l32 + 16 * ((2 * ks + hi) ^ ((l32 >> 1) & 7u));
  c.mhat[0] = c.mhat[1] = 0.f; c.lsum[0] = c.lsum[1] = 0.f; c.fct[0] = c.fct[1] = 1.f; c.pend = false; c.first = false;
  acc_zero_all(make_iseq<128>{});
  k_reads<0>(c, make_iseq<FW>{});
  float l_light[2] = {1.f, 1.f};
  char* ctx = ws + WS_CTX + h * 256;
  int nst = 12;
  asm volatile("s_waitcnt vmcnt(0)" ::: "memory"); nst = 0;

  phaseA<0, true>(c, 0, 0); phaseB<0, true>(c, 0);
  for (int t = 1; t < 17; t += 2) {
    phaseA<1, false>(c, t, nst); nst = 0;
    if (t + 1 == c.n1 && !c.helper) {
#pragma unroll
      for (int d0 = 0; d0 < 8; ++d0) c.kb_[d0] += L_SB;
    }
    phaseB<1, false>(c, t);
    const bool sw = (t + 1 == c.n1);
    if (sw && c.helper) {
      q_ld_all(ldsb + L_X + 16384 * wid + lane_id() * 16, make_iseq<16>{});
      c.mhat[0] = c.mhat[1] = 0.f; negm_bcast(c.NEGM[0], 0.f); negm_bcast(c.NEGM[1], 0.f);
      c.first = true; c.row0 = rowH + 64 * (int)wid;
      asm volatile("s_waitcnt lgkmcnt(0)\n\ts_nop 7\n\ts_nop 7" ::: "memory");
    }
    phaseA<0, false>(c, t + 1, 0);
    if (sw && c.helper) { l_light[0] = c.lsum[0]; l_light[1] = c.lsum[1]; c.lsum[0] = c.lsum[1] = 0.f; }
    phaseB<0, false>(c, t + 1);
    if (sw) {
      if (c.helper) {
        asm volatile("s_nop 7\n\ts_nop 7" ::: "memory");
        LAS char* stg = lds + L_X + 16384 * wid;
        const unsigned ln = lane_id(), ln32 = ln & 31u, lnh = ln >> 5;
        o_stage<0>(stg, ln32, lnh, __builtin_amdgcn_rcpf(l_light[0]), make_iseq<16>{});
        o_stage<1>(stg + 8192, ln32, lnh, __builtin_amdgcn_rcpf(l_light[1]), make_iseq<16>{});
        acc_zero_all(make_iseq<128>{});
        asm volatile("s_waitcnt lgkmcnt(0)" ::: "memory");
        char* dst = ctx + (size_t)(rowL + 64 * wid) * 8192;
        o_store32(stg, dst, ln); o_store32(stg + 8192, dst + 32 * 8192, ln);
        asm volatile("s_waitcnt lgkmcnt(0)" ::: "memory");
        nst = 16;
      } else {
#pragma unroll
        for (int ks = 0; ks < 4; ++ks) c.vb_[ks] += L_SB;
      }
    }
  }

  asm volatile("s_waitcnt vmcnt(0) lgkmcnt(0)\n\ts_barrier" ::: "memory");
  v_reads<0>(c, make_iseq<16>{});
#pragma unroll
  for (int e = 0; e < 32; ++e) c.S[0][1][e >> 4][e & 15] = __builtin_amdgcn_exp2f(c.S[0][1][e >> 4][e & 15]);
  packs<0>(c, make_iseq<32>{});
  rowsums<0>(c, iseq<8, 9, 10, 11, 12, 13, 14, 15, 24, 25, 26, 27, 28, 29, 30, 31>{});
  lsum_merge(c);
  asm volatile("s_waitcnt lgkmcnt(0)\n\ts_nop 1" : "+v"(c.Pk[0][0]), "+v"(c.Pk[0][1]), "+v"(c.Pk[0][2]), "+v"(c.Pk[0][3]), "+v"(c.Pk[1][0]), "+v"(c.Pk[1][1]), "+v"(c.Pk[1][2]), "+v"(c.Pk[1][3])::"memory");
  pv_all(c, make_iseq<32>{});
  asm volatile("s_nop 7\n\ts_nop 7\n\ts_barrier" ::: "memory");
  {
    const unsigned xw = 20480 * wid, xr = 20480 * (wid ^ 2);
    const unsigned lane = lane_id(), l32 = lane & 31u, hi = lane >> 5;
    if (c.helper) { o_xw_all<1>(ldsb + xw + lane * 16, make_iseq<16>{}); *(LAS f32x2*)(lds + xw + 16384 + lane * 8) = f32x2{c.mhat[1], c.lsum[1]}; }
    else          { o_xw_all<0>(ldsb + xw + lane * 16, make_iseq<16>{}); *(LAS f32x2*)(lds + xw + 16384 + lane * 8) = f32x2{c.mhat[0], c.lsum[0]}; }
    raw_barrier();
    const f32x2 ml = *(LAS const f32x2*)(lds + xr + 16384 + lane * 8);
    const float m1 = c.helper ? c.mhat[0] : c.mhat[1], l1 = c.helper ? c.lsum[0] : c.lsum[1];
    const float m = fmaxf(m1, ml[0]), e1 = __builtin_amdgcn_exp2f(m1 - m), e2 = __builtin_amdgcn_exp2f(ml[0] - m);
    const float il = __builtin_amdgcn_rcpf(l1 * e1 + ml[1] * e2);
    LAS char* stg = lds + L_X + 8192 * wid;
    if (c.helper) o_merge<0>(lds + xr + lane * 16, stg, l32, hi, e1 * il, e2 * il, make_iseq<16>{});
    else          o_merge<1>(lds + xr + lane * 16, stg, l32, hi, e1 * il, e2 * il, make_iseq<16>{});
    asm volatile("s_waitcnt lgkmcnt(0)" ::: "memory");
    o_store32(stg, ctx + (size_t)(rowH + 64 * (wid & 1) + 32 * (wid >> 1)) * 8192, lane);
  }

}
struct OC {
  LAS char* lds; srd_t sa, sb; unsigned va[2], vb, soa, sob, m0w, astep;
  unsigned a_rd[4], b_rd[2];
  bf16x8 fa[2][2]; s16x4 bl[2][2], bh[2][2];
  f32x16 acc[2][2];
};
template <int SL, int Q> DI void o3_dma1(OC& c, unsigned soa, unsigned sob) {
  constexpr unsigned st = SL * 32768;
  if constexpr (Q < 4) dma16i<st + Q * 1024, 0>(c.m0w, c.va[Q & 1], c.sa, soa + (unsigned)Q * c.astep);
  else dma16i<st + 16384 + (Q - 4) * 1024, (Q - 4) * 4096>(c.m0w, c.vb, c.sb, sob);
}
template <int SL, int... Qs> DI void o3_dmas(OC& c, unsigned soa, unsigned sob, iseq<Qs...>) { (o3_dma1<SL, Qs>(c, soa, sob), ...); }
template <int SL, int KS, int R> DI void o3_read(OC& c) {
  LAS const char* st = c.lds + SL * 32768;
  if constexpr (R < 2) c.fa[KS & 1][R] = *(LAS const bf16x8*)(st + c.a_rd[KS] + R * 4096);
  else { constexpr int j = (R - 2) >> 1, t = (R - 2) & 1; (t ? c.bh[KS & 1][j] : c.bl[KS & 1][j]) = trread(st + c.b_rd[j] + KS * 4096 + t * 1024); }
}
template <int SL, int KS, int... Rs> DI void o3_reads(OC& c, iseq<Rs...>) { (o3_read<SL, KS, Rs>(c), ...); }
template <int SL, int M> DI void o3_gap(OC& c, bool more, unsigned soa, unsigned sob) {
  constexpr int ks = M >> 2, i = (M >> 1) & 1, j = M & 1, g = M & 3;
  const bf16x8 fb = __builtin_shufflevector(c.bl[ks & 1][j], c.bh[ks & 1][j], 0, 1, 2, 3, 4, 5, 6, 7);
  c.acc[i][j] = MFMA32(fb, c.fa[ks & 1][i], c.acc[i][j]);
  constexpr int ord[6] = {2, 3, 0, 4, 5, 1};
  constexpr int nsl = ks == 3 ? (SL + 1) & 3 : SL, nks = (ks + 1) & 3;
  if constexpr (g < 2) { o3_read<nsl, nks, ord[2 * g]>(c); o3_read<nsl, nks, ord[2 * g + 1]>(c); }
  else o3_read<nsl, nks, ord[2 + g]>(c);
  if constexpr ((M & 1) == 0) { if (more) o3_dma1<(SL + 3) & 3, (M >> 1)>(c, soa, sob); }
  SBAR();
}
template <int SL, int... Ms> DI void o3_gaps(OC& c, bool more, unsigned soa, unsigned sob, iseq<Ms...>) { (o3_gap<SL, Ms>(c, more, soa, sob), ...); }
template <int SL> DI void o3_step(OC& c, int s, int ns) {
  if (s + 2 < ns) wait_vm<8>(); else wait_vm<0>();
  asm volatile("s_barrier" ::: "memory");
  SBAR();
  o3_gaps<SL>(c, s + 3 < ns, c.soa + (unsigned)(s + 3) * 128u, c.sob + (unsigned)(s + 3) * 65536u, make_iseq<16>{});
}

__global__ __launch_bounds__(256, 1) void k3_out(const bf16_t* __restrict__ x, const bf16_t* __restrict__ wo, const bf16_t* __restrict__ w2, bf16_t* __restrict__ out, char* __restrict__ ws) {
  extern __shared__ __attribute__((aligned(16))) char smem[];
  LAS char* lds = (LAS char*)smem;
  const unsigned ldsb = (unsigned)(size_t)lds;
  const unsigned tid = threadIdx.x, lane = tid & 63u, l32 = lane & 31u, hi = lane >> 5;
  const unsigned wid = __builtin_amdgcn_readfirstlane(tid >> 6), wr = wid >> 1, wc = wid & 1;
  const int id = blockIdx.x, xc = id & 7, jj = id >> 3, part = xc & 3, tn = jj & 3, tm = (xc >> 2) * 8 + (jj >> 2);

  OC c; c.lds = lds; c.m0w = ldsb + 4096 * wid;
  const bool isg = part == 3;
  const unsigned apitch = isg ? 2752u : 8192u;
  const int k0 = part == 0 ? 0 : (part == 1 ? 22 : 43), ns = (part == 1 || part == 2) ? 21 : 22;
  c.sa = isg ? make_srd(ws + WS_H, SEQ * DFF * 2) : make_srd(ws + WS_CTX, SEQ * 4096 * 2);
  c.sb = isg ? make_srd(w2, DFF * DM * 2) : make_srd(wo, 4096 * DM * 2);
#pragma unroll
  for (int par = 0; par < 2; ++par) c.va[par] = (32 * wid + (lane >> 3)) * apitch + 16 * ((lane & 7) ^ ((4 * par + (lane >> 4)) & 7));
  c.astep = 8 * apitch;
  c.soa = (unsigned)(tm * 128) * apitch + (isg ? 0u : (unsigned)k0 * 128u);
  c.vb = (16 * wid + (lane >> 4)) * 1024 + 16 * ((lane & 15) ^ ((lane >> 4) << 2));
  c.sob = tn * 256 + (isg ? 0u : (unsigned)k0 * 65536u);
#pragma unroll
  for (int ks = 0; ks < 4; ++ks) c.a_rd[ks] = 128 * (64 * wr + l32) + 16 * ((2 * ks + hi) ^ ((l32 >> 1) & 7u));
  { const unsigned blk = (lane >> 4) & 1, q4 = (lane & 15) >> 2, pp = lane & 3;
#pragma unroll
    for (int j = 0; j < 2; ++j) c.b_rd[j] = 16384 + 256 * (8 * hi + q4) + (((2 * wc + j) ^ q4) << 6) + 32 * blk + 8 * pp; }
#pragma unroll
  for (int i = 0; i < 2; ++i)
#pragma unroll
    for (int j = 0; j < 2; ++j) c.acc[i][j] = f32x16{};
  o3_dmas<0>(c, c.soa, c.sob, make_iseq<8>{}); o3_dmas<1>(c, c.soa + 128, c.sob + 65536, make_iseq<8>{}); o3_dmas<2>(c, c.soa + 256, c.sob + 131072, make_iseq<8>{});
  wait_vm<16>(); raw_barrier();

  o3_reads<0, 0>(c, make_iseq<6>{});
  for (int s = 0; s < 20; s += 4) { o3_step<0>(c, s, ns); o3_step<1>(c, s + 1, ns); o3_step<2>(c, s + 2, ns); o3_step<3>(c, s + 3, ns); }
  o3_step<0>(c, 20, ns);
  if (ns == 22) o3_step<1>(c, 21, ns);

  const int tile = tm * 4 + tn;
  srd_t sx = make_srd(ws + WS_PART, 16u << 20);
  const unsigned xbase = (unsigned)tile * 262144u + wid * 16384u + lane * 16u;
#pragma unroll
  for (int b = 0; b < 4; ++b) {
    if (b != part) {
      const f32x16& a = c.acc[b >> 1][b & 1];
#pragma unroll
      for (int e = 0; e < 4; ++e) { u32x4 v = {__float_as_uint(a[4 * e]), __float_as_uint(a[4 * e + 1]), __float_as_uint(a[4 * e + 2]), __float_as_uint(a[4 * e + 3])};
        __builtin_amdgcn_raw_buffer_store_b128(v, sx, xbase + part * 65536u + b * 4096u + e * 1024u, 0, 16); }
    }
  }
  asm volatile("s_waitcnt vmcnt(0)" ::: "memory");
  __syncthreads();
  unsigned* cnt = (unsigned*)(ws + WS_FLAG) + tile * 64;
  if (tid == 0) {
    __hip_atomic_fetch_add(cnt, 1u, __ATOMIC_RELAXED, __HIP_MEMORY_SCOPE_AGENT);
    unsigned spins = 0;
    while (__hip_atomic_load(cnt, __ATOMIC_RELAXED, __HIP_MEMORY_SCOPE_AGENT) < 4u && ++spins < 2000000u) __builtin_amdgcn_s_sleep(1);
  }
  __syncthreads();
  f32x16 own = part == 0 ? c.acc[0][0] : (part == 1 ? c.acc[0][1] : (part == 2 ? c.acc[1][0] : c.acc[1][1]));
  f32x16 s = isg ? f32x16{} : own, g = own;
#pragma unroll
  for (int p = 0; p < 4; ++p) {
    if (p != part) {
#pragma unroll
      for (int e = 0; e < 4; ++e) { const u32x4 v = __builtin_amdgcn_raw_buffer_load_b128(sx, xbase + p * 65536u + part * 4096u + e * 1024u, 0, 16);
#pragma unroll
        for (int k = 0; k < 4; ++k) { if (p == 3) g[4 * e + k] = __uint_as_float(v[k]); else s[4 * e + k] += __uint_as_float(v[k]); } }
    }
  }
  const size_t off = (size_t)(tm * 128 + 64 * wr + 32 * (part >> 1) + l32) * DM + tn * 128 + 64 * wc + 32 * (part & 1) + 4 * hi;
#pragma unroll
  for (int e = 0; e < 4; ++e) {
    const u32x2 xv = *(const u32x2*)(x + off + 8 * e);
    float o[4];
#pragma unroll
    for (int k = 0; k < 4; ++k) {
      const float xf = __uint_as_float((k & 1) ? (xv[k >> 1] & 0xffff0000u) : (xv[k >> 1] << 16));
      const float sb = rbf(s[4 * e + k]), gb = rbf(g[4 * e + k]);
      const float sg = rbf(gb / (1.f + __expf(-gb)));
      o[k] = xf + rbf(sb * sg);
    }
    *(u32x2*)(out + off + 8 * e) = u32x2{cvtpk(o[0], o[1]), cvtpk(o[2], o[3])};
  }

}

extern "C" void kernel_launch(void* const* d_in, const int* in_sizes, int n_in, void* d_out, int out_size, void* d_ws, size_t ws_size, hipStream_t stream) {
  const bf16_t* x = (const bf16_t*)d_in[0]; const bf16_t* ckv = (const bf16_t*)d_in[1]; const bf16_t* wq = (const bf16_t*)d_in[2];
  const bf16_t* wuk = (const bf16_t*)d_in[3]; const bf16_t* wuv = (const bf16_t*)d_in[4]; const bf16_t* wo = (const bf16_t*)d_in[5];
  const bf16_t* w1 = (const bf16_t*)d_in[7]; const bf16_t* w2 = (const bf16_t*)d_in[8];
  (void)hipFuncSetAttribute((const void*)k1_kv, hipFuncAttributeMaxDynamicSharedMemorySize, 131072);
  (void)hipFuncSetAttribute((const void*)k2_attn, hipFuncAttributeMaxDynamicSharedMemorySize, 163840);
  (void)hipFuncSetAttribute((const void*)k3_out, hipFuncAttributeMaxDynamicSharedMemorySize, 131072);
  k1_kv<<<256, 256, 131072, stream>>>(x, ckv, wuk, wuv, (char*)d_ws);
  k2_attn<<<256, 256, 163840, stream>>>(wq, w1, (char*)d_ws);
  k3_out<<<256, 256, 131072, stream>>>(x, wo, w2, (bf16_t*)d_out, (char*)d_ws);
}
```

```cpp
#include <hip/hip_runtime.h>
#include <stdint.h>
#include <utility>

typedef unsigned short bf16_t;
typedef float f32x4 __attribute__((ext_vector_type(4)));
typedef float f32x2 __attribute__((ext_vector_type(2)));
typedef unsigned u32x4 __attribute__((ext_vector_type(4)));
typedef unsigned u32x2 __attribute__((ext_vector_type(2)));
typedef __bf16 bf16x2v __attribute__((ext_vector_type(2)));
typedef __amdgpu_buffer_rsrc_t srd_t;
#define LAS __attribute__((address_space(3)))
#define DI __device__ __forceinline__
template <int... Is> using iseq = std::integer_sequence<int, Is...>;
template <int N> using make_iseq = std::make_integer_sequence<int, N>;

constexpr int SEQ = 2048, NH = 32, HD = 128, DM = 512, DFF = 1376;
constexpr size_t WS_K = 0, WS_V = 16u << 20, WS_XN = 32u << 20, WS_H = 34u << 20, WS_CTX = 40u << 20, WS_PART = 56u << 20, WS_FLAG = 72u << 20;

DI unsigned cvtpk(float lo, float hi) { f32x2 v = {lo, hi}; bf16x2v b = __builtin_convertvector(v, bf16x2v); return __builtin_bit_cast(unsigned, b); }
DI float rbf(float x) { return __uint_as_float(cvtpk(x, 0.f) << 16); }
DI srd_t make_srd(const void* p, unsigned bytes) { return __builtin_amdgcn_make_buffer_rsrc((void*)p, (short)0, (int)bytes, 0x00020000); }
DI void dma16(unsigned m0v, unsigned voff, srd_t srd, unsigned soff) {
  asm volatile("s_mov_b32 m0, %0\n\ts_nop 0\n\tbuffer_load_dwordx4 %1, %2, %3 offen sc1 lds" ::"s"(m0v), "v"(voff), "s"(srd), "s"(soff) : "m0", "memory");
}
template <int N> DI void wait_vm() { asm volatile("s_waitcnt vmcnt(%0)" ::"i"(N) : "memory"); }
DI void raw_barrier() { asm volatile("s_waitcnt lgkmcnt(0)\n\ts_barrier" ::: "memory"); }
DI unsigned lane_id() { unsigned l; asm volatile("v_mbcnt_lo_u32_b32 %0, -1, 0\n\tv_mbcnt_hi_u32_b32 %0, -1, %0" : "=v"(l)); return l; }
#define OWN_REGS() asm volatile("" ::: "v255", "a255")
#define KATTR __launch_bounds__(256, 1) __attribute__((amdgpu_num_vgpr(64)))

struct KVC { srd_t sa, sb; unsigned va, vb[2], soa, sob, m0w, a_rd[2][2], b_rd[2][4]; };
struct PC { srd_t tsrd; unsigned te[8], tl, tvo, tso, tch[2], tcs[4]; srd_t sx, sq, s1; unsigned vx, vq, v1, sox, soq, so1, m0x, m0q, m01, x_rd[2][2], xs_rd[2][2], q_rd[2][4], f_rd[2]; };
struct AC {
  unsigned kb_[8], vb_[4], vlin; srd_t srdK, srdV;
  unsigned m0w, sofw, soVA, soKA, soVB, soKB, u3, lim3; int t, n1, n2, row0; bool helper, first, pend;
  float mhat[2]; int pad0; float fct[2]; int pad1; float l_light[2]; int pad2; float rs[2][4], pmx[2][2];
};
struct OC { srd_t sa, sb; unsigned va[4], vb, m0w, a_rd[2][4], b_rd[2][2]; };
#define ACL64 "a0","a1","a2","a3","a4","a5","a6","a7","a8","a9","a10","a11","a12","a13","a14","a15","a16","a17","a18","a19","a20","a21","a22","a23","a24","a25","a26","a27","a28","a29","a30","a31","a32","a33","a34","a35","a36","a37","a38","a39","a40","a41","a42","a43","a44","a45","a46","a47","a48","a49","a50","a51","a52","a53","a54","a55","a56","a57","a58","a59","a60","a61","a62","a63"
DI void kv_pro(KVC& c) {
  asm volatile("s_add_u32 m0, %0, 0\n\ts_nop 0\n\tbuffer_load_dwordx4 %1, %2, %3 offen offset:0 lds" :  : "s"(c.m0w), "v"(c.va), "s"(c.sa), "s"(c.soa) : "memory", "m0", "scc");
  unsigned so1;
  asm volatile("s_add_u32 m0, %1, 1024\n\ts_add_u32 %0, %4, 16384\n\tbuffer_load_dwordx4 %2, %3, %0 offen offset:0 lds" : "=&s"(so1) : "s"(c.m0w), "v"(c.va), "s"(c.sa), "s"(c.soa) : "memory", "m0", "scc");
  unsigned so2;
  asm volatile("s_add_u32 m0, %1, 2048\n\ts_add_u32 %0, %4, 32768\n\tbuffer_load_dwordx4 %2, %3, %0 offen offset:0 lds" : "=&s"(so2) : "s"(c.m0w), "v"(c.va), "s"(c.sa), "s"(c.soa) : "memory", "m0", "scc");
  unsigned so3;
  asm volatile("s_add_u32 m0, %1, 3072\n\ts_add_u32 %0, %4, 49152\n\tbuffer_load_dwordx4 %2, %3, %0 offen offset:0 lds" : "=&s"(so3) : "s"(c.m0w), "v"(c.va), "s"(c.sa), "s"(c.soa) : "memory", "m0", "scc");
  asm volatile("s_add_u32 m0, %0, 16384\n\ts_nop 0\n\tbuffer_load_dwordx4 %1, %2, %3 offen offset:0 lds" :  : "s"(c.m0w), "v"(c.vb[0]), "s"(c.sb), "s"(c.sob) : "memory", "m0", "scc");
  asm volatile("s_add_u32 m0, %0, 17408\n\ts_nop 0\n\tbuffer_load_dwordx4 %1, %2, %3 offen offset:0 lds" :  : "s"(c.m0w), "v"(c.vb[1]), "s"(c.sb), "s"(c.sob) : "memory", "m0", "scc");
  unsigned so4;
  asm volatile("s_add_u32 m0, %1, 18432\n\ts_add_u32 %0, %4, 32768\n\tbuffer_load_dwordx4 %2, %3, %0 offen offset:0 lds" : "=&s"(so4) : "s"(c.m0w), "v"(c.vb[0]), "s"(c.sb), "s"(c.sob) : "memory", "m0", "scc");
  unsigned so5;
  asm volatile("s_add_u32 m0, %1, 19456\n\ts_add_u32 %0, %4, 32768\n\tbuffer_load_dwordx4 %2, %3, %0 offen offset:0 lds" : "=&s"(so5) : "s"(c.m0w), "v"(c.vb[1]), "s"(c.sb), "s"(c.sob) : "memory", "m0", "scc", ACL64);
  unsigned so6;
  asm volatile("s_add_u32 m0, %1, 32768\n\ts_add_u32 %0, %4, 64\n\tbuffer_load_dwordx4 %2, %3, %0 offen offset:0 lds" : "=&s"(so6) : "s"(c.m0w), "v"(c.va), "s"(c.sa), "s"(c.soa) : "memory", "m0", "scc");
  unsigned so7;
  asm volatile("s_add_u32 m0, %1, 33792\n\ts_add_u32 %0, %4, 16448\n\tbuffer_load_dwordx4 %2, %3, %0 offen offset:0 lds" : "=&s"(so7) : "s"(c.m0w), "v"(c.va), "s"(c.sa), "s"(c.soa) : "memory", "m0", "scc");
  unsigned so8;
  asm volatile("s_add_u32 m0, %1, 34816\n\ts_add_u32 %0, %4, 32832\n\tbuffer_load_dwordx4 %2, %3, %0 offen offset:0 lds" : "=&s"(so8) : "s"(c.m0w), "v"(c.va), "s"(c.sa), "s"(c.soa) : "memory", "m0", "scc");
  unsigned so9;
  asm volatile("s_add_u32 m0, %1, 35840\n\ts_add_u32 %0, %4, 49216\n\tbuffer_load_dwordx4 %2, %3, %0 offen offset:0 lds" : "=&s"(so9) : "s"(c.m0w), "v"(c.va), "s"(c.sa), "s"(c.soa) : "memory", "m0", "scc");
  unsigned so10;
  asm volatile("s_add_u32 m0, %1, 49152\n\ts_add_u32 %0, %4, 262144\n\tbuffer_load_dwordx4 %2, %3, %0 offen offset:0 lds" : "=&s"(so10) : "s"(c.m0w), "v"(c.vb[0]), "s"(c.sb), "s"(c.sob) : "memory", "m0", "scc");
  unsigned so11;
  asm volatile("s_add_u32 m0, %1, 50176\n\ts_add_u32 %0, %4, 262144\n\tbuffer_load_dwordx4 %2, %3, %0 offen offset:0 lds" : "=&s"(so11) : "s"(c.m0w), "v"(c.vb[1]), "s"(c.sb), "s"(c.sob) : "memory", "m0", "scc");
  unsigned so12;
  asm volatile("s_add_u32 m0, %1, 51200\n\ts_add_u32 %0, %4, 294912\n\tbuffer_load_dwordx4 %2, %3, %0 offen offset:0 lds" : "=&s"(so12) : "s"(c.m0w), "v"(c.vb[0]), "s"(c.sb), "s"(c.sob) : "memory", "m0", "scc");
  unsigned so13;
  asm volatile("s_add_u32 m0, %1, 52224\n\ts_add_u32 %0, %4, 294912\n\tbuffer_load_dwordx4 %2, %3, %0 offen offset:0 lds" : "=&s"(so13) : "s"(c.m0w), "v"(c.vb[1]), "s"(c.sb), "s"(c.sob) : "memory", "m0", "scc", ACL64);
  unsigned so14;
  asm volatile("s_add_u32 m0, %1, 65536\n\ts_add_u32 %0, %4, 128\n\tbuffer_load_dwordx4 %2, %3, %0 offen offset:0 lds" : "=&s"(so14) : "s"(c.m0w), "v"(c.va), "s"(c.sa), "s"(c.soa) : "memory", "m0", "scc");
  unsigned so15;
  asm volatile("s_add_u32 m0, %1, 66560\n\ts_add_u32 %0, %4, 16512\n\tbuffer_load_dwordx4 %2, %3, %0 offen offset:0 lds" : "=&s"(so15) : "s"(c.m0w), "v"(c.va), "s"(c.sa), "s"(c.soa) : "memory", "m0", "scc");
  unsigned so16;
  asm volatile("s_add_u32 m0, %1, 67584\n\ts_add_u32 %0, %4, 32896\n\tbuffer_load_dwordx4 %2, %3, %0 offen offset:0 lds" : "=&s"(so16) : "s"(c.m0w), "v"(c.va), "s"(c.sa), "s"(c.soa) : "memory", "m0", "scc");
  unsigned so17;
  asm volatile("s_add_u32 m0, %1, 68608\n\ts_add_u32 %0, %4, 49280\n\tbuffer_load_dwordx4 %2, %3, %0 offen offset:0 lds" : "=&s"(so17) : "s"(c.m0w), "v"(c.va), "s"(c.sa), "s"(c.soa) : "memory", "m0", "scc");
  unsigned so18;
  asm volatile("s_add_u32 m0, %1, 81920\n\ts_add_u32 %0, %4, 524288\n\tbuffer_load_dwordx4 %2, %3, %0 offen offset:0 lds" : "=&s"(so18) : "s"(c.m0w), "v"(c.vb[0]), "s"(c.sb), "s"(c.sob) : "memory", "m0", "scc");
  unsigned so19;
  asm volatile("s_add_u32 m0, %1, 82944\n\ts_add_u32 %0, %4, 524288\n\tbuffer_load_dwordx4 %2, %3, %0 offen offset:0 lds" : "=&s"(so19) : "s"(c.m0w), "v"(c.vb[1]), "s"(c.sb), "s"(c.sob) : "memory", "m0", "scc");
  unsigned so20;
  asm volatile("s_add_u32 m0, %1, 83968\n\ts_add_u32 %0, %4, 557056\n\tbuffer_load_dwordx4 %2, %3, %0 offen offset:0 lds" : "=&s"(so20) : "s"(c.m0w), "v"(c.vb[0]), "s"(c.sb), "s"(c.sob) : "memory", "m0", "scc");
  unsigned so21;
  asm volatile("s_add_u32 m0, %1, 84992\n\ts_add_u32 %0, %4, 557056\n\tbuffer_load_dwordx4 %2, %3, %0 offen offset:0 lds" : "=&s"(so21) : "s"(c.m0w), "v"(c.vb[1]), "s"(c.sb), "s"(c.sob) : "memory", "m0", "scc", ACL64);
}
#define KV_BODY_K(c, p) do { \
  asm volatile("ds_read_b64_tr_b16 v[80:81], %0 offset:16384\n\tds_read_b64_tr_b16 v[82:83], %0 offset:18432\n\tds_read_b128 v[64:67], %1 offset:0\n\tds_read_b64_tr_b16 v[84:85], %2 offset:16384\n\tds_read_b64_tr_b16 v[86:87], %2 offset:18432\n\tds_read_b64_tr_b16 v[88:89], %3 offset:16384\n\tds_read_b64_tr_b16 v[90:91], %3 offset:18432\n\tds_read_b64_tr_b16 v[92:93], %4 offset:16384\n\tds_read_b64_tr_b16 v[94:95], %4 offset:18432\n\tds_read_b128 v[68:71], %1 offset:2048\n\tds_read_b128 v[72:75], %1 offset:4096\n\tds_read_b128 v[76:79], %1 offset:6144" :  : "v"(c.b_rd[0][0]), "v"(c.a_rd[0][0]), "v"(c.b_rd[0][1]), "v"(c.b_rd[0][2]), "v"(c.b_rd[0][3]) : "memory"); \
  asm volatile("s_waitcnt vmcnt(8)\n\ts_barrier" ::: "memory"); \
  KV_HOOK(0); \
  asm volatile("s_waitcnt lgkmcnt(9)\n\tv_mfma_f32_32x32x16_bf16 a[0:15], v[80:83], v[64:67], 0\n\tds_read_b64_tr_b16 v[112:113], %0 offset:24576" :  : "v"(c.b_rd[0][0]) : "memory"); \
  unsigned so22; \
  asm volatile("s_waitcnt lgkmcnt(8)\n\tv_mfma_f32_32x32x16_bf16 a[16:31], v[84:87], v[64:67], 0\n\tds_read_b64_tr_b16 v[114:115], %1 offset:26624\n\ts_add_u32 m0, %2, 98304\n\ts_add_u32 %0, %5, 192\n\tbuffer_load_dwordx4 %3, %4, %0 offen offset:0 lds" : "=&s"(so22) : "v"(c.b_rd[0][0]), "s"(c.m0w), "v"(c.va), "s"(c.sa), "s"(c.soa) : "memory", "m0", "scc"); \
  asm volatile("s_waitcnt lgkmcnt(7)\n\tv_mfma_f32_32x32x16_bf16 a[32:47], v[88:91], v[64:67], 0\n\tds_read_b128 v[96:99], %0 offset:0" :  : "v"(c.a_rd[0][1]) : "memory"); \
  asm volatile("s_waitcnt lgkmcnt(6)\n\tv_mfma_f32_32x32x16_bf16 a[48:63], v[92:95], v[64:67], 0\n\tds_read_b64_tr_b16 v[116:117], %0 offset:24576" :  : "v"(c.b_rd[0][1]) : "memory"); \
  asm volatile("s_waitcnt lgkmcnt(6)\n\tv_mfma_f32_32x32x16_bf16 a[64:79], v[80:83], v[68:71], 0\n\tds_read_b64_tr_b16 v[118:119], %0 offset:26624" :  : "v"(c.b_rd[0][1]) : "memory"); \
  unsigned so23; \
  asm volatile("v_mfma_f32_32x32x16_bf16 a[80:95], v[84:87], v[68:71], 0\n\tds_read_b64_tr_b16 v[120:121], %1 offset:24576\n\ts_add_u32 m0, %2, 99328\n\ts_add_u32 %0, %5, 16576\n\tbuffer_load_dwordx4 %3, %4, %0 offen offset:0 lds" : "=&s"(so23) : "v"(c.b_rd[0][2]), "s"(c.m0w), "v"(c.va), "s"(c.sa), "s"(c.soa) : "memory", "m0", "scc"); \
  asm volatile("v_mfma_f32_32x32x16_bf16 a[96:111], v[88:91], v[68:71], 0\n\tds_read_b64_tr_b16 v[122:123], %0 offset:26624" :  : "v"(c.b_rd[0][2]) : "memory", ACL64); \
  asm volatile("v_mfma_f32_32x32x16_bf16 a[112:127], v[92:95], v[68:71], 0\n\tds_read_b64_tr_b16 v[124:125], %0 offset:24576" :  : "v"(c.b_rd[0][3]) : "memory"); \
  asm volatile("s_waitcnt lgkmcnt(9)\n\tv_mfma_f32_32x32x16_bf16 a[128:143], v[80:83], v[72:75], 0\n\tds_read_b64_tr_b16 v[126:127], %0 offset:26624" :  : "v"(c.b_rd[0][3]) : "memory"); \
  unsigned so24; \
  asm volatile("v_mfma_f32_32x32x16_bf16 a[144:159], v[84:87], v[72:75], 0\n\tds_read_b128 v[100:103], %1 offset:2048\n\ts_add_u32 m0, %2, 100352\n\ts_add_u32 %0, %5, 32960\n\tbuffer_load_dwordx4 %3, %4, %0 offen offset:0 lds" : "=&s"(so24) : "v"(c.a_rd[0][1]), "s"(c.m0w), "v"(c.va), "s"(c.sa), "s"(c.soa) : "memory", "m0", "scc"); \
  asm volatile("v_mfma_f32_32x32x16_bf16 a[160:175], v[88:91], v[72:75], 0\n\tds_read_b128 v[104:107], %0 offset:4096" :  : "v"(c.a_rd[0][1]) : "memory"); \
  asm volatile("v_mfma_f32_32x32x16_bf16 a[176:191], v[92:95], v[72:75], 0\n\tds_read_b128 v[108:111], %0 offset:6144" :  : "v"(c.a_rd[0][1]) : "memory"); \
  asm volatile("s_waitcnt lgkmcnt(12)\n\tv_mfma_f32_32x32x16_bf16 a[192:207], v[80:83], v[76:79], 0" :  :  : "memory"); \
  unsigned so25; \
  asm volatile("v_mfma_f32_32x32x16_bf16 a[208:223], v[84:87], v[76:79], 0\n\ts_add_u32 m0, %1, 101376\n\ts_add_u32 %0, %4, 49344\n\tbuffer_load_dwordx4 %2, %3, %0 offen offset:0 lds" : "=&s"(so25) : "s"(c.m0w), "v"(c.va), "s"(c.sa), "s"(c.soa) : "memory", "m0", "scc"); \
  asm volatile("v_mfma_f32_32x32x16_bf16 a[224:239], v[88:91], v[76:79], 0" :  :  : "memory", ACL64); \
  asm volatile("v_mfma_f32_32x32x16_bf16 a[240:255], v[92:95], v[76:79], 0" :  :  : "memory"); \
  asm volatile("s_waitcnt lgkmcnt(9)\n\tv_mfma_f32_32x32x16_bf16 a[0:15], v[112:115], v[96:99], a[0:15]\n\tds_read_b64_tr_b16 v[80:81], %0 offset:49152" :  : "v"(c.b_rd[0][0]) : "memory"); \
  unsigned so26; \
  asm volatile("s_waitcnt lgkmcnt(8)\n\tv_mfma_f32_32x32x16_bf16 a[16:31], v[116:119], v[96:99], a[16:31]\n\tds_read_b64_tr_b16 v[82:83], %1 offset:51200\n\ts_add_u32 m0, %2, 114688\n\ts_add_u32 %0, %5, 786432\n\tbuffer_load_dwordx4 %3, %4, %0 offen offset:0 lds" : "=&s"(so26) : "v"(c.b_rd[0][0]), "s"(c.m0w), "v"(c.vb[0]), "s"(c.sb), "s"(c.sob) : "memory", "m0", "scc"); \
  asm volatile("s_waitcnt lgkmcnt(7)\n\tv_mfma_f32_32x32x16_bf16 a[32:47], v[120:123], v[96:99], a[32:47]\n\tds_read_b128 v[64:67], %0 offset:32768" :  : "v"(c.a_rd[0][0]) : "memory"); \
  asm volatile("s_waitcnt lgkmcnt(6)\n\tv_mfma_f32_32x32x16_bf16 a[48:63], v[124:127], v[96:99], a[48:63]\n\tds_read_b64_tr_b16 v[84:85], %0 offset:49152" :  : "v"(c.b_rd[0][1]) : "memory"); \
  asm volatile("s_waitcnt lgkmcnt(6)\n\tv_mfma_f32_32x32x16_bf16 a[64:79], v[112:115], v[100:103], a[64:79]\n\tds_read_b64_tr_b16 v[86:87], %0 offset:51200" :  : "v"(c.b_rd[0][1]) : "memory"); \
  unsigned so27; \
  asm volatile("v_mfma_f32_32x32x16_bf16 a[80:95], v[116:119], v[100:103], a[80:95]\n\tds_read_b64_tr_b16 v[88:89], %1 offset:49152\n\ts_add_u32 m0, %2, 115712\n\ts_add_u32 %0, %5, 786432\n\tbuffer_load_dwordx4 %3, %4, %0 offen offset:0 lds" : "=&s"(so27) : "v"(c.b_rd[0][2]), "s"(c.m0w), "v"(c.vb[1]), "s"(c.sb), "s"(c.sob) : "memory", "m0", "scc"); \
  asm volatile("v_mfma_f32_32x32x16_bf16 a[96:111], v[120:123], v[100:103], a[96:111]\n\tds_read_b64_tr_b16 v[90:91], %0 offset:51200" :  : "v"(c.b_rd[0][2]) : "memory", ACL64); \
  asm volatile("v_mfma_f32_32x32x16_bf16 a[112:127], v[124:127], v[100:103], a[112:127]\n\tds_read_b64_tr_b16 v[92:93], %0 offset:49152" :  : "v"(c.b_rd[0][3]) : "memory"); \
  asm volatile("s_waitcnt lgkmcnt(9)\n\tv_mfma_f32_32x32x16_bf16 a[128:143], v[112:115], v[104:107], a[128:143]\n\tds_read_b64_tr_b16 v[94:95], %0 offset:51200" :  : "v"(c.b_rd[0][3]) : "memory"); \
  unsigned so28; \
  asm volatile("v_mfma_f32_32x32x16_bf16 a[144:159], v[116:119], v[104:107], a[144:159]\n\tds_read_b128 v[68:71], %1 offset:34816\n\ts_add_u32 m0, %2, 116736\n\ts_add_u32 %0, %5, 819200\n\tbuffer_load_dwordx4 %3, %4, %0 offen offset:0 lds" : "=&s"(so28) : "v"(c.a_rd[0][0]), "s"(c.m0w), "v"(c.vb[0]), "s"(c.sb), "s"(c.sob) : "memory", "m0", "scc"); \
  asm volatile("v_mfma_f32_32x32x16_bf16 a[160:175], v[120:123], v[104:107], a[160:175]\n\tds_read_b128 v[72:75], %0 offset:36864" :  : "v"(c.a_rd[0][0]) : "memory"); \
  asm volatile("v_mfma_f32_32x32x16_bf16 a[176:191], v[124:127], v[104:107], a[176:191]\n\tds_read_b128 v[76:79], %0 offset:38912" :  : "v"(c.a_rd[0][0]) : "memory"); \
  asm volatile("s_waitcnt lgkmcnt(12)\n\tv_mfma_f32_32x32x16_bf16 a[192:207], v[112:115], v[108:111], a[192:207]" :  :  : "memory"); \
  unsigned so29; \
  asm volatile("v_mfma_f32_32x32x16_bf16 a[208:223], v[116:119], v[108:111], a[208:223]\n\ts_add_u32 m0, %1, 117760\n\ts_add_u32 %0, %4, 819200\n\tbuffer_load_dwordx4 %2, %3, %0 offen offset:0 lds" : "=&s"(so29) : "s"(c.m0w), "v"(c.vb[1]), "s"(c.sb), "s"(c.sob) : "memory", "m0", "scc"); \
  asm volatile("v_mfma_f32_32x32x16_bf16 a[224:239], v[120:123], v[108:111], a[224:239]" :  :  : "memory", ACL64); \
  asm volatile("v_mfma_f32_32x32x16_bf16 a[240:255], v[124:127], v[108:111], a[240:255]" :  :  : "memory"); \
  asm volatile("s_waitcnt vmcnt(8)\n\ts_barrier" ::: "memory"); \
  KV_HOOK(1); \
  asm volatile("s_waitcnt lgkmcnt(9)\n\tv_mfma_f32_32x32x16_bf16 a[0:15], v[80:83], v[64:67], a[0:15]\n\tds_read_b64_tr_b16 v[112:113], %0 offset:57344" :  : "v"(c.b_rd[0][0]) : "memory"); \
  unsigned so30; \
  asm volatile("s_waitcnt lgkmcnt(8)\n\tv_mfma_f32_32x32x16_bf16 a[16:31], v[84:87], v[64:67], a[16:31]\n\tds_read_b64_tr_b16 v[114:115], %1 offset:59392\n\ts_add_u32 m0, %2, 0\n\ts_add_u32 %0, %5, 256\n\tbuffer_load_dwordx4 %3, %4, %0 offen offset:0 lds" : "=&s"(so30) : "v"(c.b_rd[0][0]), "s"(c.m0w), "v"(c.va), "s"(c.sa), "s"(c.soa) : "memory", "m0", "scc"); \
  asm volatile("s_waitcnt lgkmcnt(7)\n\tv_mfma_f32_32x32x16_bf16 a[32:47], v[88:91], v[64:67], a[32:47]\n\tds_read_b128 v[96:99], %0 offset:32768" :  : "v"(c.a_rd[0][1]) : "memory"); \
  asm volatile("s_waitcnt lgkmcnt(6)\n\tv_mfma_f32_32x32x16_bf16 a[48:63], v[92:95], v[64:67], a[48:63]\n\tds_read_b64_tr_b16 v[116:117], %0 offset:57344" :  : "v"(c.b_rd[0][1]) : "memory"); \
  asm volatile("s_waitcnt lgkmcnt(6)\n\tv_mfma_f32_32x32x16_bf16 a[64:79], v[80:83], v[68:71], a[64:79]\n\tds_read_b64_tr_b16 v[118:119], %0 offset:59392" :  : "v"(c.b_rd[0][1]) : "memory"); \
  unsigned so31; \
  asm volatile("v_mfma_f32_32x32x16_bf16 a[80:95], v[84:87], v[68:71], a[80:95]\n\tds_read_b64_tr_b16 v[120:121], %1 offset:57344\n\ts_add_u32 m0, %2, 1024\n\ts_add_u32 %0, %5, 16640\n\tbuffer_load_dwordx4 %3, %4, %0 offen offset:0 lds" : "=&s"(so31) : "v"(c.b_rd[0][2]), "s"(c.m0w), "v"(c.va), "s"(c.sa), "s"(c.soa) : "memory", "m0", "scc"); \
  asm volatile("v_mfma_f32_32x32x16_bf16 a[96:111], v[88:91], v[68:71], a[96:111]\n\tds_read_b64_tr_b16 v[122:123], %0 offset:59392" :  : "v"(c.b_rd[0][2]) : "memory", ACL64); \
  asm volatile("v_mfma_f32_32x32x16_bf16 a[112:127], v[92:95], v[68:71], a[112:127]\n\tds_read_b64_tr_b16 v[124:125], %0 offset:57344" :  : "v"(c.b_rd[0][3]) : "memory"); \
  asm volatile("s_waitcnt lgkmcnt(9)\n\tv_mfma_f32_32x32x16_bf16 a[128:143], v[80:83], v[72:75], a[128:143]\n\tds_read_b64_tr_b16 v[126:127], %0 offset:59392" :  : "v"(c.b_rd[0][3]) : "memory"); \
  unsigned so32; \
  asm volatile("v_mfma_f32_32x32x16_bf16 a[144:159], v[84:87], v[72:75], a[144:159]\n\tds_read_b128 v[100:103], %1 offset:34816\n\ts_add_u32 m0, %2, 2048\n\ts_add_u32 %0, %5, 33024\n\tbuffer_load_dwordx4 %3, %4, %0 offen offset:0 lds" : "=&s"(so32) : "v"(c.a_rd[0][1]), "s"(c.m0w), "v"(c.va), "s"(c.sa), "s"(c.soa) : "memory", "m0", "scc"); \
  asm volatile("v_mfma_f32_32x32x16_bf16 a[160:175], v[88:91], v[72:75], a[160:175]\n\tds_read_b128 v[104:107], %0 offset:36864" :  : "v"(c.a_rd[0][1]) : "memory"); \
  asm volatile("v_mfma_f32_32x32x16_bf16 a[176:191], v[92:95], v[72:75], a[176:191]\n\tds_read_b128 v[108:111], %0 offset:38912" :  : "v"(c.a_rd[0][1]) : "memory"); \
  asm volatile("s_waitcnt lgkmcnt(12)\n\tv_mfma_f32_32x32x16_bf16 a[192:207], v[80:83], v[76:79], a[192:207]" :  :  : "memory"); \
  unsigned so33; \
  asm volatile("v_mfma_f32_32x32x16_bf16 a[208:223], v[84:87], v[76:79], a[208:223]\n\ts_add_u32 m0, %1, 3072\n\ts_add_u32 %0, %4, 49408\n\tbuffer_load_dwordx4 %2, %3, %0 offen offset:0 lds" : "=&s"(so33) : "s"(c.m0w), "v"(c.va), "s"(c.sa), "s"(c.soa) : "memory", "m0", "scc"); \
  asm volatile("v_mfma_f32_32x32x16_bf16 a[224:239], v[88:91], v[76:79], a[224:239]" :  :  : "memory", ACL64); \
  asm volatile("v_mfma_f32_32x32x16_bf16 a[240:255], v[92:95], v[76:79], a[240:255]" :  :  : "memory"); \
  asm volatile("s_waitcnt lgkmcnt(9)\n\tv_mfma_f32_32x32x16_bf16 a[0:15], v[112:115], v[96:99], a[0:15]\n\tds_read_b64_tr_b16 v[80:81], %0 offset:16384" :  : "v"(c.b_rd[1][0]) : "memory"); \
  unsigned so34; \
  asm volatile("s_waitcnt lgkmcnt(8)\n\tv_mfma_f32_32x32x16_bf16 a[16:31], v[116:119], v[96:99], a[16:31]\n\tds_read_b64_tr_b16 v[82:83], %1 offset:18432\n\ts_add_u32 m0, %2, 16384\n\ts_add_u32 %0, %5, 1048576\n\tbuffer_load_dwordx4 %3, %4, %0 offen offset:0 lds" : "=&s"(so34) : "v"(c.b_rd[1][0]), "s"(c.m0w), "v"(c.vb[0]), "s"(c.sb), "s"(c.sob) : "memory", "m0", "scc"); \
  asm volatile("s_waitcnt lgkmcnt(7)\n\tv_mfma_f32_32x32x16_bf16 a[32:47], v[120:123], v[96:99], a[32:47]\n\tds_read_b128 v[64:67], %0 offset:0" :  : "v"(c.a_rd[1][0]) : "memory"); \
  asm volatile("s_waitcnt lgkmcnt(6)\n\tv_mfma_f32_32x32x16_bf16 a[48:63], v[124:127], v[96:99], a[48:63]\n\tds_read_b64_tr_b16 v[84:85], %0 offset:16384" :  : "v"(c.b_rd[1][1]) : "memory"); \
  asm volatile("s_waitcnt lgkmcnt(6)\n\tv_mfma_f32_32x32x16_bf16 a[64:79], v[112:115], v[100:103], a[64:79]\n\tds_read_b64_tr_b16 v[86:87], %0 offset:18432" :  : "v"(c.b_rd[1][1]) : "memory"); \
  unsigned so35; \
  asm volatile("v_mfma_f32_32x32x16_bf16 a[80:95], v[116:119], v[100:103], a[80:95]\n\tds_read_b64_tr_b16 v[88:89], %1 offset:16384\n\ts_add_u32 m0, %2, 17408\n\ts_add_u32 %0, %5, 1048576\n\tbuffer_load_dwordx4 %3, %4, %0 offen offset:0 lds" : "=&s"(so35) : "v"(c.b_rd[1][2]), "s"(c.m0w), "v"(c.vb[1]), "s"(c.sb), "s"(c.sob) : "memory", "m0", "scc"); \
  asm volatile("v_mfma_f32_32x32x16_bf16 a[96:111], v[120:123], v[100:103], a[96:111]\n\tds_read_b64_tr_b16 v[90:91], %0 offset:18432" :  : "v"(c.b_rd[1][2]) : "memory", ACL64); \
  asm volatile("v_mfma_f32_32x32x16_bf16 a[112:127], v[124:127], v[100:103], a[112:127]\n\tds_read_b64_tr_b16 v[92:93], %0 offset:16384" :  : "v"(c.b_rd[1][3]) : "memory"); \
  asm volatile("s_waitcnt lgkmcnt(9)\n\tv_mfma_f32_32x32x16_bf16 a[128:143], v[112:115], v[104:107], a[128:143]\n\tds_read_b64_tr_b16 v[94:95], %0 offset:18432" :  : "v"(c.b_rd[1][3]) : "memory"); \
  unsigned so36; \
  asm volatile("v_mfma_f32_32x32x16_bf16 a[144:159], v[116:119], v[104:107], a[144:159]\n\tds_read_b128 v[68:71], %1 offset:2048\n\ts_add_u32 m0, %2, 18432\n\ts_add_u32 %0, %5, 1081344\n\tbuffer_load_dwordx4 %3, %4, %0 offen offset:0 lds" : "=&s"(so36) : "v"(c.a_rd[1][0]), "s"(c.m0w), "v"(c.vb[0]), "s"(c.sb), "s"(c.sob) : "memory", "m0", "scc"); \
  asm volatile("v_mfma_f32_32x32x16_bf16 a[160:175], v[120:123], v[104:107], a[160:175]\n\tds_read_b128 v[72:75], %0 offset:4096" :  : "v"(c.a_rd[1][0]) : "memory"); \
  asm volatile("v_mfma_f32_32x32x16_bf16 a[176:191], v[124:127], v[104:107], a[176:191]\n\tds_read_b128 v[76:79], %0 offset:6144" :  : "v"(c.a_rd[1][0]) : "memory"); \
  asm volatile("s_waitcnt lgkmcnt(12)\n\tv_mfma_f32_32x32x16_bf16 a[192:207], v[112:115], v[108:111], a[192:207]" :  :  : "memory"); \
  unsigned so37; \
  asm volatile("v_mfma_f32_32x32x16_bf16 a[208:223], v[116:119], v[108:111], a[208:223]\n\ts_add_u32 m0, %1, 19456\n\ts_add_u32 %0, %4, 1081344\n\tbuffer_load_dwordx4 %2, %3, %0 offen offset:0 lds" : "=&s"(so37) : "s"(c.m0w), "v"(c.vb[1]), "s"(c.sb), "s"(c.sob) : "memory", "m0", "scc"); \
  asm volatile("v_mfma_f32_32x32x16_bf16 a[224:239], v[120:123], v[108:111], a[224:239]" :  :  : "memory", ACL64); \
  asm volatile("v_mfma_f32_32x32x16_bf16 a[240:255], v[124:127], v[108:111], a[240:255]" :  :  : "memory"); \
  asm volatile("s_waitcnt vmcnt(8)\n\ts_barrier" ::: "memory"); \
  KV_HOOK(2); \
  asm volatile("s_waitcnt lgkmcnt(9)\n\tv_mfma_f32_32x32x16_bf16 a[0:15], v[80:83], v[64:67], a[0:15]\n\tds_read_b64_tr_b16 v[112:113], %0 offset:24576" :  : "v"(c.b_rd[1][0]) : "memory"); \
  unsigned so38; \
  asm volatile("s_waitcnt lgkmcnt(8)\n\tv_mfma_f32_32x32x16_bf16 a[16:31], v[84:87], v[64:67], a[16:31]\n\tds_read_b64_tr_b16 v[114:115], %1 offset:26624\n\ts_add_u32 m0, %2, 32768\n\ts_add_u32 %0, %5, 320\n\tbuffer_load_dwordx4 %3, %4, %0 offen offset:0 lds" : "=&s"(so38) : "v"(c.b_rd[1][0]), "s"(c.m0w), "v"(c.va), "s"(c.sa), "s"(c.soa) : "memory", "m0", "scc"); \
  asm volatile("s_waitcnt lgkmcnt(7)\n\tv_mfma_f32_32x32x16_bf16 a[32:47], v[88:91], v[64:67], a[32:47]\n\tds_read_b128 v[96:99], %0 offset:0" :  : "v"(c.a_rd[1][1]) : "memory"); \
  asm volatile("s_waitcnt lgkmcnt(6)\n\tv_mfma_f32_32x32x16_bf16 a[48:63], v[92:95], v[64:67], a[48:63]\n\tds_read_b64_tr_b16 v[116:117], %0 offset:24576" :  : "v"(c.b_rd[1][1]) : "memory"); \
  asm volatile("s_waitcnt lgkmcnt(6)\n\tv_mfma_f32_32x32x16_bf16 a[64:79], v[80:83], v[68:71], a[64:79]\n\tds_read_b64_tr_b16 v[118:119], %0 offset:26624" :  : "v"(c.b_rd[1][1]) : "memory"); \
  unsigned so39; \
  asm volatile("v_mfma_f32_32x32x16_bf16 a[80:95], v[84:87], v[68:71], a[80:95]\n\tds_read_b64_tr_b16 v[120:121], %1 offset:24576\n\ts_add_u32 m0, %2, 33792\n\ts_add_u32 %0, %5, 16704\n\tbuffer_load_dwordx4 %3, %4, %0 offen offset:0 lds" : "=&s"(so39) : "v"(c.b_rd[1][2]), "s"(c.m0w), "v"(c.va), "s"(c.sa), "s"(c.soa) : "memory", "m0", "scc"); \
  asm volatile("v_mfma_f32_32x32x16_bf16 a[96:111], v[88:91], v[68:71], a[96:111]\n\tds_read_b64_tr_b16 v[122:123], %0 offset:26624" :  : "v"(c.b_rd[1][2]) : "memory", ACL64); \
  asm volatile("v_mfma_f32_32x32x16_bf16 a[112:127], v[92:95], v[68:71], a[112:127]\n\tds_read_b64_tr_b16 v[124:125], %0 offset:24576" :  : "v"(c.b_rd[1][3]) : "memory"); \
  asm volatile("s_waitcnt lgkmcnt(9)\n\tv_mfma_f32_32x32x16_bf16 a[128:143], v[80:83], v[72:75], a[128:143]\n\tds_read_b64_tr_b16 v[126:127], %0 offset:26624" :  : "v"(c.b_rd[1][3]) : "memory"); \
  unsigned so40; \
  asm volatile("v_mfma_f32_32x32x16_bf16 a[144:159], v[84:87], v[72:75], a[144:159]\n\tds_read_b128 v[100:103], %1 offset:2048\n\ts_add_u32 m0, %2, 34816\n\ts_add_u32 %0, %5, 33088\n\tbuffer_load_dwordx4 %3, %4, %0 offen offset:0 lds" : "=&s"(so40) : "v"(c.a_rd[1][1]), "s"(c.m0w), "v"(c.va), "s"(c.sa), "s"(c.soa) : "memory", "m0", "scc"); \
  asm volatile("v_mfma_f32_32x32x16_bf16 a[160:175], v[88:91], v[72:75], a[160:175]\n\tds_read_b128 v[104:107], %0 offset:4096" :  : "v"(c.a_rd[1][1]) : "memory"); \
  asm volatile("v_mfma_f32_32x32x16_bf16 a[176:191], v[92:95], v[72:75], a[176:191]\n\tds_read_b128 v[108:111], %0 offset:6144" :  : "v"(c.a_rd[1][1]) : "memory"); \
  asm volatile("s_waitcnt lgkmcnt(12)\n\tv_mfma_f32_32x32x16_bf16 a[192:207], v[80:83], v[76:79], a[192:207]" :  :  : "memory"); \
  unsigned so41; \
  asm volatile("v_mfma_f32_32x32x16_bf16 a[208:223], v[84:87], v[76:79], a[208:223]\n\ts_add_u32 m0, %1, 35840\n\ts_add_u32 %0, %4, 49472\n\tbuffer_load_dwordx4 %2, %3, %0 offen offset:0 lds" : "=&s"(so41) : "s"(c.m0w), "v"(c.va), "s"(c.sa), "s"(c.soa) : "memory", "m0", "scc"); \
  asm volatile("v_mfma_f32_32x32x16_bf16 a[224:239], v[88:91], v[76:79], a[224:239]" :  :  : "memory", ACL64); \
  asm volatile("v_mfma_f32_32x32x16_bf16 a[240:255], v[92:95], v[76:79], a[240:255]" :  :  : "memory"); \
  asm volatile("s_waitcnt lgkmcnt(9)\n\tv_mfma_f32_32x32x16_bf16 a[0:15], v[112:115], v[96:99], a[0:15]\n\tds_read_b64_tr_b16 v[80:81], %0 offset:49152" :  : "v"(c.b_rd[1][0]) : "memory"); \
  unsigned so42; \
  asm volatile("s_waitcnt lgkmcnt(8)\n\tv_mfma_f32_32x32x16_bf16 a[16:31], v[116:119], v[96:99], a[16:31]\n\tds_read_b64_tr_b16 v[82:83], %1 offset:51200\n\ts_add_u32 m0, %2, 49152\n\ts_add_u32 %0, %5, 1310720\n\tbuffer_load_dwordx4 %3, %4, %0 offen offset:0 lds" : "=&s"(so42) : "v"(c.b_rd[1][0]), "s"(c.m0w), "v"(c.vb[0]), "s"(c.sb), "s"(c.sob) : "memory", "m0", "scc"); \
  asm volatile("s_waitcnt lgkmcnt(7)\n\tv_mfma_f32_32x32x16_bf16 a[32:47], v[120:123], v[96:99], a[32:47]\n\tds_read_b128 v[64:67], %0 offset:32768" :  : "v"(c.a_rd[1][0]) : "memory"); \
  asm volatile("s_waitcnt lgkmcnt(6)\n\tv_mfma_f32_32x32x16_bf16 a[48:63], v[124:127], v[96:99], a[48:63]\n\tds_read_b64_tr_b16 v[84:85], %0 offset:49152" :  : "v"(c.b_rd[1][1]) : "memory"); \
  asm volatile("s_waitcnt lgkmcnt(6)\n\tv_mfma_f32_32x32x16_bf16 a[64:79], v[112:115], v[100:103], a[64:79]\n\tds_read_b64_tr_b16 v[86:87], %0 offset:51200" :  : "v"(c.b_rd[1][1]) : "memory"); \
  unsigned so43; \
  asm volatile("v_mfma_f32_32x32x16_bf16 a[80:95], v[116:119], v[100:103], a[80:95]\n\tds_read_b64_tr_b16 v[88:89], %1 offset:49152\n\ts_add_u32 m0, %2, 50176\n\ts_add_u32 %0, %5, 1310720\n\tbuffer_load_dwordx4 %3, %4, %0 offen offset:0 lds" : "=&s"(so43) : "v"(c.b_rd[1][2]), "s"(c.m0w), "v"(c.vb[1]), "s"(c.sb), "s"(c.sob) : "memory", "m0", "scc"); \
  asm volatile("v_mfma_f32_32x32x16_bf16 a[96:111], v[120:123], v[100:103], a[96:111]\n\tds_read_b64_tr_b16 v[90:91], %0 offset:51200" :  : "v"(c.b_rd[1][2]) : "memory", ACL64); \
  asm volatile("v_mfma_f32_32x32x16_bf16 a[112:127], v[124:127], v[100:103], a[112:127]\n\tds_read_b64_tr_b16 v[92:93], %0 offset:49152" :  : "v"(c.b_rd[1][3]) : "memory"); \
  asm volatile("s_waitcnt lgkmcnt(9)\n\tv_mfma_f32_32x32x16_bf16 a[128:143], v[112:115], v[104:107], a[128:143]\n\tds_read_b64_tr_b16 v[94:95], %0 offset:51200" :  : "v"(c.b_rd[1][3]) : "memory"); \
  unsigned so44; \
  asm volatile("v_mfma_f32_32x32x16_bf16 a[144:159], v[116:119], v[104:107], a[144:159]\n\tds_read_b128 v[68:71], %1 offset:34816\n\ts_add_u32 m0, %2, 51200\n\ts_add_u32 %0, %5, 1343488\n\tbuffer_load_dwordx4 %3, %4, %0 offen offset:0 lds" : "=&s"(so44) : "v"(c.a_rd[1][0]), "s"(c.m0w), "v"(c.vb[0]), "s"(c.sb), "s"(c.sob) : "memory", "m0", "scc"); \
  asm volatile("v_mfma_f32_32x32x16_bf16 a[160:175], v[120:123], v[104:107], a[160:175]\n\tds_read_b128 v[72:75], %0 offset:36864" :  : "v"(c.a_rd[1][0]) : "memory"); \
  asm volatile("v_mfma_f32_32x32x16_bf16 a[176:191], v[124:127], v[104:107], a[176:191]\n\tds_read_b128 v[76:79], %0 offset:38912" :  : "v"(c.a_rd[1][0]) : "memory"); \
  asm volatile("s_waitcnt lgkmcnt(12)\n\tv_mfma_f32_32x32x16_bf16 a[192:207], v[112:115], v[108:111], a[192:207]" :  :  : "memory"); \
  unsigned so45; \
  asm volatile("v_mfma_f32_32x32x16_bf16 a[208:223], v[116:119], v[108:111], a[208:223]\n\ts_add_u32 m0, %1, 52224\n\ts_add_u32 %0, %4, 1343488\n\tbuffer_load_dwordx4 %2, %3, %0 offen offset:0 lds" : "=&s"(so45) : "s"(c.m0w), "v"(c.vb[1]), "s"(c.sb), "s"(c.sob) : "memory", "m0", "scc"); \
  asm volatile("v_mfma_f32_32x32x16_bf16 a[224:239], v[120:123], v[108:111], a[224:239]" :  :  : "memory", ACL64); \
  asm volatile("v_mfma_f32_32x32x16_bf16 a[240:255], v[124:127], v[108:111], a[240:255]" :  :  : "memory"); \
  asm volatile("s_waitcnt vmcnt(8)\n\ts_barrier" ::: "memory"); \
  KV_HOOK(3); \
  asm volatile("s_waitcnt lgkmcnt(9)\n\tv_mfma_f32_32x32x16_bf16 a[0:15], v[80:83], v[64:67], a[0:15]\n\tds_read_b64_tr_b16 v[112:113], %0 offset:57344" :  : "v"(c.b_rd[1][0]) : "memory"); \
  unsigned so46; \
  asm volatile("s_waitcnt lgkmcnt(8)\n\tv_mfma_f32_32x32x16_bf16 a[16:31], v[84:87], v[64:67], a[16:31]\n\tds_read_b64_tr_b16 v[114:115], %1 offset:59392\n\ts_add_u32 m0, %2, 65536\n\ts_add_u32 %0, %5, 384\n\tbuffer_load_dwordx4 %3, %4, %0 offen offset:0 lds" : "=&s"(so46) : "v"(c.b_rd[1][0]), "s"(c.m0w), "v"(c.va), "s"(c.sa), "s"(c.soa) : "memory", "m0", "scc"); \
  asm volatile("s_waitcnt lgkmcnt(7)\n\tv_mfma_f32_32x32x16_bf16 a[32:47], v[88:91], v[64:67], a[32:47]\n\tds_read_b128 v[96:99], %0 offset:32768" :  : "v"(c.a_rd[1][1]) : "memory"); \
  asm volatile("s_waitcnt lgkmcnt(6)\n\tv_mfma_f32_32x32x16_bf16 a[48:63], v[92:95], v[64:67], a[48:63]\n\tds_read_b64_tr_b16 v[116:117], %0 offset:57344" :  : "v"(c.b_rd[1][1]) : "memory"); \
  asm volatile("s_waitcnt lgkmcnt(6)\n\tv_mfma_f32_32x32x16_bf16 a[64:79], v[80:83], v[68:71], a[64:79]\n\tds_read_b64_tr_b16 v[118:119], %0 offset:59392" :  : "v"(c.b_rd[1][1]) : "memory"); \
  unsigned so47; \
  asm volatile("v_mfma_f32_32x32x16_bf16 a[80:95], v[84:87], v[68:71], a[80:95]\n\tds_read_b64_tr_b16 v[120:121], %1 offset:57344\n\ts_add_u32 m0, %2, 66560\n\ts_add_u32 %0, %5, 16768\n\tbuffer_load_dwordx4 %3, %4, %0 offen offset:0 lds" : "=&s"(so47) : "v"(c.b_rd[1][2]), "s"(c.m0w), "v"(c.va), "s"(c.sa), "s"(c.soa) : "memory", "m0", "scc"); \
  asm volatile("v_mfma_f32_32x32x16_bf16 a[96:111], v[88:91], v[68:71], a[96:111]\n\tds_read_b64_tr_b16 v[122:123], %0 offset:59392" :  : "v"(c.b_rd[1][2]) : "memory", ACL64); \
  asm volatile("v_mfma_f32_32x32x16_bf16 a[112:127], v[92:95], v[68:71], a[112:127]\n\tds_read_b64_tr_b16 v[124:125], %0 offset:57344" :  : "v"(c.b_rd[1][3]) : "memory"); \
  asm volatile("s_waitcnt lgkmcnt(9)\n\tv_mfma_f32_32x32x16_bf16 a[128:143], v[80:83], v[72:75], a[128:143]\n\tds_read_b64_tr_b16 v[126:127], %0 offset:59392" :  : "v"(c.b_rd[1][3]) : "memory"); \
  unsigned so48; \
  asm volatile("v_mfma_f32_32x32x16_bf16 a[144:159], v[84:87], v[72:75], a[144:159]\n\tds_read_b128 v[100:103], %1 offset:34816\n\ts_add_u32 m0, %2, 67584\n\ts_add_u32 %0, %5, 33152\n\tbuffer_load_dwordx4 %3, %4, %0 offen offset:0 lds" : "=&s"(so48) : "v"(c.a_rd[1][1]), "s"(c.m0w), "v"(c.va), "s"(c.sa), "s"(c.soa) : "memory", "m0", "scc"); \
  asm volatile("v_mfma_f32_32x32x16_bf16 a[160:175], v[88:91], v[72:75], a[160:175]\n\tds_read_b128 v[104:107], %0 offset:36864" :  : "v"(c.a_rd[1][1]) : "memory"); \
  asm volatile("v_mfma_f32_32x32x16_bf16 a[176:191], v[92:95], v[72:75], a[176:191]\n\tds_read_b128 v[108:111], %0 offset:38912" :  : "v"(c.a_rd[1][1]) : "memory"); \
  asm volatile("s_waitcnt lgkmcnt(12)\n\tv_mfma_f32_32x32x16_bf16 a[192:207], v[80:83], v[76:79], a[192:207]" :  :  : "memory"); \
  unsigned so49; \
  asm volatile("v_mfma_f32_32x32x16_bf16 a[208:223], v[84:87], v[76:79], a[208:223]\n\ts_add_u32 m0, %1, 68608\n\ts_add_u32 %0, %4, 49536\n\tbuffer_load_dwordx4 %2, %3, %0 offen offset:0 lds" : "=&s"(so49) : "s"(c.m0w), "v"(c.va), "s"(c.sa), "s"(c.soa) : "memory", "m0", "scc"); \
  asm volatile("v_mfma_f32_32x32x16_bf16 a[224:239], v[88:91], v[76:79], a[224:239]" :  :  : "memory", ACL64); \
  asm volatile("v_mfma_f32_32x32x16_bf16 a[240:255], v[92:95], v[76:79], a[240:255]" :  :  : "memory"); \
  asm volatile("s_waitcnt lgkmcnt(9)\n\tv_mfma_f32_32x32x16_bf16 a[0:15], v[112:115], v[96:99], a[0:15]\n\tds_read_b64_tr_b16 v[80:81], %0 offset:16384" :  : "v"(c.b_rd[0][0]) : "memory"); \
  unsigned so50; \
  asm volatile("s_waitcnt lgkmcnt(8)\n\tv_mfma_f32_32x32x16_bf16 a[16:31], v[116:119], v[96:99], a[16:31]\n\tds_read_b64_tr_b16 v[82:83], %1 offset:18432\n\ts_add_u32 m0, %2, 81920\n\ts_add_u32 %0, %5, 1572864\n\tbuffer_load_dwordx4 %3, %4, %0 offen offset:0 lds" : "=&s"(so50) : "v"(c.b_rd[0][0]), "s"(c.m0w), "v"(c.vb[0]), "s"(c.sb), "s"(c.sob) : "memory", "m0", "scc"); \
  asm volatile("s_waitcnt lgkmcnt(7)\n\tv_mfma_f32_32x32x16_bf16 a[32:47], v[120:123], v[96:99], a[32:47]\n\tds_read_b128 v[64:67], %0 offset:0" :  : "v"(c.a_rd[0][0]) : "memory"); \
  asm volatile("s_waitcnt lgkmcnt(6)\n\tv_mfma_f32_32x32x16_bf16 a[48:63], v[124:127], v[96:99], a[48:63]\n\tds_read_b64_tr_b16 v[84:85], %0 offset:16384" :  : "v"(c.b_rd[0][1]) : "memory"); \
  asm volatile("s_waitcnt lgkmcnt(6)\n\tv_mfma_f32_32x32x16_bf16 a[64:79], v[112:115], v[100:103], a[64:79]\n\tds_read_b64_tr_b16 v[86:87], %0 offset:18432" :  : "v"(c.b_rd[0][1]) : "memory"); \
  unsigned so51; \
  asm volatile("v_mfma_f32_32x32x16_bf16 a[80:95], v[116:119], v[100:103], a[80:95]\n\tds_read_b64_tr_b16 v[88:89], %1 offset:16384\n\ts_add_u32 m0, %2, 82944\n\ts_add_u32 %0, %5, 1572864\n\tbuffer_load_dwordx4 %3, %4, %0 offen offset:0 lds" : "=&s"(so51) : "v"(c.b_rd[0][2]), "s"(c.m0w), "v"(c.vb[1]), "s"(c.sb), "s"(c.sob) : "memory", "m0", "scc"); \
  asm volatile("v_mfma_f32_32x32x16_bf16 a[96:111], v[120:123], v[100:103], a[96:111]\n\tds_read_b64_tr_b16 v[90:91], %0 offset:18432" :  : "v"(c.b_rd[0][2]) : "memory", ACL64); \
  asm volatile("v_mfma_f32_32x32x16_bf16 a[112:127], v[124:127], v[100:103], a[112:127]\n\tds_read_b64_tr_b16 v[92:93], %0 offset:16384" :  : "v"(c.b_rd[0][3]) : "memory"); \
  asm volatile("s_waitcnt lgkmcnt(9)\n\tv_mfma_f32_32x32x16_bf16 a[128:143], v[112:115], v[104:107], a[128:143]\n\tds_read_b64_tr_b16 v[94:95], %0 offset:18432" :  : "v"(c.b_rd[0][3]) : "memory"); \
  unsigned so52; \
  asm volatile("v_mfma_f32_32x32x16_bf16 a[144:159], v[116:119], v[104:107], a[144:159]\n\tds_read_b128 v[68:71], %1 offset:2048\n\ts_add_u32 m0, %2, 83968\n\ts_add_u32 %0, %5, 1605632\n\tbuffer_load_dwordx4 %3, %4, %0 offen offset:0 lds" : "=&s"(so52) : "v"(c.a_rd[0][0]), "s"(c.m0w), "v"(c.vb[0]), "s"(c.sb), "s"(c.sob) : "memory", "m0", "scc"); \
  asm volatile("v_mfma_f32_32x32x16_bf16 a[160:175], v[120:123], v[104:107], a[160:175]\n\tds_read_b128 v[72:75], %0 offset:4096" :  : "v"(c.a_rd[0][0]) : "memory"); \
  asm volatile("v_mfma_f32_32x32x16_bf16 a[176:191], v[124:127], v[104:107], a[176:191]\n\tds_read_b128 v[76:79], %0 offset:6144" :  : "v"(c.a_rd[0][0]) : "memory"); \
  asm volatile("s_waitcnt lgkmcnt(12)\n\tv_mfma_f32_32x32x16_bf16 a[192:207], v[112:115], v[108:111], a[192:207]" :  :  : "memory"); \
  unsigned so53; \
  asm volatile("v_mfma_f32_32x32x16_bf16 a[208:223], v[116:119], v[108:111], a[208:223]\n\ts_add_u32 m0, %1, 84992\n\ts_add_u32 %0, %4, 1605632\n\tbuffer_load_dwordx4 %2, %3, %0 offen offset:0 lds" : "=&s"(so53) : "s"(c.m0w), "v"(c.vb[1]), "s"(c.sb), "s"(c.sob) : "memory", "m0", "scc"); \
  asm volatile("v_mfma_f32_32x32x16_bf16 a[224:239], v[120:123], v[108:111], a[224:239]" :  :  : "memory", ACL64); \
  asm volatile("v_mfma_f32_32x32x16_bf16 a[240:255], v[124:127], v[108:111], a[240:255]" :  :  : "memory"); \
  asm volatile("s_waitcnt vmcnt(8)\n\ts_barrier" ::: "memory"); \
  KV_HOOK(4); \
  asm volatile("s_waitcnt lgkmcnt(9)\n\tv_mfma_f32_32x32x16_bf16 a[0:15], v[80:83], v[64:67], a[0:15]\n\tds_read_b64_tr_b16 v[112:113], %0 offset:24576" :  : "v"(c.b_rd[0][0]) : "memory"); \
  unsigned so54; \
  asm volatile("s_waitcnt lgkmcnt(8)\n\tv_mfma_f32_32x32x16_bf16 a[16:31], v[84:87], v[64:67], a[16:31]\n\tds_read_b64_tr_b16 v[114:115], %1 offset:26624\n\ts_add_u32 m0, %2, 98304\n\ts_add_u32 %0, %5, 448\n\tbuffer_load_dwordx4 %3, %4, %0 offen offset:0 lds" : "=&s"(so54) : "v"(c.b_rd[0][0]), "s"(c.m0w), "v"(c.va), "s"(c.sa), "s"(c.soa) : "memory", "m0", "scc"); \
  asm volatile("s_waitcnt lgkmcnt(7)\n\tv_mfma_f32_32x32x16_bf16 a[32:47], v[88:91], v[64:67], a[32:47]\n\tds_read_b128 v[96:99], %0 offset:0" :  : "v"(c.a_rd[0][1]) : "memory"); \
  asm volatile("s_waitcnt lgkmcnt(6)\n\tv_mfma_f32_32x32x16_bf16 a[48:63], v[92:95], v[64:67], a[48:63]\n\tds_read_b64_tr_b16 v[116:117], %0 offset:24576" :  : "v"(c.b_rd[0][1]) : "memory"); \
  asm volatile("s_waitcnt lgkmcnt(6)\n\tv_mfma_f32_32x32x16_bf16 a[64:79], v[80:83], v[68:71], a[64:79]\n\tds_read_b64_tr_b16 v[118:119], %0 offset:26624" :  : "v"(c.b_rd[0][1]) : "memory"); \
  unsigned so55; \
  asm volatile("v_mfma_f32_32x32x16_bf16 a[80:95], v[84:87], v[68:71], a[80:95]\n\tds_read_b64_tr_b16 v[120:121], %1 offset:24576\n\ts_add_u32 m0, %2, 99328\n\ts_add_u32 %0, %5, 16832\n\tbuffer_load_dwordx4 %3, %4, %0 offen offset:0 lds" : "=&s"(so55) : "v"(c.b_rd[0][2]), "s"(c.m0w), "v"(c.va), "s"(c.sa), "s"(c.soa) : "memory", "m0", "scc"); \
  asm volatile("v_mfma_f32_32x32x16_bf16 a[96:111], v[88:91], v[68:71], a[96:111]\n\tds_read_b64_tr_b16 v[122:123], %0 offset:26624" :  : "v"(c.b_rd[0][2]) : "memory", ACL64); \
  asm volatile("v_mfma_f32_32x32x16_bf16 a[112:127], v[92:95], v[68:71], a[112:127]\n\tds_read_b64_tr_b16 v[124:125], %0 offset:24576" :  : "v"(c.b_rd[0][3]) : "memory"); \
  asm volatile("s_waitcnt lgkmcnt(9)\n\tv_mfma_f32_32x32x16_bf16 a[128:143], v[80:83], v[72:75], a[128:143]\n\tds_read_b64_tr_b16 v[126:127], %0 offset:26624" :  : "v"(c.b_rd[0][3]) : "memory"); \
  unsigned so56; \
  asm volatile("v_mfma_f32_32x32x16_bf16 a[144:159], v[84:87], v[72:75], a[144:159]\n\tds_read_b128 v[100:103], %1 offset:2048\n\ts_add_u32 m0, %2, 100352\n\ts_add_u32 %0, %5, 33216\n\tbuffer_load_dwordx4 %3, %4, %0 offen offset:0 lds" : "=&s"(so56) : "v"(c.a_rd[0][1]), "s"(c.m0w), "v"(c.va), "s"(c.sa), "s"(c.soa) : "memory", "m0", "scc"); \
  asm volatile("v_mfma_f32_32x32x16_bf16 a[160:175], v[88:91], v[72:75], a[160:175]\n\tds_read_b128 v[104:107], %0 offset:4096" :  : "v"(c.a_rd[0][1]) : "memory"); \
  asm volatile("v_mfma_f32_32x32x16_bf16 a[176:191], v[92:95], v[72:75], a[176:191]\n\tds_read_b128 v[108:111], %0 offset:6144" :  : "v"(c.a_rd[0][1]) : "memory"); \
  asm volatile("s_waitcnt lgkmcnt(12)\n\tv_mfma_f32_32x32x16_bf16 a[192:207], v[80:83], v[76:79], a[192:207]" :  :  : "memory"); \
  unsigned so57; \
  asm volatile("v_mfma_f32_32x32x16_bf16 a[208:223], v[84:87], v[76:79], a[208:223]\n\ts_add_u32 m0, %1, 101376\n\ts_add_u32 %0, %4, 49600\n\tbuffer_load_dwordx4 %2, %3, %0 offen offset:0 lds" : "=&s"(so57) : "s"(c.m0w), "v"(c.va), "s"(c.sa), "s"(c.soa) : "memory", "m0", "scc"); \
  asm volatile("v_mfma_f32_32x32x16_bf16 a[224:239], v[88:91], v[76:79], a[224:239]" :  :  : "memory", ACL64); \
  asm volatile("v_mfma_f32_32x32x16_bf16 a[240:255], v[92:95], v[76:79], a[240:255]" :  :  : "memory"); \
  asm volatile("s_waitcnt lgkmcnt(9)\n\tv_mfma_f32_32x32x16_bf16 a[0:15], v[112:115], v[96:99], a[0:15]\n\tds_read_b64_tr_b16 v[80:81], %0 offset:49152" :  : "v"(c.b_rd[0][0]) : "memory"); \
  unsigned so58; \
  asm volatile("s_waitcnt lgkmcnt(8)\n\tv_mfma_f32_32x32x16_bf16 a[16:31], v[116:119], v[96:99], a[16:31]\n\tds_read_b64_tr_b16 v[82:83], %1 offset:51200\n\ts_add_u32 m0, %2, 114688\n\ts_add_u32 %0, %5, 1835008\n\tbuffer_load_dwordx4 %3, %4, %0 offen offset:0 lds" : "=&s"(so58) : "v"(c.b_rd[0][0]), "s"(c.m0w), "v"(c.vb[0]), "s"(c.sb), "s"(c.sob) : "memory", "m0", "scc"); \
  asm volatile("s_waitcnt lgkmcnt(7)\n\tv_mfma_f32_32x32x16_bf16 a[32:47], v[120:123], v[96:99], a[32:47]\n\tds_read_b128 v[64:67], %0 offset:32768" :  : "v"(c.a_rd[0][0]) : "memory"); \
  asm volatile("s_waitcnt lgkmcnt(6)\n\tv_mfma_f32_32x32x16_bf16 a[48:63], v[124:127], v[96:99], a[48:63]\n\tds_read_b64_tr_b16 v[84:85], %0 offset:49152" :  : "v"(c.b_rd[0][1]) : "memory"); \
  asm volatile("s_waitcnt lgkmcnt(6)\n\tv_mfma_f32_32x32x16_bf16 a[64:79], v[112:115], v[100:103], a[64:79]\n\tds_read_b64_tr_b16 v[86:87], %0 offset:51200" :  : "v"(c.b_rd[0][1]) : "memory"); \
  unsigned so59; \
  asm volatile("v_mfma_f32_32x32x16_bf16 a[80:95], v[116:119], v[100:103], a[80:95]\n\tds_read_b64_tr_b16 v[88:89], %1 offset:49152\n\ts_add_u32 m0, %2, 115712\n\ts_add_u32 %0, %5, 1835008\n\tbuffer_load_dwordx4 %3, %4, %0 offen offset:0 lds" : "=&s"(so59) : "v"(c.b_rd[0][2]), "s"(c.m0w), "v"(c.vb[1]), "s"(c.sb), "s"(c.sob) : "memory", "m0", "scc"); \
  asm volatile("v_mfma_f32_32x32x16_bf16 a[96:111], v[120:123], v[100:103], a[96:111]\n\tds_read_b64_tr_b16 v[90:91], %0 offset:51200" :  : "v"(c.b_rd[0][2]) : "memory", ACL64); \
  asm volatile("v_mfma_f32_32x32x16_bf16 a[112:127], v[124:127], v[100:103], a[112:127]\n\tds_read_b64_tr_b16 v[92:93], %0 offset:49152" :  : "v"(c.b_rd[0][3]) : "memory"); \
  asm volatile("s_waitcnt lgkmcnt(9)\n\tv_mfma_f32_32x32x16_bf16 a[128:143], v[112:115], v[104:107], a[128:143]\n\tds_read_b64_tr_b16 v[94:95], %0 offset:51200" :  : "v"(c.b_rd[0][3]) : "memory"); \
  unsigned so60; \
  asm volatile("v_mfma_f32_32x32x16_bf16 a[144:159], v[116:119], v[104:107], a[144:159]\n\tds_read_b128 v[68:71], %1 offset:34816\n\ts_add_u32 m0, %2, 116736\n\ts_add_u32 %0, %5, 1867776\n\tbuffer_load_dwordx4 %3, %4, %0 offen offset:0 lds" : "=&s"(so60) : "v"(c.a_rd[0][0]), "s"(c.m0w), "v"(c.vb[0]), "s"(c.sb), "s"(c.sob) : "memory", "m0", "scc"); \
  asm volatile("v_mfma_f32_32x32x16_bf16 a[160:175], v[120:123], v[104:107], a[160:175]\n\tds_read_b128 v[72:75], %0 offset:36864" :  : "v"(c.a_rd[0][0]) : "memory"); \
  asm volatile("v_mfma_f32_32x32x16_bf16 a[176:191], v[124:127], v[104:107], a[176:191]\n\tds_read_b128 v[76:79], %0 offset:38912" :  : "v"(c.a_rd[0][0]) : "memory"); \
  asm volatile("s_waitcnt lgkmcnt(12)\n\tv_mfma_f32_32x32x16_bf16 a[192:207], v[112:115], v[108:111], a[192:207]" :  :  : "memory"); \
  unsigned so61; \
  asm volatile("v_mfma_f32_32x32x16_bf16 a[208:223], v[116:119], v[108:111], a[208:223]\n\ts_add_u32 m0, %1, 117760\n\ts_add_u32 %0, %4, 1867776\n\tbuffer_load_dwordx4 %2, %3, %0 offen offset:0 lds" : "=&s"(so61) : "s"(c.m0w), "v"(c.vb[1]), "s"(c.sb), "s"(c.sob) : "memory", "m0", "scc"); \
  asm volatile("v_mfma_f32_32x32x16_bf16 a[224:239], v[120:123], v[108:111], a[224:239]" :  :  : "memory", ACL64); \
  asm volatile("v_mfma_f32_32x32x16_bf16 a[240:255], v[124:127], v[108:111], a[240:255]" :  :  : "memory"); \
  asm volatile("s_waitcnt vmcnt(8)\n\ts_barrier" ::: "memory"); \
  KV_HOOK(5); \
  asm volatile("s_waitcnt lgkmcnt(9)\n\tv_mfma_f32_32x32x16_bf16 a[0:15], v[80:83], v[64:67], a[0:15]\n\tds_read_b64_tr_b16 v[112:113], %0 offset:57344" :  : "v"(c.b_rd[0][0]) : "memory"); \
  unsigned so62; \
  asm volatile("s_waitcnt lgkmcnt(8)\n\tv_mfma_f32_32x32x16_bf16 a[16:31], v[84:87], v[64:67], a[16:31]\n\tds_read_b64_tr_b16 v[114:115], %1 offset:59392\n\ts_add_u32 m0, %2, 0\n\ts_add_u32 %0, %5, 512\n\tbuffer_load_dwordx4 %3, %4, %0 offen offset:0 lds" : "=&s"(so62) : "v"(c.b_rd[0][0]), "s"(c.m0w), "v"(c.va), "s"(c.sa), "s"(c.soa) : "memory", "m0", "scc"); \
  asm volatile("s_waitcnt lgkmcnt(7)\n\tv_mfma_f32_32x32x16_bf16 a[32:47], v[88:91], v[64:67], a[32:47]\n\tds_read_b128 v[96:99], %0 offset:32768" :  : "v"(c.a_rd[0][1]) : "memory"); \
  asm volatile("s_waitcnt lgkmcnt(6)\n\tv_mfma_f32_32x32x16_bf16 a[48:63], v[92:95], v[64:67], a[48:63]\n\tds_read_b64_tr_b16 v[116:117], %0 offset:57344" :  : "v"(c.b_rd[0][1]) : "memory"); \
  asm volatile("s_waitcnt lgkmcnt(6)\n\tv_mfma_f32_32x32x16_bf16 a[64:79], v[80:83], v[68:71], a[64:79]\n\tds_read_b64_tr_b16 v[118:119], %0 offset:59392" :  : "v"(c.b_rd[0][1]) : "memory"); \
  unsigned so63; \
  asm volatile("v_mfma_f32_32x32x16_bf16 a[80:95], v[84:87], v[68:71], a[80:95]\n\tds_read_b64_tr_b16 v[120:121], %1 offset:57344\n\ts_add_u32 m0, %2, 1024\n\ts_add_u32 %0, %5, 16896\n\tbuffer_load_dwordx4 %3, %4, %0 offen offset:0 lds" : "=&s"(so63) : "v"(c.b_rd[0][2]), "s"(c.m0w), "v"(c.va), "s"(c.sa), "s"(c.soa) : "memory", "m0", "scc"); \
  asm volatile("v_mfma_f32_32x32x16_bf16 a[96:111], v[88:91], v[68:71], a[96:111]\n\tds_read_b64_tr_b16 v[122:123], %0 offset:59392" :  : "v"(c.b_rd[0][2]) : "memory", ACL64); \
  asm volatile("v_mfma_f32_32x32x16_bf16 a[112:127], v[92:95], v[68:71], a[112:127]\n\tds_read_b64_tr_b16 v[124:125], %0 offset:57344" :  : "v"(c.b_rd[0][3]) : "memory"); \
  asm volatile("s_waitcnt lgkmcnt(9)\n\tv_mfma_f32_32x32x16_bf16 a[128:143], v[80:83], v[72:75], a[128:143]\n\tds_read_b64_tr_b16 v[126:127], %0 offset:59392" :  : "v"(c.b_rd[0][3]) : "memory"); \
  unsigned so64; \
  asm volatile("v_mfma_f32_32x32x16_bf16 a[144:159], v[84:87], v[72:75], a[144:159]\n\tds_read_b128 v[100:103], %1 offset:34816\n\ts_add_u32 m0, %2, 2048\n\ts_add_u32 %0, %5, 33280\n\tbuffer_load_dwordx4 %3, %4, %0 offen offset:0 lds" : "=&s"(so64) : "v"(c.a_rd[0][1]), "s"(c.m0w), "v"(c.va), "s"(c.sa), "s"(c.soa) : "memory", "m0", "scc"); \
  asm volatile("v_mfma_f32_32x32x16_bf16 a[160:175], v[88:91], v[72:75], a[160:175]\n\tds_read_b128 v[104:107], %0 offset:36864" :  : "v"(c.a_rd[0][1]) : "memory"); \
  asm volatile("v_mfma_f32_32x32x16_bf16 a[176:191], v[92:95], v[72:75], a[176:191]\n\tds_read_b128 v[108:111], %0 offset:38912" :  : "v"(c.a_rd[0][1]) : "memory"); \
  asm volatile("s_waitcnt lgkmcnt(12)\n\tv_mfma_f32_32x32x16_bf16 a[192:207], v[80:83], v[76:79], a[192:207]" :  :  : "memory"); \
  unsigned so65; \
  asm volatile("v_mfma_f32_32x32x16_bf16 a[208:223], v[84:87], v[76:79], a[208:223]\n\ts_add_u32 m0, %1, 3072\n\ts_add_u32 %0, %4, 49664\n\tbuffer_load_dwordx4 %2, %3, %0 offen offset:0 lds" : "=&s"(so65) : "s"(c.m0w), "v"(c.va), "s"(c.sa), "s"(c.soa) : "memory", "m0", "scc"); \
  asm volatile("v_mfma_f32_32x32x16_bf16 a[224:239], v[88:91], v[76:79], a[224:239]" :  :  : "memory", ACL64); \
  asm volatile("v_mfma_f32_32x32x16_bf16 a[240:255], v[92:95], v[76:79], a[240:255]" :  :  : "memory"); \
  asm volatile("s_waitcnt lgkmcnt(9)\n\tv_mfma_f32_32x32x16_bf16 a[0:15], v[112:115], v[96:99], a[0:15]\n\tds_read_b64_tr_b16 v[80:81], %0 offset:16384" :  : "v"(c.b_rd[1][0]) : "memory"); \
  unsigned so66; \
  asm volatile("s_waitcnt lgkmcnt(8)\n\tv_mfma_f32_32x32x16_bf16 a[16:31], v[116:119], v[96:99], a[16:31]\n\tds_read_b64_tr_b16 v[82:83], %1 offset:18432\n\ts_add_u32 m0, %2, 16384\n\ts_add_u32 %0, %5, 2097152\n\tbuffer_load_dwordx4 %3, %4, %0 offen offset:0 lds" : "=&s"(so66) : "v"(c.b_rd[1][0]), "s"(c.m0w), "v"(c.vb[0]), "s"(c.sb), "s"(c.sob) : "memory", "m0", "scc"); \
  asm volatile("s_waitcnt lgkmcnt(7)\n\tv_mfma_f32_32x32x16_bf16 a[32:47], v[120:123], v[96:99], a[32:47]\n\tds_read_b128 v[64:67], %0 offset:0" :  : "v"(c.a_rd[1][0]) : "memory"); \
  asm volatile("s_waitcnt lgkmcnt(6)\n\tv_mfma_f32_32x32x16_bf16 a[48:63], v[124:127], v[96:99], a[48:63]\n\tds_read_b64_tr_b16 v[84:85], %0 offset:16384" :  : "v"(c.b_rd[1][1]) : "memory"); \
  asm volatile("s_waitcnt lgkmcnt(6)\n\tv_mfma_f32_32x32x16_bf16 a[64:79], v[112:115], v[100:103], a[64:79]\n\tds_read_b64_tr_b16 v[86:87], %0 offset:18432" :  : "v"(c.b_rd[1][1]) : "memory"); \
  unsigned so67; \
  asm volatile("v_mfma_f32_32x32x16_bf16 a[80:95], v[116:119], v[100:103], a[80:95]\n\tds_read_b64_tr_b16 v[88:89], %1 offset:16384\n\ts_add_u32 m0, %2, 17408\n\ts_add_u32 %0, %5, 2097152\n\tbuffer_load_dwordx4 %3, %4, %0 offen offset:0 lds" : "=&s"(so67) : "v"(c.b_rd[1][2]), "s"(c.m0w), "v"(c.vb[1]), "s"(c.sb), "s"(c.sob) : "memory", "m0", "scc"); \
  asm volatile("v_mfma_f32_32x32x16_bf16 a[96:111], v[120:123], v[100:103], a[96:111]\n\tds_read_b64_tr_b16 v[90:91], %0 offset:18432" :  : "v"(c.b_rd[1][2]) : "memory", ACL64); \
  asm volatile("v_mfma_f32_32x32x16_bf16 a[112:127], v[124:127], v[100:103], a[112:127]\n\tds_read_b64_tr_b16 v[92:93], %0 offset:16384" :  : "v"(c.b_rd[1][3]) : "memory"); \
  asm volatile("s_waitcnt lgkmcnt(9)\n\tv_mfma_f32_32x32x16_bf16 a[128:143], v[112:115], v[104:107], a[128:143]\n\tds_read_b64_tr_b16 v[94:95], %0 offset:18432" :  : "v"(c.b_rd[1][3]) : "memory"); \
  unsigned so68; \
  asm volatile("v_mfma_f32_32x32x16_bf16 a[144:159], v[116:119], v[104:107], a[144:159]\n\tds_read_b128 v[68:71], %1 offset:2048\n\ts_add_u32 m0, %2, 18432\n\ts_add_u32 %0, %5, 2129920\n\tbuffer_load_dwordx4 %3, %4, %0 offen offset:0 lds" : "=&s"(so68) : "v"(c.a_rd[1][0]), "s"(c.m0w), "v"(c.vb[0]), "s"(c.sb), "s"(c.sob) : "memory", "m0", "scc"); \
  asm volatile("v_mfma_f32_32x32x16_bf16 a[160:175], v[120:123], v[104:107], a[160:175]\n\tds_read_b128 v[72:75], %0 offset:4096" :  : "v"(c.a_rd[1][0]) : "memory"); \
  asm volatile("v_mfma_f32_32x32x16_bf16 a[176:191], v[124:127], v[104:107], a[176:191]\n\tds_read_b128 v[76:79], %0 offset:6144" :  : "v"(c.a_rd[1][0]) : "memory"); \
  asm volatile("s_waitcnt lgkmcnt(12)\n\tv_mfma_f32_32x32x16_bf16 a[192:207], v[112:115], v[108:111], a[192:207]" :  :  : "memory"); \
  unsigned so69; \
  asm volatile("v_mfma_f32_32x32x16_bf16 a[208:223], v[116:119], v[108:111], a[208:223]\n\ts_add_u32 m0, %1, 19456\n\ts_add_u32 %0, %4, 2129920\n\tbuffer_load_dwordx4 %2, %3, %0 offen offset:0 lds" : "=&s"(so69) : "s"(c.m0w), "v"(c.vb[1]), "s"(c.sb), "s"(c.sob) : "memory", "m0", "scc"); \
  asm volatile("v_mfma_f32_32x32x16_bf16 a[224:239], v[120:123], v[108:111], a[224:239]" :  :  : "memory", ACL64); \
  asm volatile("v_mfma_f32_32x32x16_bf16 a[240:255], v[124:127], v[108:111], a[240:255]" :  :  : "memory"); \
  asm volatile("s_waitcnt vmcnt(8)\n\ts_barrier" ::: "memory"); \
  KV_HOOK(6); \
  asm volatile("s_waitcnt lgkmcnt(9)\n\tv_mfma_f32_32x32x16_bf16 a[0:15], v[80:83], v[64:67], a[0:15]\n\tds_read_b64_tr_b16 v[112:113], %0 offset:24576" :  : "v"(c.b_rd[1][0]) : "memory"); \
  unsigned so70; \
  asm volatile("s_waitcnt lgkmcnt(8)\n\tv_mfma_f32_32x32x16_bf16 a[16:31], v[84:87], v[64:67], a[16:31]\n\tds_read_b64_tr_b16 v[114:115], %1 offset:26624\n\ts_add_u32 m0, %2, 32768\n\ts_add_u32 %0, %5, 576\n\tbuffer_load_dwordx4 %3, %4, %0 offen offset:0 lds" : "=&s"(so70) : "v"(c.b_rd[1][0]), "s"(c.m0w), "v"(c.va), "s"(c.sa), "s"(c.soa) : "memory", "m0", "scc"); \
  asm volatile("s_waitcnt lgkmcnt(7)\n\tv_mfma_f32_32x32x16_bf16 a[32:47], v[88:91], v[64:67], a[32:47]\n\tds_read_b128 v[96:99], %0 offset:0" :  : "v"(c.a_rd[1][1]) : "memory"); \
  asm volatile("s_waitcnt lgkmcnt(6)\n\tv_mfma_f32_32x32x16_bf16 a[48:63], v[92:95], v[64:67], a[48:63]\n\tds_read_b64_tr_b16 v[116:117], %0 offset:24576" :  : "v"(c.b_rd[1][1]) : "memory"); \
  asm volatile("s_waitcnt lgkmcnt(6)\n\tv_mfma_f32_32x32x16_bf16 a[64:79], v[80:83], v[68:71], a[64:79]\n\tds_read_b64_tr_b16 v[118:119], %0 offset:26624" :  : "v"(c.b_rd[1][1]) : "memory"); \
  unsigned so71; \
  asm volatile("v_mfma_f32_32x32x16_bf16 a[80:95], v[84:87], v[68:71], a[80:95]\n\tds_read_b64_tr_b16 v[120:121], %1 offset:24576\n\ts_add_u32 m0, %2, 33792\n\ts_add_u32 %0, %5, 16960\n\tbuffer_load_dwordx4 %3, %4, %0 offen offset:0 lds" : "=&s"(so71) : "v"(c.b_rd[1][2]), "s"(c.m0w), "v"(c.va), "s"(c.sa), "s"(c.soa) : "memory", "m0", "scc"); \
  asm volatile("v_mfma_f32_32x32x16_bf16 a[96:111], v[88:91], v[68:71], a[96:111]\n\tds_read_b64_tr_b16 v[122:123], %0 offset:26624" :  : "v"(c.b_rd[1][2]) : "memory", ACL64); \
  asm volatile("v_mfma_f32_32x32x16_bf16 a[112:127], v[92:95], v[68:71], a[112:127]\n\tds_read_b64_tr_b16 v[124:125], %0 offset:24576" :  : "v"(c.b_rd[1][3]) : "memory"); \
  asm volatile("s_waitcnt lgkmcnt(9)\n\tv_mfma_f32_32x32x16_bf16 a[128:143], v[80:83], v[72:75], a[128:143]\n\tds_read_b64_tr_b16 v[126:127], %0 offset:26624" :  : "v"(c.b_rd[1][3]) : "memory"); \
  unsigned so72; \
  asm volatile("v_mfma_f32_32x32x16_bf16 a[144:159], v[84:87], v[72:75], a[144:159]\n\tds_read_b128 v[100:103], %1 offset:2048\n\ts_add_u32 m0, %2, 34816\n\ts_add_u32 %0, %5, 33344\n\tbuffer_load_dwordx4 %3, %4, %0 offen offset:0 lds" : "=&s"(so72) : "v"(c.a_rd[1][1]), "s"(c.m0w), "v"(c.va), "s"(c.sa), "s"(c.soa) : "memory", "m0", "scc"); \
  asm volatile("v_mfma_f32_32x32x16_bf16 a[160:175], v[88:91], v[72:75], a[160:175]\n\tds_read_b128 v[104:107], %0 offset:4096" :  : "v"(c.a_rd[1][1]) : "memory"); \
  asm volatile("v_mfma_f32_32x32x16_bf16 a[176:191], v[92:95], v[72:75], a[176:191]\n\tds_read_b128 v[108:111], %0 offset:6144" :  : "v"(c.a_rd[1][1]) : "memory"); \
  asm volatile("s_waitcnt lgkmcnt(12)\n\tv_mfma_f32_32x32x16_bf16 a[192:207], v[80:83], v[76:79], a[192:207]" :  :  : "memory"); \
  unsigned so73; \
  asm volatile("v_mfma_f32_32x32x16_bf16 a[208:223], v[84:87], v[76:79], a[208:223]\n\ts_add_u32 m0, %1, 35840\n\ts_add_u32 %0, %4, 49728\n\tbuffer_load_dwordx4 %2, %3, %0 offen offset:0 lds" : "=&s"(so73) : "s"(c.m0w), "v"(c.va), "s"(c.sa), "s"(c.soa) : "memory", "m0", "scc"); \
  asm volatile("v_mfma_f32_32x32x16_bf16 a[224:239], v[88:91], v[76:79], a[224:239]" :  :  : "memory", ACL64); \
  asm volatile("v_mfma_f32_32x32x16_bf16 a[240:255], v[92:95], v[76:79], a[240:255]" :  :  : "memory"); \
  asm volatile("s_waitcnt lgkmcnt(9)\n\tv_mfma_f32_32x32x16_bf16 a[0:15], v[112:115], v[96:99], a[0:15]\n\tds_read_b64_tr_b16 v[80:81], %0 offset:49152" :  : "v"(c.b_rd[1][0]) : "memory"); \
  unsigned so74; \
  asm volatile("s_waitcnt lgkmcnt(8)\n\tv_mfma_f32_32x32x16_bf16 a[16:31], v[116:119], v[96:99], a[16:31]\n\tds_read_b64_tr_b16 v[82:83], %1 offset:51200\n\ts_add_u32 m0, %2, 49152\n\ts_add_u32 %0, %5, 2359296\n\tbuffer_load_dwordx4 %3, %4, %0 offen offset:0 lds" : "=&s"(so74) : "v"(c.b_rd[1][0]), "s"(c.m0w), "v"(c.vb[0]), "s"(c.sb), "s"(c.sob) : "memory", "m0", "scc"); \
  asm volatile("s_waitcnt lgkmcnt(7)\n\tv_mfma_f32_32x32x16_bf16 a[32:47], v[120:123], v[96:99], a[32:47]\n\tds_read_b128 v[64:67], %0 offset:32768" :  : "v"(c.a_rd[1][0]) : "memory"); \
  asm volatile("s_waitcnt lgkmcnt(6)\n\tv_mfma_f32_32x32x16_bf16 a[48:63], v[124:127], v[96:99], a[48:63]\n\tds_read_b64_tr_b16 v[84:85], %0 offset:49152" :  : "v"(c.b_rd[1][1]) : "memory"); \
  asm volatile("s_waitcnt lgkmcnt(6)\n\tv_mfma_f32_32x32x16_bf16 a[64:79], v[112:115], v[100:103], a[64:79]\n\tds_read_b64_tr_b16 v[86:87], %0 offset:51200" :  : "v"(c.b_rd[1][1]) : "memory"); \
  unsigned so75; \
  asm volatile("v_mfma_f32_32x32x16_bf16 a[80:95], v[116:119], v[100:103], a[80:95]\n\tds_read_b64_tr_b16 v[88:89], %1 offset:49152\n\ts_add_u32 m0, %2, 50176\n\ts_add_u32 %0, %5, 2359296\n\tbuffer_load_dwordx4 %3, %4, %0 offen offset:0 lds" : "=&s"(so75) : "v"(c.b_rd[1][2]), "s"(c.m0w), "v"(c.vb[1]), "s"(c.sb), "s"(c.sob) : "memory", "m0", "scc"); \
  asm volatile("v_mfma_f32_32x32x16_bf16 a[96:111], v[120:123], v[100:103], a[96:111]\n\tds_read_b64_tr_b16 v[90:91], %0 offset:51200" :  : "v"(c.b_rd[1][2]) : "memory", ACL64); \
  asm volatile("v_mfma_f32_32x32x16_bf16 a[112:127], v[124:127], v[100:103], a[112:127]\n\tds_read_b64_tr_b16 v[92:93], %0 offset:49152" :  : "v"(c.b_rd[1][3]) : "memory"); \
  asm volatile("s_waitcnt lgkmcnt(9)\n\tv_mfma_f32_32x32x16_bf16 a[128:143], v[112:115], v[104:107], a[128:143]\n\tds_read_b64_tr_b16 v[94:95], %0 offset:51200" :  : "v"(c.b_rd[1][3]) : "memory"); \
  unsigned so76; \
  asm volatile("v_mfma_f32_32x32x16_bf16 a[144:159], v[116:119], v[104:107], a[144:159]\n\tds_read_b128 v[68:71], %1 offset:34816\n\ts_add_u32 m0, %2, 51200\n\ts_add_u32 %0, %5, 2392064\n\tbuffer_load_dwordx4 %3, %4, %0 offen offset:0 lds" : "=&s"(so76) : "v"(c.a_rd[1][0]), "s"(c.m0w), "v"(c.vb[0]), "s"(c.sb), "s"(c.sob) : "memory", "m0", "scc"); \
  asm volatile("v_mfma_f32_32x32x16_bf16 a[160:175], v[120:123], v[104:107], a[160:175]\n\tds_read_b128 v[72:75], %0 offset:36864" :  : "v"(c.a_rd[1][0]) : "memory"); \
  asm volatile("v_mfma_f32_32x32x16_bf16 a[176:191], v[124:127], v[104:107], a[176:191]\n\tds_read_b128 v[76:79], %0 offset:38912" :  : "v"(c.a_rd[1][0]) : "memory"); \
  asm volatile("s_waitcnt lgkmcnt(12)\n\tv_mfma_f32_32x32x16_bf16 a[192:207], v[112:115], v[108:111], a[192:207]" :  :  : "memory"); \
  unsigned so77; \
  asm volatile("v_mfma_f32_32x32x16_bf16 a[208:223], v[116:119], v[108:111], a[208:223]\n\ts_add_u32 m0, %1, 52224\n\ts_add_u32 %0, %4, 2392064\n\tbuffer_load_dwordx4 %2, %3, %0 offen offset:0 lds" : "=&s"(so77) : "s"(c.m0w), "v"(c.vb[1]), "s"(c.sb), "s"(c.sob) : "memory", "m0", "scc"); \
  asm volatile("v_mfma_f32_32x32x16_bf16 a[224:239], v[120:123], v[108:111], a[224:239]" :  :  : "memory", ACL64); \
  asm volatile("v_mfma_f32_32x32x16_bf16 a[240:255], v[124:127], v[108:111], a[240:255]" :  :  : "memory"); \
  asm volatile("s_waitcnt vmcnt(8)\n\ts_barrier" ::: "memory"); \
  KV_HOOK(7); \
  asm volatile("s_waitcnt lgkmcnt(9)\n\tv_mfma_f32_32x32x16_bf16 a[0:15], v[80:83], v[64:67], a[0:15]\n\tds_read_b64_tr_b16 v[112:113], %0 offset:57344" :  : "v"(c.b_rd[1][0]) : "memory"); \
  unsigned so78; \
  asm volatile("s_waitcnt lgkmcnt(8)\n\tv_mfma_f32_32x32x16_bf16 a[16:31], v[84:87], v[64:67], a[16:31]\n\tds_read_b64_tr_b16 v[114:115], %1 offset:59392\n\ts_add_u32 m0, %2, 65536\n\ts_add_u32 %0, %5, 640\n\tbuffer_load_dwordx4 %3, %4, %0 offen offset:0 lds" : "=&s"(so78) : "v"(c.b_rd[1][0]), "s"(c.m0w), "v"(c.va), "s"(c.sa), "s"(c.soa) : "memory", "m0", "scc"); \
  asm volatile("s_waitcnt lgkmcnt(7)\n\tv_mfma_f32_32x32x16_bf16 a[32:47], v[88:91], v[64:67], a[32:47]\n\tds_read_b128 v[96:99], %0 offset:32768" :  : "v"(c.a_rd[1][1]) : "memory"); \
  asm volatile("s_waitcnt lgkmcnt(6)\n\tv_mfma_f32_32x32x16_bf16 a[48:63], v[92:95], v[64:67], a[48:63]\n\tds_read_b64_tr_b16 v[116:117], %0 offset:57344" :  : "v"(c.b_rd[1][1]) : "memory"); \
  asm volatile("s_waitcnt lgkmcnt(6)\n\tv_mfma_f32_32x32x16_bf16 a[64:79], v[80:83], v[68:71], a[64:79]\n\tds_read_b64_tr_b16 v[118:119], %0 offset:59392" :  : "v"(c.b_rd[1][1]) : "memory"); \
  unsigned so79; \
  asm volatile("v_mfma_f32_32x32x16_bf16 a[80:95], v[84:87], v[68:71], a[80:95]\n\tds_read_b64_tr_b16 v[120:121], %1 offset:57344\n\ts_add_u32 m0, %2, 66560\n\ts_add_u32 %0, %5, 17024\n\tbuffer_load_dwordx4 %3, %4, %0 offen offset:0 lds" : "=&s"(so79) : "v"(c.b_rd[1][2]), "s"(c.m0w), "v"(c.va), "s"(c.sa), "s"(c.soa) : "memory", "m0", "scc"); \
  asm volatile("v_mfma_f32_32x32x16_bf16 a[96:111], v[88:91], v[68:71], a[96:111]\n\tds_read_b64_tr_b16 v[122:123], %0 offset:59392" :  : "v"(c.b_rd[1][2]) : "memory", ACL64); \
  asm volatile("v_mfma_f32_32x32x16_bf16 a[112:127], v[92:95], v[68:71], a[112:127]\n\tds_read_b64_tr_b16 v[124:125], %0 offset:57344" :  : "v"(c.b_rd[1][3]) : "memory"); \
  asm volatile("s_waitcnt lgkmcnt(9)\n\tv_mfma_f32_32x32x16_bf16 a[128:143], v[80:83], v[72:75], a[128:143]\n\tds_read_b64_tr_b16 v[126:127], %0 offset:59392" :  : "v"(c.b_rd[1][3]) : "memory"); \
  unsigned so80; \
  asm volatile("v_mfma_f32_32x32x16_bf16 a[144:159], v[84:87], v[72:75], a[144:159]\n\tds_read_b128 v[100:103], %1 offset:34816\n\ts_add_u32 m0, %2, 67584\n\ts_add_u32 %0, %5, 33408\n\tbuffer_load_dwordx4 %3, %4, %0 offen offset:0 lds" : "=&s"(so80) : "v"(c.a_rd[1][1]), "s"(c.m0w), "v"(c.va), "s"(c.sa), "s"(c.soa) : "memory", "m0", "scc"); \
  asm volatile("v_mfma_f32_32x32x16_bf16 a[160:175], v[88:91], v[72:75], a[160:175]\n\tds_read_b128 v[104:107], %0 offset:36864" :  : "v"(c.a_rd[1][1]) : "memory"); \
  asm volatile("v_mfma_f32_32x32x16_bf16 a[176:191], v[92:95], v[72:75], a[176:191]\n\tds_read_b128 v[108:111], %0 offset:38912" :  : "v"(c.a_rd[1][1]) : "memory"); \
  asm volatile("s_waitcnt lgkmcnt(12)\n\tv_mfma_f32_32x32x16_bf16 a[192:207], v[80:83], v[76:79], a[192:207]" :  :  : "memory"); \
  unsigned so81; \
  asm volatile("v_mfma_f32_32x32x16_bf16 a[208:223], v[84:87], v[76:79], a[208:223]\n\ts_add_u32 m0, %1, 68608\n\ts_add_u32 %0, %4, 49792\n\tbuffer_load_dwordx4 %2, %3, %0 offen offset:0 lds" : "=&s"(so81) : "s"(c.m0w), "v"(c.va), "s"(c.sa), "s"(c.soa) : "memory", "m0", "scc"); \
  asm volatile("v_mfma_f32_32x32x16_bf16 a[224:239], v[88:91], v[76:79], a[224:239]" :  :  : "memory", ACL64); \
  asm volatile("v_mfma_f32_32x32x16_bf16 a[240:255], v[92:95], v[76:79], a[240:255]" :  :  : "memory"); \
  asm volatile("s_waitcnt lgkmcnt(9)\n\tv_mfma_f32_32x32x16_bf16 a[0:15], v[112:115], v[96:99], a[0:15]\n\tds_read_b64_tr_b16 v[80:81], %0 offset:16384" :  : "v"(c.b_rd[0][0]) : "memory"); \
  unsigned so82; \
  asm volatile("s_waitcnt lgkmcnt(8)\n\tv_mfma_f32_32x32x16_bf16 a[16:31], v[116:119], v[96:99], a[16:31]\n\tds_read_b64_tr_b16 v[82:83], %1 offset:18432\n\ts_add_u32 m0, %2, 81920\n\ts_add_u32 %0, %5, 2621440\n\tbuffer_load_dwordx4 %3, %4, %0 offen offset:0 lds" : "=&s"(so82) : "v"(c.b_rd[0][0]), "s"(c.m0w), "v"(c.vb[0]), "s"(c.sb), "s"(c.sob) : "memory", "m0", "scc"); \
  asm volatile("s_waitcnt lgkmcnt(7)\n\tv_mfma_f32_32x32x16_bf16 a[32:47], v[120:123], v[96:99], a[32:47]\n\tds_read_b128 v[64:67], %0 offset:0" :  : "v"(c.a_rd[0][0]) : "memory"); \
  asm volatile("s_waitcnt lgkmcnt(6)\n\tv_mfma_f32_32x32x16_bf16 a[48:63], v[124:127], v[96:99], a[48:63]\n\tds_read_b64_tr_b16 v[84:85], %0 offset:16384" :  : "v"(c.b_rd[0][1]) : "memory"); \
  asm volatile("s_waitcnt lgkmcnt(6)\n\tv_mfma_f32_32x32x16_bf16 a[64:79], v[112:115], v[100:103], a[64:79]\n\tds_read_b64_tr_b16 v[86:87], %0 offset:18432" :  : "v"(c.b_rd[0][1]) : "memory"); \
  unsigned so83; \
  asm volatile("v_mfma_f32_32x32x16_bf16 a[80:95], v[116:119], v[100:103], a[80:95]\n\tds_read_b64_tr_b16 v[88:89], %1 offset:16384\n\ts_add_u32 m0, %2, 82944\n\ts_add_u32 %0, %5, 2621440\n\tbuffer_load_dwordx4 %3, %4, %0 offen offset:0 lds" : "=&s"(so83) : "v"(c.b_rd[0][2]), "s"(c.m0w), "v"(c.vb[1]), "s"(c.sb), "s"(c.sob) : "memory", "m0", "scc"); \
  asm volatile("v_mfma_f32_32x32x16_bf16 a[96:111], v[120:123], v[100:103], a[96:111]\n\tds_read_b64_tr_b16 v[90:91], %0 offset:18432" :  : "v"(c.b_rd[0][2]) : "memory", ACL64); \
  asm volatile("v_mfma_f32_32x32x16_bf16 a[112:127], v[124:127], v[100:103], a[112:127]\n\tds_read_b64_tr_b16 v[92:93], %0 offset:16384" :  : "v"(c.b_rd[0][3]) : "memory"); \
  asm volatile("s_waitcnt lgkmcnt(9)\n\tv_mfma_f32_32x32x16_bf16 a[128:143], v[112:115], v[104:107], a[128:143]\n\tds_read_b64_tr_b16 v[94:95], %0 offset:18432" :  : "v"(c.b_rd[0][3]) : "memory"); \
  unsigned so84; \
  asm volatile("v_mfma_f32_32x32x16_bf16 a[144:159], v[116:119], v[104:107], a[144:159]\n\tds_read_b128 v[68:71], %1 offset:2048\n\ts_add_u32 m0, %2, 83968\n\ts_add_u32 %0, %5, 2654208\n\tbuffer_load_dwordx4 %3, %4, %0 offen offset:0 lds" : "=&s"(so84) : "v"(c.a_rd[0][0]), "s"(c.m0w), "v"(c.vb[0]), "s"(c.sb), "s"(c.sob) : "memory", "m0", "scc"); \
  asm volatile("v_mfma_f32_32x32x16_bf16 a[160:175], v[120:123], v[104:107], a[160:175]\n\tds_read_b128 v[72:75], %0 offset:4096" :  : "v"(c.a_rd[0][0]) : "memory"); \
  asm volatile("v_mfma_f32_32x32x16_bf16 a[176:191], v[124:127], v[104:107], a[176:191]\n\tds_read_b128 v[76:79], %0 offset:6144" :  : "v"(c.a_rd[0][0]) : "memory"); \
  asm volatile("s_waitcnt lgkmcnt(12)\n\tv_mfma_f32_32x32x16_bf16 a[192:207], v[112:115], v[108:111], a[192:207]" :  :  : "memory"); \
  unsigned so85; \
  asm volatile("v_mfma_f32_32x32x16_bf16 a[208:223], v[116:119], v[108:111], a[208:223]\n\ts_add_u32 m0, %1, 84992\n\ts_add_u32 %0, %4, 2654208\n\tbuffer_load_dwordx4 %2, %3, %0 offen offset:0 lds" : "=&s"(so85) : "s"(c.m0w), "v"(c.vb[1]), "s"(c.sb), "s"(c.sob) : "memory", "m0", "scc"); \
  asm volatile("v_mfma_f32_32x32x16_bf16 a[224:239], v[120:123], v[108:111], a[224:239]" :  :  : "memory", ACL64); \
  asm volatile("v_mfma_f32_32x32x16_bf16 a[240:255], v[124:127], v[108:111], a[240:255]" :  :  : "memory"); \
  asm volatile("s_waitcnt vmcnt(8)\n\ts_barrier" ::: "memory"); \
  KV_HOOK(8); \
  asm volatile("s_waitcnt lgkmcnt(9)\n\tv_mfma_f32_32x32x16_bf16 a[0:15], v[80:83], v[64:67], a[0:15]\n\tds_read_b64_tr_b16 v[112:113], %0 offset:24576" :  : "v"(c.b_rd[0][0]) : "memory"); \
  unsigned so86; \
  asm volatile("s_waitcnt lgkmcnt(8)\n\tv_mfma_f32_32x32x16_bf16 a[16:31], v[84:87], v[64:67], a[16:31]\n\tds_read_b64_tr_b16 v[114:115], %1 offset:26624\n\ts_add_u32 m0, %2, 98304\n\ts_add_u32 %0, %5, 704\n\tbuffer_load_dwordx4 %3, %4, %0 offen offset:0 lds" : "=&s"(so86) : "v"(c.b_rd[0][0]), "s"(c.m0w), "v"(c.va), "s"(c.sa), "s"(c.soa) : "memory", "m0", "scc"); \
  asm volatile("s_waitcnt lgkmcnt(7)\n\tv_mfma_f32_32x32x16_bf16 a[32:47], v[88:91], v[64:67], a[32:47]\n\tds_read_b128 v[96:99], %0 offset:0" :  : "v"(c.a_rd[0][1]) : "memory"); \
  asm volatile("s_waitcnt lgkmcnt(6)\n\tv_mfma_f32_32x32x16_bf16 a[48:63], v[92:95], v[64:67], a[48:63]\n\tds_read_b64_tr_b16 v[116:117], %0 offset:24576" :  : "v"(c.b_rd[0][1]) : "memory"); \
  asm volatile("s_waitcnt lgkmcnt(6)\n\tv_mfma_f32_32x32x16_bf16 a[64:79], v[80:83], v[68:71], a[64:79]\n\tds_read_b64_tr_b16 v[118:119], %0 offset:26624" :  : "v"(c.b_rd[0][1]) : "memory"); \
  unsigned so87; \
  asm volatile("v_mfma_f32_32x32x16_bf16 a[80:95], v[84:87], v[68:71], a[80:95]\n\tds_read_b64_tr_b16 v[120:121], %1 offset:24576\n\ts_add_u32 m0, %2, 99328\n\ts_add_u32 %0, %5, 17088\n\tbuffer_load_dwordx4 %3, %4, %0 offen offset:0 lds" : "=&s"(so87) : "v"(c.b_rd[0][2]), "s"(c.m0w), "v"(c.va), "s"(c.sa), "s"(c.soa) : "memory", "m0", "scc"); \
  asm volatile("v_mfma_f32_32x32x16_bf16 a[96:111], v[88:91], v[68:71], a[96:111]\n\tds_read_b64_tr_b16 v[122:123], %0 offset:26624" :  : "v"(c.b_rd[0][2]) : "memory", ACL64); \
  asm volatile("v_mfma_f32_32x32x16_bf16 a[112:127], v[92:95], v[68:71], a[112:127]\n\tds_read_b64_tr_b16 v[124:125], %0 offset:24576" :  : "v"(c.b_rd[0][3]) : "memory"); \
  asm volatile("s_waitcnt lgkmcnt(9)\n\tv_mfma_f32_32x32x16_bf16 a[128:143], v[80:83], v[72:75], a[128:143]\n\tds_read_b64_tr_b16 v[126:127], %0 offset:26624" :  : "v"(c.b_rd[0][3]) : "memory"); \
  unsigned so88; \
  asm volatile("v_mfma_f32_32x32x16_bf16 a[144:159], v[84:87], v[72:75], a[144:159]\n\tds_read_b128 v[100:103], %1 offset:2048\n\ts_add_u32 m0, %2, 100352\n\ts_add_u32 %0, %5, 33472\n\tbuffer_load_dwordx4 %3, %4, %0 offen offset:0 lds" : "=&s"(so88) : "v"(c.a_rd[0][1]), "s"(c.m0w), "v"(c.va), "s"(c.sa), "s"(c.soa) : "memory", "m0", "scc"); \
  asm volatile("v_mfma_f32_32x32x16_bf16 a[160:175], v[88:91], v[72:75], a[160:175]\n\tds_read_b128 v[104:107], %0 offset:4096" :  : "v"(c.a_rd[0][1]) : "memory"); \
  asm volatile("v_mfma_f32_32x32x16_bf16 a[176:191], v[92:95], v[72:75], a[176:191]\n\tds_read_b128 v[108:111], %0 offset:6144" :  : "v"(c.a_rd[0][1]) : "memory"); \
  asm volatile("s_waitcnt lgkmcnt(12)\n\tv_mfma_f32_32x32x16_bf16 a[192:207], v[80:83], v[76:79], a[192:207]" :  :  : "memory"); \
  unsigned so89; \
  asm volatile("v_mfma_f32_32x32x16_bf16 a[208:223], v[84:87], v[76:79], a[208:223]\n\ts_add_u32 m0, %1, 101376\n\ts_add_u32 %0, %4, 49856\n\tbuffer_load_dwordx4 %2, %3, %0 offen offset:0 lds" : "=&s"(so89) : "s"(c.m0w), "v"(c.va), "s"(c.sa), "s"(c.soa) : "memory", "m0", "scc"); \
  asm volatile("v_mfma_f32_32x32x16_bf16 a[224:239], v[88:91], v[76:79], a[224:239]" :  :  : "memory", ACL64); \
  asm volatile("v_mfma_f32_32x32x16_bf16 a[240:255], v[92:95], v[76:79], a[240:255]" :  :  : "memory"); \
  asm volatile("s_waitcnt lgkmcnt(9)\n\tv_mfma_f32_32x32x16_bf16 a[0:15], v[112:115], v[96:99], a[0:15]\n\tds_read_b64_tr_b16 v[80:81], %0 offset:49152" :  : "v"(c.b_rd[0][0]) : "memory"); \
  unsigned so90; \
  asm volatile("s_waitcnt lgkmcnt(8)\n\tv_mfma_f32_32x32x16_bf16 a[16:31], v[116:119], v[96:99], a[16:31]\n\tds_read_b64_tr_b16 v[82:83], %1 offset:51200\n\ts_add_u32 m0, %2, 114688\n\ts_add_u32 %0, %5, 2883584\n\tbuffer_load_dwordx4 %3, %4, %0 offen offset:0 lds" : "=&s"(so90) : "v"(c.b_rd[0][0]), "s"(c.m0w), "v"(c.vb[0]), "s"(c.sb), "s"(c.sob) : "memory", "m0", "scc"); \
  asm volatile("s_waitcnt lgkmcnt(7)\n\tv_mfma_f32_32x32x16_bf16 a[32:47], v[120:123], v[96:99], a[32:47]\n\tds_read_b128 v[64:67], %0 offset:32768" :  : "v"(c.a_rd[0][0]) : "memory"); \
  asm volatile("s_waitcnt lgkmcnt(6)\n\tv_mfma_f32_32x32x16_bf16 a[48:63], v[124:127], v[96:99], a[48:63]\n\tds_read_b64_tr_b16 v[84:85], %0 offset:49152" :  : "v"(c.b_rd[0][1]) : "memory"); \
  asm volatile("s_waitcnt lgkmcnt(6)\n\tv_mfma_f32_32x32x16_bf16 a[64:79], v[112:115], v[100:103], a[64:79]\n\tds_read_b64_tr_b16 v[86:87], %0 offset:51200" :  : "v"(c.b_rd[0][1]) : "memory"); \
  unsigned so91; \
  asm volatile("v_mfma_f32_32x32x16_bf16 a[80:95], v[116:119], v[100:103], a[80:95]\n\tds_read_b64_tr_b16 v[88:89], %1 offset:49152\n\ts_add_u32 m0, %2, 115712\n\ts_add_u32 %0, %5, 2883584\n\tbuffer_load_dwordx4 %3, %4, %0 offen offset:0 lds" : "=&s"(so91) : "v"(c.b_rd[0][2]), "s"(c.m0w), "v"(c.vb[1]), "s"(c.sb), "s"(c.sob) : "memory", "m0", "scc"); \
  asm volatile("v_mfma_f32_32x32x16_bf16 a[96:111], v[120:123], v[100:103], a[96:111]\n\tds_read_b64_tr_b16 v[90:91], %0 offset:51200" :  : "v"(c.b_rd[0][2]) : "memory", ACL64); \
  asm volatile("v_mfma_f32_32x32x16_bf16 a[112:127], v[124:127], v[100:103], a[112:127]\n\tds_read_b64_tr_b16 v[92:93], %0 offset:49152" :  : "v"(c.b_rd[0][3]) : "memory"); \
  asm volatile("s_waitcnt lgkmcnt(9)\n\tv_mfma_f32_32x32x16_bf16 a[128:143], v[112:115], v[104:107], a[128:143]\n\tds_read_b64_tr_b16 v[94:95], %0 offset:51200" :  : "v"(c.b_rd[0][3]) : "memory"); \
  unsigned so92; \
  asm volatile("v_mfma_f32_32x32x16_bf16 a[144:159], v[116:119], v[104:107], a[144:159]\n\tds_read_b128 v[68:71], %1 offset:34816\n\ts_add_u32 m0, %2, 116736\n\ts_add_u32 %0, %5, 2916352\n\tbuffer_load_dwordx4 %3, %4, %0 offen offset:0 lds" : "=&s"(so92) : "v"(c.a_rd[0][0]), "s"(c.m0w), "v"(c.vb[0]), "s"(c.sb), "s"(c.sob) : "memory", "m0", "scc"); \
  asm volatile("v_mfma_f32_32x32x16_bf16 a[160:175], v[120:123], v[104:107], a[160:175]\n\tds_read_b128 v[72:75], %0 offset:36864" :  : "v"(c.a_rd[0][0]) : "memory"); \
  asm volatile("v_mfma_f32_32x32x16_bf16 a[176:191], v[124:127], v[104:107], a[176:191]\n\tds_read_b128 v[76:79], %0 offset:38912" :  : "v"(c.a_rd[0][0]) : "memory"); \
  asm volatile("s_waitcnt lgkmcnt(12)\n\tv_mfma_f32_32x32x16_bf16 a[192:207], v[112:115], v[108:111], a[192:207]" :  :  : "memory"); \
  unsigned so93; \
  asm volatile("v_mfma_f32_32x32x16_bf16 a[208:223], v[116:119], v[108:111], a[208:223]\n\ts_add_u32 m0, %1, 117760\n\ts_add_u32 %0, %4, 2916352\n\tbuffer_load_dwordx4 %2, %3, %0 offen offset:0 lds" : "=&s"(so93) : "s"(c.m0w), "v"(c.vb[1]), "s"(c.sb), "s"(c.sob) : "memory", "m0", "scc"); \
  asm volatile("v_mfma_f32_32x32x16_bf16 a[224:239], v[120:123], v[108:111], a[224:239]" :  :  : "memory", ACL64); \
  asm volatile("v_mfma_f32_32x32x16_bf16 a[240:255], v[124:127], v[108:111], a[240:255]" :  :  : "memory"); \
  asm volatile("s_waitcnt vmcnt(8)\n\ts_barrier" ::: "memory"); \
  KV_HOOK(9); \
  asm volatile("s_waitcnt lgkmcnt(9)\n\tv_mfma_f32_32x32x16_bf16 a[0:15], v[80:83], v[64:67], a[0:15]\n\tds_read_b64_tr_b16 v[112:113], %0 offset:57344" :  : "v"(c.b_rd[0][0]) : "memory"); \
  unsigned so94; \
  asm volatile("s_waitcnt lgkmcnt(8)\n\tv_mfma_f32_32x32x16_bf16 a[16:31], v[84:87], v[64:67], a[16:31]\n\tds_read_b64_tr_b16 v[114:115], %1 offset:59392\n\ts_add_u32 m0, %2, 0\n\ts_add_u32 %0, %5, 768\n\tbuffer_load_dwordx4 %3, %4, %0 offen offset:0 lds" : "=&s"(so94) : "v"(c.b_rd[0][0]), "s"(c.m0w), "v"(c.va), "s"(c.sa), "s"(c.soa) : "memory", "m0", "scc"); \
  asm volatile("s_waitcnt lgkmcnt(7)\n\tv_mfma_f32_32x32x16_bf16 a[32:47], v[88:91], v[64:67], a[32:47]\n\tds_read_b128 v[96:99], %0 offset:32768" :  : "v"(c.a_rd[0][1]) : "memory"); \
  asm volatile("s_waitcnt lgkmcnt(6)\n\tv_mfma_f32_32x32x16_bf16 a[48:63], v[92:95], v[64:67], a[48:63]\n\tds_read_b64_tr_b16 v[116:117], %0 offset:57344" :  : "v"(c.b_rd[0][1]) : "memory"); \
  asm volatile("s_waitcnt lgkmcnt(6)\n\tv_mfma_f32_32x32x16_bf16 a[64:79], v[80:83], v[68:71], a[64:79]\n\tds_read_b64_tr_b16 v[118:119], %0 offset:59392" :  : "v"(c.b_rd[0][1]) : "memory"); \
  unsigned so95; \
  asm volatile("v_mfma_f32_32x32x16_bf16 a[80:95], v[84:87], v[68:71], a[80:95]\n\tds_read_b64_tr_b16 v[120:121], %1 offset:57344\n\ts_add_u32 m0, %2, 1024\n\ts_add_u32 %0, %5, 17152\n\tbuffer_load_dwordx4 %3, %4, %0 offen offset:0 lds" : "=&s"(so95) : "v"(c.b_rd[0][2]), "s"(c.m0w), "v"(c.va), "s"(c.sa), "s"(c.soa) : "memory", "m0", "scc"); \
  asm volatile("v_mfma_f32_32x32x16_bf16 a[96:111], v[88:91], v[68:71], a[96:111]\n\tds_read_b64_tr_b16 v[122:123], %0 offset:59392" :  : "v"(c.b_rd[0][2]) : "memory", ACL64); \
  asm volatile("v_mfma_f32_32x32x16_bf16 a[112:127], v[92:95], v[68:71], a[112:127]\n\tds_read_b64_tr_b16 v[124:125], %0 offset:57344" :  : "v"(c.b_rd[0][3]) : "memory"); \
  asm volatile("s_waitcnt lgkmcnt(9)\n\tv_mfma_f32_32x32x16_bf16 a[128:143], v[80:83], v[72:75], a[128:143]\n\tds_read_b64_tr_b16 v[126:127], %0 offset:59392" :  : "v"(c.b_rd[0][3]) : "memory"); \
  unsigned so96; \
  asm volatile("v_mfma_f32_32x32x16_bf16 a[144:159], v[84:87], v[72:75], a[144:159]\n\tds_read_b128 v[100:103], %1 offset:34816\n\ts_add_u32 m0, %2, 2048\n\ts_add_u32 %0, %5, 33536\n\tbuffer_load_dwordx4 %3, %4, %0 offen offset:0 lds" : "=&s"(so96) : "v"(c.a_rd[0][1]), "s"(c.m0w), "v"(c.va), "s"(c.sa), "s"(c.soa) : "memory", "m0", "scc"); \
  asm volatile("v_mfma_f32_32x32x16_bf16 a[160:175], v[88:91], v[72:75], a[160:175]\n\tds_read_b128 v[104:107], %0 offset:36864" :  : "v"(c.a_rd[0][1]) : "memory"); \
  asm volatile("v_mfma_f32_32x32x16_bf16 a[176:191], v[92:95], v[72:75], a[176:191]\n\tds_read_b128 v[108:111], %0 offset:38912" :  : "v"(c.a_rd[0][1]) : "memory"); \
  asm volatile("s_waitcnt lgkmcnt(12)\n\tv_mfma_f32_32x32x16_bf16 a[192:207], v[80:83], v[76:79], a[192:207]" :  :  : "memory"); \
  unsigned so97; \
  asm volatile("v_mfma_f32_32x32x16_bf16 a[208:223], v[84:87], v[76:79], a[208:223]\n\ts_add_u32 m0, %1, 3072\n\ts_add_u32 %0, %4, 49920\n\tbuffer_load_dwordx4 %2, %3, %0 offen offset:0 lds" : "=&s"(so97) : "s"(c.m0w), "v"(c.va), "s"(c.sa), "s"(c.soa) : "memory", "m0", "scc"); \
  asm volatile("v_mfma_f32_32x32x16_bf16 a[224:239], v[88:91], v[76:79], a[224:239]" :  :  : "memory", ACL64); \
  asm volatile("v_mfma_f32_32x32x16_bf16 a[240:255], v[92:95], v[76:79], a[240:255]" :  :  : "memory"); \
  asm volatile("s_waitcnt lgkmcnt(9)\n\tv_mfma_f32_32x32x16_bf16 a[0:15], v[112:115], v[96:99], a[0:15]\n\tds_read_b64_tr_b16 v[80:81], %0 offset:16384" :  : "v"(c.b_rd[1][0]) : "memory"); \
  unsigned so98; \
  asm volatile("s_waitcnt lgkmcnt(8)\n\tv_mfma_f32_32x32x16_bf16 a[16:31], v[116:119], v[96:99], a[16:31]\n\tds_read_b64_tr_b16 v[82:83], %1 offset:18432\n\ts_add_u32 m0, %2, 16384\n\ts_add_u32 %0, %5, 3145728\n\tbuffer_load_dwordx4 %3, %4, %0 offen offset:0 lds" : "=&s"(so98) : "v"(c.b_rd[1][0]), "s"(c.m0w), "v"(c.vb[0]), "s"(c.sb), "s"(c.sob) : "memory", "m0", "scc"); \
  asm volatile("s_waitcnt lgkmcnt(7)\n\tv_mfma_f32_32x32x16_bf16 a[32:47], v[120:123], v[96:99], a[32:47]\n\tds_read_b128 v[64:67], %0 offset:0" :  : "v"(c.a_rd[1][0]) : "memory"); \
  asm volatile("s_waitcnt lgkmcnt(6)\n\tv_mfma_f32_32x32x16_bf16 a[48:63], v[124:127], v[96:99], a[48:63]\n\tds_read_b64_tr_b16 v[84:85], %0 offset:16384" :  : "v"(c.b_rd[1][1]) : "memory"); \
  asm volatile("s_waitcnt lgkmcnt(6)\n\tv_mfma_f32_32x32x16_bf16 a[64:79], v[112:115], v[100:103], a[64:79]\n\tds_read_b64_tr_b16 v[86:87], %0 offset:18432" :  : "v"(c.b_rd[1][1]) : "memory"); \
  unsigned so99; \
  asm volatile("v_mfma_f32_32x32x16_bf16 a[80:95], v[116:119], v[100:103], a[80:95]\n\tds_read_b64_tr_b16 v[88:89], %1 offset:16384\n\ts_add_u32 m0, %2, 17408\n\ts_add_u32 %0, %5, 3145728\n\tbuffer_load_dwordx4 %3, %4, %0 offen offset:0 lds" : "=&s"(so99) : "v"(c.b_rd[1][2]), "s"(c.m0w), "v"(c.vb[1]), "s"(c.sb), "s"(c.sob) : "memory", "m0", "scc"); \
  asm volatile("v_mfma_f32_32x32x16_bf16 a[96:111], v[120:123], v[100:103], a[96:111]\n\tds_read_b64_tr_b16 v[90:91], %0 offset:18432" :  : "v"(c.b_rd[1][2]) : "memory", ACL64); \
  asm volatile("v_mfma_f32_32x32x16_bf16 a[112:127], v[124:127], v[100:103], a[112:127]\n\tds_read_b64_tr_b16 v[92:93], %0 offset:16384" :  : "v"(c.b_rd[1][3]) : "memory"); \
  asm volatile("s_waitcnt lgkmcnt(9)\n\tv_mfma_f32_32x32x16_bf16 a[128:143], v[112:115], v[104:107], a[128:143]\n\tds_read_b64_tr_b16 v[94:95], %0 offset:18432" :  : "v"(c.b_rd[1][3]) : "memory"); \
  unsigned so100; \
  asm volatile("v_mfma_f32_32x32x16_bf16 a[144:159], v[116:119], v[104:107], a[144:159]\n\tds_read_b128 v[68:71], %1 offset:2048\n\ts_add_u32 m0, %2, 18432\n\ts_add_u32 %0, %5, 3178496\n\tbuffer_load_dwordx4 %3, %4, %0 offen offset:0 lds" : "=&s"(so100) : "v"(c.a_rd[1][0]), "s"(c.m0w), "v"(c.vb[0]), "s"(c.sb), "s"(c.sob) : "memory", "m0", "scc"); \
  asm volatile("v_mfma_f32_32x32x16_bf16 a[160:175], v[120:123], v[104:107], a[160:175]\n\tds_read_b128 v[72:75], %0 offset:4096" :  : "v"(c.a_rd[1][0]) : "memory"); \
  asm volatile("v_mfma_f32_32x32x16_bf16 a[176:191], v[124:127], v[104:107], a[176:191]\n\tds_read_b128 v[76:79], %0 offset:6144" :  : "v"(c.a_rd[1][0]) : "memory"); \
  asm volatile("s_waitcnt lgkmcnt(12)\n\tv_mfma_f32_32x32x16_bf16 a[192:207], v[112:115], v[108:111], a[192:207]" :  :  : "memory"); \
  unsigned so101; \
  asm volatile("v_mfma_f32_32x32x16_bf16 a[208:223], v[116:119], v[108:111], a[208:223]\n\ts_add_u32 m0, %1, 19456\n\ts_add_u32 %0, %4, 3178496\n\tbuffer_load_dwordx4 %2, %3, %0 offen offset:0 lds" : "=&s"(so101) : "s"(c.m0w), "v"(c.vb[1]), "s"(c.sb), "s"(c.sob) : "memory", "m0", "scc"); \
  asm volatile("v_mfma_f32_32x32x16_bf16 a[224:239], v[120:123], v[108:111], a[224:239]" :  :  : "memory", ACL64); \
  asm volatile("v_mfma_f32_32x32x16_bf16 a[240:255], v[124:127], v[108:111], a[240:255]" :  :  : "memory"); \
  asm volatile("s_waitcnt vmcnt(8)\n\ts_barrier" ::: "memory"); \
  KV_HOOK(10); \
  asm volatile("s_waitcnt lgkmcnt(9)\n\tv_mfma_f32_32x32x16_bf16 a[0:15], v[80:83], v[64:67], a[0:15]\n\tds_read_b64_tr_b16 v[112:113], %0 offset:24576" :  : "v"(c.b_rd[1][0]) : "memory"); \
  unsigned so102; \
  asm volatile("s_waitcnt lgkmcnt(8)\n\tv_mfma_f32_32x32x16_bf16 a[16:31], v[84:87], v[64:67], a[16:31]\n\tds_read_b64_tr_b16 v[114:115], %1 offset:26624\n\ts_add_u32 m0, %2, 32768\n\ts_add_u32 %0, %5, 832\n\tbuffer_load_dwordx4 %3, %4, %0 offen offset:0 lds" : "=&s"(so102) : "v"(c.b_rd[1][0]), "s"(c.m0w), "v"(c.va), "s"(c.sa), "s"(c.soa) : "memory", "m0", "scc"); \
  asm volatile("s_waitcnt lgkmcnt(7)\n\tv_mfma_f32_32x32x16_bf16 a[32:47], v[88:91], v[64:67], a[32:47]\n\tds_read_b128 v[96:99], %0 offset:0" :  : "v"(c.a_rd[1][1]) : "memory"); \
  asm volatile("s_waitcnt lgkmcnt(6)\n\tv_mfma_f32_32x32x16_bf16 a[48:63], v[92:95], v[64:67], a[48:63]\n\tds_read_b64_tr_b16 v[116:117], %0 offset:24576" :  : "v"(c.b_rd[1][1]) : "memory"); \
  asm volatile("s_waitcnt lgkmcnt(6)\n\tv_mfma_f32_32x32x16_bf16 a[64:79], v[80:83], v[68:71], a[64:79]\n\tds_read_b64_tr_b16 v[118:119], %0 offset:26624" :  : "v"(c.b_rd[1][1]) : "memory"); \
  unsigned so103; \
  asm volatile("v_mfma_f32_32x32x16_bf16 a[80:95], v[84:87], v[68:71], a[80:95]\n\tds_read_b64_tr_b16 v[120:121], %1 offset:24576\n\ts_add_u32 m0, %2, 33792\n\ts_add_u32 %0, %5, 17216\n\tbuffer_load_dwordx4 %3, %4, %0 offen offset:0 lds" : "=&s"(so103) : "v"(c.b_rd[1][2]), "s"(c.m0w), "v"(c.va), "s"(c.sa), "s"(c.soa) : "memory", "m0", "scc"); \
  asm volatile("v_mfma_f32_32x32x16_bf16 a[96:111], v[88:91], v[68:71], a[96:111]\n\tds_read_b64_tr_b16 v[122:123], %0 offset:26624" :  : "v"(c.b_rd[1][2]) : "memory", ACL64); \
  asm volatile("v_mfma_f32_32x32x16_bf16 a[112:127], v[92:95], v[68:71], a[112:127]\n\tds_read_b64_tr_b16 v[124:125], %0 offset:24576" :  : "v"(c.b_rd[1][3]) : "memory"); \
  asm volatile("s_waitcnt lgkmcnt(9)\n\tv_mfma_f32_32x32x16_bf16 a[128:143], v[80:83], v[72:75], a[128:143]\n\tds_read_b64_tr_b16 v[126:127], %0 offset:26624" :  : "v"(c.b_rd[1][3]) : "memory"); \
  unsigned so104; \
  asm volatile("v_mfma_f32_32x32x16_bf16 a[144:159], v[84:87], v[72:75], a[144:159]\n\tds_read_b128 v[100:103], %1 offset:2048\n\ts_add_u32 m0, %2, 34816\n\ts_add_u32 %0, %5, 33600\n\tbuffer_load_dwordx4 %3, %4, %0 offen offset:0 lds" : "=&s"(so104) : "v"(c.a_rd[1][1]), "s"(c.m0w), "v"(c.va), "s"(c.sa), "s"(c.soa) : "memory", "m0", "scc"); \
  asm volatile("v_mfma_f32_32x32x16_bf16 a[160:175], v[88:91], v[72:75], a[160:175]\n\tds_read_b128 v[104:107], %0 offset:4096" :  : "v"(c.a_rd[1][1]) : "memory"); \
  asm volatile("v_mfma_f32_32x32x16_bf16 a[176:191], v[92:95], v[72:75], a[176:191]\n\tds_read_b128 v[108:111], %0 offset:6144" :  : "v"(c.a_rd[1][1]) : "memory"); \
  asm volatile("s_waitcnt lgkmcnt(12)\n\tv_mfma_f32_32x32x16_bf16 a[192:207], v[80:83], v[76:79], a[192:207]" :  :  : "memory"); \
  unsigned so105; \
  asm volatile("v_mfma_f32_32x32x16_bf16 a[208:223], v[84:87], v[76:79], a[208:223]\n\ts_add_u32 m0, %1, 35840\n\ts_add_u32 %0, %4, 49984\n\tbuffer_load_dwordx4 %2, %3, %0 offen offset:0 lds" : "=&s"(so105) : "s"(c.m0w), "v"(c.va), "s"(c.sa), "s"(c.soa) : "memory", "m0", "scc"); \
  asm volatile("v_mfma_f32_32x32x16_bf16 a[224:239], v[88:91], v[76:79], a[224:239]" :  :  : "memory", ACL64); \
  asm volatile("v_mfma_f32_32x32x16_bf16 a[240:255], v[92:95], v[76:79], a[240:255]" :  :  : "memory"); \
  asm volatile("s_waitcnt lgkmcnt(9)\n\tv_mfma_f32_32x32x16_bf16 a[0:15], v[112:115], v[96:99], a[0:15]\n\tds_read_b64_tr_b16 v[80:81], %0 offset:49152" :  : "v"(c.b_rd[1][0]) : "memory"); \
  unsigned so106; \
  asm volatile("s_waitcnt lgkmcnt(8)\n\tv_mfma_f32_32x32x16_bf16 a[16:31], v[116:119], v[96:99], a[16:31]\n\tds_read_b64_tr_b16 v[82:83], %1 offset:51200\n\ts_add_u32 m0, %2, 49152\n\ts_add_u32 %0, %5, 3407872\n\tbuffer_load_dwordx4 %3, %4, %0 offen offset:0 lds" : "=&s"(so106) : "v"(c.b_rd[1][0]), "s"(c.m0w), "v"(c.vb[0]), "s"(c.sb), "s"(c.sob) : "memory", "m0", "scc"); \
  asm volatile("s_waitcnt lgkmcnt(7)\n\tv_mfma_f32_32x32x16_bf16 a[32:47], v[120:123], v[96:99], a[32:47]\n\tds_read_b128 v[64:67], %0 offset:32768" :  : "v"(c.a_rd[1][0]) : "memory"); \
  asm volatile("s_waitcnt lgkmcnt(6)\n\tv_mfma_f32_32x32x16_bf16 a[48:63], v[124:127], v[96:99], a[48:63]\n\tds_read_b64_tr_b16 v[84:85], %0 offset:49152" :  : "v"(c.b_rd[1][1]) : "memory"); \
  asm volatile("s_waitcnt lgkmcnt(6)\n\tv_mfma_f32_32x32x16_bf16 a[64:79], v[112:115], v[100:103], a[64:79]\n\tds_read_b64_tr_b16 v[86:87], %0 offset:51200" :  : "v"(c.b_rd[1][1]) : "memory"); \
  unsigned so107; \
  asm volatile("v_mfma_f32_32x32x16_bf16 a[80:95], v[116:119], v[100:103], a[80:95]\n\tds_read_b64_tr_b16 v[88:89], %1 offset:49152\n\ts_add_u32 m0, %2, 50176\n\ts_add_u32 %0, %5, 3407872\n\tbuffer_load_dwordx4 %3, %4, %0 offen offset:0 lds" : "=&s"(so107) : "v"(c.b_rd[1][2]), "s"(c.m0w), "v"(c.vb[1]), "s"(c.sb), "s"(c.sob) : "memory", "m0", "scc"); \
  asm volatile("v_mfma_f32_32x32x16_bf16 a[96:111], v[120:123], v[100:103], a[96:111]\n\tds_read_b64_tr_b16 v[90:91], %0 offset:51200" :  : "v"(c.b_rd[1][2]) : "memory", ACL64); \
  asm volatile("v_mfma_f32_32x32x16_bf16 a[112:127], v[124:127], v[100:103], a[112:127]\n\tds_read_b64_tr_b16 v[92:93], %0 offset:49152" :  : "v"(c.b_rd[1][3]) : "memory"); \
  asm volatile("s_waitcnt lgkmcnt(9)\n\tv_mfma_f32_32x32x16_bf16 a[128:143], v[112:115], v[104:107], a[128:143]\n\tds_read_b64_tr_b16 v[94:95], %0 offset:51200" :  : "v"(c.b_rd[1][3]) : "memory"); \
  unsigned so108; \
  asm volatile("v_mfma_f32_32x32x16_bf16 a[144:159], v[116:119], v[104:107], a[144:159]\n\tds_read_b128 v[68:71], %1 offset:34816\n\ts_add_u32 m0, %2, 51200\n\ts_add_u32 %0, %5, 3440640\n\tbuffer_load_dwordx4 %3, %4, %0 offen offset:0 lds" : "=&s"(so108) : "v"(c.a_rd[1][0]), "s"(c.m0w), "v"(c.vb[0]), "s"(c.sb), "s"(c.sob) : "memory", "m0", "scc"); \
  asm volatile("v_mfma_f32_32x32x16_bf16 a[160:175], v[120:123], v[104:107], a[160:175]\n\tds_read_b128 v[72:75], %0 offset:36864" :  : "v"(c.a_rd[1][0]) : "memory"); \
  asm volatile("v_mfma_f32_32x32x16_bf16 a[176:191], v[124:127], v[104:107], a[176:191]\n\tds_read_b128 v[76:79], %0 offset:38912" :  : "v"(c.a_rd[1][0]) : "memory"); \
  asm volatile("s_waitcnt lgkmcnt(12)\n\tv_mfma_f32_32x32x16_bf16 a[192:207], v[112:115], v[108:111], a[192:207]" :  :  : "memory"); \
  unsigned so109; \
  asm volatile("v_mfma_f32_32x32x16_bf16 a[208:223], v[116:119], v[108:111], a[208:223]\n\ts_add_u32 m0, %1, 52224\n\ts_add_u32 %0, %4, 3440640\n\tbuffer_load_dwordx4 %2, %3, %0 offen offset:0 lds" : "=&s"(so109) : "s"(c.m0w), "v"(c.vb[1]), "s"(c.sb), "s"(c.sob) : "memory", "m0", "scc"); \
  asm volatile("v_mfma_f32_32x32x16_bf16 a[224:239], v[120:123], v[108:111], a[224:239]" :  :  : "memory", ACL64); \
  asm volatile("v_mfma_f32_32x32x16_bf16 a[240:255], v[124:127], v[108:111], a[240:255]" :  :  : "memory"); \
  asm volatile("s_waitcnt vmcnt(8)\n\ts_barrier" ::: "memory"); \
  KV_HOOK(11); \
  asm volatile("s_waitcnt lgkmcnt(9)\n\tv_mfma_f32_32x32x16_bf16 a[0:15], v[80:83], v[64:67], a[0:15]\n\tds_read_b64_tr_b16 v[112:113], %0 offset:57344" :  : "v"(c.b_rd[1][0]) : "memory"); \
  unsigned so110; \
  asm volatile("s_waitcnt lgkmcnt(8)\n\tv_mfma_f32_32x32x16_bf16 a[16:31], v[84:87], v[64:67], a[16:31]\n\tds_read_b64_tr_b16 v[114:115], %1 offset:59392\n\ts_add_u32 m0, %2, 65536\n\ts_add_u32 %0, %5, 896\n\tbuffer_load_dwordx4 %3, %4, %0 offen offset:0 lds" : "=&s"(so110) : "v"(c.b_rd[1][0]), "s"(c.m0w), "v"(c.va), "s"(c.sa), "s"(c.soa) : "memory", "m0", "scc"); \
  asm volatile("s_waitcnt lgkmcnt(7)\n\tv_mfma_f32_32x32x16_bf16 a[32:47], v[88:91], v[64:67], a[32:47]\n\tds_read_b128 v[96:99], %0 offset:32768" :  : "v"(c.a_rd[1][1]) : "memory"); \
  asm volatile("s_waitcnt lgkmcnt(6)\n\tv_mfma_f32_32x32x16_bf16 a[48:63], v[92:95], v[64:67], a[48:63]\n\tds_read_b64_tr_b16 v[116:117], %0 offset:57344" :  : "v"(c.b_rd[1][1]) : "memory"); \
  asm volatile("s_waitcnt lgkmcnt(6)\n\tv_mfma_f32_32x32x16_bf16 a[64:79], v[80:83], v[68:71], a[64:79]\n\tds_read_b64_tr_b16 v[118:119], %0 offset:59392" :  : "v"(c.b_rd[1][1]) : "memory"); \
  unsigned so111; \
  asm volatile("v_mfma_f32_32x32x16_bf16 a[80:95], v[84:87], v[68:71], a[80:95]\n\tds_read_b64_tr_b16 v[120:121], %1 offset:57344\n\ts_add_u32 m0, %2, 66560\n\ts_add_u32 %0, %5, 17280\n\tbuffer_load_dwordx4 %3, %4, %0 offen offset:0 lds" : "=&s"(so111) : "v"(c.b_rd[1][2]), "s"(c.m0w), "v"(c.va), "s"(c.sa), "s"(c.soa) : "memory", "m0", "scc"); \
  asm volatile("v_mfma_f32_32x32x16_bf16 a[96:111], v[88:91], v[68:71], a[96:111]\n\tds_read_b64_tr_b16 v[122:123], %0 offset:59392" :  : "v"(c.b_rd[1][2]) : "memory", ACL64); \
  asm volatile("v_mfma_f32_32x32x16_bf16 a[112:127], v[92:95], v[68:71], a[112:127]\n\tds_read_b64_tr_b16 v[124:125], %0 offset:57344" :  : "v"(c.b_rd[1][3]) : "memory"); \
  asm volatile("s_waitcnt lgkmcnt(9)\n\tv_mfma_f32_32x32x16_bf16 a[128:143], v[80:83], v[72:75], a[128:143]\n\tds_read_b64_tr_b16 v[126:127], %0 offset:59392" :  : "v"(c.b_rd[1][3]) : "memory"); \
  unsigned so112; \
  asm volatile("v_mfma_f32_32x32x16_bf16 a[144:159], v[84:87], v[72:75], a[144:159]\n\tds_read_b128 v[100:103], %1 offset:34816\n\ts_add_u32 m0, %2, 67584\n\ts_add_u32 %0, %5, 33664\n\tbuffer_load_dwordx4 %3, %4, %0 offen offset:0 lds" : "=&s"(so112) : "v"(c.a_rd[1][1]), "s"(c.m0w), "v"(c.va), "s"(c.sa), "s"(c.soa) : "memory", "m0", "scc"); \
  asm volatile("v_mfma_f32_32x32x16_bf16 a[160:175], v[88:91], v[72:75], a[160:175]\n\tds_read_b128 v[104:107], %0 offset:36864" :  : "v"(c.a_rd[1][1]) : "memory"); \
  asm volatile("v_mfma_f32_32x32x16_bf16 a[176:191], v[92:95], v[72:75], a[176:191]\n\tds_read_b128 v[108:111], %0 offset:38912" :  : "v"(c.a_rd[1][1]) : "memory"); \
  asm volatile("s_waitcnt lgkmcnt(12)\n\tv_mfma_f32_32x32x16_bf16 a[192:207], v[80:83], v[76:79], a[192:207]" :  :  : "memory"); \
  unsigned so113; \
  asm volatile("v_mfma_f32_32x32x16_bf16 a[208:223], v[84:87], v[76:79], a[208:223]\n\ts_add_u32 m0, %1, 68608\n\ts_add_u32 %0, %4, 50048\n\tbuffer_load_dwordx4 %2, %3, %0 offen offset:0 lds" : "=&s"(so113) : "s"(c.m0w), "v"(c.va), "s"(c.sa), "s"(c.soa) : "memory", "m0", "scc"); \
  asm volatile("v_mfma_f32_32x32x16_bf16 a[224:239], v[88:91], v[76:79], a[224:239]" :  :  : "memory", ACL64); \
  asm volatile("v_mfma_f32_32x32x16_bf16 a[240:255], v[92:95], v[76:79], a[240:255]" :  :  : "memory"); \
  asm volatile("s_waitcnt lgkmcnt(9)\n\tv_mfma_f32_32x32x16_bf16 a[0:15], v[112:115], v[96:99], a[0:15]\n\tds_read_b64_tr_b16 v[80:81], %0 offset:16384" :  : "v"(c.b_rd[0][0]) : "memory"); \
  unsigned so114; \
  asm volatile("s_waitcnt lgkmcnt(8)\n\tv_mfma_f32_32x32x16_bf16 a[16:31], v[116:119], v[96:99], a[16:31]\n\tds_read_b64_tr_b16 v[82:83], %1 offset:18432\n\ts_add_u32 m0, %2, 81920\n\ts_add_u32 %0, %5, 3670016\n\tbuffer_load_dwordx4 %3, %4, %0 offen offset:0 lds" : "=&s"(so114) : "v"(c.b_rd[0][0]), "s"(c.m0w), "v"(c.vb[0]), "s"(c.sb), "s"(c.sob) : "memory", "m0", "scc"); \
  asm volatile("s_waitcnt lgkmcnt(7)\n\tv_mfma_f32_32x32x16_bf16 a[32:47], v[120:123], v[96:99], a[32:47]\n\tds_read_b128 v[64:67], %0 offset:0" :  : "v"(c.a_rd[0][0]) : "memory"); \
  asm volatile("s_waitcnt lgkmcnt(6)\n\tv_mfma_f32_32x32x16_bf16 a[48:63], v[124:127], v[96:99], a[48:63]\n\tds_read_b64_tr_b16 v[84:85], %0 offset:16384" :  : "v"(c.b_rd[0][1]) : "memory"); \
  asm volatile("s_waitcnt lgkmcnt(6)\n\tv_mfma_f32_32x32x16_bf16 a[64:79], v[112:115], v[100:103], a[64:79]\n\tds_read_b64_tr_b16 v[86:87], %0 offset:18432" :  : "v"(c.b_rd[0][1]) : "memory"); \
  unsigned so115; \
  asm volatile("v_mfma_f32_32x32x16_bf16 a[80:95], v[116:119], v[100:103], a[80:95]\n\tds_read_b64_tr_b16 v[88:89], %1 offset:16384\n\ts_add_u32 m0, %2, 82944\n\ts_add_u32 %0, %5, 3670016\n\tbuffer_load_dwordx4 %3, %4, %0 offen offset:0 lds" : "=&s"(so115) : "v"(c.b_rd[0][2]), "s"(c.m0w), "v"(c.vb[1]), "s"(c.sb), "s"(c.sob) : "memory", "m0", "scc"); \
  asm volatile("v_mfma_f32_32x32x16_bf16 a[96:111], v[120:123], v[100:103], a[96:111]\n\tds_read_b64_tr_b16 v[90:91], %0 offset:18432" :  : "v"(c.b_rd[0][2]) : "memory", ACL64); \
  asm volatile("v_mfma_f32_32x32x16_bf16 a[112:127], v[124:127], v[100:103], a[112:127]\n\tds_read_b64_tr_b16 v[92:93], %0 offset:16384" :  : "v"(c.b_rd[0][3]) : "memory"); \
  asm volatile("s_waitcnt lgkmcnt(9)\n\tv_mfma_f32_32x32x16_bf16 a[128:143], v[112:115], v[104:107], a[128:143]\n\tds_read_b64_tr_b16 v[94:95], %0 offset:18432" :  : "v"(c.b_rd[0][3]) : "memory"); \
  unsigned so116; \
  asm volatile("v_mfma_f32_32x32x16_bf16 a[144:159], v[116:119], v[104:107], a[144:159]\n\tds_read_b128 v[68:71], %1 offset:2048\n\ts_add_u32 m0, %2, 83968\n\ts_add_u32 %0, %5, 3702784\n\tbuffer_load_dwordx4 %3, %4, %0 offen offset:0 lds" : "=&s"(so116) : "v"(c.a_rd[0][0]), "s"(c.m0w), "v"(c.vb[0]), "s"(c.sb), "s"(c.sob) : "memory", "m0", "scc"); \
  asm volatile("v_mfma_f32_32x32x16_bf16 a[160:175], v[120:123], v[104:107], a[160:175]\n\tds_read_b128 v[72:75], %0 offset:4096" :  : "v"(c.a_rd[0][0]) : "memory"); \
  asm volatile("v_mfma_f32_32x32x16_bf16 a[176:191], v[124:127], v[104:107], a[176:191]\n\tds_read_b128 v[76:79], %0 offset:6144" :  : "v"(c.a_rd[0][0]) : "memory"); \
  asm volatile("s_waitcnt lgkmcnt(12)\n\tv_mfma_f32_32x32x16_bf16 a[192:207], v[112:115], v[108:111], a[192:207]" :  :  : "memory"); \
  unsigned so117; \
  asm volatile("v_mfma_f32_32x32x16_bf16 a[208:223], v[116:119], v[108:111], a[208:223]\n\ts_add_u32 m0, %1, 84992\n\ts_add_u32 %0, %4, 3702784\n\tbuffer_load_dwordx4 %2, %3, %0 offen offset:0 lds" : "=&s"(so117) : "s"(c.m0w), "v"(c.vb[1]), "s"(c.sb), "s"(c.sob) : "memory", "m0", "scc"); \
  asm volatile("v_mfma_f32_32x32x16_bf16 a[224:239], v[120:123], v[108:111], a[224:239]" :  :  : "memory", ACL64); \
  asm volatile("v_mfma_f32_32x32x16_bf16 a[240:255], v[124:127], v[108:111], a[240:255]" :  :  : "memory"); \
  asm volatile("s_waitcnt vmcnt(8)\n\ts_barrier" ::: "memory"); \
  KV_HOOK(12); \
  asm volatile("s_waitcnt lgkmcnt(9)\n\tv_mfma_f32_32x32x16_bf16 a[0:15], v[80:83], v[64:67], a[0:15]\n\tds_read_b64_tr_b16 v[112:113], %0 offset:24576" :  : "v"(c.b_rd[0][0]) : "memory"); \
  unsigned so118; \
  asm volatile("s_waitcnt lgkmcnt(8)\n\tv_mfma_f32_32x32x16_bf16 a[16:31], v[84:87], v[64:67], a[16:31]\n\tds_read_b64_tr_b16 v[114:115], %1 offset:26624\n\ts_add_u32 m0, %2, 98304\n\ts_add_u32 %0, %5, 960\n\tbuffer_load_dwordx4 %3, %4, %0 offen offset:0 lds" : "=&s"(so118) : "v"(c.b_rd[0][0]), "s"(c.m0w), "v"(c.va), "s"(c.sa), "s"(c.soa) : "memory", "m0", "scc"); \
  asm volatile("s_waitcnt lgkmcnt(7)\n\tv_mfma_f32_32x32x16_bf16 a[32:47], v[88:91], v[64:67], a[32:47]\n\tds_read_b128 v[96:99], %0 offset:0" :  : "v"(c.a_rd[0][1]) : "memory"); \
  asm volatile("s_waitcnt lgkmcnt(6)\n\tv_mfma_f32_32x32x16_bf16 a[48:63], v[92:95], v[64:67], a[48:63]\n\tds_read_b64_tr_b16 v[116:117], %0 offset:24576" :  : "v"(c.b_rd[0][1]) : "memory"); \
  asm volatile("s_waitcnt lgkmcnt(6)\n\tv_mfma_f32_32x32x16_bf16 a[64:79], v[80:83], v[68:71], a[64:79]\n\tds_read_b64_tr_b16 v[118:119], %0 offset:26624" :  : "v"(c.b_rd[0][1]) : "memory"); \
  unsigned so119; \
  asm volatile("v_mfma_f32_32x32x16_bf16 a[80:95], v[84:87], v[68:71], a[80:95]\n\tds_read_b64_tr_b16 v[120:121], %1 offset:24576\n\ts_add_u32 m0, %2, 99328\n\ts_add_u32 %0, %5, 17344\n\tbuffer_load_dwordx4 %3, %4, %0 offen offset:0 lds" : "=&s"(so119) : "v"(c.b_rd[0][2]), "s"(c.m0w), "v"(c.va), "s"(c.sa), "s"(c.soa) : "memory", "m0", "scc"); \
  asm volatile("v_mfma_f32_32x32x16_bf16 a[96:111], v[88:91], v[68:71], a[96:111]\n\tds_read_b64_tr_b16 v[122:123], %0 offset:26624" :  : "v"(c.b_rd[0][2]) : "memory", ACL64); \
  asm volatile("v_mfma_f32_32x32x16_bf16 a[112:127], v[92:95], v[68:71], a[112:127]\n\tds_read_b64_tr_b16 v[124:125], %0 offset:24576" :  : "v"(c.b_rd[0][3]) : "memory"); \
  asm volatile("s_waitcnt lgkmcnt(9)\n\tv_mfma_f32_32x32x16_bf16 a[128:143], v[80:83], v[72:75], a[128:143]\n\tds_read_b64_tr_b16 v[126:127], %0 offset:26624" :  : "v"(c.b_rd[0][3]) : "memory"); \
  unsigned so120; \
  asm volatile("v_mfma_f32_32x32x16_bf16 a[144:159], v[84:87], v[72:75], a[144:159]\n\tds_read_b128 v[100:103], %1 offset:2048\n\ts_add_u32 m0, %2, 100352\n\ts_add_u32 %0, %5, 33728\n\tbuffer_load_dwordx4 %3, %4, %0 offen offset:0 lds" : "=&s"(so120) : "v"(c.a_rd[0][1]), "s"(c.m0w), "v"(c.va), "s"(c.sa), "s"(c.soa) : "memory", "m0", "scc"); \
  asm volatile("v_mfma_f32_32x32x16_bf16 a[160:175], v[88:91], v[72:75], a[160:175]\n\tds_read_b128 v[104:107], %0 offset:4096" :  : "v"(c.a_rd[0][1]) : "memory"); \
  asm volatile("v_mfma_f32_32x32x16_bf16 a[176:191], v[92:95], v[72:75], a[176:191]\n\tds_read_b128 v[108:111], %0 offset:6144" :  : "v"(c.a_rd[0][1]) : "memory"); \
  asm volatile("s_waitcnt lgkmcnt(12)\n\tv_mfma_f32_32x32x16_bf16 a[192:207], v[80:83], v[76:79], a[192:207]" :  :  : "memory"); \
  unsigned so121; \
  asm volatile("v_mfma_f32_32x32x16_bf16 a[208:223], v[84:87], v[76:79], a[208:223]\n\ts_add_u32 m0, %1, 101376\n\ts_add_u32 %0, %4, 50112\n\tbuffer_load_dwordx4 %2, %3, %0 offen offset:0 lds" : "=&s"(so121) : "s"(c.m0w), "v"(c.va), "s"(c.sa), "s"(c.soa) : "memory", "m0", "scc"); \
  asm volatile("v_mfma_f32_32x32x16_bf16 a[224:239], v[88:91], v[76:79], a[224:239]" :  :  : "memory", ACL64); \
  asm volatile("v_mfma_f32_32x32x16_bf16 a[240:255], v[92:95], v[76:79], a[240:255]" :  :  : "memory"); \
  asm volatile("s_waitcnt lgkmcnt(9)\n\tv_mfma_f32_32x32x16_bf16 a[0:15], v[112:115], v[96:99], a[0:15]\n\tds_read_b64_tr_b16 v[80:81], %0 offset:49152" :  : "v"(c.b_rd[0][0]) : "memory"); \
  unsigned so122; \
  asm volatile("s_waitcnt lgkmcnt(8)\n\tv_mfma_f32_32x32x16_bf16 a[16:31], v[116:119], v[96:99], a[16:31]\n\tds_read_b64_tr_b16 v[82:83], %1 offset:51200\n\ts_add_u32 m0, %2, 114688\n\ts_add_u32 %0, %5, 3932160\n\tbuffer_load_dwordx4 %3, %4, %0 offen offset:0 lds" : "=&s"(so122) : "v"(c.b_rd[0][0]), "s"(c.m0w), "v"(c.vb[0]), "s"(c.sb), "s"(c.sob) : "memory", "m0", "scc"); \
  asm volatile("s_waitcnt lgkmcnt(7)\n\tv_mfma_f32_32x32x16_bf16 a[32:47], v[120:123], v[96:99], a[32:47]\n\tds_read_b128 v[64:67], %0 offset:32768" :  : "v"(c.a_rd[0][0]) : "memory"); \
  asm volatile("s_waitcnt lgkmcnt(6)\n\tv_mfma_f32_32x32x16_bf16 a[48:63], v[124:127], v[96:99], a[48:63]\n\tds_read_b64_tr_b16 v[84:85], %0 offset:49152" :  : "v"(c.b_rd[0][1]) : "memory"); \
  asm volatile("s_waitcnt lgkmcnt(6)\n\tv_mfma_f32_32x32x16_bf16 a[64:79], v[112:115], v[100:103], a[64:79]\n\tds_read_b64_tr_b16 v[86:87], %0 offset:51200" :  : "v"(c.b_rd[0][1]) : "memory"); \
  unsigned so123; \
  asm volatile("v_mfma_f32_32x32x16_bf16 a[80:95], v[116:119], v[100:103], a[80:95]\n\tds_read_b64_tr_b16 v[88:89], %1 offset:49152\n\ts_add_u32 m0, %2, 115712\n\ts_add_u32 %0, %5, 3932160\n\tbuffer_load_dwordx4 %3, %4, %0 offen offset:0 lds" : "=&s"(so123) : "v"(c.b_rd[0][2]), "s"(c.m0w), "v"(c.vb[1]), "s"(c.sb), "s"(c.sob) : "memory", "m0", "scc"); \
  asm volatile("v_mfma_f32_32x32x16_bf16 a[96:111], v[120:123], v[100:103], a[96:111]\n\tds_read_b64_tr_b16 v[90:91], %0 offset:51200" :  : "v"(c.b_rd[0][2]) : "memory", ACL64); \
  asm volatile("v_mfma_f32_32x32x16_bf16 a[112:127], v[124:127], v[100:103], a[112:127]\n\tds_read_b64_tr_b16 v[92:93], %0 offset:49152" :  : "v"(c.b_rd[0][3]) : "memory"); \
  asm volatile("s_waitcnt lgkmcnt(9)\n\tv_mfma_f32_32x32x16_bf16 a[128:143], v[112:115], v[104:107], a[128:143]\n\tds_read_b64_tr_b16 v[94:95], %0 offset:51200" :  : "v"(c.b_rd[0][3]) : "memory"); \
  unsigned so124; \
  asm volatile("v_mfma_f32_32x32x16_bf16 a[144:159], v[116:119], v[104:107], a[144:159]\n\tds_read_b128 v[68:71], %1 offset:34816\n\ts_add_u32 m0, %2, 116736\n\ts_add_u32 %0, %5, 3964928\n\tbuffer_load_dwordx4 %3, %4, %0 offen offset:0 lds" : "=&s"(so124) : "v"(c.a_rd[0][0]), "s"(c.m0w), "v"(c.vb[0]), "s"(c.sb), "s"(c.sob) : "memory", "m0", "scc"); \
  asm volatile("v_mfma_f32_32x32x16_bf16 a[160:175], v[120:123], v[104:107], a[160:175]\n\tds_read_b128 v[72:75], %0 offset:36864" :  : "v"(c.a_rd[0][0]) : "memory"); \
  asm volatile("v_mfma_f32_32x32x16_bf16 a[176:191], v[124:127], v[104:107], a[176:191]\n\tds_read_b128 v[76:79], %0 offset:38912" :  : "v"(c.a_rd[0][0]) : "memory"); \
  asm volatile("s_waitcnt lgkmcnt(12)\n\tv_mfma_f32_32x32x16_bf16 a[192:207], v[112:115], v[108:111], a[192:207]" :  :  : "memory"); \
  unsigned so125; \
  asm volatile("v_mfma_f32_32x32x16_bf16 a[208:223], v[116:119], v[108:111], a[208:223]\n\ts_add_u32 m0, %1, 117760\n\ts_add_u32 %0, %4, 3964928\n\tbuffer_load_dwordx4 %2, %3, %0 offen offset:0 lds" : "=&s"(so125) : "s"(c.m0w), "v"(c.vb[1]), "s"(c.sb), "s"(c.sob) : "memory", "m0", "scc"); \
  asm volatile("v_mfma_f32_32x32x16_bf16 a[224:239], v[120:123], v[108:111], a[224:239]" :  :  : "memory", ACL64); \
  asm volatile("v_mfma_f32_32x32x16_bf16 a[240:255], v[124:127], v[108:111], a[240:255]" :  :  : "memory"); \
  asm volatile("s_waitcnt vmcnt(8)\n\ts_barrier" ::: "memory"); \
  KV_HOOK(13); \
  asm volatile("s_waitcnt lgkmcnt(9)\n\tv_mfma_f32_32x32x16_bf16 a[0:15], v[80:83], v[64:67], a[0:15]\n\tds_read_b64_tr_b16 v[112:113], %0 offset:57344" :  : "v"(c.b_rd[0][0]) : "memory"); \
  asm volatile("s_waitcnt lgkmcnt(8)\n\tv_mfma_f32_32x32x16_bf16 a[16:31], v[84:87], v[64:67], a[16:31]\n\tds_read_b64_tr_b16 v[114:115], %0 offset:59392\n\ts_add_u32 m0, %1, 135168\n\ts_nop 0\n\tbuffer_load_dwordx4 %2, %3, %4 offen offset:0 sc1 lds" :  : "v"(c.b_rd[0][0]), "s"(p.m0x), "v"(p.vx), "s"(p.sx), "s"(p.sox) : "memory", "m0", "scc"); \
  asm volatile("s_waitcnt lgkmcnt(7)\n\tv_mfma_f32_32x32x16_bf16 a[32:47], v[88:91], v[64:67], a[32:47]\n\tds_read_b128 v[96:99], %0 offset:32768" :  : "v"(c.a_rd[0][1]) : "memory"); \
  asm volatile("s_waitcnt lgkmcnt(6)\n\tv_mfma_f32_32x32x16_bf16 a[48:63], v[92:95], v[64:67], a[48:63]\n\tds_read_b64_tr_b16 v[116:117], %0 offset:57344" :  : "v"(c.b_rd[0][1]) : "memory"); \
  asm volatile("s_waitcnt lgkmcnt(6)\n\tv_mfma_f32_32x32x16_bf16 a[64:79], v[80:83], v[68:71], a[64:79]\n\tds_read_b64_tr_b16 v[118:119], %0 offset:59392" :  : "v"(c.b_rd[0][1]) : "memory"); \
  unsigned so126; \
  asm volatile("v_mfma_f32_32x32x16_bf16 a[80:95], v[84:87], v[68:71], a[80:95]\n\tds_read_b64_tr_b16 v[120:121], %1 offset:57344\n\ts_add_u32 m0, %2, 136192\n\ts_add_u32 %0, %5, 16384\n\tbuffer_load_dwordx4 %3, %4, %0 offen offset:0 sc1 lds" : "=&s"(so126) : "v"(c.b_rd[0][2]), "s"(p.m0x), "v"(p.vx), "s"(p.sx), "s"(p.sox) : "memory", "m0", "scc"); \
  asm volatile("v_mfma_f32_32x32x16_bf16 a[96:111], v[88:91], v[68:71], a[96:111]\n\tds_read_b64_tr_b16 v[122:123], %0 offset:59392" :  : "v"(c.b_rd[0][2]) : "memory", ACL64); \
  asm volatile("v_mfma_f32_32x32x16_bf16 a[112:127], v[92:95], v[68:71], a[112:127]\n\tds_read_b64_tr_b16 v[124:125], %0 offset:57344" :  : "v"(c.b_rd[0][3]) : "memory"); \
  asm volatile("s_waitcnt lgkmcnt(9)\n\tv_mfma_f32_32x32x16_bf16 a[128:143], v[80:83], v[72:75], a[128:143]\n\tds_read_b64_tr_b16 v[126:127], %0 offset:59392" :  : "v"(c.b_rd[0][3]) : "memory"); \
  unsigned so127; \
  asm volatile("v_mfma_f32_32x32x16_bf16 a[144:159], v[84:87], v[72:75], a[144:159]\n\tds_read_b128 v[100:103], %1 offset:34816\n\ts_add_u32 m0, %2, 137216\n\ts_add_u32 %0, %5, 32768\n\tbuffer_load_dwordx4 %3, %4, %0 offen offset:0 sc1 lds" : "=&s"(so127) : "v"(c.a_rd[0][1]), "s"(p.m0x), "v"(p.vx), "s"(p.sx), "s"(p.sox) : "memory", "m0", "scc"); \
  asm volatile("v_mfma_f32_32x32x16_bf16 a[160:175], v[88:91], v[72:75], a[160:175]\n\tds_read_b128 v[104:107], %0 offset:36864" :  : "v"(c.a_rd[0][1]) : "memory"); \
  asm volatile("v_mfma_f32_32x32x16_bf16 a[176:191], v[92:95], v[72:75], a[176:191]\n\tds_read_b128 v[108:111], %0 offset:38912" :  : "v"(c.a_rd[0][1]) : "memory"); \
  asm volatile("s_waitcnt lgkmcnt(12)\n\tv_mfma_f32_32x32x16_bf16 a[192:207], v[80:83], v[76:79], a[192:207]" :  :  : "memory"); \
  unsigned so128; \
  asm volatile("v_mfma_f32_32x32x16_bf16 a[208:223], v[84:87], v[76:79], a[208:223]\n\ts_add_u32 m0, %1, 138240\n\ts_add_u32 %0, %4, 49152\n\tbuffer_load_dwordx4 %2, %3, %0 offen offset:0 sc1 lds" : "=&s"(so128) : "s"(p.m0x), "v"(p.vx), "s"(p.sx), "s"(p.sox) : "memory", "m0", "scc"); \
  asm volatile("v_mfma_f32_32x32x16_bf16 a[224:239], v[88:91], v[76:79], a[224:239]" :  :  : "memory", ACL64); \
  asm volatile("v_mfma_f32_32x32x16_bf16 a[240:255], v[92:95], v[76:79], a[240:255]" :  :  : "memory"); \
  asm volatile("s_waitcnt lgkmcnt(9)\n\tv_mfma_f32_32x32x16_bf16 a[0:15], v[112:115], v[96:99], a[0:15]\n\tds_read_b64_tr_b16 v[80:81], %0 offset:16384" :  : "v"(c.b_rd[1][0]) : "memory"); \
  asm volatile("s_waitcnt lgkmcnt(8)\n\tv_mfma_f32_32x32x16_bf16 a[16:31], v[116:119], v[96:99], a[16:31]\n\tds_read_b64_tr_b16 v[82:83], %0 offset:18432\n\ts_add_u32 m0, %1, 135168\n\ts_nop 0\n\tbuffer_load_dwordx4 %2, %3, %4 offen offset:0 lds" :  : "v"(c.b_rd[1][0]), "s"(p.m0q), "v"(p.vq), "s"(p.sq), "s"(p.soq) : "memory", "m0", "scc"); \
  asm volatile("s_waitcnt lgkmcnt(7)\n\tv_mfma_f32_32x32x16_bf16 a[32:47], v[120:123], v[96:99], a[32:47]\n\tds_read_b128 v[64:67], %0 offset:0" :  : "v"(c.a_rd[1][0]) : "memory"); \
  asm volatile("s_waitcnt lgkmcnt(6)\n\tv_mfma_f32_32x32x16_bf16 a[48:63], v[124:127], v[96:99], a[48:63]\n\tds_read_b64_tr_b16 v[84:85], %0 offset:16384" :  : "v"(c.b_rd[1][1]) : "memory"); \
  asm volatile("s_waitcnt lgkmcnt(6)\n\tv_mfma_f32_32x32x16_bf16 a[64:79], v[112:115], v[100:103], a[64:79]\n\tds_read_b64_tr_b16 v[86:87], %0 offset:18432" :  : "v"(c.b_rd[1][1]) : "memory"); \
  unsigned so129; \
  asm volatile("v_mfma_f32_32x32x16_bf16 a[80:95], v[116:119], v[100:103], a[80:95]\n\tds_read_b64_tr_b16 v[88:89], %1 offset:16384\n\ts_add_u32 m0, %2, 136192\n\ts_add_u32 %0, %5, 32768\n\tbuffer_load_dwordx4 %3, %4, %0 offen offset:0 lds" : "=&s"(so129) : "v"(c.b_rd[1][2]), "s"(p.m0q), "v"(p.vq), "s"(p.sq), "s"(p.soq) : "memory", "m0", "scc"); \
  asm volatile("v_mfma_f32_32x32x16_bf16 a[96:111], v[120:123], v[100:103], a[96:111]\n\tds_read_b64_tr_b16 v[90:91], %0 offset:18432" :  : "v"(c.b_rd[1][2]) : "memory", ACL64); \
  asm volatile("v_mfma_f32_32x32x16_bf16 a[112:127], v[124:127], v[100:103], a[112:127]\n\tds_read_b64_tr_b16 v[92:93], %0 offset:16384" :  : "v"(c.b_rd[1][3]) : "memory"); \
  asm volatile("s_waitcnt lgkmcnt(9)\n\tv_mfma_f32_32x32x16_bf16 a[128:143], v[112:115], v[104:107], a[128:143]\n\tds_read_b64_tr_b16 v[94:95], %0 offset:18432" :  : "v"(c.b_rd[1][3]) : "memory"); \
  asm volatile("v_mfma_f32_32x32x16_bf16 a[144:159], v[116:119], v[104:107], a[144:159]\n\tds_read_b128 v[68:71], %0 offset:2048\n\ts_add_u32 m0, %1, 135168\n\ts_nop 0\n\tbuffer_load_dwordx4 %2, %3, %4 offen offset:0 lds" :  : "v"(c.a_rd[1][0]), "s"(p.m01), "v"(p.v1), "s"(p.s1), "s"(p.so1) : "memory", "m0", "scc"); \
  asm volatile("v_mfma_f32_32x32x16_bf16 a[160:175], v[120:123], v[104:107], a[160:175]\n\tds_read_b128 v[72:75], %0 offset:4096" :  : "v"(c.a_rd[1][0]) : "memory"); \
  asm volatile("v_mfma_f32_32x32x16_bf16 a[176:191], v[124:127], v[104:107], a[176:191]\n\tds_read_b128 v[76:79], %0 offset:6144" :  : "v"(c.a_rd[1][0]) : "memory"); \
  asm volatile("s_waitcnt lgkmcnt(12)\n\tv_mfma_f32_32x32x16_bf16 a[192:207], v[112:115], v[108:111], a[192:207]" :  :  : "memory"); \
  asm volatile("v_mfma_f32_32x32x16_bf16 a[208:223], v[116:119], v[108:111], a[208:223]" :  :  : "memory"); \
  asm volatile("v_mfma_f32_32x32x16_bf16 a[224:239], v[120:123], v[108:111], a[224:239]" :  :  : "memory", ACL64); \
  asm volatile("v_mfma_f32_32x32x16_bf16 a[240:255], v[124:127], v[108:111], a[240:255]" :  :  : "memory"); \
  asm volatile("s_waitcnt vmcnt(7)\n\ts_barrier" ::: "memory"); \
  KV_HOOK(14); \
  asm volatile("s_waitcnt lgkmcnt(9)\n\tv_mfma_f32_32x32x16_bf16 a[0:15], v[80:83], v[64:67], a[0:15]\n\tds_read_b64_tr_b16 v[112:113], %0 offset:24576" :  : "v"(c.b_rd[1][0]) : "memory"); \
  asm volatile("s_waitcnt lgkmcnt(8)\n\tv_mfma_f32_32x32x16_bf16 a[16:31], v[84:87], v[64:67], a[16:31]\n\tds_read_b64_tr_b16 v[114:115], %0 offset:26624" :  : "v"(c.b_rd[1][0]) : "memory"); \
  asm volatile("s_waitcnt lgkmcnt(7)\n\tv_mfma_f32_32x32x16_bf16 a[32:47], v[88:91], v[64:67], a[32:47]\n\tds_read_b128 v[96:99], %0 offset:0" :  : "v"(c.a_rd[1][1]) : "memory"); \
  asm volatile("s_waitcnt lgkmcnt(6)\n\tv_mfma_f32_32x32x16_bf16 a[48:63], v[92:95], v[64:67], a[48:63]\n\tds_read_b64_tr_b16 v[116:117], %0 offset:24576" :  : "v"(c.b_rd[1][1]) : "memory"); \
  asm volatile("s_waitcnt lgkmcnt(6)\n\tv_mfma_f32_32x32x16_bf16 a[64:79], v[80:83], v[68:71], a[64:79]\n\tds_read_b64_tr_b16 v[118:119], %0 offset:26624" :  : "v"(c.b_rd[1][1]) : "memory"); \
  asm volatile("v_mfma_f32_32x32x16_bf16 a[80:95], v[84:87], v[68:71], a[80:95]\n\tds_read_b64_tr_b16 v[120:121], %0 offset:24576" :  : "v"(c.b_rd[1][2]) : "memory"); \
  asm volatile("v_mfma_f32_32x32x16_bf16 a[96:111], v[88:91], v[68:71], a[96:111]\n\tds_read_b64_tr_b16 v[122:123], %0 offset:26624" :  : "v"(c.b_rd[1][2]) : "memory", ACL64); \
  asm volatile("v_mfma_f32_32x32x16_bf16 a[112:127], v[92:95], v[68:71], a[112:127]\n\tds_read_b64_tr_b16 v[124:125], %0 offset:24576" :  : "v"(c.b_rd[1][3]) : "memory"); \
  asm volatile("s_waitcnt lgkmcnt(9)\n\tv_mfma_f32_32x32x16_bf16 a[128:143], v[80:83], v[72:75], a[128:143]\n\tds_read_b64_tr_b16 v[126:127], %0 offset:26624" :  : "v"(c.b_rd[1][3]) : "memory"); \
  asm volatile("v_mfma_f32_32x32x16_bf16 a[144:159], v[84:87], v[72:75], a[144:159]\n\tds_read_b128 v[100:103], %0 offset:2048" :  : "v"(c.a_rd[1][1]) : "memory"); \
  asm volatile("v_mfma_f32_32x32x16_bf16 a[160:175], v[88:91], v[72:75], a[160:175]\n\tds_read_b128 v[104:107], %0 offset:4096" :  : "v"(c.a_rd[1][1]) : "memory"); \
  asm volatile("v_mfma_f32_32x32x16_bf16 a[176:191], v[92:95], v[72:75], a[176:191]\n\tds_read_b128 v[108:111], %0 offset:6144" :  : "v"(c.a_rd[1][1]) : "memory"); \
  asm volatile("s_waitcnt lgkmcnt(12)\n\tv_mfma_f32_32x32x16_bf16 a[192:207], v[80:83], v[76:79], a[192:207]" :  :  : "memory"); \
  asm volatile("v_mfma_f32_32x32x16_bf16 a[208:223], v[84:87], v[76:79], a[208:223]" :  :  : "memory"); \
  asm volatile("v_mfma_f32_32x32x16_bf16 a[224:239], v[88:91], v[76:79], a[224:239]" :  :  : "memory", ACL64); \
  asm volatile("v_mfma_f32_32x32x16_bf16 a[240:255], v[92:95], v[76:79], a[240:255]" :  :  : "memory"); \
  asm volatile("s_waitcnt lgkmcnt(9)\n\tv_mfma_f32_32x32x16_bf16 a[0:15], v[112:115], v[96:99], a[0:15]\n\tds_read_b64_tr_b16 v[80:81], %0 offset:49152" :  : "v"(c.b_rd[1][0]) : "memory"); \
  asm volatile("s_waitcnt lgkmcnt(8)\n\tv_mfma_f32_32x32x16_bf16 a[16:31], v[116:119], v[96:99], a[16:31]\n\tds_read_b64_tr_b16 v[82:83], %0 offset:51200" :  : "v"(c.b_rd[1][0]) : "memory"); \
  asm volatile("s_waitcnt lgkmcnt(7)\n\tv_mfma_f32_32x32x16_bf16 a[32:47], v[120:123], v[96:99], a[32:47]\n\tds_read_b128 v[64:67], %0 offset:32768" :  : "v"(c.a_rd[1][0]) : "memory"); \
  asm volatile("s_waitcnt lgkmcnt(6)\n\tv_mfma_f32_32x32x16_bf16 a[48:63], v[124:127], v[96:99], a[48:63]\n\tds_read_b64_tr_b16 v[84:85], %0 offset:49152" :  : "v"(c.b_rd[1][1]) : "memory"); \
  asm volatile("s_waitcnt lgkmcnt(6)\n\tv_mfma_f32_32x32x16_bf16 a[64:79], v[112:115], v[100:103], a[64:79]\n\tds_read_b64_tr_b16 v[86:87], %0 offset:51200" :  : "v"(c.b_rd[1][1]) : "memory"); \
  asm volatile("v_mfma_f32_32x32x16_bf16 a[80:95], v[116:119], v[100:103], a[80:95]\n\tds_read_b64_tr_b16 v[88:89], %0 offset:49152" :  : "v"(c.b_rd[1][2]) : "memory"); \
  asm volatile("v_mfma_f32_32x32x16_bf16 a[96:111], v[120:123], v[100:103], a[96:111]\n\tds_read_b64_tr_b16 v[90:91], %0 offset:51200" :  : "v"(c.b_rd[1][2]) : "memory", ACL64); \
  asm volatile("v_mfma_f32_32x32x16_bf16 a[112:127], v[124:127], v[100:103], a[112:127]\n\tds_read_b64_tr_b16 v[92:93], %0 offset:49152" :  : "v"(c.b_rd[1][3]) : "memory"); \
  asm volatile("s_waitcnt lgkmcnt(9)\n\tv_mfma_f32_32x32x16_bf16 a[128:143], v[112:115], v[104:107], a[128:143]\n\tds_read_b64_tr_b16 v[94:95], %0 offset:51200" :  : "v"(c.b_rd[1][3]) : "memory"); \
  asm volatile("v_mfma_f32_32x32x16_bf16 a[144:159], v[116:119], v[104:107], a[144:159]\n\tds_read_b128 v[68:71], %0 offset:34816" :  : "v"(c.a_rd[1][0]) : "memory"); \
  asm volatile("v_mfma_f32_32x32x16_bf16 a[160:175], v[120:123], v[104:107], a[160:175]\n\tds_read_b128 v[72:75], %0 offset:36864" :  : "v"(c.a_rd[1][0]) : "memory"); \
  asm volatile("v_mfma_f32_32x32x16_bf16 a[176:191], v[124:127], v[104:107], a[176:191]\n\tds_read_b128 v[76:79], %0 offset:38912" :  : "v"(c.a_rd[1][0]) : "memory"); \
  asm volatile("s_waitcnt lgkmcnt(12)\n\tv_mfma_f32_32x32x16_bf16 a[192:207], v[112:115], v[108:111], a[192:207]" :  :  : "memory"); \
  asm volatile("v_mfma_f32_32x32x16_bf16 a[208:223], v[116:119], v[108:111], a[208:223]" :  :  : "memory"); \
  asm volatile("v_mfma_f32_32x32x16_bf16 a[224:239], v[120:123], v[108:111], a[224:239]" :  :  : "memory", ACL64); \
  asm volatile("v_mfma_f32_32x32x16_bf16 a[240:255], v[124:127], v[108:111], a[240:255]" :  :  : "memory"); \
  asm volatile("s_waitcnt vmcnt(7)\n\ts_barrier" ::: "memory"); \
  KV_HOOK(15); \
  asm volatile("s_waitcnt lgkmcnt(9)\n\tv_mfma_f32_32x32x16_bf16 a[0:15], v[80:83], v[64:67], a[0:15]\n\tds_read_b64_tr_b16 v[112:113], %0 offset:57344" :  : "v"(c.b_rd[1][0]) : "memory"); \
  unsigned so130; \
  asm volatile("s_waitcnt lgkmcnt(8)\n\tv_mfma_f32_32x32x16_bf16 a[16:31], v[84:87], v[64:67], a[16:31]\n\tds_read_b64_tr_b16 v[114:115], %1 offset:59392\n\ts_add_u32 m0, %2, 49152\n\ts_add_u32 %0, %5, 64\n\tbuffer_load_dwordx4 %3, %4, %0 offen offset:0 sc1 lds" : "=&s"(so130) : "v"(c.b_rd[1][0]), "s"(p.m0x), "v"(p.vx), "s"(p.sx), "s"(p.sox) : "memory", "m0", "scc"); \
  asm volatile("s_waitcnt lgkmcnt(7)\n\tv_mfma_f32_32x32x16_bf16 a[32:47], v[88:91], v[64:67], a[32:47]\n\tds_read_b128 v[96:99], %0 offset:32768" :  : "v"(c.a_rd[1][1]) : "memory"); \
  asm volatile("s_waitcnt lgkmcnt(6)\n\tv_mfma_f32_32x32x16_bf16 a[48:63], v[92:95], v[64:67], a[48:63]\n\tds_read_b64_tr_b16 v[116:117], %0 offset:57344" :  : "v"(c.b_rd[1][1]) : "memory"); \
  asm volatile("s_waitcnt lgkmcnt(6)\n\tv_mfma_f32_32x32x16_bf16 a[64:79], v[80:83], v[68:71], a[64:79]\n\tds_read_b64_tr_b16 v[118:119], %0 offset:59392" :  : "v"(c.b_rd[1][1]) : "memory"); \
  unsigned so131; \
  asm volatile("v_mfma_f32_32x32x16_bf16 a[80:95], v[84:87], v[68:71], a[80:95]\n\tds_read_b64_tr_b16 v[120:121], %1 offset:57344\n\ts_add_u32 m0, %2, 50176\n\ts_add_u32 %0, %5, 16448\n\tbuffer_load_dwordx4 %3, %4, %0 offen offset:0 sc1 lds" : "=&s"(so131) : "v"(c.b_rd[1][2]), "s"(p.m0x), "v"(p.vx), "s"(p.sx), "s"(p.sox) : "memory", "m0", "scc"); \
  asm volatile("v_mfma_f32_32x32x16_bf16 a[96:111], v[88:91], v[68:71], a[96:111]\n\tds_read_b64_tr_b16 v[122:123], %0 offset:59392" :  : "v"(c.b_rd[1][2]) : "memory", ACL64); \
  asm volatile("v_mfma_f32_32x32x16_bf16 a[112:127], v[92:95], v[68:71], a[112:127]\n\tds_read_b64_tr_b16 v[124:125], %0 offset:57344" :  : "v"(c.b_rd[1][3]) : "memory"); \
  asm volatile("s_waitcnt lgkmcnt(9)\n\tv_mfma_f32_32x32x16_bf16 a[128:143], v[80:83], v[72:75], a[128:143]\n\tds_read_b64_tr_b16 v[126:127], %0 offset:59392" :  : "v"(c.b_rd[1][3]) : "memory"); \
  unsigned so132; \
  asm volatile("v_mfma_f32_32x32x16_bf16 a[144:159], v[84:87], v[72:75], a[144:159]\n\tds_read_b128 v[100:103], %1 offset:34816\n\ts_add_u32 m0, %2, 51200\n\ts_add_u32 %0, %5, 32832\n\tbuffer_load_dwordx4 %3, %4, %0 offen offset:0 sc1 lds" : "=&s"(so132) : "v"(c.a_rd[1][1]), "s"(p.m0x), "v"(p.vx), "s"(p.sx), "s"(p.sox) : "memory", "m0", "scc"); \
  asm volatile("v_mfma_f32_32x32x16_bf16 a[160:175], v[88:91], v[72:75], a[160:175]\n\tds_read_b128 v[104:107], %0 offset:36864" :  : "v"(c.a_rd[1][1]) : "memory"); \
  asm volatile("v_mfma_f32_32x32x16_bf16 a[176:191], v[92:95], v[72:75], a[176:191]\n\tds_read_b128 v[108:111], %0 offset:38912" :  : "v"(c.a_rd[1][1]) : "memory"); \
  asm volatile("s_waitcnt lgkmcnt(12)\n\tv_mfma_f32_32x32x16_bf16 a[192:207], v[80:83], v[76:79], a[192:207]" :  :  : "memory"); \
  unsigned so133; \
  asm volatile("v_mfma_f32_32x32x16_bf16 a[208:223], v[84:87], v[76:79], a[208:223]\n\ts_add_u32 m0, %1, 52224\n\ts_add_u32 %0, %4, 49216\n\tbuffer_load_dwordx4 %2, %3, %0 offen offset:0 sc1 lds" : "=&s"(so133) : "s"(p.m0x), "v"(p.vx), "s"(p.sx), "s"(p.sox) : "memory", "m0", "scc"); \
  asm volatile("v_mfma_f32_32x32x16_bf16 a[224:239], v[88:91], v[76:79], a[224:239]" :  :  : "memory", ACL64); \
  asm volatile("v_mfma_f32_32x32x16_bf16 a[240:255], v[92:95], v[76:79], a[240:255]" :  :  : "memory"); \
  asm volatile("s_waitcnt lgkmcnt(9)\n\tv_mfma_f32_32x32x16_bf16 a[0:15], v[112:115], v[96:99], a[0:15]" :  :  : "memory"); \
  unsigned so134; \
  asm volatile("s_waitcnt lgkmcnt(7)\n\tv_mfma_f32_32x32x16_bf16 a[16:31], v[116:119], v[96:99], a[16:31]\n\ts_add_u32 m0, %1, 49152\n\ts_add_u32 %0, %4, 262144\n\tbuffer_load_dwordx4 %2, %3, %0 offen offset:0 lds" : "=&s"(so134) : "s"(p.m0q), "v"(p.vq), "s"(p.sq), "s"(p.soq) : "memory", "m0", "scc"); \
  asm volatile("s_waitcnt lgkmcnt(5)\n\tv_mfma_f32_32x32x16_bf16 a[32:47], v[120:123], v[96:99], a[32:47]" :  :  : "memory"); \
  asm volatile("s_waitcnt lgkmcnt(3)\n\tv_mfma_f32_32x32x16_bf16 a[48:63], v[124:127], v[96:99], a[48:63]" :  :  : "memory"); \
  asm volatile("s_waitcnt lgkmcnt(2)\n\tv_mfma_f32_32x32x16_bf16 a[64:79], v[112:115], v[100:103], a[64:79]" :  :  : "memory"); \
  unsigned so135; \
  asm volatile("v_mfma_f32_32x32x16_bf16 a[80:95], v[116:119], v[100:103], a[80:95]\n\ts_add_u32 m0, %1, 50176\n\ts_add_u32 %0, %4, 294912\n\tbuffer_load_dwordx4 %2, %3, %0 offen offset:0 lds" : "=&s"(so135) : "s"(p.m0q), "v"(p.vq), "s"(p.sq), "s"(p.soq) : "memory", "m0", "scc"); \
  asm volatile("v_mfma_f32_32x32x16_bf16 a[96:111], v[120:123], v[100:103], a[96:111]" :  :  : "memory", ACL64); \
  asm volatile("v_mfma_f32_32x32x16_bf16 a[112:127], v[124:127], v[100:103], a[112:127]" :  :  : "memory"); \
  asm volatile("s_waitcnt lgkmcnt(1)\n\tv_mfma_f32_32x32x16_bf16 a[128:143], v[112:115], v[104:107], a[128:143]" :  :  : "memory"); \
  unsigned so136; \
  asm volatile("v_mfma_f32_32x32x16_bf16 a[144:159], v[116:119], v[104:107], a[144:159]\n\ts_add_u32 m0, %1, 49152\n\ts_add_u32 %0, %4, 88064\n\tbuffer_load_dwordx4 %2, %3, %0 offen offset:0 lds" : "=&s"(so136) : "s"(p.m01), "v"(p.v1), "s"(p.s1), "s"(p.so1) : "memory", "m0", "scc"); \
  asm volatile("v_mfma_f32_32x32x16_bf16 a[160:175], v[120:123], v[104:107], a[160:175]" :  :  : "memory"); \
  asm volatile("v_mfma_f32_32x32x16_bf16 a[176:191], v[124:127], v[104:107], a[176:191]" :  :  : "memory"); \
  asm volatile("s_waitcnt lgkmcnt(0)\n\tv_mfma_f32_32x32x16_bf16 a[192:207], v[112:115], v[108:111], a[192:207]" :  :  : "memory"); \
  asm volatile("v_mfma_f32_32x32x16_bf16 a[208:223], v[116:119], v[108:111], a[208:223]" :  :  : "memory"); \
  asm volatile("v_mfma_f32_32x32x16_bf16 a[224:239], v[120:123], v[108:111], a[224:239]" :  :  : "memory", ACL64); \
  asm volatile("v_mfma_f32_32x32x16_bf16 a[240:255], v[124:127], v[108:111], a[240:255]" :  :  : "memory"); \
} while (0)
#define KV_BODY_V(c, p) do { \
  asm volatile("ds_read_b64_tr_b16 v[80:81], %0 offset:16384\n\tds_read_b64_tr_b16 v[82:83], %0 offset:18432\n\tds_read_b128 v[64:67], %1 offset:0\n\tds_read_b64_tr_b16 v[84:85], %2 offset:16384\n\tds_read_b64_tr_b16 v[86:87], %2 offset:18432\n\tds_read_b64_tr_b16 v[88:89], %3 offset:16384\n\tds_read_b64_tr_b16 v[90:91], %3 offset:18432\n\tds_read_b64_tr_b16 v[92:93], %4 offset:16384\n\tds_read_b64_tr_b16 v[94:95], %4 offset:18432\n\tds_read_b128 v[68:71], %1 offset:2048\n\tds_read_b128 v[72:75], %1 offset:4096\n\tds_read_b128 v[76:79], %1 offset:6144" :  : "v"(c.b_rd[0][0]), "v"(c.a_rd[0][0]), "v"(c.b_rd[0][1]), "v"(c.b_rd[0][2]), "v"(c.b_rd[0][3]) : "memory"); \
  asm volatile("s_waitcnt vmcnt(8)\n\ts_barrier" ::: "memory"); \
  KV_HOOK(0); \
  asm volatile("s_waitcnt lgkmcnt(9)\n\tv_mfma_f32_32x32x16_bf16 a[0:15], v[64:67], v[80:83], 0\n\tds_read_b64_tr_b16 v[112:113], %0 offset:24576" :  : "v"(c.b_rd[0][0]) : "memory"); \
  unsigned so158; \
  asm volatile("s_waitcnt lgkmcnt(8)\n\tv_mfma_f32_32x32x16_bf16 a[32:47], v[64:67], v[84:87], 0\n\tds_read_b64_tr_b16 v[114:115], %1 offset:26624\n\ts_add_u32 m0, %2, 98304\n\ts_add_u32 %0, %5, 192\n\tbuffer_load_dwordx4 %3, %4, %0 offen offset:0 lds" : "=&s"(so158) : "v"(c.b_rd[0][0]), "s"(c.m0w), "v"(c.va), "s"(c.sa), "s"(c.soa) : "memory", "m0", "scc"); \
  asm volatile("s_waitcnt lgkmcnt(7)\n\tv_mfma_f32_32x32x16_bf16 a[64:79], v[64:67], v[88:91], 0\n\tds_read_b128 v[96:99], %0 offset:0" :  : "v"(c.a_rd[0][1]) : "memory"); \
  asm volatile("s_waitcnt lgkmcnt(6)\n\tv_mfma_f32_32x32x16_bf16 a[96:111], v[64:67], v[92:95], 0\n\tds_read_b64_tr_b16 v[116:117], %0 offset:24576" :  : "v"(c.b_rd[0][1]) : "memory"); \
  asm volatile("s_waitcnt lgkmcnt(6)\n\tv_mfma_f32_32x32x16_bf16 a[16:31], v[68:71], v[80:83], 0\n\tds_read_b64_tr_b16 v[118:119], %0 offset:26624" :  : "v"(c.b_rd[0][1]) : "memory"); \
  unsigned so159; \
  asm volatile("v_mfma_f32_32x32x16_bf16 a[48:63], v[68:71], v[84:87], 0\n\tds_read_b64_tr_b16 v[120:121], %1 offset:24576\n\ts_add_u32 m0, %2, 99328\n\ts_add_u32 %0, %5, 16576\n\tbuffer_load_dwordx4 %3, %4, %0 offen offset:0 lds" : "=&s"(so159) : "v"(c.b_rd[0][2]), "s"(c.m0w), "v"(c.va), "s"(c.sa), "s"(c.soa) : "memory", "m0", "scc", ACL64); \
  asm volatile("v_mfma_f32_32x32x16_bf16 a[80:95], v[68:71], v[88:91], 0\n\tds_read_b64_tr_b16 v[122:123], %0 offset:26624" :  : "v"(c.b_rd[0][2]) : "memory"); \
  asm volatile("v_mfma_f32_32x32x16_bf16 a[112:127], v[68:71], v[92:95], 0\n\tds_read_b64_tr_b16 v[124:125], %0 offset:24576" :  : "v"(c.b_rd[0][3]) : "memory"); \
  asm volatile("s_waitcnt lgkmcnt(9)\n\tv_mfma_f32_32x32x16_bf16 a[128:143], v[72:75], v[80:83], 0\n\tds_read_b64_tr_b16 v[126:127], %0 offset:26624" :  : "v"(c.b_rd[0][3]) : "memory"); \
  unsigned so160; \
  asm volatile("v_mfma_f32_32x32x16_bf16 a[160:175], v[72:75], v[84:87], 0\n\tds_read_b128 v[100:103], %1 offset:2048\n\ts_add_u32 m0, %2, 100352\n\ts_add_u32 %0, %5, 32960\n\tbuffer_load_dwordx4 %3, %4, %0 offen offset:0 lds" : "=&s"(so160) : "v"(c.a_rd[0][1]), "s"(c.m0w), "v"(c.va), "s"(c.sa), "s"(c.soa) : "memory", "m0", "scc"); \
  asm volatile("v_mfma_f32_32x32x16_bf16 a[192:207], v[72:75], v[88:91], 0\n\tds_read_b128 v[104:107], %0 offset:4096" :  : "v"(c.a_rd[0][1]) : "memory"); \
  asm volatile("v_mfma_f32_32x32x16_bf16 a[224:239], v[72:75], v[92:95], 0\n\tds_read_b128 v[108:111], %0 offset:6144" :  : "v"(c.a_rd[0][1]) : "memory"); \
  asm volatile("s_waitcnt lgkmcnt(12)\n\tv_mfma_f32_32x32x16_bf16 a[144:159], v[76:79], v[80:83], 0" :  :  : "memory"); \
  unsigned so161; \
  asm volatile("v_mfma_f32_32x32x16_bf16 a[176:191], v[76:79], v[84:87], 0\n\ts_add_u32 m0, %1, 101376\n\ts_add_u32 %0, %4, 49344\n\tbuffer_load_dwordx4 %2, %3, %0 offen offset:0 lds" : "=&s"(so161) : "s"(c.m0w), "v"(c.va), "s"(c.sa), "s"(c.soa) : "memory", "m0", "scc", ACL64); \
  asm volatile("v_mfma_f32_32x32x16_bf16 a[208:223], v[76:79], v[88:91], 0" :  :  : "memory"); \
  asm volatile("v_mfma_f32_32x32x16_bf16 a[240:255], v[76:79], v[92:95], 0" :  :  : "memory"); \
  asm volatile("s_waitcnt lgkmcnt(9)\n\tv_mfma_f32_32x32x16_bf16 a[0:15], v[96:99], v[112:115], a[0:15]\n\tds_read_b64_tr_b16 v[80:81], %0 offset:49152" :  : "v"(c.b_rd[0][0]) : "memory"); \
  unsigned so162; \
  asm volatile("s_waitcnt lgkmcnt(8)\n\tv_mfma_f32_32x32x16_bf16 a[32:47], v[96:99], v[116:119], a[32:47]\n\tds_read_b64_tr_b16 v[82:83], %1 offset:51200\n\ts_add_u32 m0, %2, 114688\n\ts_add_u32 %0, %5, 786432\n\tbuffer_load_dwordx4 %3, %4, %0 offen offset:0 lds" : "=&s"(so162) : "v"(c.b_rd[0][0]), "s"(c.m0w), "v"(c.vb[0]), "s"(c.sb), "s"(c.sob) : "memory", "m0", "scc"); \
  asm volatile("s_waitcnt lgkmcnt(7)\n\tv_mfma_f32_32x32x16_bf16 a[64:79], v[96:99], v[120:123], a[64:79]\n\tds_read_b128 v[64:67], %0 offset:32768" :  : "v"(c.a_rd[0][0]) : "memory"); \
  asm volatile("s_waitcnt lgkmcnt(6)\n\tv_mfma_f32_32x32x16_bf16 a[96:111], v[96:99], v[124:127], a[96:111]\n\tds_read_b64_tr_b16 v[84:85], %0 offset:49152" :  : "v"(c.b_rd[0][1]) : "memory"); \
  asm volatile("s_waitcnt lgkmcnt(6)\n\tv_mfma_f32_32x32x16_bf16 a[16:31], v[100:103], v[112:115], a[16:31]\n\tds_read_b64_tr_b16 v[86:87], %0 offset:51200" :  : "v"(c.b_rd[0][1]) : "memory"); \
  unsigned so163; \
  asm volatile("v_mfma_f32_32x32x16_bf16 a[48:63], v[100:103], v[116:119], a[48:63]\n\tds_read_b64_tr_b16 v[88:89], %1 offset:49152\n\ts_add_u32 m0, %2, 115712\n\ts_add_u32 %0, %5, 786432\n\tbuffer_load_dwordx4 %3, %4, %0 offen offset:0 lds" : "=&s"(so163) : "v"(c.b_rd[0][2]), "s"(c.m0w), "v"(c.vb[1]), "s"(c.sb), "s"(c.sob) : "memory", "m0", "scc", ACL64); \
  asm volatile("v_mfma_f32_32x32x16_bf16 a[80:95], v[100:103], v[120:123], a[80:95]\n\tds_read_b64_tr_b16 v[90:91], %0 offset:51200" :  : "v"(c.b_rd[0][2]) : "memory"); \
  asm volatile("v_mfma_f32_32x32x16_bf16 a[112:127], v[100:103], v[124:127], a[112:127]\n\tds_read_b64_tr_b16 v[92:93], %0 offset:49152" :  : "v"(c.b_rd[0][3]) : "memory"); \
  asm volatile("s_waitcnt lgkmcnt(9)\n\tv_mfma_f32_32x32x16_bf16 a[128:143], v[104:107], v[112:115], a[128:143]\n\tds_read_b64_tr_b16 v[94:95], %0 offset:51200" :  : "v"(c.b_rd[0][3]) : "memory"); \
  unsigned so164; \
  asm volatile("v_mfma_f32_32x32x16_bf16 a[160:175], v[104:107], v[116:119], a[160:175]\n\tds_read_b128 v[68:71], %1 offset:34816\n\ts_add_u32 m0, %2, 116736\n\ts_add_u32 %0, %5, 819200\n\tbuffer_load_dwordx4 %3, %4, %0 offen offset:0 lds" : "=&s"(so164) : "v"(c.a_rd[0][0]), "s"(c.m0w), "v"(c.vb[0]), "s"(c.sb), "s"(c.sob) : "memory", "m0", "scc"); \
  asm volatile("v_mfma_f32_32x32x16_bf16 a[192:207], v[104:107], v[120:123], a[192:207]\n\tds_read_b128 v[72:75], %0 offset:36864" :  : "v"(c.a_rd[0][0]) : "memory"); \
  asm volatile("v_mfma_f32_32x32x16_bf16 a[224:239], v[104:107], v[124:127], a[224:239]\n\tds_read_b128 v[76:79], %0 offset:38912" :  : "v"(c.a_rd[0][0]) : "memory"); \
  asm volatile("s_waitcnt lgkmcnt(12)\n\tv_mfma_f32_32x32x16_bf16 a[144:159], v[108:111], v[112:115], a[144:159]" :  :  : "memory"); \
  unsigned so165; \
  asm volatile("v_mfma_f32_32x32x16_bf16 a[176:191], v[108:111], v[116:119], a[176:191]\n\ts_add_u32 m0, %1, 117760\n\ts_add_u32 %0, %4, 819200\n\tbuffer_load_dwordx4 %2, %3, %0 offen offset:0 lds" : "=&s"(so165) : "s"(c.m0w), "v"(c.vb[1]), "s"(c.sb), "s"(c.sob) : "memory", "m0", "scc", ACL64); \
  asm volatile("v_mfma_f32_32x32x16_bf16 a[208:223], v[108:111], v[120:123], a[208:223]" :  :  : "memory"); \
  asm volatile("v_mfma_f32_32x32x16_bf16 a[240:255], v[108:111], v[124:127], a[240:255]" :  :  : "memory"); \
  asm volatile("s_waitcnt vmcnt(8)\n\ts_barrier" ::: "memory"); \
  KV_HOOK(1); \
  asm volatile("s_waitcnt lgkmcnt(9)\n\tv_mfma_f32_32x32x16_bf16 a[0:15], v[64:67], v[80:83], a[0:15]\n\tds_read_b64_tr_b16 v[112:113], %0 offset:57344" :  : "v"(c.b_rd[0][0]) : "memory"); \
  unsigned so166; \
  asm volatile("s_waitcnt lgkmcnt(8)\n\tv_mfma_f32_32x32x16_bf16 a[32:47], v[64:67], v[84:87], a[32:47]\n\tds_read_b64_tr_b16 v[114:115], %1 offset:59392\n\ts_add_u32 m0, %2, 0\n\ts_add_u32 %0, %5, 256\n\tbuffer_load_dwordx4 %3, %4, %0 offen offset:0 lds" : "=&s"(so166) : "v"(c.b_rd[0][0]), "s"(c.m0w), "v"(c.va), "s"(c.sa), "s"(c.soa) : "memory", "m0", "scc"); \
  asm volatile("s_waitcnt lgkmcnt(7)\n\tv_mfma_f32_32x32x16_bf16 a[64:79], v[64:67], v[88:91], a[64:79]\n\tds_read_b128 v[96:99], %0 offset:32768" :  : "v"(c.a_rd[0][1]) : "memory"); \
  asm volatile("s_waitcnt lgkmcnt(6)\n\tv_mfma_f32_32x32x16_bf16 a[96:111], v[64:67], v[92:95], a[96:111]\n\tds_read_b64_tr_b16 v[116:117], %0 offset:57344" :  : "v"(c.b_rd[0][1]) : "memory"); \
  asm volatile("s_waitcnt lgkmcnt(6)\n\tv_mfma_f32_32x32x16_bf16 a[16:31], v[68:71], v[80:83], a[16:31]\n\tds_read_b64_tr_b16 v[118:119], %0 offset:59392" :  : "v"(c.b_rd[0][1]) : "memory"); \
  unsigned so167; \
  asm volatile("v_mfma_f32_32x32x16_bf16 a[48:63], v[68:71], v[84:87], a[48:63]\n\tds_read_b64_tr_b16 v[120:121], %1 offset:57344\n\ts_add_u32 m0, %2, 1024\n\ts_add_u32 %0, %5, 16640\n\tbuffer_load_dwordx4 %3, %4, %0 offen offset:0 lds" : "=&s"(so167) : "v"(c.b_rd[0][2]), "s"(c.m0w), "v"(c.va), "s"(c.sa), "s"(c.soa) : "memory", "m0", "scc", ACL64); \
  asm volatile("v_mfma_f32_32x32x16_bf16 a[80:95], v[68:71], v[88:91], a[80:95]\n\tds_read_b64_tr_b16 v[122:123], %0 offset:59392" :  : "v"(c.b_rd[0][2]) : "memory"); \
  asm volatile("v_mfma_f32_32x32x16_bf16 a[112:127], v[68:71], v[92:95], a[112:127]\n\tds_read_b64_tr_b16 v[124:125], %0 offset:57344" :  : "v"(c.b_rd[0][3]) : "memory"); \
  asm volatile("s_waitcnt lgkmcnt(9)\n\tv_mfma_f32_32x32x16_bf16 a[128:143], v[72:75], v[80:83], a[128:143]\n\tds_read_b64_tr_b16 v[126:127], %0 offset:59392" :  : "v"(c.b_rd[0][3]) : "memory"); \
  unsigned so168; \
  asm volatile("v_mfma_f32_32x32x16_bf16 a[160:175], v[72:75], v[84:87], a[160:175]\n\tds_read_b128 v[100:103], %1 offset:34816\n\ts_add_u32 m0, %2, 2048\n\ts_add_u32 %0, %5, 33024\n\tbuffer_load_dwordx4 %3, %4, %0 offen offset:0 lds" : "=&s"(so168) : "v"(c.a_rd[0][1]), "s"(c.m0w), "v"(c.va), "s"(c.sa), "s"(c.soa) : "memory", "m0", "scc"); \
  asm volatile("v_mfma_f32_32x32x16_bf16 a[192:207], v[72:75], v[88:91], a[192:207]\n\tds_read_b128 v[104:107], %0 offset:36864" :  : "v"(c.a_rd[0][1]) : "memory"); \
  asm volatile("v_mfma_f32_32x32x16_bf16 a[224:239], v[72:75], v[92:95], a[224:239]\n\tds_read_b128 v[108:111], %0 offset:38912" :  : "v"(c.a_rd[0][1]) : "memory"); \
  asm volatile("s_waitcnt lgkmcnt(12)\n\tv_mfma_f32_32x32x16_bf16 a[144:159], v[76:79], v[80:83], a[144:159]" :  :  : "memory"); \
  unsigned so169; \
  asm volatile("v_mfma_f32_32x32x16_bf16 a[176:191], v[76:79], v[84:87], a[176:191]\n\ts_add_u32 m0, %1, 3072\n\ts_add_u32 %0, %4, 49408\n\tbuffer_load_dwordx4 %2, %3, %0 offen offset:0 lds" : "=&s"(so169) : "s"(c.m0w), "v"(c.va), "s"(c.sa), "s"(c.soa) : "memory", "m0", "scc", ACL64); \
  asm volatile("v_mfma_f32_32x32x16_bf16 a[208:223], v[76:79], v[88:91], a[208:223]" :  :  : "memory"); \
  asm volatile("v_mfma_f32_32x32x16_bf16 a[240:255], v[76:79], v[92:95], a[240:255]" :  :  : "memory"); \
  asm volatile("s_waitcnt lgkmcnt(9)\n\tv_mfma_f32_32x32x16_bf16 a[0:15], v[96:99], v[112:115], a[0:15]\n\tds_read_b64_tr_b16 v[80:81], %0 offset:16384" :  : "v"(c.b_rd[1][0]) : "memory"); \
  unsigned so170; \
  asm volatile("s_waitcnt lgkmcnt(8)\n\tv_mfma_f32_32x32x16_bf16 a[32:47], v[96:99], v[116:119], a[32:47]\n\tds_read_b64_tr_b16 v[82:83], %1 offset:18432\n\ts_add_u32 m0, %2, 16384\n\ts_add_u32 %0, %5, 1048576\n\tbuffer_load_dwordx4 %3, %4, %0 offen offset:0 lds" : "=&s"(so170) : "v"(c.b_rd[1][0]), "s"(c.m0w), "v"(c.vb[0]), "s"(c.sb), "s"(c.sob) : "memory", "m0", "scc"); \
  asm volatile("s_waitcnt lgkmcnt(7)\n\tv_mfma_f32_32x32x16_bf16 a[64:79], v[96:99], v[120:123], a[64:79]\n\tds_read_b128 v[64:67], %0 offset:0" :  : "v"(c.a_rd[1][0]) : "memory"); \
  asm volatile("s_waitcnt lgkmcnt(6)\n\tv_mfma_f32_32x32x16_bf16 a[96:111], v[96:99], v[124:127], a[96:111]\n\tds_read_b64_tr_b16 v[84:85], %0 offset:16384" :  : "v"(c.b_rd[1][1]) : "memory"); \
  asm volatile("s_waitcnt lgkmcnt(6)\n\tv_mfma_f32_32x32x16_bf16 a[16:31], v[100:103], v[112:115], a[16:31]\n\tds_read_b64_tr_b16 v[86:87], %0 offset:18432" :  : "v"(c.b_rd[1][1]) : "memory"); \
  unsigned so171; \
  asm volatile("v_mfma_f32_32x32x16_bf16 a[48:63], v[100:103], v[116:119], a[48:63]\n\tds_read_b64_tr_b16 v[88:89], %1 offset:16384\n\ts_add_u32 m0, %2, 17408\n\ts_add_u32 %0, %5, 1048576\n\tbuffer_load_dwordx4 %3, %4, %0 offen offset:0 lds" : "=&s"(so171) : "v"(c.b_rd[1][2]), "s"(c.m0w), "v"(c.vb[1]), "s"(c.sb), "s"(c.sob) : "memory", "m0", "scc", ACL64); \
  asm volatile("v_mfma_f32_32x32x16_bf16 a[80:95], v[100:103], v[120:123], a[80:95]\n\tds_read_b64_tr_b16 v[90:91], %0 offset:18432" :  : "v"(c.b_rd[1][2]) : "memory"); \
  asm volatile("v_mfma_f32_32x32x16_bf16 a[112:127], v[100:103], v[124:127], a[112:127]\n\tds_read_b64_tr_b16 v[92:93], %0 offset:16384" :  : "v"(c.b_rd[1][3]) : "memory"); \
  asm volatile("s_waitcnt lgkmcnt(9)\n\tv_mfma_f32_32x32x16_bf16 a[128:143], v[104:107], v[112:115], a[128:143]\n\tds_read_b64_tr_b16 v[94:95], %0 offset:18432" :  : "v"(c.b_rd[1][3]) : "memory"); \
  unsigned so172; \
  asm volatile("v_mfma_f32_32x32x16_bf16 a[160:175], v[104:107], v[116:119], a[160:175]\n\tds_read_b128 v[68:71], %1 offset:2048\n\ts_add_u32 m0, %2, 18432\n\ts_add_u32 %0, %5, 1081344\n\tbuffer_load_dwordx4 %3, %4, %0 offen offset:0 lds" : "=&s"(so172) : "v"(c.a_rd[1][0]), "s"(c.m0w), "v"(c.vb[0]), "s"(c.sb), "s"(c.sob) : "memory", "m0", "scc"); \
  asm volatile("v_mfma_f32_32x32x16_bf16 a[192:207], v[104:107], v[120:123], a[192:207]\n\tds_read_b128 v[72:75], %0 offset:4096" :  : "v"(c.a_rd[1][0]) : "memory"); \
  asm volatile("v_mfma_f32_32x32x16_bf16 a[224:239], v[104:107], v[124:127], a[224:239]\n\tds_read_b128 v[76:79], %0 offset:6144" :  : "v"(c.a_rd[1][0]) : "memory"); \
  asm volatile("s_waitcnt lgkmcnt(12)\n\tv_mfma_f32_32x32x16_bf16 a[144:159], v[108:111], v[112:115], a[144:159]" :  :  : "memory"); \
  unsigned so173; \
  asm volatile("v_mfma_f32_32x32x16_bf16 a[176:191], v[108:111], v[116:119], a[176:191]\n\ts_add_u32 m0, %1, 19456\n\ts_add_u32 %0, %4, 1081344\n\tbuffer_load_dwordx4 %2, %3, %0 offen offset:0 lds" : "=&s"(so173) : "s"(c.m0w), "v"(c.vb[1]), "s"(c.sb), "s"(c.sob) : "memory", "m0", "scc", ACL64); \
  asm volatile("v_mfma_f32_32x32x16_bf16 a[208:223], v[108:111], v[120:123], a[208:223]" :  :  : "memory"); \
  asm volatile("v_mfma_f32_32x32x16_bf16 a[240:255], v[108:111], v[124:127], a[240:255]" :  :  : "memory"); \
  asm volatile("s_waitcnt vmcnt(8)\n\ts_barrier" ::: "memory"); \
  KV_HOOK(2); \
  asm volatile("s_waitcnt lgkmcnt(9)\n\tv_mfma_f32_32x32x16_bf16 a[0:15], v[64:67], v[80:83], a[0:15]\n\tds_read_b64_tr_b16 v[112:113], %0 offset:24576" :  : "v"(c.b_rd[1][0]) : "memory"); \
  unsigned so174; \
  asm volatile("s_waitcnt lgkmcnt(8)\n\tv_mfma_f32_32x32x16_bf16 a[32:47], v[64:67], v[84:87], a[32:47]\n\tds_read_b64_tr_b16 v[114:115], %1 offset:26624\n\ts_add_u32 m0, %2, 32768\n\ts_add_u32 %0, %5, 320\n\tbuffer_load_dwordx4 %3, %4, %0 offen offset:0 lds" : "=&s"(so174) : "v"(c.b_rd[1][0]), "s"(c.m0w), "v"(c.va), "s"(c.sa), "s"(c.soa) : "memory", "m0", "scc"); \
  asm volatile("s_waitcnt lgkmcnt(7)\n\tv_mfma_f32_32x32x16_bf16 a[64:79], v[64:67], v[88:91], a[64:79]\n\tds_read_b128 v[96:99], %0 offset:0" :  : "v"(c.a_rd[1][1]) : "memory"); \
  asm volatile("s_waitcnt lgkmcnt(6)\n\tv_mfma_f32_32x32x16_bf16 a[96:111], v[64:67], v[92:95], a[96:111]\n\tds_read_b64_tr_b16 v[116:117], %0 offset:24576" :  : "v"(c.b_rd[1][1]) : "memory"); \
  asm volatile("s_waitcnt lgkmcnt(6)\n\tv_mfma_f32_32x32x16_bf16 a[16:31], v[68:71], v[80:83], a[16:31]\n\tds_read_b64_tr_b16 v[118:119], %0 offset:26624" :  : "v"(c.b_rd[1][1]) : "memory"); \
  unsigned so175; \
  asm volatile("v_mfma_f32_32x32x16_bf16 a[48:63], v[68:71], v[84:87], a[48:63]\n\tds_read_b64_tr_b16 v[120:121], %1 offset:24576\n\ts_add_u32 m0, %2, 33792\n\ts_add_u32 %0, %5, 16704\n\tbuffer_load_dwordx4 %3, %4, %0 offen offset:0 lds" : "=&s"(so175) : "v"(c.b_rd[1][2]), "s"(c.m0w), "v"(c.va), "s"(c.sa), "s"(c.soa) : "memory", "m0", "scc", ACL64); \
  asm volatile("v_mfma_f32_32x32x16_bf16 a[80:95], v[68:71], v[88:91], a[80:95]\n\tds_read_b64_tr_b16 v[122:123], %0 offset:26624" :  : "v"(c.b_rd[1][2]) : "memory"); \
  asm volatile("v_mfma_f32_32x32x16_bf16 a[112:127], v[68:71], v[92:95], a[112:127]\n\tds_read_b64_tr_b16 v[124:125], %0 offset:24576" :  : "v"(c.b_rd[1][3]) : "memory"); \
  asm volatile("s_waitcnt lgkmcnt(9)\n\tv_mfma_f32_32x32x16_bf16 a[128:143], v[72:75], v[80:83], a[128:143]\n\tds_read_b64_tr_b16 v[126:127], %0 offset:26624" :  : "v"(c.b_rd[1][3]) : "memory"); \
  unsigned so176; \
  asm volatile("v_mfma_f32_32x32x16_bf16 a[160:175], v[72:75], v[84:87], a[160:175]\n\tds_read_b128 v[100:103], %1 offset:2048\n\ts_add_u32 m0, %2, 34816\n\ts_add_u32 %0, %5, 33088\n\tbuffer_load_dwordx4 %3, %4, %0 offen offset:0 lds" : "=&s"(so176) : "v"(c.a_rd[1][1]), "s"(c.m0w), "v"(c.va), "s"(c.sa), "s"(c.soa) : "memory", "m0", "scc"); \
  asm volatile("v_mfma_f32_32x32x16_bf16 a[192:207], v[72:75], v[88:91], a[192:207]\n\tds_read_b128 v[104:107], %0 offset:4096" :  : "v"(c.a_rd[1][1]) : "memory"); \
  asm volatile("v_mfma_f32_32x32x16_bf16 a[224:239], v[72:75], v[92:95], a[224:239]\n\tds_read_b128 v[108:111], %0 offset:6144" :  : "v"(c.a_rd[1][1]) : "memory"); \
  asm volatile("s_waitcnt lgkmcnt(12)\n\tv_mfma_f32_32x32x16_bf16 a[144:159], v[76:79], v[80:83], a[144:159]" :  :  : "memory"); \
  unsigned so177; \
  asm volatile("v_mfma_f32_32x32x16_bf16 a[176:191], v[76:79], v[84:87], a[176:191]\n\ts_add_u32 m0, %1, 35840\n\ts_add_u32 %0, %4, 49472\n\tbuffer_load_dwordx4 %2, %3, %0 offen offset:0 lds" : "=&s"(so177) : "s"(c.m0w), "v"(c.va), "s"(c.sa), "s"(c.soa) : "memory", "m0", "scc", ACL64); \
  asm volatile("v_mfma_f32_32x32x16_bf16 a[208:223], v[76:79], v[88:91], a[208:223]" :  :  : "memory"); \
  asm volatile("v_mfma_f32_32x32x16_bf16 a[240:255], v[76:79], v[92:95], a[240:255]" :  :  : "memory"); \
  asm volatile("s_waitcnt lgkmcnt(9)\n\tv_mfma_f32_32x32x16_bf16 a[0:15], v[96:99], v[112:115], a[0:15]\n\tds_read_b64_tr_b16 v[80:81], %0 offset:49152" :  : "v"(c.b_rd[1][0]) : "memory"); \
  unsigned so178; \
  asm volatile("s_waitcnt lgkmcnt(8)\n\tv_mfma_f32_32x32x16_bf16 a[32:47], v[96:99], v[116:119], a[32:47]\n\tds_read_b64_tr_b16 v[82:83], %1 offset:51200\n\ts_add_u32 m0, %2, 49152\n\ts_add_u32 %0, %5, 1310720\n\tbuffer_load_dwordx4 %3, %4, %0 offen offset:0 lds" : "=&s"(so178) : "v"(c.b_rd[1][0]), "s"(c.m0w), "v"(c.vb[0]), "s"(c.sb), "s"(c.sob) : "memory", "m0", "scc"); \
  asm volatile("s_waitcnt lgkmcnt(7)\n\tv_mfma_f32_32x32x16_bf16 a[64:79], v[96:99], v[120:123], a[64:79]\n\tds_read_b128 v[64:67], %0 offset:32768" :  : "v"(c.a_rd[1][0]) : "memory"); \
  asm volatile("s_waitcnt lgkmcnt(6)\n\tv_mfma_f32_32x32x16_bf16 a[96:111], v[96:99], v[124:127], a[96:111]\n\tds_read_b64_tr_b16 v[84:85], %0 offset:49152" :  : "v"(c.b_rd[1][1]) : "memory"); \
  asm volatile("s_waitcnt lgkmcnt(6)\n\tv_mfma_f32_32x32x16_bf16 a[16:31], v[100:103], v[112:115], a[16:31]\n\tds_read_b64_tr_b16 v[86:87], %0 offset:51200" :  : "v"(c.b_rd[1][1]) : "memory"); \
  unsigned so179; \
  asm volatile("v_mfma_f32_32x32x16_bf16 a[48:63], v[100:103], v[116:119], a[48:63]\n\tds_read_b64_tr_b16 v[88:89], %1 offset:49152\n\ts_add_u32 m0, %2, 50176\n\ts_add_u32 %0, %5, 1310720\n\tbuffer_load_dwordx4 %3, %4, %0 offen offset:0 lds" : "=&s"(so179) : "v"(c.b_rd[1][2]), "s"(c.m0w), "v"(c.vb[1]), "s"(c.sb), "s"(c.sob) : "memory", "m0", "scc", ACL64); \
  asm volatile("v_mfma_f32_32x32x16_bf16 a[80:95], v[100:103], v[120:123], a[80:95]\n\tds_read_b64_tr_b16 v[90:91], %0 offset:51200" :  : "v"(c.b_rd[1][2]) : "memory"); \
  asm volatile("v_mfma_f32_32x32x16_bf16 a[112:127], v[100:103], v[124:127], a[112:127]\n\tds_read_b64_tr_b16 v[92:93], %0 offset:49152" :  : "v"(c.b_rd[1][3]) : "memory"); \
  asm volatile("s_waitcnt lgkmcnt(9)\n\tv_mfma_f32_32x32x16_bf16 a[128:143], v[104:107], v[112:115], a[128:143]\n\tds_read_b64_tr_b16 v[94:95], %0 offset:51200" :  : "v"(c.b_rd[1][3]) : "memory"); \
  unsigned so180; \
  asm volatile("v_mfma_f32_32x32x16_bf16 a[160:175], v[104:107], v[116:119], a[160:175]\n\tds_read_b128 v[68:71], %1 offset:34816\n\ts_add_u32 m0, %2, 51200\n\ts_add_u32 %0, %5, 1343488\n\tbuffer_load_dwordx4 %3, %4, %0 offen offset:0 lds" : "=&s"(so180) : "v"(c.a_rd[1][0]), "s"(c.m0w), "v"(c.vb[0]), "s"(c.sb), "s"(c.sob) : "memory", "m0", "scc"); \
  asm volatile("v_mfma_f32_32x32x16_bf16 a[192:207], v[104:107], v[120:123], a[192:207]\n\tds_read_b128 v[72:75], %0 offset:36864" :  : "v"(c.a_rd[1][0]) : "memory"); \
  asm volatile("v_mfma_f32_32x32x16_bf16 a[224:239], v[104:107], v[124:127], a[224:239]\n\tds_read_b128 v[76:79], %0 offset:38912" :  : "v"(c.a_rd[1][0]) : "memory"); \
  asm volatile("s_waitcnt lgkmcnt(12)\n\tv_mfma_f32_32x32x16_bf16 a[144:159], v[108:111], v[112:115], a[144:159]" :  :  : "memory"); \
  unsigned so181; \
  asm volatile("v_mfma_f32_32x32x16_bf16 a[176:191], v[108:111], v[116:119], a[176:191]\n\ts_add_u32 m0, %1, 52224\n\ts_add_u32 %0, %4, 1343488\n\tbuffer_load_dwordx4 %2, %3, %0 offen offset:0 lds" : "=&s"(so181) : "s"(c.m0w), "v"(c.vb[1]), "s"(c.sb), "s"(c.sob) : "memory", "m0", "scc", ACL64); \
  asm volatile("v_mfma_f32_32x32x16_bf16 a[208:223], v[108:111], v[120:123], a[208:223]" :  :  : "memory"); \
  asm volatile("v_mfma_f32_32x32x16_bf16 a[240:255], v[108:111], v[124:127], a[240:255]" :  :  : "memory"); \
  asm volatile("s_waitcnt vmcnt(8)\n\ts_barrier" ::: "memory"); \
  KV_HOOK(3); \
  asm volatile("s_waitcnt lgkmcnt(9)\n\tv_mfma_f32_32x32x16_bf16 a[0:15], v[64:67], v[80:83], a[0:15]\n\tds_read_b64_tr_b16 v[112:113], %0 offset:57344" :  : "v"(c.b_rd[1][0]) : "memory"); \
  unsigned so182; \
  asm volatile("s_waitcnt lgkmcnt(8)\n\tv_mfma_f32_32x32x16_bf16 a[32:47], v[64:67], v[84:87], a[32:47]\n\tds_read_b64_tr_b16 v[114:115], %1 offset:59392\n\ts_add_u32 m0, %2, 65536\n\ts_add_u32 %0, %5, 384\n\tbuffer_load_dwordx4 %3, %4, %0 offen offset:0 lds" : "=&s"(so182) : "v"(c.b_rd[1][0]), "s"(c.m0w), "v"(c.va), "s"(c.sa), "s"(c.soa) : "memory", "m0", "scc"); \
  asm volatile("s_waitcnt lgkmcnt(7)\n\tv_mfma_f32_32x32x16_bf16 a[64:79], v[64:67], v[88:91], a[64:79]\n\tds_read_b128 v[96:99], %0 offset:32768" :  : "v"(c.a_rd[1][1]) : "memory"); \
  asm volatile("s_waitcnt lgkmcnt(6)\n\tv_mfma_f32_32x32x16_bf16 a[96:111], v[64:67], v[92:95], a[96:111]\n\tds_read_b64_tr_b16 v[116:117], %0 offset:57344" :  : "v"(c.b_rd[1][1]) : "memory"); \
  asm volatile("s_waitcnt lgkmcnt(6)\n\tv_mfma_f32_32x32x16_bf16 a[16:31], v[68:71], v[80:83], a[16:31]\n\tds_read_b64_tr_b16 v[118:119], %0 offset:59392" :  : "v"(c.b_rd[1][1]) : "memory"); \
  unsigned so183; \
  asm volatile("v_mfma_f32_32x32x16_bf16 a[48:63], v[68:71], v[84:87], a[48:63]\n\tds_read_b64_tr_b16 v[120:121], %1 offset:57344\n\ts_add_u32 m0, %2, 66560\n\ts_add_u32 %0, %5, 16768\n\tbuffer_load_dwordx4 %3, %4, %0 offen offset:0 lds" : "=&s"(so183) : "v"(c.b_rd[1][2]), "s"(c.m0w), "v"(c.va), "s"(c.sa), "s"(c.soa) : "memory", "m0", "scc", ACL64); \
  asm volatile("v_mfma_f32_32x32x16_bf16 a[80:95], v[68:71], v[88:91], a[80:95]\n\tds_read_b64_tr_b16 v[122:123], %0 offset:59392" :  : "v"(c.b_rd[1][2]) : "memory"); \
  asm volatile("v_mfma_f32_32x32x16_bf16 a[112:127], v[68:71], v[92:95], a[112:127]\n\tds_read_b64_tr_b16 v[124:125], %0 offset:57344" :  : "v"(c.b_rd[1][3]) : "memory"); \
  asm volatile("s_waitcnt lgkmcnt(9)\n\tv_mfma_f32_32x32x16_bf16 a[128:143], v[72:75], v[80:83], a[128:143]\n\tds_read_b64_tr_b16 v[126:127], %0 offset:59392" :  : "v"(c.b_rd[1][3]) : "memory"); \
  unsigned so184; \
  asm volatile("v_mfma_f32_32x32x16_bf16 a[160:175], v[72:75], v[84:87], a[160:175]\n\tds_read_b128 v[100:103], %1 offset:34816\n\ts_add_u32 m0, %2, 67584\n\ts_add_u32 %0, %5, 33152\n\tbuffer_load_dwordx4 %3, %4, %0 offen offset:0 lds" : "=&s"(so184) : "v"(c.a_rd[1][1]), "s"(c.m0w), "v"(c.va), "s"(c.sa), "s"(c.soa) : "memory", "m0", "scc"); \
  asm volatile("v_mfma_f32_32x32x16_bf16 a[192:207], v[72:75], v[88:91], a[192:207]\n\tds_read_b128 v[104:107], %0 offset:36864" :  : "v"(c.a_rd[1][1]) : "memory"); \
  asm volatile("v_mfma_f32_32x32x16_bf16 a[224:239], v[72:75], v[92:95], a[224:239]\n\tds_read_b128 v[108:111], %0 offset:38912" :  : "v"(c.a_rd[1][1]) : "memory"); \
  asm volatile("s_waitcnt lgkmcnt(12)\n\tv_mfma_f32_32x32x16_bf16 a[144:159], v[76:79], v[80:83], a[144:159]" :  :  : "memory"); \
  unsigned so185; \
  asm volatile("v_mfma_f32_32x32x16_bf16 a[176:191], v[76:79], v[84:87], a[176:191]\n\ts_add_u32 m0, %1, 68608\n\ts_add_u32 %0, %4, 49536\n\tbuffer_load_dwordx4 %2, %3, %0 offen offset:0 lds" : "=&s"(so185) : "s"(c.m0w), "v"(c.va), "s"(c.sa), "s"(c.soa) : "memory", "m0", "scc", ACL64); \
  asm volatile("v_mfma_f32_32x32x16_bf16 a[208:223], v[76:79], v[88:91], a[208:223]" :  :  : "memory"); \
  asm volatile("v_mfma_f32_32x32x16_bf16 a[240:255], v[76:79], v[92:95], a[240:255]" :  :  : "memory"); \
  asm volatile("s_waitcnt lgkmcnt(9)\n\tv_mfma_f32_32x32x16_bf16 a[0:15], v[96:99], v[112:115], a[0:15]\n\tds_read_b64_tr_b16 v[80:81], %0 offset:16384" :  : "v"(c.b_rd[0][0]) : "memory"); \
  unsigned so186; \
  asm volatile("s_waitcnt lgkmcnt(8)\n\tv_mfma_f32_32x32x16_bf16 a[32:47], v[96:99], v[116:119], a[32:47]\n\tds_read_b64_tr_b16 v[82:83], %1 offset:18432\n\ts_add_u32 m0, %2, 81920\n\ts_add_u32 %0, %5, 1572864\n\tbuffer_load_dwordx4 %3, %4, %0 offen offset:0 lds" : "=&s"(so186) : "v"(c.b_rd[0][0]), "s"(c.m0w), "v"(c.vb[0]), "s"(c.sb), "s"(c.sob) : "memory", "m0", "scc"); \
  asm volatile("s_waitcnt lgkmcnt(7)\n\tv_mfma_f32_32x32x16_bf16 a[64:79], v[96:99], v[120:123], a[64:79]\n\tds_read_b128 v[64:67], %0 offset:0" :  : "v"(c.a_rd[0][0]) : "memory"); \
  asm volatile("s_waitcnt lgkmcnt(6)\n\tv_mfma_f32_32x32x16_bf16 a[96:111], v[96:99], v[124:127], a[96:111]\n\tds_read_b64_tr_b16 v[84:85], %0 offset:16384" :  : "v"(c.b_rd[0][1]) : "memory"); \
  asm volatile("s_waitcnt lgkmcnt(6)\n\tv_mfma_f32_32x32x16_bf16 a[16:31], v[100:103], v[112:115], a[16:31]\n\tds_read_b64_tr_b16 v[86:87], %0 offset:18432" :  : "v"(c.b_rd[0][1]) : "memory"); \
  unsigned so187; \
  asm volatile("v_mfma_f32_32x32x16_bf16 a[48:63], v[100:103], v[116:119], a[48:63]\n\tds_read_b64_tr_b16 v[88:89], %1 offset:16384\n\ts_add_u32 m0, %2, 82944\n\ts_add_u32 %0, %5, 1572864\n\tbuffer_load_dwordx4 %3, %4, %0 offen offset:0 lds" : "=&s"(so187) : "v"(c.b_rd[0][2]), "s"(c.m0w), "v"(c.vb[1]), "s"(c.sb), "s"(c.sob) : "memory", "m0", "scc", ACL64); \
  asm volatile("v_mfma_f32_32x32x16_bf16 a[80:95], v[100:103], v[120:123], a[80:95]\n\tds_read_b64_tr_b16 v[90:91], %0 offset:18432" :  : "v"(c.b_rd[0][2]) : "memory"); \
  asm volatile("v_mfma_f32_32x32x16_bf16 a[112:127], v[100:103], v[124:127], a[112:127]\n\tds_read_b64_tr_b16 v[92:93], %0 offset:16384" :  : "v"(c.b_rd[0][3]) : "memory"); \
  asm volatile("s_waitcnt lgkmcnt(9)\n\tv_mfma_f32_32x32x16_bf16 a[128:143], v[104:107], v[112:115], a[128:143]\n\tds_read_b64_tr_b16 v[94:95], %0 offset:18432" :  : "v"(c.b_rd[0][3]) : "memory"); \
  unsigned so188; \
  asm volatile("v_mfma_f32_32x32x16_bf16 a[160:175], v[104:107], v[116:119], a[160:175]\n\tds_read_b128 v[68:71], %1 offset:2048\n\ts_add_u32 m0, %2, 83968\n\ts_add_u32 %0, %5, 1605632\n\tbuffer_load_dwordx4 %3, %4, %0 offen offset:0 lds" : "=&s"(so188) : "v"(c.a_rd[0][0]), "s"(c.m0w), "v"(c.vb[0]), "s"(c.sb), "s"(c.sob) : "memory", "m0", "scc"); \
  asm volatile("v_mfma_f32_32x32x16_bf16 a[192:207], v[104:107], v[120:123], a[192:207]\n\tds_read_b128 v[72:75], %0 offset:4096" :  : "v"(c.a_rd[0][0]) : "memory"); \
  asm volatile("v_mfma_f32_32x32x16_bf16 a[224:239], v[104:107], v[124:127], a[224:239]\n\tds_read_b128 v[76:79], %0 offset:6144" :  : "v"(c.a_rd[0][0]) : "memory"); \
  asm volatile("s_waitcnt lgkmcnt(12)\n\tv_mfma_f32_32x32x16_bf16 a[144:159], v[108:111], v[112:115], a[144:159]" :  :  : "memory"); \
  unsigned so189; \
  asm volatile("v_mfma_f32_32x32x16_bf16 a[176:191], v[108:111], v[116:119], a[176:191]\n\ts_add_u32 m0, %1, 84992\n\ts_add_u32 %0, %4, 1605632\n\tbuffer_load_dwordx4 %2, %3, %0 offen offset:0 lds" : "=&s"(so189) : "s"(c.m0w), "v"(c.vb[1]), "s"(c.sb), "s"(c.sob) : "memory", "m0", "scc", ACL64); \
  asm volatile("v_mfma_f32_32x32x16_bf16 a[208:223], v[108:111], v[120:123], a[208:223]" :  :  : "memory"); \
  asm volatile("v_mfma_f32_32x32x16_bf16 a[240:255], v[108:111], v[124:127], a[240:255]" :  :  : "memory"); \
  asm volatile("s_waitcnt vmcnt(8)\n\ts_barrier" ::: "memory"); \
  KV_HOOK(4); \
  asm volatile("s_waitcnt lgkmcnt(9)\n\tv_mfma_f32_32x32x16_bf16 a[0:15], v[64:67], v[80:83], a[0:15]\n\tds_read_b64_tr_b16 v[112:113], %0 offset:24576" :  : "v"(c.b_rd[0][0]) : "memory"); \
  unsigned so190; \
  asm volatile("s_waitcnt lgkmcnt(8)\n\tv_mfma_f32_32x32x16_bf16 a[32:47], v[64:67], v[84:87], a[32:47]\n\tds_read_b64_tr_b16 v[114:115], %1 offset:26624\n\ts_add_u32 m0, %2, 98304\n\ts_add_u32 %0, %5, 448\n\tbuffer_load_dwordx4 %3, %4, %0 offen offset:0 lds" : "=&s"(so190) : "v"(c.b_rd[0][0]), "s"(c.m0w), "v"(c.va), "s"(c.sa), "s"(c.soa) : "memory", "m0", "scc"); \
  asm volatile("s_waitcnt lgkmcnt(7)\n\tv_mfma_f32_32x32x16_bf16 a[64:79], v[64:67], v[88:91], a[64:79]\n\tds_read_b128 v[96:99], %0 offset:0" :  : "v"(c.a_rd[0][1]) : "memory"); \
  asm volatile("s_waitcnt lgkmcnt(6)\n\tv_mfma_f32_32x32x16_bf16 a[96:111], v[64:67], v[92:95], a[96:111]\n\tds_read_b64_tr_b16 v[116:117], %0 offset:24576" :  : "v"(c.b_rd[0][1]) : "memory"); \
  asm volatile("s_waitcnt lgkmcnt(6)\n\tv_mfma_f32_32x32x16_bf16 a[16:31], v[68:71], v[80:83], a[16:31]\n\tds_read_b64_tr_b16 v[118:119], %0 offset:26624" :  : "v"(c.b_rd[0][1]) : "memory"); \
  unsigned so191; \
  asm volatile("v_mfma_f32_32x32x16_bf16 a[48:63], v[68:71], v[84:87], a[48:63]\n\tds_read_b64_tr_b16 v[120:121], %1 offset:24576\n\ts_add_u32 m0, %2, 99328\n\ts_add_u32 %0, %5, 16832\n\tbuffer_load_dwordx4 %3, %4, %0 offen offset:0 lds" : "=&s"(so191) : "v"(c.b_rd[0][2]), "s"(c.m0w), "v"(c.va), "s"(c.sa), "s"(c.soa) : "memory", "m0", "scc", ACL64); \
  asm volatile("v_mfma_f32_32x32x16_bf16 a[80:95], v[68:71], v[88:91], a[80:95]\n\tds_read_b64_tr_b16 v[122:123], %0 offset:26624" :  : "v"(c.b_rd[0][2]) : "memory"); \
  asm volatile("v_mfma_f32_32x32x16_bf16 a[112:127], v[68:71], v[92:95], a[112:127]\n\tds_read_b64_tr_b16 v[124:125], %0 offset:24576" :  : "v"(c.b_rd[0][3]) : "memory"); \
  asm volatile("s_waitcnt lgkmcnt(9)\n\tv_mfma_f32_32x32x16_bf16 a[128:143], v[72:75], v[80:83], a[128:143]\n\tds_read_b64_tr_b16 v[126:127], %0 offset:26624" :  : "v"(c.b_rd[0][3]) : "memory"); \
  unsigned so192; \
  asm volatile("v_mfma_f32_32x32x16_bf16 a[160:175], v[72:75], v[84:87], a[160:175]\n\tds_read_b128 v[100:103], %1 offset:2048\n\ts_add_u32 m0, %2, 100352\n\ts_add_u32 %0, %5, 33216\n\tbuffer_load_dwordx4 %3, %4, %0 offen offset:0 lds" : "=&s"(so192) : "v"(c.a_rd[0][1]), "s"(c.m0w), "v"(c.va), "s"(c.sa), "s"(c.soa) : "memory", "m0", "scc"); \
  asm volatile("v_mfma_f32_32x32x16_bf16 a[192:207], v[72:75], v[88:91], a[192:207]\n\tds_read_b128 v[104:107], %0 offset:4096" :  : "v"(c.a_rd[0][1]) : "memory"); \
  asm volatile("v_mfma_f32_32x32x16_bf16 a[224:239], v[72:75], v[92:95], a[224:239]\n\tds_read_b128 v[108:111], %0 offset:6144" :  : "v"(c.a_rd[0][1]) : "memory"); \
  asm volatile("s_waitcnt lgkmcnt(12)\n\tv_mfma_f32_32x32x16_bf16 a[144:159], v[76:79], v[80:83], a[144:159]" :  :  : "memory"); \
  unsigned so193; \
  asm volatile("v_mfma_f32_32x32x16_bf16 a[176:191], v[76:79], v[84:87], a[176:191]\n\ts_add_u32 m0, %1, 101376\n\ts_add_u32 %0, %4, 49600\n\tbuffer_load_dwordx4 %2, %3, %0 offen offset:0 lds" : "=&s"(so193) : "s"(c.m0w), "v"(c.va), "s"(c.sa), "s"(c.soa) : "memory", "m0", "scc", ACL64); \
  asm volatile("v_mfma_f32_32x32x16_bf16 a[208:223], v[76:79], v[88:91], a[208:223]" :  :  : "memory"); \
  asm volatile("v_mfma_f32_32x32x16_bf16 a[240:255], v[76:79], v[92:95], a[240:255]" :  :  : "memory"); \
  asm volatile("s_waitcnt lgkmcnt(9)\n\tv_mfma_f32_32x32x16_bf16 a[0:15], v[96:99], v[112:115], a[0:15]\n\tds_read_b64_tr_b16 v[80:81], %0 offset:49152" :  : "v"(c.b_rd[0][0]) : "memory"); \
  unsigned so194; \
  asm volatile("s_waitcnt lgkmcnt(8)\n\tv_mfma_f32_32x32x16_bf16 a[32:47], v[96:99], v[116:119], a[32:47]\n\tds_read_b64_tr_b16 v[82:83], %1 offset:51200\n\ts_add_u32 m0, %2, 114688\n\ts_add_u32 %0, %5, 1835008\n\tbuffer_load_dwordx4 %3, %4, %0 offen offset:0 lds" : "=&s"(so194) : "v"(c.b_rd[0][0]), "s"(c.m0w), "v"(c.vb[0]), "s"(c.sb), "s"(c.sob) : "memory", "m0", "scc"); \
  asm volatile("s_waitcnt lgkmcnt(7)\n\tv_mfma_f32_32x32x16_bf16 a[64:79], v[96:99], v[120:123], a[64:79]\n\tds_read_b128 v[64:67], %0 offset:32768" :  : "v"(c.a_rd[0][0]) : "memory"); \
  asm volatile("s_waitcnt lgkmcnt(6)\n\tv_mfma_f32_32x32x16_bf16 a[96:111], v[96:99], v[124:127], a[96:111]\n\tds_read_b64_tr_b16 v[84:85], %0 offset:49152" :  : "v"(c.b_rd[0][1]) : "memory"); \
  asm volatile("s_waitcnt lgkmcnt(6)\n\tv_mfma_f32_32x32x16_bf16 a[16:31], v[100:103], v[112:115], a[16:31]\n\tds_read_b64_tr_b16 v[86:87], %0 offset:51200" :  : "v"(c.b_rd[0][1]) : "memory"); \
  unsigned so195; \
  asm volatile("v_mfma_f32_32x32x16_bf16 a[48:63], v[100:103], v[116:119], a[48:63]\n\tds_read_b64_tr_b16 v[88:89], %1 offset:49152\n\ts_add_u32 m0, %2, 115712\n\ts_add_u32 %0, %5, 1835008\n\tbuffer_load_dwordx4 %3, %4, %0 offen offset:0 lds" : "=&s"(so195) : "v"(c.b_rd[0][2]), "s"(c.m0w), "v"(c.vb[1]), "s"(c.sb), "s"(c.sob) : "memory", "m0", "scc", ACL64); \
  asm volatile("v_mfma_f32_32x32x16_bf16 a[80:95], v[100:103], v[120:123], a[80:95]\n\tds_read_b64_tr_b16 v[90:91], %0 offset:51200" :  : "v"(c.b_rd[0][2]) : "memory"); \
  asm volatile("v_mfma_f32_32x32x16_bf16 a[112:127], v[100:103], v[124:127], a[112:127]\n\tds_read_b64_tr_b16 v[92:93], %0 offset:49152" :  : "v"(c.b_rd[0][3]) : "memory"); \
  asm volatile("s_waitcnt lgkmcnt(9)\n\tv_mfma_f32_32x32x16_bf16 a[128:143], v[104:107], v[112:115], a[128:143]\n\tds_read_b64_tr_b16 v[94:95], %0 offset:51200" :  : "v"(c.b_rd[0][3]) : "memory"); \
  unsigned so196; \
  asm volatile("v_mfma_f32_32x32x16_bf16 a[160:175], v[104:107], v[116:119], a[160:175]\n\tds_read_b128 v[68:71], %1 offset:34816\n\ts_add_u32 m0, %2, 116736\n\ts_add_u32 %0, %5, 1867776\n\tbuffer_load_dwordx4 %3, %4, %0 offen offset:0 lds" : "=&s"(so196) : "v"(c.a_rd[0][0]), "s"(c.m0w), "v"(c.vb[0]), "s"(c.sb), "s"(c.sob) : "memory", "m0", "scc"); \
  asm volatile("v_mfma_f32_32x32x16_bf16 a[192:207], v[104:107], v[120:123], a[192:207]\n\tds_read_b128 v[72:75], %0 offset:36864" :  : "v"(c.a_rd[0][0]) : "memory"); \
  asm volatile("v_mfma_f32_32x32x16_bf16 a[224:239], v[104:107], v[124:127], a[224:239]\n\tds_read_b128 v[76:79], %0 offset:38912" :  : "v"(c.a_rd[0][0]) : "memory"); \
  asm volatile("s_waitcnt lgkmcnt(12)\n\tv_mfma_f32_32x32x16_bf16 a[144:159], v[108:111], v[112:115], a[144:159]" :  :  : "memory"); \
  unsigned so197; \
  asm volatile("v_mfma_f32_32x32x16_bf16 a[176:191], v[108:111], v[116:119], a[176:191]\n\ts_add_u32 m0, %1, 117760\n\ts_add_u32 %0, %4, 1867776\n\tbuffer_load_dwordx4 %2, %3, %0 offen offset:0 lds" : "=&s"(so197) : "s"(c.m0w), "v"(c.vb[1]), "s"(c.sb), "s"(c.sob) : "memory", "m0", "scc", ACL64); \
  asm volatile("v_mfma_f32_32x32x16_bf16 a[208:223], v[108:111], v[120:123], a[208:223]" :  :  : "memory"); \
  asm volatile("v_mfma_f32_32x32x16_bf16 a[240:255], v[108:111], v[124:127], a[240:255]" :  :  : "memory"); \
  asm volatile("s_waitcnt vmcnt(8)\n\ts_barrier" ::: "memory"); \
  KV_HOOK(5); \
  asm volatile("s_waitcnt lgkmcnt(9)\n\tv_mfma_f32_32x32x16_bf16 a[0:15], v[64:67], v[80:83], a[0:15]\n\tds_read_b64_tr_b16 v[112:113], %0 offset:57344" :  : "v"(c.b_rd[0][0]) : "memory"); \
  unsigned so198; \
  asm volatile("s_waitcnt lgkmcnt(8)\n\tv_mfma_f32_32x32x16_bf16 a[32:47], v[64:67], v[84:87], a[32:47]\n\tds_read_b64_tr_b16 v[114:115], %1 offset:59392\n\ts_add_u32 m0, %2, 0\n\ts_add_u32 %0, %5, 512\n\tbuffer_load_dwordx4 %3, %4, %0 offen offset:0 lds" : "=&s"(so198) : "v"(c.b_rd[0][0]), "s"(c.m0w), "v"(c.va), "s"(c.sa), "s"(c.soa) : "memory", "m0", "scc"); \
  asm volatile("s_waitcnt lgkmcnt(7)\n\tv_mfma_f32_32x32x16_bf16 a[64:79], v[64:67], v[88:91], a[64:79]\n\tds_read_b128 v[96:99], %0 offset:32768" :  : "v"(c.a_rd[0][1]) : "memory"); \
  asm volatile("s_waitcnt lgkmcnt(6)\n\tv_mfma_f32_32x32x16_bf16 a[96:111], v[64:67], v[92:95], a[96:111]\n\tds_read_b64_tr_b16 v[116:117], %0 offset:57344" :  : "v"(c.b_rd[0][1]) : "memory"); \
  asm volatile("s_waitcnt lgkmcnt(6)\n\tv_mfma_f32_32x32x16_bf16 a[16:31], v[68:71], v[80:83], a[16:31]\n\tds_read_b64_tr_b16 v[118:119], %0 offset:59392" :  : "v"(c.b_rd[0][1]) : "memory"); \
  unsigned so199; \
  asm volatile("v_mfma_f32_32x32x16_bf16 a[48:63], v[68:71], v[84:87], a[48:63]\n\tds_read_b64_tr_b16 v[120:121], %1 offset:57344\n\ts_add_u32 m0, %2, 1024\n\ts_add_u32 %0, %5, 16896\n\tbuffer_load_dwordx4 %3, %4, %0 offen offset:0 lds" : "=&s"(so199) : "v"(c.b_rd[0][2]), "s"(c.m0w), "v"(c.va), "s"(c.sa), "s"(c.soa) : "memory", "m0", "scc", ACL64); \
  asm volatile("v_mfma_f32_32x32x16_bf16 a[80:95], v[68:71], v[88:91], a[80:95]\n\tds_read_b64_tr_b16 v[122:123], %0 offset:59392" :  : "v"(c.b_rd[0][2]) : "memory"); \
  asm volatile("v_mfma_f32_32x32x16_bf16 a[112:127], v[68:71], v[92:95], a[112:127]\n\tds_read_b64_tr_b16 v[124:125], %0 offset:57344" :  : "v"(c.b_rd[0][3]) : "memory"); \
  asm volatile("s_waitcnt lgkmcnt(9)\n\tv_mfma_f32_32x32x16_bf16 a[128:143], v[72:75], v[80:83], a[128:143]\n\tds_read_b64_tr_b16 v[126:127], %0 offset:59392" :  : "v"(c.b_rd[0][3]) : "memory"); \
  unsigned so200; \
  asm volatile("v_mfma_f32_32x32x16_bf16 a[160:175], v[72:75], v[84:87], a[160:175]\n\tds_read_b128 v[100:103], %1 offset:34816\n\ts_add_u32 m0, %2, 2048\n\ts_add_u32 %0, %5, 33280\n\tbuffer_load_dwordx4 %3, %4, %0 offen offset:0 lds" : "=&s"(so200) : "v"(c.a_rd[0][1]), "s"(c.m0w), "v"(c.va), "s"(c.sa), "s"(c.soa) : "memory", "m0", "scc"); \
  asm volatile("v_mfma_f32_32x32x16_bf16 a[192:207], v[72:75], v[88:91], a[192:207]\n\tds_read_b128 v[104:107], %0 offset:36864" :  : "v"(c.a_rd[0][1]) : "memory"); \
  asm volatile("v_mfma_f32_32x32x16_bf16 a[224:239], v[72:75], v[92:95], a[224:239]\n\tds_read_b128 v[108:111], %0 offset:38912" :  : "v"(c.a_rd[0][1]) : "memory"); \
  asm volatile("s_waitcnt lgkmcnt(12)\n\tv_mfma_f32_32x32x16_bf16 a[144:159], v[76:79], v[80:83], a[144:159]" :  :  : "memory"); \
  unsigned so201; \
  asm volatile("v_mfma_f32_32x32x16_bf16 a[176:191], v[76:79], v[84:87], a[176:191]\n\ts_add_u32 m0, %1, 3072\n\ts_add_u32 %0, %4, 49664\n\tbuffer_load_dwordx4 %2, %3, %0 offen offset:0 lds" : "=&s"(so201) : "s"(c.m0w), "v"(c.va), "s"(c.sa), "s"(c.soa) : "memory", "m0", "scc", ACL64); \
  asm volatile("v_mfma_f32_32x32x16_bf16 a[208:223], v[76:79], v[88:91], a[208:223]" :  :  : "memory"); \
  asm volatile("v_mfma_f32_32x32x16_bf16 a[240:255], v[76:79], v[92:95], a[240:255]" :  :  : "memory"); \
  asm volatile("s_waitcnt lgkmcnt(9)\n\tv_mfma_f32_32x32x16_bf16 a[0:15], v[96:99], v[112:115], a[0:15]\n\tds_read_b64_tr_b16 v[80:81], %0 offset:16384" :  : "v"(c.b_rd[1][0]) : "memory"); \
  unsigned so202; \
  asm volatile("s_waitcnt lgkmcnt(8)\n\tv_mfma_f32_32x32x16_bf16 a[32:47], v[96:99], v[116:119], a[32:47]\n\tds_read_b64_tr_b16 v[82:83], %1 offset:18432\n\ts_add_u32 m0, %2, 16384\n\ts_add_u32 %0, %5, 2097152\n\tbuffer_load_dwordx4 %3, %4, %0 offen offset:0 lds" : "=&s"(so202) : "v"(c.b_rd[1][0]), "s"(c.m0w), "v"(c.vb[0]), "s"(c.sb), "s"(c.sob) : "memory", "m0", "scc"); \
  asm volatile("s_waitcnt lgkmcnt(7)\n\tv_mfma_f32_32x32x16_bf16 a[64:79], v[96:99], v[120:123], a[64:79]\n\tds_read_b128 v[64:67], %0 offset:0" :  : "v"(c.a_rd[1][0]) : "memory"); \
  asm volatile("s_waitcnt lgkmcnt(6)\n\tv_mfma_f32_32x32x16_bf16 a[96:111], v[96:99], v[124:127], a[96:111]\n\tds_read_b64_tr_b16 v[84:85], %0 offset:16384" :  : "v"(c.b_rd[1][1]) : "memory"); \
  asm volatile("s_waitcnt lgkmcnt(6)\n\tv_mfma_f32_32x32x16_bf16 a[16:31], v[100:103], v[112:115], a[16:31]\n\tds_read_b64_tr_b16 v[86:87], %0 offset:18432" :  : "v"(c.b_rd[1][1]) : "memory"); \
  unsigned so203; \
  asm volatile("v_mfma_f32_32x32x16_bf16 a[48:63], v[100:103], v[116:119], a[48:63]\n\tds_read_b64_tr_b16 v[88:89], %1 offset:16384\n\ts_add_u32 m0, %2, 17408\n\ts_add_u32 %0, %5, 2097152\n\tbuffer_load_dwordx4 %3, %4, %0 offen offset:0 lds" : "=&s"(so203) : "v"(c.b_rd[1][2]), "s"(c.m0w), "v"(c.vb[1]), "s"(c.sb), "s"(c.sob) : "memory", "m0", "scc", ACL64); \
  asm volatile("v_mfma_f32_32x32x16_bf16 a[80:95], v[100:103], v[120:123], a[80:95]\n\tds_read_b64_tr_b16 v[90:91], %0 offset:18432" :  : "v"(c.b_rd[1][2]) : "memory"); \
  asm volatile("v_mfma_f32_32x32x16_bf16 a[112:127], v[100:103], v[124:127], a[112:127]\n\tds_read_b64_tr_b16 v[92:93], %0 offset:16384" :  : "v"(c.b_rd[1][3]) : "memory"); \
  asm volatile("s_waitcnt lgkmcnt(9)\n\tv_mfma_f32_32x32x16_bf16 a[128:143], v[104:107], v[112:115], a[128:143]\n\tds_read_b64_tr_b16 v[94:95], %0 offset:18432" :  : "v"(c.b_rd[1][3]) : "memory"); \
  unsigned so204; \
  asm volatile("v_mfma_f32_32x32x16_bf16 a[160:175], v[104:107], v[116:119], a[160:175]\n\tds_read_b128 v[68:71], %1 offset:2048\n\ts_add_u32 m0, %2, 18432\n\ts_add_u32 %0, %5, 2129920\n\tbuffer_load_dwordx4 %3, %4, %0 offen offset:0 lds" : "=&s"(so204) : "v"(c.a_rd[1][0]), "s"(c.m0w), "v"(c.vb[0]), "s"(c.sb), "s"(c.sob) : "memory", "m0", "scc"); \
  asm volatile("v_mfma_f32_32x32x16_bf16 a[192:207], v[104:107], v[120:123], a[192:207]\n\tds_read_b128 v[72:75], %0 offset:4096" :  : "v"(c.a_rd[1][0]) : "memory"); \
  asm volatile("v_mfma_f32_32x32x16_bf16 a[224:239], v[104:107], v[124:127], a[224:239]\n\tds_read_b128 v[76:79], %0 offset:6144" :  : "v"(c.a_rd[1][0]) : "memory"); \
  asm volatile("s_waitcnt lgkmcnt(12)\n\tv_mfma_f32_32x32x16_bf16 a[144:159], v[108:111], v[112:115], a[144:159]" :  :  : "memory"); \
  unsigned so205; \
  asm volatile("v_mfma_f32_32x32x16_bf16 a[176:191], v[108:111], v[116:119], a[176:191]\n\ts_add_u32 m0, %1, 19456\n\ts_add_u32 %0, %4, 2129920\n\tbuffer_load_dwordx4 %2, %3, %0 offen offset:0 lds" : "=&s"(so205) : "s"(c.m0w), "v"(c.vb[1]), "s"(c.sb), "s"(c.sob) : "memory", "m0", "scc", ACL64); \
  asm volatile("v_mfma_f32_32x32x16_bf16 a[208:223], v[108:111], v[120:123], a[208:223]" :  :  : "memory"); \
  asm volatile("v_mfma_f32_32x32x16_bf16 a[240:255], v[108:111], v[124:127], a[240:255]" :  :  : "memory"); \
  asm volatile("s_waitcnt vmcnt(8)\n\ts_barrier" ::: "memory"); \
  KV_HOOK(6); \
  asm volatile("s_waitcnt lgkmcnt(9)\n\tv_mfma_f32_32x32x16_bf16 a[0:15], v[64:67], v[80:83], a[0:15]\n\tds_read_b64_tr_b16 v[112:113], %0 offset:24576" :  : "v"(c.b_rd[1][0]) : "memory"); \
  unsigned so206; \
  asm volatile("s_waitcnt lgkmcnt(8)\n\tv_mfma_f32_32x32x16_bf16 a[32:47], v[64:67], v[84:87], a[32:47]\n\tds_read_b64_tr_b16 v[114:115], %1 offset:26624\n\ts_add_u32 m0, %2, 32768\n\ts_add_u32 %0, %5, 576\n\tbuffer_load_dwordx4 %3, %4, %0 offen offset:0 lds" : "=&s"(so206) : "v"(c.b_rd[1][0]), "s"(c.m0w), "v"(c.va), "s"(c.sa), "s"(c.soa) : "memory", "m0", "scc"); \
  asm volatile("s_waitcnt lgkmcnt(7)\n\tv_mfma_f32_32x32x16_bf16 a[64:79], v[64:67], v[88:91], a[64:79]\n\tds_read_b128 v[96:99], %0 offset:0" :  : "v"(c.a_rd[1][1]) : "memory"); \
  asm volatile("s_waitcnt lgkmcnt(6)\n\tv_mfma_f32_32x32x16_bf16 a[96:111], v[64:67], v[92:95], a[96:111]\n\tds_read_b64_tr_b16 v[116:117], %0 offset:24576" :  : "v"(c.b_rd[1][1]) : "memory"); \
  asm volatile("s_waitcnt lgkmcnt(6)\n\tv_mfma_f32_32x32x16_bf16 a[16:31], v[68:71], v[80:83], a[16:31]\n\tds_read_b64_tr_b16 v[118:119], %0 offset:26624" :  : "v"(c.b_rd[1][1]) : "memory"); \
  unsigned so207; \
  asm volatile("v_mfma_f32_32x32x16_bf16 a[48:63], v[68:71], v[84:87], a[48:63]\n\tds_read_b64_tr_b16 v[120:121], %1 offset:24576\n\ts_add_u32 m0, %2, 33792\n\ts_add_u32 %0, %5, 16960\n\tbuffer_load_dwordx4 %3, %4, %0 offen offset:0 lds" : "=&s"(so207) : "v"(c.b_rd[1][2]), "s"(c.m0w), "v"(c.va), "s"(c.sa), "s"(c.soa) : "memory", "m0", "scc", ACL64); \
  asm volatile("v_mfma_f32_32x32x16_bf16 a[80:95], v[68:71], v[88:91], a[80:95]\n\tds_read_b64_tr_b16 v[122:123], %0 offset:26624" :  : "v"(c.b_rd[1][2]) : "memory"); \
  asm volatile("v_mfma_f32_32x32x16_bf16 a[112:127], v[68:71], v[92:95], a[112:127]\n\tds_read_b64_tr_b16 v[124:125], %0 offset:24576" :  : "v"(c.b_rd[1][3]) : "memory"); \
  asm volatile("s_waitcnt lgkmcnt(9)\n\tv_mfma_f32_32x32x16_bf16 a[128:143], v[72:75], v[80:83], a[128:143]\n\tds_read_b64_tr_b16 v[126:127], %0 offset:26624" :  : "v"(c.b_rd[1][3]) : "memory"); \
  unsigned so208; \
  asm volatile("v_mfma_f32_32x32x16_bf16 a[160:175], v[72:75], v[84:87], a[160:175]\n\tds_read_b128 v[100:103], %1 offset:2048\n\ts_add_u32 m0, %2, 34816\n\ts_add_u32 %0, %5, 33344\n\tbuffer_load_dwordx4 %3, %4, %0 offen offset:0 lds" : "=&s"(so208) : "v"(c.a_rd[1][1]), "s"(c.m0w), "v"(c.va), "s"(c.sa), "s"(c.soa) : "memory", "m0", "scc"); \
  asm volatile("v_mfma_f32_32x32x16_bf16 a[192:207], v[72:75], v[88:91], a[192:207]\n\tds_read_b128 v[104:107], %0 offset:4096" :  : "v"(c.a_rd[1][1]) : "memory"); \
  asm volatile("v_mfma_f32_32x32x16_bf16 a[224:239], v[72:75], v[92:95], a[224:239]\n\tds_read_b128 v[108:111], %0 offset:6144" :  : "v"(c.a_rd[1][1]) : "memory"); \
  asm volatile("s_waitcnt lgkmcnt(12)\n\tv_mfma_f32_32x32x16_bf16 a[144:159], v[76:79], v[80:83], a[144:159]" :  :  : "memory"); \
  unsigned so209; \
  asm volatile("v_mfma_f32_32x32x16_bf16 a[176:191], v[76:79], v[84:87], a[176:191]\n\ts_add_u32 m0, %1, 35840\n\ts_add_u32 %0, %4, 49728\n\tbuffer_load_dwordx4 %2, %3, %0 offen offset:0 lds" : "=&s"(so209) : "s"(c.m0w), "v"(c.va), "s"(c.sa), "s"(c.soa) : "memory", "m0", "scc", ACL64); \
  asm volatile("v_mfma_f32_32x32x16_bf16 a[208:223], v[76:79], v[88:91], a[208:223]" :  :  : "memory"); \
  asm volatile("v_mfma_f32_32x32x16_bf16 a[240:255], v[76:79], v[92:95], a[240:255]" :  :  : "memory"); \
  asm volatile("s_waitcnt lgkmcnt(9)\n\tv_mfma_f32_32x32x16_bf16 a[0:15], v[96:99], v[112:115], a[0:15]\n\tds_read_b64_tr_b16 v[80:81], %0 offset:49152" :  : "v"(c.b_rd[1][0]) : "memory"); \
  unsigned so210; \
  asm volatile("s_waitcnt lgkmcnt(8)\n\tv_mfma_f32_32x32x16_bf16 a[32:47], v[96:99], v[116:119], a[32:47]\n\tds_read_b64_tr_b16 v[82:83], %1 offset:51200\n\ts_add_u32 m0, %2, 49152\n\ts_add_u32 %0, %5, 2359296\n\tbuffer_load_dwordx4 %3, %4, %0 offen offset:0 lds" : "=&s"(so210) : "v"(c.b_rd[1][0]), "s"(c.m0w), "v"(c.vb[0]), "s"(c.sb), "s"(c.sob) : "memory", "m0", "scc"); \
  asm volatile("s_waitcnt lgkmcnt(7)\n\tv_mfma_f32_32x32x16_bf16 a[64:79], v[96:99], v[120:123], a[64:79]\n\tds_read_b128 v[64:67], %0 offset:32768" :  : "v"(c.a_rd[1][0]) : "memory"); \
  asm volatile("s_waitcnt lgkmcnt(6)\n\tv_mfma_f32_32x32x16_bf16 a[96:111], v[96:99], v[124:127], a[96:111]\n\tds_read_b64_tr_b16 v[84:85], %0 offset:49152" :  : "v"(c.b_rd[1][1]) : "memory"); \
  asm volatile("s_waitcnt lgkmcnt(6)\n\tv_mfma_f32_32x32x16_bf16 a[16:31], v[100:103], v[112:115], a[16:31]\n\tds_read_b64_tr_b16 v[86:87], %0 offset:51200" :  : "v"(c.b_rd[1][1]) : "memory"); \
  unsigned so211; \
  asm volatile("v_mfma_f32_32x32x16_bf16 a[48:63], v[100:103], v[116:119], a[48:63]\n\tds_read_b64_tr_b16 v[88:89], %1 offset:49152\n\ts_add_u32 m0, %2, 50176\n\ts_add_u32 %0, %5, 2359296\n\tbuffer_load_dwordx4 %3, %4, %0 offen offset:0 lds" : "=&s"(so211) : "v"(c.b_rd[1][2]), "s"(c.m0w), "v"(c.vb[1]), "s"(c.sb), "s"(c.sob) : "memory", "m0", "scc", ACL64); \
  asm volatile("v_mfma_f32_32x32x16_bf16 a[80:95], v[100:103], v[120:123], a[80:95]\n\tds_read_b64_tr_b16 v[90:91], %0 offset:51200" :  : "v"(c.b_rd[1][2]) : "memory"); \
  asm volatile("v_mfma_f32_32x32x16_bf16 a[112:127], v[100:103], v[124:127], a[112:127]\n\tds_read_b64_tr_b16 v[92:93], %0 offset:49152" :  : "v"(c.b_rd[1][3]) : "memory"); \
  asm volatile("s_waitcnt lgkmcnt(9)\n\tv_mfma_f32_32x32x16_bf16 a[128:143], v[104:107], v[112:115], a[128:143]\n\tds_read_b64_tr_b16 v[94:95], %0 offset:51200" :  : "v"(c.b_rd[1][3]) : "memory"); \
  unsigned so212; \
  asm volatile("v_mfma_f32_32x32x16_bf16 a[160:175], v[104:107], v[116:119], a[160:175]\n\tds_read_b128 v[68:71], %1 offset:34816\n\ts_add_u32 m0, %2, 51200\n\ts_add_u32 %0, %5, 2392064\n\tbuffer_load_dwordx4 %3, %4, %0 offen offset:0 lds" : "=&s"(so212) : "v"(c.a_rd[1][0]), "s"(c.m0w), "v"(c.vb[0]), "s"(c.sb), "s"(c.sob) : "memory", "m0", "scc"); \
  asm volatile("v_mfma_f32_32x32x16_bf16 a[192:207], v[104:107], v[120:123], a[192:207]\n\tds_read_b128 v[72:75], %0 offset:36864" :  : "v"(c.a_rd[1][0]) : "memory"); \
  asm volatile("v_mfma_f32_32x32x16_bf16 a[224:239], v[104:107], v[124:127], a[224:239]\n\tds_read_b128 v[76:79], %0 offset:38912" :  : "v"(c.a_rd[1][0]) : "memory"); \
  asm volatile("s_waitcnt lgkmcnt(12)\n\tv_mfma_f32_32x32x16_bf16 a[144:159], v[108:111], v[112:115], a[144:159]" :  :  : "memory"); \
  unsigned so213; \
  asm volatile("v_mfma_f32_32x32x16_bf16 a[176:191], v[108:111], v[116:119], a[176:191]\n\ts_add_u32 m0, %1, 52224\n\ts_add_u32 %0, %4, 2392064\n\tbuffer_load_dwordx4 %2, %3, %0 offen offset:0 lds" : "=&s"(so213) : "s"(c.m0w), "v"(c.vb[1]), "s"(c.sb), "s"(c.sob) : "memory", "m0", "scc", ACL64); \
  asm volatile("v_mfma_f32_32x32x16_bf16 a[208:223], v[108:111], v[120:123], a[208:223]" :  :  : "memory"); \
  asm volatile("v_mfma_f32_32x32x16_bf16 a[240:255], v[108:111], v[124:127], a[240:255]" :  :  : "memory"); \
  asm volatile("s_waitcnt vmcnt(8)\n\ts_barrier" ::: "memory"); \
  KV_HOOK(7); \
  asm volatile("s_waitcnt lgkmcnt(9)\n\tv_mfma_f32_32x32x16_bf16 a[0:15], v[64:67], v[80:83], a[0:15]\n\tds_read_b64_tr_b16 v[112:113], %0 offset:57344" :  : "v"(c.b_rd[1][0]) : "memory"); \
  unsigned so214; \
  asm volatile("s_waitcnt lgkmcnt(8)\n\tv_mfma_f32_32x32x16_bf16 a[32:47], v[64:67], v[84:87], a[32:47]\n\tds_read_b64_tr_b16 v[114:115], %1 offset:59392\n\ts_add_u32 m0, %2, 65536\n\ts_add_u32 %0, %5, 640\n\tbuffer_load_dwordx4 %3, %4, %0 offen offset:0 lds" : "=&s"(so214) : "v"(c.b_rd[1][0]), "s"(c.m0w), "v"(c.va), "s"(c.sa), "s"(c.soa) : "memory", "m0", "scc"); \
  asm volatile("s_waitcnt lgkmcnt(7)\n\tv_mfma_f32_32x32x16_bf16 a[64:79], v[64:67], v[88:91], a[64:79]\n\tds_read_b128 v[96:99], %0 offset:32768" :  : "v"(c.a_rd[1][1]) : "memory"); \
  asm volatile("s_waitcnt lgkmcnt(6)\n\tv_mfma_f32_32x32x16_bf16 a[96:111], v[64:67], v[92:95], a[96:111]\n\tds_read_b64_tr_b16 v[116:117], %0 offset:57344" :  : "v"(c.b_rd[1][1]) : "memory"); \
  asm volatile("s_waitcnt lgkmcnt(6)\n\tv_mfma_f32_32x32x16_bf16 a[16:31], v[68:71], v[80:83], a[16:31]\n\tds_read_b64_tr_b16 v[118:119], %0 offset:59392" :  : "v"(c.b_rd[1][1]) : "memory"); \
  unsigned so215; \
  asm volatile("v_mfma_f32_32x32x16_bf16 a[48:63], v[68:71], v[84:87], a[48:63]\n\tds_read_b64_tr_b16 v[120:121], %1 offset:57344\n\ts_add_u32 m0, %2, 66560\n\ts_add_u32 %0, %5, 17024\n\tbuffer_load_dwordx4 %3, %4, %0 offen offset:0 lds" : "=&s"(so215) : "v"(c.b_rd[1][2]), "s"(c.m0w), "v"(c.va), "s"(c.sa), "s"(c.soa) : "memory", "m0", "scc", ACL64); \
  asm volatile("v_mfma_f32_32x32x16_bf16 a[80:95], v[68:71], v[88:91], a[80:95]\n\tds_read_b64_tr_b16 v[122:123], %0 offset:59392" :  : "v"(c.b_rd[1][2]) : "memory"); \
  asm volatile("v_mfma_f32_32x32x16_bf16 a[112:127], v[68:71], v[92:95], a[112:127]\n\tds_read_b64_tr_b16 v[124:125], %0 offset:57344" :  : "v"(c.b_rd[1][3]) : "memory"); \
  asm volatile("s_waitcnt lgkmcnt(9)\n\tv_mfma_f32_32x32x16_bf16 a[128:143], v[72:75], v[80:83], a[128:143]\n\tds_read_b64_tr_b16 v[126:127], %0 offset:59392" :  : "v"(c.b_rd[1][3]) : "memory"); \
  unsigned so216; \
  asm volatile("v_mfma_f32_32x32x16_bf16 a[160:175], v[72:75], v[84:87], a[160:175]\n\tds_read_b128 v[100:103], %1 offset:34816\n\ts_add_u32 m0, %2, 67584\n\ts_add_u32 %0, %5, 33408\n\tbuffer_load_dwordx4 %3, %4, %0 offen offset:0 lds" : "=&s"(so216) : "v"(c.a_rd[1][1]), "s"(c.m0w), "v"(c.va), "s"(c.sa), "s"(c.soa) : "memory", "m0", "scc"); \
  asm volatile("v_mfma_f32_32x32x16_bf16 a[192:207], v[72:75], v[88:91], a[192:207]\n\tds_read_b128 v[104:107], %0 offset:36864" :  : "v"(c.a_rd[1][1]) : "memory"); \
  asm volatile("v_mfma_f32_32x32x16_bf16 a[224:239], v[72:75], v[92:95], a[224:239]\n\tds_read_b128 v[108:111], %0 offset:38912" :  : "v"(c.a_rd[1][1]) : "memory"); \
  asm volatile("s_waitcnt lgkmcnt(12)\n\tv_mfma_f32_32x32x16_bf16 a[144:159], v[76:79], v[80:83], a[144:159]" :  :  : "memory"); \
  unsigned so217; \
  asm volatile("v_mfma_f32_32x32x16_bf16 a[176:191], v[76:79], v[84:87], a[176:191]\n\ts_add_u32 m0, %1, 68608\n\ts_add_u32 %0, %4, 49792\n\tbuffer_load_dwordx4 %2, %3, %0 offen offset:0 lds" : "=&s"(so217) : "s"(c.m0w), "v"(c.va), "s"(c.sa), "s"(c.soa) : "memory", "m0", "scc", ACL64); \
  asm volatile("v_mfma_f32_32x32x16_bf16 a[208:223], v[76:79], v[88:91], a[208:223]" :  :  : "memory"); \
  asm volatile("v_mfma_f32_32x32x16_bf16 a[240:255], v[76:79], v[92:95], a[240:255]" :  :  : "memory"); \
  asm volatile("s_waitcnt lgkmcnt(9)\n\tv_mfma_f32_32x32x16_bf16 a[0:15], v[96:99], v[112:115], a[0:15]\n\tds_read_b64_tr_b16 v[80:81], %0 offset:16384" :  : "v"(c.b_rd[0][0]) : "memory"); \
  unsigned so218; \
  asm volatile("s_waitcnt lgkmcnt(8)\n\tv_mfma_f32_32x32x16_bf16 a[32:47], v[96:99], v[116:119], a[32:47]\n\tds_read_b64_tr_b16 v[82:83], %1 offset:18432\n\ts_add_u32 m0, %2, 81920\n\ts_add_u32 %0, %5, 2621440\n\tbuffer_load_dwordx4 %3, %4, %0 offen offset:0 lds" : "=&s"(so218) : "v"(c.b_rd[0][0]), "s"(c.m0w), "v"(c.vb[0]), "s"(c.sb), "s"(c.sob) : "memory", "m0", "scc"); \
  asm volatile("s_waitcnt lgkmcnt(7)\n\tv_mfma_f32_32x32x16_bf16 a[64:79], v[96:99], v[120:123], a[64:79]\n\tds_read_b128 v[64:67], %0 offset:0" :  : "v"(c.a_rd[0][0]) : "memory"); \
  asm volatile("s_waitcnt lgkmcnt(6)\n\tv_mfma_f32_32x32x16_bf16 a[96:111], v[96:99], v[124:127], a[96:111]\n\tds_read_b64_tr_b16 v[84:85], %0 offset:16384" :  : "v"(c.b_rd[0][1]) : "memory"); \
  asm volatile("s_waitcnt lgkmcnt(6)\n\tv_mfma_f32_32x32x16_bf16 a[16:31], v[100:103], v[112:115], a[16:31]\n\tds_read_b64_tr_b16 v[86:87], %0 offset:18432" :  : "v"(c.b_rd[0][1]) : "memory"); \
  unsigned so219; \
  asm volatile("v_mfma_f32_32x32x16_bf16 a[48:63], v[100:103], v[116:119], a[48:63]\n\tds_read_b64_tr_b16 v[88:89], %1 offset:16384\n\ts_add_u32 m0, %2, 82944\n\ts_add_u32 %0, %5, 2621440\n\tbuffer_load_dwordx4 %3, %4, %0 offen offset:0 lds" : "=&s"(so219) : "v"(c.b_rd[0][2]), "s"(c.m0w), "v"(c.vb[1]), "s"(c.sb), "s"(c.sob) : "memory", "m0", "scc", ACL64); \
  asm volatile("v_mfma_f32_32x32x16_bf16 a[80:95], v[100:103], v[120:123], a[80:95]\n\tds_read_b64_tr_b16 v[90:91], %0 offset:18432" :  : "v"(c.b_rd[0][2]) : "memory"); \
  asm volatile("v_mfma_f32_32x32x16_bf16 a[112:127], v[100:103], v[124:127], a[112:127]\n\tds_read_b64_tr_b16 v[92:93], %0 offset:16384" :  : "v"(c.b_rd[0][3]) : "memory"); \
  asm volatile("s_waitcnt lgkmcnt(9)\n\tv_mfma_f32_32x32x16_bf16 a[128:143], v[104:107], v[112:115], a[128:143]\n\tds_read_b64_tr_b16 v[94:95], %0 offset:18432" :  : "v"(c.b_rd[0][3]) : "memory"); \
  unsigned so220; \
  asm volatile("v_mfma_f32_32x32x16_bf16 a[160:175], v[104:107], v[116:119], a[160:175]\n\tds_read_b128 v[68:71], %1 offset:2048\n\ts_add_u32 m0, %2, 83968\n\ts_add_u32 %0, %5, 2654208\n\tbuffer_load_dwordx4 %3, %4, %0 offen offset:0 lds" : "=&s"(so220) : "v"(c.a_rd[0][0]), "s"(c.m0w), "v"(c.vb[0]), "s"(c.sb), "s"(c.sob) : "memory", "m0", "scc"); \
  asm volatile("v_mfma_f32_32x32x16_bf16 a[192:207], v[104:107], v[120:123], a[192:207]\n\tds_read_b128 v[72:75], %0 offset:4096" :  : "v"(c.a_rd[0][0]) : "memory"); \
  asm volatile("v_mfma_f32_32x32x16_bf16 a[224:239], v[104:107], v[124:127], a[224:239]\n\tds_read_b128 v[76:79], %0 offset:6144" :  : "v"(c.a_rd[0][0]) : "memory"); \
  asm volatile("s_waitcnt lgkmcnt(12)\n\tv_mfma_f32_32x32x16_bf16 a[144:159], v[108:111], v[112:115], a[144:159]" :  :  : "memory"); \
  unsigned so221; \
  asm volatile("v_mfma_f32_32x32x16_bf16 a[176:191], v[108:111], v[116:119], a[176:191]\n\ts_add_u32 m0, %1, 84992\n\ts_add_u32 %0, %4, 2654208\n\tbuffer_load_dwordx4 %2, %3, %0 offen offset:0 lds" : "=&s"(so221) : "s"(c.m0w), "v"(c.vb[1]), "s"(c.sb), "s"(c.sob) : "memory", "m0", "scc", ACL64); \
  asm volatile("v_mfma_f32_32x32x16_bf16 a[208:223], v[108:111], v[120:123], a[208:223]" :  :  : "memory"); \
  asm volatile("v_mfma_f32_32x32x16_bf16 a[240:255], v[108:111], v[124:127], a[240:255]" :  :  : "memory"); \
  asm volatile("s_waitcnt vmcnt(8)\n\ts_barrier" ::: "memory"); \
  KV_HOOK(8); \
  asm volatile("s_waitcnt lgkmcnt(9)\n\tv_mfma_f32_32x32x16_bf16 a[0:15], v[64:67], v[80:83], a[0:15]\n\tds_read_b64_tr_b16 v[112:113], %0 offset:24576" :  : "v"(c.b_rd[0][0]) : "memory"); \
  unsigned so222; \
  asm volatile("s_waitcnt lgkmcnt(8)\n\tv_mfma_f32_32x32x16_bf16 a[32:47], v[64:67], v[84:87], a[32:47]\n\tds_read_b64_tr_b16 v[114:115], %1 offset:26624\n\ts_add_u32 m0, %2, 98304\n\ts_add_u32 %0, %5, 704\n\tbuffer_load_dwordx4 %3, %4, %0 offen offset:0 lds" : "=&s"(so222) : "v"(c.b_rd[0][0]), "s"(c.m0w), "v"(c.va), "s"(c.sa), "s"(c.soa) : "memory", "m0", "scc"); \
  asm volatile("s_waitcnt lgkmcnt(7)\n\tv_mfma_f32_32x32x16_bf16 a[64:79], v[64:67], v[88:91], a[64:79]\n\tds_read_b128 v[96:99], %0 offset:0" :  : "v"(c.a_rd[0][1]) : "memory"); \
  asm volatile("s_waitcnt lgkmcnt(6)\n\tv_mfma_f32_32x32x16_bf16 a[96:111], v[64:67], v[92:95], a[96:111]\n\tds_read_b64_tr_b16 v[116:117], %0 offset:24576" :  : "v"(c.b_rd[0][1]) : "memory"); \
  asm volatile("s_waitcnt lgkmcnt(6)\n\tv_mfma_f32_32x32x16_bf16 a[16:31], v[68:71], v[80:83], a[16:31]\n\tds_read_b64_tr_b16 v[118:119], %0 offset:26624" :  : "v"(c.b_rd[0][1]) : "memory"); \
  unsigned so223; \
  asm volatile("v_mfma_f32_32x32x16_bf16 a[48:63], v[68:71], v[84:87], a[48:63]\n\tds_read_b64_tr_b16 v[120:121], %1 offset:24576\n\ts_add_u32 m0, %2, 99328\n\ts_add_u32 %0, %5, 17088\n\tbuffer_load_dwordx4 %3, %4, %0 offen offset:0 lds" : "=&s"(so223) : "v"(c.b_rd[0][2]), "s"(c.m0w), "v"(c.va), "s"(c.sa), "s"(c.soa) : "memory", "m0", "scc", ACL64); \
  asm volatile("v_mfma_f32_32x32x16_bf16 a[80:95], v[68:71], v[88:91], a[80:95]\n\tds_read_b64_tr_b16 v[122:123], %0 offset:26624" :  : "v"(c.b_rd[0][2]) : "memory"); \
  asm volatile("v_mfma_f32_32x32x16_bf16 a[112:127], v[68:71], v[92:95], a[112:127]\n\tds_read_b64_tr_b16 v[124:125], %0 offset:24576" :  : "v"(c.b_rd[0][3]) : "memory"); \
  asm volatile("s_waitcnt lgkmcnt(9)\n\tv_mfma_f32_32x32x16_bf16 a[128:143], v[72:75], v[80:83], a[128:143]\n\tds_read_b64_tr_b16 v[126:127], %0 offset:26624" :  : "v"(c.b_rd[0][3]) : "memory"); \
  unsigned so224; \
  asm volatile("v_mfma_f32_32x32x16_bf16 a[160:175], v[72:75], v[84:87], a[160:175]\n\tds_read_b128 v[100:103], %1 offset:2048\n\ts_add_u32 m0, %2, 100352\n\ts_add_u32 %0, %5, 33472\n\tbuffer_load_dwordx4 %3, %4, %0 offen offset:0 lds" : "=&s"(so224) : "v"(c.a_rd[0][1]), "s"(c.m0w), "v"(c.va), "s"(c.sa), "s"(c.soa) : "memory", "m0", "scc"); \
  asm volatile("v_mfma_f32_32x32x16_bf16 a[192:207], v[72:75], v[88:91], a[192:207]\n\tds_read_b128 v[104:107], %0 offset:4096" :  : "v"(c.a_rd[0][1]) : "memory"); \
  asm volatile("v_mfma_f32_32x32x16_bf16 a[224:239], v[72:75], v[92:95], a[224:239]\n\tds_read_b128 v[108:111], %0 offset:6144" :  : "v"(c.a_rd[0][1]) : "memory"); \
  asm volatile("s_waitcnt lgkmcnt(12)\n\tv_mfma_f32_32x32x16_bf16 a[144:159], v[76:79], v[80:83], a[144:159]" :  :  : "memory"); \
  unsigned so225; \
  asm volatile("v_mfma_f32_32x32x16_bf16 a[176:191], v[76:79], v[84:87], a[176:191]\n\ts_add_u32 m0, %1, 101376\n\ts_add_u32 %0, %4, 49856\n\tbuffer_load_dwordx4 %2, %3, %0 offen offset:0 lds" : "=&s"(so225) : "s"(c.m0w), "v"(c.va), "s"(c.sa), "s"(c.soa) : "memory", "m0", "scc", ACL64); \
  asm volatile("v_mfma_f32_32x32x16_bf16 a[208:223], v[76:79], v[88:91], a[208:223]" :  :  : "memory"); \
  asm volatile("v_mfma_f32_32x32x16_bf16 a[240:255], v[76:79], v[92:95], a[240:255]" :  :  : "memory"); \
  asm volatile("s_waitcnt lgkmcnt(9)\n\tv_mfma_f32_32x32x16_bf16 a[0:15], v[96:99], v[112:115], a[0:15]\n\tds_read_b64_tr_b16 v[80:81], %0 offset:49152" :  : "v"(c.b_rd[0][0]) : "memory"); \
  unsigned so226; \
  asm volatile("s_waitcnt lgkmcnt(8)\n\tv_mfma_f32_32x32x16_bf16 a[32:47], v[96:99], v[116:119], a[32:47]\n\tds_read_b64_tr_b16 v[82:83], %1 offset:51200\n\ts_add_u32 m0, %2, 114688\n\ts_add_u32 %0, %5, 2883584\n\tbuffer_load_dwordx4 %3, %4, %0 offen offset:0 lds" : "=&s"(so226) : "v"(c.b_rd[0][0]), "s"(c.m0w), "v"(c.vb[0]), "s"(c.sb), "s"(c.sob) : "memory", "m0", "scc"); \
  asm volatile("s_waitcnt lgkmcnt(7)\n\tv_mfma_f32_32x32x16_bf16 a[64:79], v[96:99], v[120:123], a[64:79]\n\tds_read_b128 v[64:67], %0 offset:32768" :  : "v"(c.a_rd[0][0]) : "memory"); \
  asm volatile("s_waitcnt lgkmcnt(6)\n\tv_mfma_f32_32x32x16_bf16 a[96:111], v[96:99], v[124:127], a[96:111]\n\tds_read_b64_tr_b16 v[84:85], %0 offset:49152" :  : "v"(c.b_rd[0][1]) : "memory"); \
  asm volatile("s_waitcnt lgkmcnt(6)\n\tv_mfma_f32_32x32x16_bf16 a[16:31], v[100:103], v[112:115], a[16:31]\n\tds_read_b64_tr_b16 v[86:87], %0 offset:51200" :  : "v"(c.b_rd[0][1]) : "memory"); \
  unsigned so227; \
  asm volatile("v_mfma_f32_32x32x16_bf16 a[48:63], v[100:103], v[116:119], a[48:63]\n\tds_read_b64_tr_b16 v[88:89], %1 offset:49152\n\ts_add_u32 m0, %2, 115712\n\ts_add_u32 %0, %5, 2883584\n\tbuffer_load_dwordx4 %3, %4, %0 offen offset:0 lds" : "=&s"(so227) : "v"(c.b_rd[0][2]), "s"(c.m0w), "v"(c.vb[1]), "s"(c.sb), "s"(c.sob) : "memory", "m0", "scc", ACL64); \
  asm volatile("v_mfma_f32_32x32x16_bf16 a[80:95], v[100:103], v[120:123], a[80:95]\n\tds_read_b64_tr_b16 v[90:91], %0 offset:51200" :  : "v"(c.b_rd[0][2]) : "memory"); \
  asm volatile("v_mfma_f32_32x32x16_bf16 a[112:127], v[100:103], v[124:127], a[112:127]\n\tds_read_b64_tr_b16 v[92:93], %0 offset:49152" :  : "v"(c.b_rd[0][3]) : "memory"); \
  asm volatile("s_waitcnt lgkmcnt(9)\n\tv_mfma_f32_32x32x16_bf16 a[128:143], v[104:107], v[112:115], a[128:143]\n\tds_read_b64_tr_b16 v[94:95], %0 offset:51200" :  : "v"(c.b_rd[0][3]) : "memory"); \
  unsigned so228; \
  asm volatile("v_mfma_f32_32x32x16_bf16 a[160:175], v[104:107], v[116:119], a[160:175]\n\tds_read_b128 v[68:71], %1 offset:34816\n\ts_add_u32 m0, %2, 116736\n\ts_add_u32 %0, %5, 2916352\n\tbuffer_load_dwordx4 %3, %4, %0 offen offset:0 lds" : "=&s"(so228) : "v"(c.a_rd[0][0]), "s"(c.m0w), "v"(c.vb[0]), "s"(c.sb), "s"(c.sob) : "memory", "m0", "scc"); \
  asm volatile("v_mfma_f32_32x32x16_bf16 a[192:207], v[104:107], v[120:123], a[192:207]\n\tds_read_b128 v[72:75], %0 offset:36864" :  : "v"(c.a_rd[0][0]) : "memory"); \
  asm volatile("v_mfma_f32_32x32x16_bf16 a[224:239], v[104:107], v[124:127], a[224:239]\n\tds_read_b128 v[76:79], %0 offset:38912" :  : "v"(c.a_rd[0][0]) : "memory"); \
  asm volatile("s_waitcnt lgkmcnt(12)\n\tv_mfma_f32_32x32x16_bf16 a[144:159], v[108:111], v[112:115], a[144:159]" :  :  : "memory"); \
  unsigned so229; \
  asm volatile("v_mfma_f32_32x32x16_bf16 a[176:191], v[108:111], v[116:119], a[176:191]\n\ts_add_u32 m0, %1, 117760\n\ts_add_u32 %0, %4, 2916352\n\tbuffer_load_dwordx4 %2, %3, %0 offen offset:0 lds" : "=&s"(so229) : "s"(c.m0w), "v"(c.vb[1]), "s"(c.sb), "s"(c.sob) : "memory", "m0", "scc", ACL64); \
  asm volatile("v_mfma_f32_32x32x16_bf16 a[208:223], v[108:111], v[120:123], a[208:223]" :  :  : "memory"); \
  asm volatile("v_mfma_f32_32x32x16_bf16 a[240:255], v[108:111], v[124:127], a[240:255]" :  :  : "memory"); \
  asm volatile("s_waitcnt vmcnt(8)\n\ts_barrier" ::: "memory"); \
  KV_HOOK(9); \
  asm volatile("s_waitcnt lgkmcnt(9)\n\tv_mfma_f32_32x32x16_bf16 a[0:15], v[64:67], v[80:83], a[0:15]\n\tds_read_b64_tr_b16 v[112:113], %0 offset:57344" :  : "v"(c.b_rd[0][0]) : "memory"); \
  unsigned so230; \
  asm volatile("s_waitcnt lgkmcnt(8)\n\tv_mfma_f32_32x32x16_bf16 a[32:47], v[64:67], v[84:87], a[32:47]\n\tds_read_b64_tr_b16 v[114:115], %1 offset:59392\n\ts_add_u32 m0, %2, 0\n\ts_add_u32 %0, %5, 768\n\tbuffer_load_dwordx4 %3, %4, %0 offen offset:0 lds" : "=&s"(so230) : "v"(c.b_rd[0][0]), "s"(c.m0w), "v"(c.va), "s"(c.sa), "s"(c.soa) : "memory", "m0", "scc"); \
  asm volatile("s_waitcnt lgkmcnt(7)\n\tv_mfma_f32_32x32x16_bf16 a[64:79], v[64:67], v[88:91], a[64:79]\n\tds_read_b128 v[96:99], %0 offset:32768" :  : "v"(c.a_rd[0][1]) : "memory"); \
  asm volatile("s_waitcnt lgkmcnt(6)\n\tv_mfma_f32_32x32x16_bf16 a[96:111], v[64:67], v[92:95], a[96:111]\n\tds_read_b64_tr_b16 v[116:117], %0 offset:57344" :  : "v"(c.b_rd[0][1]) : "memory"); \
  asm volatile("s_waitcnt lgkmcnt(6)\n\tv_mfma_f32_32x32x16_bf16 a[16:31], v[68:71], v[80:83], a[16:31]\n\tds_read_b64_tr_b16 v[118:119], %0 offset:59392" :  : "v"(c.b_rd[0][1]) : "memory"); \
  unsigned so231; \
  asm volatile("v_mfma_f32_32x32x16_bf16 a[48:63], v[68:71], v[84:87], a[48:63]\n\tds_read_b64_tr_b16 v[120:121], %1 offset:57344\n\ts_add_u32 m0, %2, 1024\n\ts_add_u32 %0, %5, 17152\n\tbuffer_load_dwordx4 %3, %4, %0 offen offset:0 lds" : "=&s"(so231) : "v"(c.b_rd[0][2]), "s"(c.m0w), "v"(c.va), "s"(c.sa), "s"(c.soa) : "memory", "m0", "scc", ACL64); \
  asm volatile("v_mfma_f32_32x32x16_bf16 a[80:95], v[68:71], v[88:91], a[80:95]\n\tds_read_b64_tr_b16 v[122:123], %0 offset:59392" :  : "v"(c.b_rd[0][2]) : "memory"); \
  asm volatile("v_mfma_f32_32x32x16_bf16 a[112:127], v[68:71], v[92:95], a[112:127]\n\tds_read_b64_tr_b16 v[124:125], %0 offset:57344" :  : "v"(c.b_rd[0][3]) : "memory"); \
  asm volatile("s_waitcnt lgkmcnt(9)\n\tv_mfma_f32_32x32x16_bf16 a[128:143], v[72:75], v[80:83], a[128:143]\n\tds_read_b64_tr_b16 v[126:127], %0 offset:59392" :  : "v"(c.b_rd[0][3]) : "memory"); \
  unsigned so232; \
  asm volatile("v_mfma_f32_32x32x16_bf16 a[160:175], v[72:75], v[84:87], a[160:175]\n\tds_read_b128 v[100:103], %1 offset:34816\n\ts_add_u32 m0, %2, 2048\n\ts_add_u32 %0, %5, 33536\n\tbuffer_load_dwordx4 %3, %4, %0 offen offset:0 lds" : "=&s"(so232) : "v"(c.a_rd[0][1]), "s"(c.m0w), "v"(c.va), "s"(c.sa), "s"(c.soa) : "memory", "m0", "scc"); \
  asm volatile("v_mfma_f32_32x32x16_bf16 a[192:207], v[72:75], v[88:91], a[192:207]\n\tds_read_b128 v[104:107], %0 offset:36864" :  : "v"(c.a_rd[0][1]) : "memory"); \
  asm volatile("v_mfma_f32_32x32x16_bf16 a[224:239], v[72:75], v[92:95], a[224:239]\n\tds_read_b128 v[108:111], %0 offset:38912" :  : "v"(c.a_rd[0][1]) : "memory"); \
  asm volatile("s_waitcnt lgkmcnt(12)\n\tv_mfma_f32_32x32x16_bf16 a[144:159], v[76:79], v[80:83], a[144:159]" :  :  : "memory"); \
  unsigned so233; \
  asm volatile("v_mfma_f32_32x32x16_bf16 a[176:191], v[76:79], v[84:87], a[176:191]\n\ts_add_u32 m0, %1, 3072\n\ts_add_u32 %0, %4, 49920\n\tbuffer_load_dwordx4 %2, %3, %0 offen offset:0 lds" : "=&s"(so233) : "s"(c.m0w), "v"(c.va), "s"(c.sa), "s"(c.soa) : "memory", "m0", "scc", ACL64); \
  asm volatile("v_mfma_f32_32x32x16_bf16 a[208:223], v[76:79], v[88:91], a[208:223]" :  :  : "memory"); \
  asm volatile("v_mfma_f32_32x32x16_bf16 a[240:255], v[76:79], v[92:95], a[240:255]" :  :  : "memory"); \
  asm volatile("s_waitcnt lgkmcnt(9)\n\tv_mfma_f32_32x32x16_bf16 a[0:15], v[96:99], v[112:115], a[0:15]\n\tds_read_b64_tr_b16 v[80:81], %0 offset:16384" :  : "v"(c.b_rd[1][0]) : "memory"); \
  unsigned so234; \
  asm volatile("s_waitcnt lgkmcnt(8)\n\tv_mfma_f32_32x32x16_bf16 a[32:47], v[96:99], v[116:119], a[32:47]\n\tds_read_b64_tr_b16 v[82:83], %1 offset:18432\n\ts_add_u32 m0, %2, 16384\n\ts_add_u32 %0, %5, 3145728\n\tbuffer_load_dwordx4 %3, %4, %0 offen offset:0 lds" : "=&s"(so234) : "v"(c.b_rd[1][0]), "s"(c.m0w), "v"(c.vb[0]), "s"(c.sb), "s"(c.sob) : "memory", "m0", "scc"); \
  asm volatile("s_waitcnt lgkmcnt(7)\n\tv_mfma_f32_32x32x16_bf16 a[64:79], v[96:99], v[120:123], a[64:79]\n\tds_read_b128 v[64:67], %0 offset:0" :  : "v"(c.a_rd[1][0]) : "memory"); \
  asm volatile("s_waitcnt lgkmcnt(6)\n\tv_mfma_f32_32x32x16_bf16 a[96:111], v[96:99], v[124:127], a[96:111]\n\tds_read_b64_tr_b16 v[84:85], %0 offset:16384" :  : "v"(c.b_rd[1][1]) : "memory"); \
  asm volatile("s_waitcnt lgkmcnt(6)\n\tv_mfma_f32_32x32x16_bf16 a[16:31], v[100:103], v[112:115], a[16:31]\n\tds_read_b64_tr_b16 v[86:87], %0 offset:18432" :  : "v"(c.b_rd[1][1]) : "memory"); \
  unsigned so235; \
  asm volatile("v_mfma_f32_32x32x16_bf16 a[48:63], v[100:103], v[116:119], a[48:63]\n\tds_read_b64_tr_b16 v[88:89], %1 offset:16384\n\ts_add_u32 m0, %2, 17408\n\ts_add_u32 %0, %5, 3145728\n\tbuffer_load_dwordx4 %3, %4, %0 offen offset:0 lds" : "=&s"(so235) : "v"(c.b_rd[1][2]), "s"(c.m0w), "v"(c.vb[1]), "s"(c.sb), "s"(c.sob) : "memory", "m0", "scc", ACL64); \
  asm volatile("v_mfma_f32_32x32x16_bf16 a[80:95], v[100:103], v[120:123], a[80:95]\n\tds_read_b64_tr_b16 v[90:91], %0 offset:18432" :  : "v"(c.b_rd[1][2]) : "memory"); \
  asm volatile("v_mfma_f32_32x32x16_bf16 a[112:127], v[100:103], v[124:127], a[112:127]\n\tds_read_b64_tr_b16 v[92:93], %0 offset:16384" :  : "v"(c.b_rd[1][3]) : "memory"); \
  asm volatile("s_waitcnt lgkmcnt(9)\n\tv_mfma_f32_32x32x16_bf16 a[128:143], v[104:107], v[112:115], a[128:143]\n\tds_read_b64_tr_b16 v[94:95], %0 offset:18432" :  : "v"(c.b_rd[1][3]) : "memory"); \
  unsigned so236; \
  asm volatile("v_mfma_f32_32x32x16_bf16 a[160:175], v[104:107], v[116:119], a[160:175]\n\tds_read_b128 v[68:71], %1 offset:2048\n\ts_add_u32 m0, %2, 18432\n\ts_add_u32 %0, %5, 3178496\n\tbuffer_load_dwordx4 %3, %4, %0 offen offset:0 lds" : "=&s"(so236) : "v"(c.a_rd[1][0]), "s"(c.m0w), "v"(c.vb[0]), "s"(c.sb), "s"(c.sob) : "memory", "m0", "scc"); \
  asm volatile("v_mfma_f32_32x32x16_bf16 a[192:207], v[104:107], v[120:123], a[192:207]\n\tds_read_b128 v[72:75], %0 offset:4096" :  : "v"(c.a_rd[1][0]) : "memory"); \
  asm volatile("v_mfma_f32_32x32x16_bf16 a[224:239], v[104:107], v[124:127], a[224:239]\n\tds_read_b128 v[76:79], %0 offset:6144" :  : "v"(c.a_rd[1][0]) : "memory"); \
  asm volatile("s_waitcnt lgkmcnt(12)\n\tv_mfma_f32_32x32x16_bf16 a[144:159], v[108:111], v[112:115], a[144:159]" :  :  : "memory"); \
  unsigned so237; \
  asm volatile("v_mfma_f32_32x32x16_bf16 a[176:191], v[108:111], v[116:119], a[176:191]\n\ts_add_u32 m0, %1, 19456\n\ts_add_u32 %0, %4, 3178496\n\tbuffer_load_dwordx4 %2, %3, %0 offen offset:0 lds" : "=&s"(so237) : "s"(c.m0w), "v"(c.vb[1]), "s"(c.sb), "s"(c.sob) : "memory", "m0", "scc", ACL64); \
  asm volatile("v_mfma_f32_32x32x16_bf16 a[208:223], v[108:111], v[120:123], a[208:223]" :  :  : "memory"); \
  asm volatile("v_mfma_f32_32x32x16_bf16 a[240:255], v[108:111], v[124:127], a[240:255]" :  :  : "memory"); \
  asm volatile("s_waitcnt vmcnt(8)\n\ts_barrier" ::: "memory"); \
  KV_HOOK(10); \
  asm volatile("s_waitcnt lgkmcnt(9)\n\tv_mfma_f32_32x32x16_bf16 a[0:15], v[64:67], v[80:83], a[0:15]\n\tds_read_b64_tr_b16 v[112:113], %0 offset:24576" :  : "v"(c.b_rd[1][0]) : "memory"); \
  unsigned so238; \
  asm volatile("s_waitcnt lgkmcnt(8)\n\tv_mfma_f32_32x32x16_bf16 a[32:47], v[64:67], v[84:87], a[32:47]\n\tds_read_b64_tr_b16 v[114:115], %1 offset:26624\n\ts_add_u32 m0, %2, 32768\n\ts_add_u32 %0, %5, 832\n\tbuffer_load_dwordx4 %3, %4, %0 offen offset:0 lds" : "=&s"(so238) : "v"(c.b_rd[1][0]), "s"(c.m0w), "v"(c.va), "s"(c.sa), "s"(c.soa) : "memory", "m0", "scc"); \
  asm volatile("s_waitcnt lgkmcnt(7)\n\tv_mfma_f32_32x32x16_bf16 a[64:79], v[64:67], v[88:91], a[64:79]\n\tds_read_b128 v[96:99], %0 offset:0" :  : "v"(c.a_rd[1][1]) : "memory"); \
  asm volatile("s_waitcnt lgkmcnt(6)\n\tv_mfma_f32_32x32x16_bf16 a[96:111], v[64:67], v[92:95], a[96:111]\n\tds_read_b64_tr_b16 v[116:117], %0 offset:24576" :  : "v"(c.b_rd[1][1]) : "memory"); \
  asm volatile("s_waitcnt lgkmcnt(6)\n\tv_mfma_f32_32x32x16_bf16 a[16:31], v[68:71], v[80:83], a[16:31]\n\tds_read_b64_tr_b16 v[118:119], %0 offset:26624" :  : "v"(c.b_rd[1][1]) : "memory"); \
  unsigned so239; \
  asm volatile("v_mfma_f32_32x32x16_bf16 a[48:63], v[68:71], v[84:87], a[48:63]\n\tds_read_b64_tr_b16 v[120:121], %1 offset:24576\n\ts_add_u32 m0, %2, 33792\n\ts_add_u32 %0, %5, 17216\n\tbuffer_load_dwordx4 %3, %4, %0 offen offset:0 lds" : "=&s"(so239) : "v"(c.b_rd[1][2]), "s"(c.m0w), "v"(c.va), "s"(c.sa), "s"(c.soa) : "memory", "m0", "scc", ACL64); \
  asm volatile("v_mfma_f32_32x32x16_bf16 a[80:95], v[68:71], v[88:91], a[80:95]\n\tds_read_b64_tr_b16 v[122:123], %0 offset:26624" :  : "v"(c.b_rd[1][2]) : "memory"); \
  asm volatile("v_mfma_f32_32x32x16_bf16 a[112:127], v[68:71], v[92:95], a[112:127]\n\tds_read_b64_tr_b16 v[124:125], %0 offset:24576" :  : "v"(c.b_rd[1][3]) : "memory"); \
  asm volatile("s_waitcnt lgkmcnt(9)\n\tv_mfma_f32_32x32x16_bf16 a[128:143], v[72:75], v[80:83], a[128:143]\n\tds_read_b64_tr_b16 v[126:127], %0 offset:26624" :  : "v"(c.b_rd[1][3]) : "memory"); \
  unsigned so240; \
  asm volatile("v_mfma_f32_32x32x16_bf16 a[160:175], v[72:75], v[84:87], a[160:175]\n\tds_read_b128 v[100:103], %1 offset:2048\n\ts_add_u32 m0, %2, 34816\n\ts_add_u32 %0, %5, 33600\n\tbuffer_load_dwordx4 %3, %4, %0 offen offset:0 lds" : "=&s"(so240) : "v"(c.a_rd[1][1]), "s"(c.m0w), "v"(c.va), "s"(c.sa), "s"(c.soa) : "memory", "m0", "scc"); \
  asm volatile("v_mfma_f32_32x32x16_bf16 a[192:207], v[72:75], v[88:91], a[192:207]\n\tds_read_b128 v[104:107], %0 offset:4096" :  : "v"(c.a_rd[1][1]) : "memory"); \
  asm volatile("v_mfma_f32_32x32x16_bf16 a[224:239], v[72:75], v[92:95], a[224:239]\n\tds_read_b128 v[108:111], %0 offset:6144" :  : "v"(c.a_rd[1][1]) : "memory"); \
  asm volatile("s_waitcnt lgkmcnt(12)\n\tv_mfma_f32_32x32x16_bf16 a[144:159], v[76:79], v[80:83], a[144:159]" :  :  : "memory"); \
  unsigned so241; \
  asm volatile("v_mfma_f32_32x32x16_bf16 a[176:191], v[76:79], v[84:87], a[176:191]\n\ts_add_u32 m0, %1, 35840\n\ts_add_u32 %0, %4, 49984\n\tbuffer_load_dwordx4 %2, %3, %0 offen offset:0 lds" : "=&s"(so241) : "s"(c.m0w), "v"(c.va), "s"(c.sa), "s"(c.soa) : "memory", "m0", "scc", ACL64); \
  asm volatile("v_mfma_f32_32x32x16_bf16 a[208:223], v[76:79], v[88:91], a[208:223]" :  :  : "memory"); \
  asm volatile("v_mfma_f32_32x32x16_bf16 a[240:255], v[76:79], v[92:95], a[240:255]" :  :  : "memory"); \
  asm volatile("s_waitcnt lgkmcnt(9)\n\tv_mfma_f32_32x32x16_bf16 a[0:15], v[96:99], v[112:115], a[0:15]\n\tds_read_b64_tr_b16 v[80:81], %0 offset:49152" :  : "v"(c.b_rd[1][0]) : "memory"); \
  unsigned so242; \
  asm volatile("s_waitcnt lgkmcnt(8)\n\tv_mfma_f32_32x32x16_bf16 a[32:47], v[96:99], v[116:119], a[32:47]\n\tds_read_b64_tr_b16 v[82:83], %1 offset:51200\n\ts_add_u32 m0, %2, 49152\n\ts_add_u32 %0, %5, 3407872\n\tbuffer_load_dwordx4 %3, %4, %0 offen offset:0 lds" : "=&s"(so242) : "v"(c.b_rd[1][0]), "s"(c.m0w), "v"(c.vb[0]), "s"(c.sb), "s"(c.sob) : "memory", "m0", "scc"); \
  asm volatile("s_waitcnt lgkmcnt(7)\n\tv_mfma_f32_32x32x16_bf16 a[64:79], v[96:99], v[120:123], a[64:79]\n\tds_read_b128 v[64:67], %0 offset:32768" :  : "v"(c.a_rd[1][0]) : "memory"); \
  asm volatile("s_waitcnt lgkmcnt(6)\n\tv_mfma_f32_32x32x16_bf16 a[96:111], v[96:99], v[124:127], a[96:111]\n\tds_read_b64_tr_b16 v[84:85], %0 offset:49152" :  : "v"(c.b_rd[1][1]) : "memory"); \
  asm volatile("s_waitcnt lgkmcnt(6)\n\tv_mfma_f32_32x32x16_bf16 a[16:31], v[100:103], v[112:115], a[16:31]\n\tds_read_b64_tr_b16 v[86:87], %0 offset:51200" :  : "v"(c.b_rd[1][1]) : "memory"); \
  unsigned so243; \
  asm volatile("v_mfma_f32_32x32x16_bf16 a[48:63], v[100:103], v[116:119], a[48:63]\n\tds_read_b64_tr_b16 v[88:89], %1 offset:49152\n\ts_add_u32 m0, %2, 50176\n\ts_add_u32 %0, %5, 3407872\n\tbuffer_load_dwordx4 %3, %4, %0 offen offset:0 lds" : "=&s"(so243) : "v"(c.b_rd[1][2]), "s"(c.m0w), "v"(c.vb[1]), "s"(c.sb), "s"(c.sob) : "memory", "m0", "scc", ACL64); \
  asm volatile("v_mfma_f32_32x32x16_bf16 a[80:95], v[100:103], v[120:123], a[80:95]\n\tds_read_b64_tr_b16 v[90:91], %0 offset:51200" :  : "v"(c.b_rd[1][2]) : "memory"); \
  asm volatile("v_mfma_f32_32x32x16_bf16 a[112:127], v[100:103], v[124:127], a[112:127]\n\tds_read_b64_tr_b16 v[92:93], %0 offset:49152" :  : "v"(c.b_rd[1][3]) : "memory"); \
  asm volatile("s_waitcnt lgkmcnt(9)\n\tv_mfma_f32_32x32x16_bf16 a[128:143], v[104:107], v[112:115], a[128:143]\n\tds_read_b64_tr_b16 v[94:95], %0 offset:51200" :  : "v"(c.b_rd[1][3]) : "memory"); \
  unsigned so244; \
  asm volatile("v_mfma_f32_32x32x16_bf16 a[160:175], v[104:107], v[116:119], a[160:175]\n\tds_read_b128 v[68:71], %1 offset:34816\n\ts_add_u32 m0, %2, 51200\n\ts_add_u32 %0, %5, 3440640\n\tbuffer_load_dwordx4 %3, %4, %0 offen offset:0 lds" : "=&s"(so244) : "v"(c.a_rd[1][0]), "s"(c.m0w), "v"(c.vb[0]), "s"(c.sb), "s"(c.sob) : "memory", "m0", "scc"); \
  asm volatile("v_mfma_f32_32x32x16_bf16 a[192:207], v[104:107], v[120:123], a[192:207]\n\tds_read_b128 v[72:75], %0 offset:36864" :  : "v"(c.a_rd[1][0]) : "memory"); \
  asm volatile("v_mfma_f32_32x32x16_bf16 a[224:239], v[104:107], v[124:127], a[224:239]\n\tds_read_b128 v[76:79], %0 offset:38912" :  : "v"(c.a_rd[1][0]) : "memory"); \
  asm volatile("s_waitcnt lgkmcnt(12)\n\tv_mfma_f32_32x32x16_bf16 a[144:159], v[108:111], v[112:115], a[144:159]" :  :  : "memory"); \
  unsigned so245; \
  asm volatile("v_mfma_f32_32x32x16_bf16 a[176:191], v[108:111], v[116:119], a[176:191]\n\ts_add_u32 m0, %1, 52224\n\ts_add_u32 %0, %4, 3440640\n\tbuffer_load_dwordx4 %2, %3, %0 offen offset:0 lds" : "=&s"(so245) : "s"(c.m0w), "v"(c.vb[1]), "s"(c.sb), "s"(c.sob) : "memory", "m0", "scc", ACL64); \
  asm volatile("v_mfma_f32_32x32x16_bf16 a[208:223], v[108:111], v[120:123], a[208:223]" :  :  : "memory"); \
  asm volatile("v_mfma_f32_32x32x16_bf16 a[240:255], v[108:111], v[124:127], a[240:255]" :  :  : "memory"); \
  asm volatile("s_waitcnt vmcnt(8)\n\ts_barrier" ::: "memory"); \
  KV_HOOK(11); \
  asm volatile("s_waitcnt lgkmcnt(9)\n\tv_mfma_f32_32x32x16_bf16 a[0:15], v[64:67], v[80:83], a[0:15]\n\tds_read_b64_tr_b16 v[112:113], %0 offset:57344" :  : "v"(c.b_rd[1][0]) : "memory"); \
  unsigned so246; \
  asm volatile("s_waitcnt lgkmcnt(8)\n\tv_mfma_f32_32x32x16_bf16 a[32:47], v[64:67], v[84:87], a[32:47]\n\tds_read_b64_tr_b16 v[114:115], %1 offset:59392\n\ts_add_u32 m0, %2, 65536\n\ts_add_u32 %0, %5, 896\n\tbuffer_load_dwordx4 %3, %4, %0 offen offset:0 lds" : "=&s"(so246) : "v"(c.b_rd[1][0]), "s"(c.m0w), "v"(c.va), "s"(c.sa), "s"(c.soa) : "memory", "m0", "scc"); \
  asm volatile("s_waitcnt lgkmcnt(7)\n\tv_mfma_f32_32x32x16_bf16 a[64:79], v[64:67], v[88:91], a[64:79]\n\tds_read_b128 v[96:99], %0 offset:32768" :  : "v"(c.a_rd[1][1]) : "memory"); \
  asm volatile("s_waitcnt lgkmcnt(6)\n\tv_mfma_f32_32x32x16_bf16 a[96:111], v[64:67], v[92:95], a[96:111]\n\tds_read_b64_tr_b16 v[116:117], %0 offset:57344" :  : "v"(c.b_rd[1][1]) : "memory"); \
  asm volatile("s_waitcnt lgkmcnt(6)\n\tv_mfma_f32_32x32x16_bf16 a[16:31], v[68:71], v[80:83], a[16:31]\n\tds_read_b64_tr_b16 v[118:119], %0 offset:59392" :  : "v"(c.b_rd[1][1]) : "memory"); \
  unsigned so247; \
  asm volatile("v_mfma_f32_32x32x16_bf16 a[48:63], v[68:71], v[84:87], a[48:63]\n\tds_read_b64_tr_b16 v[120:121], %1 offset:57344\n\ts_add_u32 m0, %2, 66560\n\ts_add_u32 %0, %5, 17280\n\tbuffer_load_dwordx4 %3, %4, %0 offen offset:0 lds" : "=&s"(so247) : "v"(c.b_rd[1][2]), "s"(c.m0w), "v"(c.va), "s"(c.sa), "s"(c.soa) : "memory", "m0", "scc", ACL64); \
  asm volatile("v_mfma_f32_32x32x16_bf16 a[80:95], v[68:71], v[88:91], a[80:95]\n\tds_read_b64_tr_b16 v[122:123], %0 offset:59392" :  : "v"(c.b_rd[1][2]) : "memory"); \
  asm volatile("v_mfma_f32_32x32x16_bf16 a[112:127], v[68:71], v[92:95], a[112:127]\n\tds_read_b64_tr_b16 v[124:125], %0 offset:57344" :  : "v"(c.b_rd[1][3]) : "memory"); \
  asm volatile("s_waitcnt lgkmcnt(9)\n\tv_mfma_f32_32x32x16_bf16 a[128:143], v[72:75], v[80:83], a[128:143]\n\tds_read_b64_tr_b16 v[126:127], %0 offset:59392" :  : "v"(c.b_rd[1][3]) : "memory"); \
  unsigned so248; \
  asm volatile("v_mfma_f32_32x32x16_bf16 a[160:175], v[72:75], v[84:87], a[160:175]\n\tds_read_b128 v[100:103], %1 offset:34816\n\ts_add_u32 m0, %2, 67584\n\ts_add_u32 %0, %5, 33664\n\tbuffer_load_dwordx4 %3, %4, %0 offen offset:0 lds" : "=&s"(so248) : "v"(c.a_rd[1][1]), "s"(c.m0w), "v"(c.va), "s"(c.sa), "s"(c.soa) : "memory", "m0", "scc"); \
  asm volatile("v_mfma_f32_32x32x16_bf16 a[192:207], v[72:75], v[88:91], a[192:207]\n\tds_read_b128 v[104:107], %0 offset:36864" :  : "v"(c.a_rd[1][1]) : "memory"); \
  asm volatile("v_mfma_f32_32x32x16_bf16 a[224:239], v[72:75], v[92:95], a[224:239]\n\tds_read_b128 v[108:111], %0 offset:38912" :  : "v"(c.a_rd[1][1]) : "memory"); \
  asm volatile("s_waitcnt lgkmcnt(12)\n\tv_mfma_f32_32x32x16_bf16 a[144:159], v[76:79], v[80:83], a[144:159]" :  :  : "memory"); \
  unsigned so249; \
  asm volatile("v_mfma_f32_32x32x16_bf16 a[176:191], v[76:79], v[84:87], a[176:191]\n\ts_add_u32 m0, %1, 68608\n\ts_add_u32 %0, %4, 50048\n\tbuffer_load_dwordx4 %2, %3, %0 offen offset:0 lds" : "=&s"(so249) : "s"(c.m0w), "v"(c.va), "s"(c.sa), "s"(c.soa) : "memory", "m0", "scc", ACL64); \
  asm volatile("v_mfma_f32_32x32x16_bf16 a[208:223], v[76:79], v[88:91], a[208:223]" :  :  : "memory"); \
  asm volatile("v_mfma_f32_32x32x16_bf16 a[240:255], v[76:79], v[92:95], a[240:255]" :  :  : "memory"); \
  asm volatile("s_waitcnt lgkmcnt(9)\n\tv_mfma_f32_32x32x16_bf16 a[0:15], v[96:99], v[112:115], a[0:15]\n\tds_read_b64_tr_b16 v[80:81], %0 offset:16384" :  : "v"(c.b_rd[0][0]) : "memory"); \
  unsigned so250; \
  asm volatile("s_waitcnt lgkmcnt(8)\n\tv_mfma_f32_32x32x16_bf16 a[32:47], v[96:99], v[116:119], a[32:47]\n\tds_read_b64_tr_b16 v[82:83], %1 offset:18432\n\ts_add_u32 m0, %2, 81920\n\ts_add_u32 %0, %5, 3670016\n\tbuffer_load_dwordx4 %3, %4, %0 offen offset:0 lds" : "=&s"(so250) : "v"(c.b_rd[0][0]), "s"(c.m0w), "v"(c.vb[0]), "s"(c.sb), "s"(c.sob) : "memory", "m0", "scc"); \
  asm volatile("s_waitcnt lgkmcnt(7)\n\tv_mfma_f32_32x32x16_bf16 a[64:79], v[96:99], v[120:123], a[64:79]\n\tds_read_b128 v[64:67], %0 offset:0" :  : "v"(c.a_rd[0][0]) : "memory"); \
  asm volatile("s_waitcnt lgkmcnt(6)\n\tv_mfma_f32_32x32x16_bf16 a[96:111], v[96:99], v[124:127], a[96:111]\n\tds_read_b64_tr_b16 v[84:85], %0 offset:16384" :  : "v"(c.b_rd[0][1]) : "memory"); \
  asm volatile("s_waitcnt lgkmcnt(6)\n\tv_mfma_f32_32x32x16_bf16 a[16:31], v[100:103], v[112:115], a[16:31]\n\tds_read_b64_tr_b16 v[86:87], %0 offset:18432" :  : "v"(c.b_rd[0][1]) : "memory"); \
  unsigned so251; \
  asm volatile("v_mfma_f32_32x32x16_bf16 a[48:63], v[100:103], v[116:119], a[48:63]\n\tds_read_b64_tr_b16 v[88:89], %1 offset:16384\n\ts_add_u32 m0, %2, 82944\n\ts_add_u32 %0, %5, 3670016\n\tbuffer_load_dwordx4 %3, %4, %0 offen offset:0 lds" : "=&s"(so251) : "v"(c.b_rd[0][2]), "s"(c.m0w), "v"(c.vb[1]), "s"(c.sb), "s"(c.sob) : "memory", "m0", "scc", ACL64); \
  asm volatile("v_mfma_f32_32x32x16_bf16 a[80:95], v[100:103], v[120:123], a[80:95]\n\tds_read_b64_tr_b16 v[90:91], %0 offset:18432" :  : "v"(c.b_rd[0][2]) : "memory"); \
  asm volatile("v_mfma_f32_32x32x16_bf16 a[112:127], v[100:103], v[124:127], a[112:127]\n\tds_read_b64_tr_b16 v[92:93], %0 offset:16384" :  : "v"(c.b_rd[0][3]) : "memory"); \
  asm volatile("s_waitcnt lgkmcnt(9)\n\tv_mfma_f32_32x32x16_bf16 a[128:143], v[104:107], v[112:115], a[128:143]\n\tds_read_b64_tr_b16 v[94:95], %0 offset:18432" :  : "v"(c.b_rd[0][3]) : "memory"); \
  unsigned so252; \
  asm volatile("v_mfma_f32_32x32x16_bf16 a[160:175], v[104:107], v[116:119], a[160:175]\n\tds_read_b128 v[68:71], %1 offset:2048\n\ts_add_u32 m0, %2, 83968\n\ts_add_u32 %0, %5, 3702784\n\tbuffer_load_dwordx4 %3, %4, %0 offen offset:0 lds" : "=&s"(so252) : "v"(c.a_rd[0][0]), "s"(c.m0w), "v"(c.vb[0]), "s"(c.sb), "s"(c.sob) : "memory", "m0", "scc"); \
  asm volatile("v_mfma_f32_32x32x16_bf16 a[192:207], v[104:107], v[120:123], a[192:207]\n\tds_read_b128 v[72:75], %0 offset:4096" :  : "v"(c.a_rd[0][0]) : "memory"); \
  asm volatile("v_mfma_f32_32x32x16_bf16 a[224:239], v[104:107], v[124:127], a[224:239]\n\tds_read_b128 v[76:79], %0 offset:6144" :  : "v"(c.a_rd[0][0]) : "memory"); \
  asm volatile("s_waitcnt lgkmcnt(12)\n\tv_mfma_f32_32x32x16_bf16 a[144:159], v[108:111], v[112:115], a[144:159]" :  :  : "memory"); \
  unsigned so253; \
  asm volatile("v_mfma_f32_32x32x16_bf16 a[176:191], v[108:111], v[116:119], a[176:191]\n\ts_add_u32 m0, %1, 84992\n\ts_add_u32 %0, %4, 3702784\n\tbuffer_load_dwordx4 %2, %3, %0 offen offset:0 lds" : "=&s"(so253) : "s"(c.m0w), "v"(c.vb[1]), "s"(c.sb), "s"(c.sob) : "memory", "m0", "scc", ACL64); \
  asm volatile("v_mfma_f32_32x32x16_bf16 a[208:223], v[108:111], v[120:123], a[208:223]" :  :  : "memory"); \
  asm volatile("v_mfma_f32_32x32x16_bf16 a[240:255], v[108:111], v[124:127], a[240:255]" :  :  : "memory"); \
  asm volatile("s_waitcnt vmcnt(8)\n\ts_barrier" ::: "memory"); \
  KV_HOOK(12); \
  asm volatile("s_waitcnt lgkmcnt(9)\n\tv_mfma_f32_32x32x16_bf16 a[0:15], v[64:67], v[80:83], a[0:15]\n\tds_read_b64_tr_b16 v[112:113], %0 offset:24576" :  : "v"(c.b_rd[0][0]) : "memory"); \
  unsigned so254; \
  asm volatile("s_waitcnt lgkmcnt(8)\n\tv_mfma_f32_32x32x16_bf16 a[32:47], v[64:67], v[84:87], a[32:47]\n\tds_read_b64_tr_b16 v[114:115], %1 offset:26624\n\ts_add_u32 m0, %2, 98304\n\ts_add_u32 %0, %5, 960\n\tbuffer_load_dwordx4 %3, %4, %0 offen offset:0 lds" : "=&s"(so254) : "v"(c.b_rd[0][0]), "s"(c.m0w), "v"(c.va), "s"(c.sa), "s"(c.soa) : "memory", "m0", "scc"); \
  asm volatile("s_waitcnt lgkmcnt(7)\n\tv_mfma_f32_32x32x16_bf16 a[64:79], v[64:67], v[88:91], a[64:79]\n\tds_read_b128 v[96:99], %0 offset:0" :  : "v"(c.a_rd[0][1]) : "memory"); \
  asm volatile("s_waitcnt lgkmcnt(6)\n\tv_mfma_f32_32x32x16_bf16 a[96:111], v[64:67], v[92:95], a[96:111]\n\tds_read_b64_tr_b16 v[116:117], %0 offset:24576" :  : "v"(c.b_rd[0][1]) : "memory"); \
  asm volatile("s_waitcnt lgkmcnt(6)\n\tv_mfma_f32_32x32x16_bf16 a[16:31], v[68:71], v[80:83], a[16:31]\n\tds_read_b64_tr_b16 v[118:119], %0 offset:26624" :  : "v"(c.b_rd[0][1]) : "memory"); \
  unsigned so255; \
  asm volatile("v_mfma_f32_32x32x16_bf16 a[48:63], v[68:71], v[84:87], a[48:63]\n\tds_read_b64_tr_b16 v[120:121], %1 offset:24576\n\ts_add_u32 m0, %2, 99328\n\ts_add_u32 %0, %5, 17344\n\tbuffer_load_dwordx4 %3, %4, %0 offen offset:0 lds" : "=&s"(so255) : "v"(c.b_rd[0][2]), "s"(c.m0w), "v"(c.va), "s"(c.sa), "s"(c.soa) : "memory", "m0", "scc", ACL64); \
  asm volatile("v_mfma_f32_32x32x16_bf16 a[80:95], v[68:71], v[88:91], a[80:95]\n\tds_read_b64_tr_b16 v[122:123], %0 offset:26624" :  : "v"(c.b_rd[0][2]) : "memory"); \
  asm volatile("v_mfma_f32_32x32x16_bf16 a[112:127], v[68:71], v[92:95], a[112:127]\n\tds_read_b64_tr_b16 v[124:125], %0 offset:24576" :  : "v"(c.b_rd[0][3]) : "memory"); \
  asm volatile("s_waitcnt lgkmcnt(9)\n\tv_mfma_f32_32x32x16_bf16 a[128:143], v[72:75], v[80:83], a[128:143]\n\tds_read_b64_tr_b16 v[126:127], %0 offset:26624" :  : "v"(c.b_rd[0][3]) : "memory"); \
  unsigned so256; \
  asm volatile("v_mfma_f32_32x32x16_bf16 a[160:175], v[72:75], v[84:87], a[160:175]\n\tds_read_b128 v[100:103], %1 offset:2048\n\ts_add_u32 m0, %2, 100352\n\ts_add_u32 %0, %5, 33728\n\tbuffer_load_dwordx4 %3, %4, %0 offen offset:0 lds" : "=&s"(so256) : "v"(c.a_rd[0][1]), "s"(c.m0w), "v"(c.va), "s"(c.sa), "s"(c.soa) : "memory", "m0", "scc"); \
  asm volatile("v_mfma_f32_32x32x16_bf16 a[192:207], v[72:75], v[88:91], a[192:207]\n\tds_read_b128 v[104:107], %0 offset:4096" :  : "v"(c.a_rd[0][1]) : "memory"); \
  asm volatile("v_mfma_f32_32x32x16_bf16 a[224:239], v[72:75], v[92:95], a[224:239]\n\tds_read_b128 v[108:111], %0 offset:6144" :  : "v"(c.a_rd[0][1]) : "memory"); \
  asm volatile("s_waitcnt lgkmcnt(12)\n\tv_mfma_f32_32x32x16_bf16 a[144:159], v[76:79], v[80:83], a[144:159]" :  :  : "memory"); \
  unsigned so257; \
  asm volatile("v_mfma_f32_32x32x16_bf16 a[176:191], v[76:79], v[84:87], a[176:191]\n\ts_add_u32 m0, %1, 101376\n\ts_add_u32 %0, %4, 50112\n\tbuffer_load_dwordx4 %2, %3, %0 offen offset:0 lds" : "=&s"(so257) : "s"(c.m0w), "v"(c.va), "s"(c.sa), "s"(c.soa) : "memory", "m0", "scc", ACL64); \
  asm volatile("v_mfma_f32_32x32x16_bf16 a[208:223], v[76:79], v[88:91], a[208:223]" :  :  : "memory"); \
  asm volatile("v_mfma_f32_32x32x16_bf16 a[240:255], v[76:79], v[92:95], a[240:255]" :  :  : "memory"); \
  asm volatile("s_waitcnt lgkmcnt(9)\n\tv_mfma_f32_32x32x16_bf16 a[0:15], v[96:99], v[112:115], a[0:15]\n\tds_read_b64_tr_b16 v[80:81], %0 offset:49152" :  : "v"(c.b_rd[0][0]) : "memory"); \
  unsigned so258; \
  asm volatile("s_waitcnt lgkmcnt(8)\n\tv_mfma_f32_32x32x16_bf16 a[32:47], v[96:99], v[116:119], a[32:47]\n\tds_read_b64_tr_b16 v[82:83], %1 offset:51200\n\ts_add_u32 m0, %2, 114688\n\ts_add_u32 %0, %5, 3932160\n\tbuffer_load_dwordx4 %3, %4, %0 offen offset:0 lds" : "=&s"(so258) : "v"(c.b_rd[0][0]), "s"(c.m0w), "v"(c.vb[0]), "s"(c.sb), "s"(c.sob) : "memory", "m0", "scc"); \
  asm volatile("s_waitcnt lgkmcnt(7)\n\tv_mfma_f32_32x32x16_bf16 a[64:79], v[96:99], v[120:123], a[64:79]\n\tds_read_b128 v[64:67], %0 offset:32768" :  : "v"(c.a_rd[0][0]) : "memory"); \
  asm volatile("s_waitcnt lgkmcnt(6)\n\tv_mfma_f32_32x32x16_bf16 a[96:111], v[96:99], v[124:127], a[96:111]\n\tds_read_b64_tr_b16 v[84:85], %0 offset:49152" :  : "v"(c.b_rd[0][1]) : "memory"); \
  asm volatile("s_waitcnt lgkmcnt(6)\n\tv_mfma_f32_32x32x16_bf16 a[16:31], v[100:103], v[112:115], a[16:31]\n\tds_read_b64_tr_b16 v[86:87], %0 offset:51200" :  : "v"(c.b_rd[0][1]) : "memory"); \
  unsigned so259; \
  asm volatile("v_mfma_f32_32x32x16_bf16 a[48:63], v[100:103], v[116:119], a[48:63]\n\tds_read_b64_tr_b16 v[88:89], %1 offset:49152\n\ts_add_u32 m0, %2, 115712\n\ts_add_u32 %0, %5, 3932160\n\tbuffer_load_dwordx4 %3, %4, %0 offen offset:0 lds" : "=&s"(so259) : "v"(c.b_rd[0][2]), "s"(c.m0w), "v"(c.vb[1]), "s"(c.sb), "s"(c.sob) : "memory", "m0", "scc", ACL64); \
  asm volatile("v_mfma_f32_32x32x16_bf16 a[80:95], v[100:103], v[120:123], a[80:95]\n\tds_read_b64_tr_b16 v[90:91], %0 offset:51200" :  : "v"(c.b_rd[0][2]) : "memory"); \
  asm volatile("v_mfma_f32_32x32x16_bf16 a[112:127], v[100:103], v[124:127], a[112:127]\n\tds_read_b64_tr_b16 v[92:93], %0 offset:49152" :  : "v"(c.b_rd[0][3]) : "memory"); \
  asm volatile("s_waitcnt lgkmcnt(9)\n\tv_mfma_f32_32x32x16_bf16 a[128:143], v[104:107], v[112:115], a[128:143]\n\tds_read_b64_tr_b16 v[94:95], %0 offset:51200" :  : "v"(c.b_rd[0][3]) : "memory"); \
  unsigned so260; \
  asm volatile("v_mfma_f32_32x32x16_bf16 a[160:175], v[104:107], v[116:119], a[160:175]\n\tds_read_b128 v[68:71], %1 offset:34816\n\ts_add_u32 m0, %2, 116736\n\ts_add_u32 %0, %5, 3964928\n\tbuffer_load_dwordx4 %3, %4, %0 offen offset:0 lds" : "=&s"(so260) : "v"(c.a_rd[0][0]), "s"(c.m0w), "v"(c.vb[0]), "s"(c.sb), "s"(c.sob) : "memory", "m0", "scc"); \
  asm volatile("v_mfma_f32_32x32x16_bf16 a[192:207], v[104:107], v[120:123], a[192:207]\n\tds_read_b128 v[72:75], %0 offset:36864" :  : "v"(c.a_rd[0][0]) : "memory"); \
  asm volatile("v_mfma_f32_32x32x16_bf16 a[224:239], v[104:107], v[124:127], a[224:239]\n\tds_read_b128 v[76:79], %0 offset:38912" :  : "v"(c.a_rd[0][0]) : "memory"); \
  asm volatile("s_waitcnt lgkmcnt(12)\n\tv_mfma_f32_32x32x16_bf16 a[144:159], v[108:111], v[112:115], a[144:159]" :  :  : "memory"); \
  unsigned so261; \
  asm volatile("v_mfma_f32_32x32x16_bf16 a[176:191], v[108:111], v[116:119], a[176:191]\n\ts_add_u32 m0, %1, 117760\n\ts_add_u32 %0, %4, 3964928\n\tbuffer_load_dwordx4 %2, %3, %0 offen offset:0 lds" : "=&s"(so261) : "s"(c.m0w), "v"(c.vb[1]), "s"(c.sb), "s"(c.sob) : "memory", "m0", "scc", ACL64); \
  asm volatile("v_mfma_f32_32x32x16_bf16 a[208:223], v[108:111], v[120:123], a[208:223]" :  :  : "memory"); \
  asm volatile("v_mfma_f32_32x32x16_bf16 a[240:255], v[108:111], v[124:127], a[240:255]" :  :  : "memory"); \
  asm volatile("s_waitcnt vmcnt(8)\n\ts_barrier" ::: "memory"); \
  KV_HOOK(13); \
  asm volatile("s_waitcnt lgkmcnt(9)\n\tv_mfma_f32_32x32x16_bf16 a[0:15], v[64:67], v[80:83], a[0:15]\n\tds_read_b64_tr_b16 v[112:113], %0 offset:57344" :  : "v"(c.b_rd[0][0]) : "memory"); \
  asm volatile("s_waitcnt lgkmcnt(8)\n\tv_mfma_f32_32x32x16_bf16 a[32:47], v[64:67], v[84:87], a[32:47]\n\tds_read_b64_tr_b16 v[114:115], %0 offset:59392\n\ts_add_u32 m0, %1, 135168\n\ts_nop 0\n\tbuffer_load_dwordx4 %2, %3, %4 offen offset:0 sc1 lds" :  : "v"(c.b_rd[0][0]), "s"(p.m0x), "v"(p.vx), "s"(p.sx), "s"(p.sox) : "memory", "m0", "scc"); \
  asm volatile("s_waitcnt lgkmcnt(7)\n\tv_mfma_f32_32x32x16_bf16 a[64:79], v[64:67], v[88:91], a[64:79]\n\tds_read_b128 v[96:99], %0 offset:32768" :  : "v"(c.a_rd[0][1]) : "memory"); \
  asm volatile("s_waitcnt lgkmcnt(6)\n\tv_mfma_f32_32x32x16_bf16 a[96:111], v[64:67], v[92:95], a[96:111]\n\tds_read_b64_tr_b16 v[116:117], %0 offset:57344" :  : "v"(c.b_rd[0][1]) : "memory"); \
  asm volatile("s_waitcnt lgkmcnt(6)\n\tv_mfma_f32_32x32x16_bf16 a[16:31], v[68:71], v[80:83], a[16:31]\n\tds_read_b64_tr_b16 v[118:119], %0 offset:59392" :  : "v"(c.b_rd[0][1]) : "memory"); \
  unsigned so262; \
  asm volatile("v_mfma_f32_32x32x16_bf16 a[48:63], v[68:71], v[84:87], a[48:63]\n\tds_read_b64_tr_b16 v[120:121], %1 offset:57344\n\ts_add_u32 m0, %2, 136192\n\ts_add_u32 %0, %5, 16384\n\tbuffer_load_dwordx4 %3, %4, %0 offen offset:0 sc1 lds" : "=&s"(so262) : "v"(c.b_rd[0][2]), "s"(p.m0x), "v"(p.vx), "s"(p.sx), "s"(p.sox) : "memory", "m0", "scc", ACL64); \
  asm volatile("v_mfma_f32_32x32x16_bf16 a[80:95], v[68:71], v[88:91], a[80:95]\n\tds_read_b64_tr_b16 v[122:123], %0 offset:59392" :  : "v"(c.b_rd[0][2]) : "memory"); \
  asm volatile("v_mfma_f32_32x32x16_bf16 a[112:127], v[68:71], v[92:95], a[112:127]\n\tds_read_b64_tr_b16 v[124:125], %0 offset:57344" :  : "v"(c.b_rd[0][3]) : "memory"); \
  asm volatile("s_waitcnt lgkmcnt(9)\n\tv_mfma_f32_32x32x16_bf16 a[128:143], v[72:75], v[80:83], a[128:143]\n\tds_read_b64_tr_b16 v[126:127], %0 offset:59392" :  : "v"(c.b_rd[0][3]) : "memory"); \
  unsigned so263; \
  asm volatile("v_mfma_f32_32x32x16_bf16 a[160:175], v[72:75], v[84:87], a[160:175]\n\tds_read_b128 v[100:103], %1 offset:34816\n\ts_add_u32 m0, %2, 137216\n\ts_add_u32 %0, %5, 32768\n\tbuffer_load_dwordx4 %3, %4, %0 offen offset:0 sc1 lds" : "=&s"(so263) : "v"(c.a_rd[0][1]), "s"(p.m0x), "v"(p.vx), "s"(p.sx), "s"(p.sox) : "memory", "m0", "scc"); \
  asm volatile("v_mfma_f32_32x32x16_bf16 a[192:207], v[72:75], v[88:91], a[192:207]\n\tds_read_b128 v[104:107], %0 offset:36864" :  : "v"(c.a_rd[0][1]) : "memory"); \
  asm volatile("v_mfma_f32_32x32x16_bf16 a[224:239], v[72:75], v[92:95], a[224:239]\n\tds_read_b128 v[108:111], %0 offset:38912" :  : "v"(c.a_rd[0][1]) : "memory"); \
  asm volatile("s_waitcnt lgkmcnt(12)\n\tv_mfma_f32_32x32x16_bf16 a[144:159], v[76:79], v[80:83], a[144:159]" :  :  : "memory"); \
  unsigned so264; \
  asm volatile("v_mfma_f32_32x32x16_bf16 a[176:191], v[76:79], v[84:87], a[176:191]\n\ts_add_u32 m0, %1, 138240\n\ts_add_u32 %0, %4, 49152\n\tbuffer_load_dwordx4 %2, %3, %0 offen offset:0 sc1 lds" : "=&s"(so264) : "s"(p.m0x), "v"(p.vx), "s"(p.sx), "s"(p.sox) : "memory", "m0", "scc", ACL64); \
  asm volatile("v_mfma_f32_32x32x16_bf16 a[208:223], v[76:79], v[88:91], a[208:223]" :  :  : "memory"); \
  asm volatile("v_mfma_f32_32x32x16_bf16 a[240:255], v[76:79], v[92:95], a[240:255]" :  :  : "memory"); \
  asm volatile("s_waitcnt lgkmcnt(9)\n\tv_mfma_f32_32x32x16_bf16 a[0:15], v[96:99], v[112:115], a[0:15]\n\tds_read_b64_tr_b16 v[80:81], %0 offset:16384" :  : "v"(c.b_rd[1][0]) : "memory"); \
  asm volatile("s_waitcnt lgkmcnt(8)\n\tv_mfma_f32_32x32x16_bf16 a[32:47], v[96:99], v[116:119], a[32:47]\n\tds_read_b64_tr_b16 v[82:83], %0 offset:18432\n\ts_add_u32 m0, %1, 135168\n\ts_nop 0\n\tbuffer_load_dwordx4 %2, %3, %4 offen offset:0 lds" :  : "v"(c.b_rd[1][0]), "s"(p.m0q), "v"(p.vq), "s"(p.sq), "s"(p.soq) : "memory", "m0", "scc"); \
  asm volatile("s_waitcnt lgkmcnt(7)\n\tv_mfma_f32_32x32x16_bf16 a[64:79], v[96:99], v[120:123], a[64:79]\n\tds_read_b128 v[64:67], %0 offset:0" :  : "v"(c.a_rd[1][0]) : "memory"); \
  asm volatile("s_waitcnt lgkmcnt(6)\n\tv_mfma_f32_32x32x16_bf16 a[96:111], v[96:99], v[124:127], a[96:111]\n\tds_read_b64_tr_b16 v[84:85], %0 offset:16384" :  : "v"(c.b_rd[1][1]) : "memory"); \
  asm volatile("s_waitcnt lgkmcnt(6)\n\tv_mfma_f32_32x32x16_bf16 a[16:31], v[100:103], v[112:115], a[16:31]\n\tds_read_b64_tr_b16 v[86:87], %0 offset:18432" :  : "v"(c.b_rd[1][1]) : "memory"); \
  unsigned so265; \
  asm volatile("v_mfma_f32_32x32x16_bf16 a[48:63], v[100:103], v[116:119], a[48:63]\n\tds_read_b64_tr_b16 v[88:89], %1 offset:16384\n\ts_add_u32 m0, %2, 136192\n\ts_add_u32 %0, %5, 32768\n\tbuffer_load_dwordx4 %3, %4, %0 offen offset:0 lds" : "=&s"(so265) : "v"(c.b_rd[1][2]), "s"(p.m0q), "v"(p.vq), "s"(p.sq), "s"(p.soq) : "memory", "m0", "scc", ACL64); \
  asm volatile("v_mfma_f32_32x32x16_bf16 a[80:95], v[100:103], v[120:123], a[80:95]\n\tds_read_b64_tr_b16 v[90:91], %0 offset:18432" :  : "v"(c.b_rd[1][2]) : "memory"); \
  asm volatile("v_mfma_f32_32x32x16_bf16 a[112:127], v[100:103], v[124:127], a[112:127]\n\tds_read_b64_tr_b16 v[92:93], %0 offset:16384" :  : "v"(c.b_rd[1][3]) : "memory"); \
  asm volatile("s_waitcnt lgkmcnt(9)\n\tv_mfma_f32_32x32x16_bf16 a[128:143], v[104:107], v[112:115], a[128:143]\n\tds_read_b64_tr_b16 v[94:95], %0 offset:18432" :  : "v"(c.b_rd[1][3]) : "memory"); \
  asm volatile("v_mfma_f32_32x32x16_bf16 a[160:175], v[104:107], v[116:119], a[160:175]\n\tds_read_b128 v[68:71], %0 offset:2048\n\ts_add_u32 m0, %1, 135168\n\ts_nop 0\n\tbuffer_load_dwordx4 %2, %3, %4 offen offset:0 lds" :  : "v"(c.a_rd[1][0]), "s"(p.m01), "v"(p.v1), "s"(p.s1), "s"(p.so1) : "memory", "m0", "scc"); \
  asm volatile("v_mfma_f32_32x32x16_bf16 a[192:207], v[104:107], v[120:123], a[192:207]\n\tds_read_b128 v[72:75], %0 offset:4096" :  : "v"(c.a_rd[1][0]) : "memory"); \
  asm volatile("v_mfma_f32_32x32x16_bf16 a[224:239], v[104:107], v[124:127], a[224:239]\n\tds_read_b128 v[76:79], %0 offset:6144" :  : "v"(c.a_rd[1][0]) : "memory"); \
  asm volatile("s_waitcnt lgkmcnt(12)\n\tv_mfma_f32_32x32x16_bf16 a[144:159], v[108:111], v[112:115], a[144:159]" :  :  : "memory"); \
  asm volatile("v_mfma_f32_32x32x16_bf16 a[176:191], v[108:111], v[116:119], a[176:191]" :  :  : "memory", ACL64); \
  asm volatile("v_mfma_f32_32x32x16_bf16 a[208:223], v[108:111], v[120:123], a[208:223]" :  :  : "memory"); \
  asm volatile("v_mfma_f32_32x32x16_bf16 a[240:255], v[108:111], v[124:127], a[240:255]" :  :  : "memory"); \
  asm volatile("s_waitcnt vmcnt(7)\n\ts_barrier" ::: "memory"); \
  KV_HOOK(14); \
  asm volatile("s_waitcnt lgkmcnt(9)\n\tv_mfma_f32_32x32x16_bf16 a[0:15], v[64:67], v[80:83], a[0:15]\n\tds_read_b64_tr_b16 v[112:113], %0 offset:24576" :  : "v"(c.b_rd[1][0]) : "memory"); \
  asm volatile("s_waitcnt lgkmcnt(8)\n\tv_mfma_f32_32x32x16_bf16 a[32:47], v[64:67], v[84:87], a[32:47]\n\tds_read_b64_tr_b16 v[114:115], %0 offset:26624" :  : "v"(c.b_rd[1][0]) : "memory"); \
  asm volatile("s_waitcnt lgkmcnt(7)\n\tv_mfma_f32_32x32x16_bf16 a[64:79], v[64:67], v[88:91], a[64:79]\n\tds_read_b128 v[96:99], %0 offset:0" :  : "v"(c.a_rd[1][1]) : "memory"); \
  asm volatile("s_waitcnt lgkmcnt(6)\n\tv_mfma_f32_32x32x16_bf16 a[96:111], v[64:67], v[92:95], a[96:111]\n\tds_read_b64_tr_b16 v[116:117], %0 offset:24576" :  : "v"(c.b_rd[1][1]) : "memory"); \
  asm volatile("s_waitcnt lgkmcnt(6)\n\tv_mfma_f32_32x32x16_bf16 a[16:31], v[68:71], v[80:83], a[16:31]\n\tds_read_b64_tr_b16 v[118:119], %0 offset:26624" :  : "v"(c.b_rd[1][1]) : "memory"); \
  asm volatile("v_mfma_f32_32x32x16_bf16 a[48:63], v[68:71], v[84:87], a[48:63]\n\tds_read_b64_tr_b16 v[120:121], %0 offset:24576" :  : "v"(c.b_rd[1][2]) : "memory", ACL64); \
  asm volatile("v_mfma_f32_32x32x16_bf16 a[80:95], v[68:71], v[88:91], a[80:95]\n\tds_read_b64_tr_b16 v[122:123], %0 offset:26624" :  : "v"(c.b_rd[1][2]) : "memory"); \
  asm volatile("v_mfma_f32_32x32x16_bf16 a[112:127], v[68:71], v[92:95], a[112:127]\n\tds_read_b64_tr_b16 v[124:125], %0 offset:24576" :  : "v"(c.b_rd[1][3]) : "memory"); \
  asm volatile("s_waitcnt lgkmcnt(9)\n\tv_mfma_f32_32x32x16_bf16 a[128:143], v[72:75], v[80:83], a[128:143]\n\tds_read_b64_tr_b16 v[126:127], %0 offset:26624" :  : "v"(c.b_rd[1][3]) : "memory"); \
  asm volatile("v_mfma_f32_32x32x16_bf16 a[160:175], v[72:75], v[84:87], a[160:175]\n\tds_read_b128 v[100:103], %0 offset:2048" :  : "v"(c.a_rd[1][1]) : "memory"); \
  asm volatile("v_mfma_f32_32x32x16_bf16 a[192:207], v[72:75], v[88:91], a[192:207]\n\tds_read_b128 v[104:107], %0 offset:4096" :  : "v"(c.a_rd[1][1]) : "memory"); \
  asm volatile("v_mfma_f32_32x32x16_bf16 a[224:239], v[72:75], v[92:95], a[224:239]\n\tds_read_b128 v[108:111], %0 offset:6144" :  : "v"(c.a_rd[1][1]) : "memory"); \
  asm volatile("s_waitcnt lgkmcnt(12)\n\tv_mfma_f32_32x32x16_bf16 a[144:159], v[76:79], v[80:83], a[144:159]" :  :  : "memory"); \
  asm volatile("v_mfma_f32_32x32x16_bf16 a[176:191], v[76:79], v[84:87], a[176:191]" :  :  : "memory", ACL64); \
  asm volatile("v_mfma_f32_32x32x16_bf16 a[208:223], v[76:79], v[88:91], a[208:223]" :  :  : "memory"); \
  asm volatile("v_mfma_f32_32x32x16_bf16 a[240:255], v[76:79], v[92:95], a[240:255]" :  :  : "memory"); \
  asm volatile("s_waitcnt lgkmcnt(9)\n\tv_mfma_f32_32x32x16_bf16 a[0:15], v[96:99], v[112:115], a[0:15]\n\tds_read_b64_tr_b16 v[80:81], %0 offset:49152" :  : "v"(c.b_rd[1][0]) : "memory"); \
  asm volatile("s_waitcnt lgkmcnt(8)\n\tv_mfma_f32_32x32x16_bf16 a[32:47], v[96:99], v[116:119], a[32:47]\n\tds_read_b64_tr_b16 v[82:83], %0 offset:51200" :  : "v"(c.b_rd[1][0]) : "memory"); \
  asm volatile("s_waitcnt lgkmcnt(7)\n\tv_mfma_f32_32x32x16_bf16 a[64:79], v[96:99], v[120:123], a[64:79]\n\tds_read_b128 v[64:67], %0 offset:32768" :  : "v"(c.a_rd[1][0]) : "memory"); \
  asm volatile("s_waitcnt lgkmcnt(6)\n\tv_mfma_f32_32x32x16_bf16 a[96:111], v[96:99], v[124:127], a[96:111]\n\tds_read_b64_tr_b16 v[84:85], %0 offset:49152" :  : "v"(c.b_rd[1][1]) : "memory"); \
  asm volatile("s_waitcnt lgkmcnt(6)\n\tv_mfma_f32_32x32x16_bf16 a[16:31], v[100:103], v[112:115], a[16:31]\n\tds_read_b64_tr_b16 v[86:87], %0 offset:51200" :  : "v"(c.b_rd[1][1]) : "memory"); \
  asm volatile("v_mfma_f32_32x32x16_bf16 a[48:63], v[100:103], v[116:119], a[48:63]\n\tds_read_b64_tr_b16 v[88:89], %0 offset:49152" :  : "v"(c.b_rd[1][2]) : "memory", ACL64); \
  asm volatile("v_mfma_f32_32x32x16_bf16 a[80:95], v[100:103], v[120:123], a[80:95]\n\tds_read_b64_tr_b16 v[90:91], %0 offset:51200" :  : "v"(c.b_rd[1][2]) : "memory"); \
  asm volatile("v_mfma_f32_32x32x16_bf16 a[112:127], v[100:103], v[124:127], a[112:127]\n\tds_read_b64_tr_b16 v[92:93], %0 offset:49152" :  : "v"(c.b_rd[1][3]) : "memory"); \
  asm volatile("s_waitcnt lgkmcnt(9)\n\tv_mfma_f32_32x32x16_bf16 a[128:143], v[104:107], v[112:115], a[128:143]\n\tds_read_b64_tr_b16 v[94:95], %0 offset:51200" :  : "v"(c.b_rd[1][3]) : "memory"); \
  asm volatile("v_mfma_f32_32x32x16_bf16 a[160:175], v[104:107], v[116:119], a[160:175]\n\tds_read_b128 v[68:71], %0 offset:34816" :  : "v"(c.a_rd[1][0]) : "memory"); \
  asm volatile("v_mfma_f32_32x32x16_bf16 a[192:207], v[104:107], v[120:123], a[192:207]\n\tds_read_b128 v[72:75], %0 offset:36864" :  : "v"(c.a_rd[1][0]) : "memory"); \
  asm volatile("v_mfma_f32_32x32x16_bf16 a[224:239], v[104:107], v[124:127], a[224:239]\n\tds_read_b128 v[76:79], %0 offset:38912" :  : "v"(c.a_rd[1][0]) : "memory"); \
  asm volatile("s_waitcnt lgkmcnt(12)\n\tv_mfma_f32_32x32x16_bf16 a[144:159], v[108:111], v[112:115], a[144:159]" :  :  : "memory"); \
  asm volatile("v_mfma_f32_32x32x16_bf16 a[176:191], v[108:111], v[116:119], a[176:191]" :  :  : "memory", ACL64); \
  asm volatile("v_mfma_f32_32x32x16_bf16 a[208:223], v[108:111], v[120:123], a[208:223]" :  :  : "memory"); \
  asm volatile("v_mfma_f32_32x32x16_bf16 a[240:255], v[108:111], v[124:127], a[240:255]" :  :  : "memory"); \
  asm volatile("s_waitcnt vmcnt(7)\n\ts_barrier" ::: "memory"); \
  KV_HOOK(15); \
  asm volatile("s_waitcnt lgkmcnt(9)\n\tv_mfma_f32_32x32x16_bf16 a[0:15], v[64:67], v[80:83], a[0:15]\n\tds_read_b64_tr_b16 v[112:113], %0 offset:57344" :  : "v"(c.b_rd[1][0]) : "memory"); \
  unsigned so266; \
  asm volatile("s_waitcnt lgkmcnt(8)\n\tv_mfma_f32_32x32x16_bf16 a[32:47], v[64:67], v[84:87], a[32:47]\n\tds_read_b64_tr_b16 v[114:115], %1 offset:59392\n\ts_add_u32 m0, %2, 49152\n\ts_add_u32 %0, %5, 64\n\tbuffer_load_dwordx4 %3, %4, %0 offen offset:0 sc1 lds" : "=&s"(so266) : "v"(c.b_rd[1][0]), "s"(p.m0x), "v"(p.vx), "s"(p.sx), "s"(p.sox) : "memory", "m0", "scc"); \
  asm volatile("s_waitcnt lgkmcnt(7)\n\tv_mfma_f32_32x32x16_bf16 a[64:79], v[64:67], v[88:91], a[64:79]\n\tds_read_b128 v[96:99], %0 offset:32768" :  : "v"(c.a_rd[1][1]) : "memory"); \
  asm volatile("s_waitcnt lgkmcnt(6)\n\tv_mfma_f32_32x32x16_bf16 a[96:111], v[64:67], v[92:95], a[96:111]\n\tds_read_b64_tr_b16 v[116:117], %0 offset:57344" :  : "v"(c.b_rd[1][1]) : "memory"); \
  asm volatile("s_waitcnt lgkmcnt(6)\n\tv_mfma_f32_32x32x16_bf16 a[16:31], v[68:71], v[80:83], a[16:31]\n\tds_read_b64_tr_b16 v[118:119], %0 offset:59392" :  : "v"(c.b_rd[1][1]) : "memory"); \
  unsigned so267; \
  asm volatile("v_mfma_f32_32x32x16_bf16 a[48:63], v[68:71], v[84:87], a[48:63]\n\tds_read_b64_tr_b16 v[120:121], %1 offset:57344\n\ts_add_u32 m0, %2, 50176\n\ts_add_u32 %0, %5, 16448\n\tbuffer_load_dwordx4 %3, %4, %0 offen offset:0 sc1 lds" : "=&s"(so267) : "v"(c.b_rd[1][2]), "s"(p.m0x), "v"(p.vx), "s"(p.sx), "s"(p.sox) : "memory", "m0", "scc", ACL64); \
  asm volatile("v_mfma_f32_32x32x16_bf16 a[80:95], v[68:71], v[88:91], a[80:95]\n\tds_read_b64_tr_b16 v[122:123], %0 offset:59392" :  : "v"(c.b_rd[1][2]) : "memory"); \
  asm volatile("v_mfma_f32_32x32x16_bf16 a[112:127], v[68:71], v[92:95], a[112:127]\n\tds_read_b64_tr_b16 v[124:125], %0 offset:57344" :  : "v"(c.b_rd[1][3]) : "memory"); \
  asm volatile("s_waitcnt lgkmcnt(9)\n\tv_mfma_f32_32x32x16_bf16 a[128:143], v[72:75], v[80:83], a[128:143]\n\tds_read_b64_tr_b16 v[126:127], %0 offset:59392" :  : "v"(c.b_rd[1][3]) : "memory"); \
  unsigned so268; \
  asm volatile("v_mfma_f32_32x32x16_bf16 a[160:175], v[72:75], v[84:87], a[160:175]\n\tds_read_b128 v[100:103], %1 offset:34816\n\ts_add_u32 m0, %2, 51200\n\ts_add_u32 %0, %5, 32832\n\tbuffer_load_dwordx4 %3, %4, %0 offen offset:0 sc1 lds" : "=&s"(so268) : "v"(c.a_rd[1][1]), "s"(p.m0x), "v"(p.vx), "s"(p.sx), "s"(p.sox) : "memory", "m0", "scc"); \
  asm volatile("v_mfma_f32_32x32x16_bf16 a[192:207], v[72:75], v[88:91], a[192:207]\n\tds_read_b128 v[104:107], %0 offset:36864" :  : "v"(c.a_rd[1][1]) : "memory"); \
  asm volatile("v_mfma_f32_32x32x16_bf16 a[224:239], v[72:75], v[92:95], a[224:239]\n\tds_read_b128 v[108:111], %0 offset:38912" :  : "v"(c.a_rd[1][1]) : "memory"); \
  asm volatile("s_waitcnt lgkmcnt(12)\n\tv_mfma_f32_32x32x16_bf16 a[144:159], v[76:79], v[80:83], a[144:159]" :  :  : "memory"); \
  unsigned so269; \
  asm volatile("v_mfma_f32_32x32x16_bf16 a[176:191], v[76:79], v[84:87], a[176:191]\n\ts_add_u32 m0, %1, 52224\n\ts_add_u32 %0, %4, 49216\n\tbuffer_load_dwordx4 %2, %3, %0 offen offset:0 sc1 lds" : "=&s"(so269) : "s"(p.m0x), "v"(p.vx), "s"(p.sx), "s"(p.sox) : "memory", "m0", "scc", ACL64); \
  asm volatile("v_mfma_f32_32x32x16_bf16 a[208:223], v[76:79], v[88:91], a[208:223]" :  :  : "memory"); \
  asm volatile("v_mfma_f32_32x32x16_bf16 a[240:255], v[76:79], v[92:95], a[240:255]" :  :  : "memory"); \
  asm volatile("s_waitcnt lgkmcnt(9)\n\tv_mfma_f32_32x32x16_bf16 a[0:15], v[96:99], v[112:115], a[0:15]" :  :  : "memory"); \
  unsigned so270; \
  asm volatile("s_waitcnt lgkmcnt(7)\n\tv_mfma_f32_32x32x16_bf16 a[32:47], v[96:99], v[116:119], a[32:47]\n\ts_add_u32 m0, %1, 49152\n\ts_add_u32 %0, %4, 262144\n\tbuffer_load_dwordx4 %2, %3, %0 offen offset:0 lds" : "=&s"(so270) : "s"(p.m0q), "v"(p.vq), "s"(p.sq), "s"(p.soq) : "memory", "m0", "scc"); \
  asm volatile("s_waitcnt lgkmcnt(5)\n\tv_mfma_f32_32x32x16_bf16 a[64:79], v[96:99], v[120:123], a[64:79]" :  :  : "memory"); \
  asm volatile("s_waitcnt lgkmcnt(3)\n\tv_mfma_f32_32x32x16_bf16 a[96:111], v[96:99], v[124:127], a[96:111]" :  :  : "memory"); \
  asm volatile("s_waitcnt lgkmcnt(2)\n\tv_mfma_f32_32x32x16_bf16 a[16:31], v[100:103], v[112:115], a[16:31]" :  :  : "memory"); \
  unsigned so271; \
  asm volatile("v_mfma_f32_32x32x16_bf16 a[48:63], v[100:103], v[116:119], a[48:63]\n\ts_add_u32 m0, %1, 50176\n\ts_add_u32 %0, %4, 294912\n\tbuffer_load_dwordx4 %2, %3, %0 offen offset:0 lds" : "=&s"(so271) : "s"(p.m0q), "v"(p.vq), "s"(p.sq), "s"(p.soq) : "memory", "m0", "scc", ACL64); \
  asm volatile("v_mfma_f32_32x32x16_bf16 a[80:95], v[100:103], v[120:123], a[80:95]" :  :  : "memory"); \
  asm volatile("v_mfma_f32_32x32x16_bf16 a[112:127], v[100:103], v[124:127], a[112:127]" :  :  : "memory"); \
  asm volatile("s_waitcnt lgkmcnt(1)\n\tv_mfma_f32_32x32x16_bf16 a[128:143], v[104:107], v[112:115], a[128:143]" :  :  : "memory"); \
  unsigned so272; \
  asm volatile("v_mfma_f32_32x32x16_bf16 a[160:175], v[104:107], v[116:119], a[160:175]\n\ts_add_u32 m0, %1, 49152\n\ts_add_u32 %0, %4, 88064\n\tbuffer_load_dwordx4 %2, %3, %0 offen offset:0 lds" : "=&s"(so272) : "s"(p.m01), "v"(p.v1), "s"(p.s1), "s"(p.so1) : "memory", "m0", "scc"); \
  asm volatile("v_mfma_f32_32x32x16_bf16 a[192:207], v[104:107], v[120:123], a[192:207]" :  :  : "memory"); \
  asm volatile("v_mfma_f32_32x32x16_bf16 a[224:239], v[104:107], v[124:127], a[224:239]" :  :  : "memory"); \
  asm volatile("s_waitcnt lgkmcnt(0)\n\tv_mfma_f32_32x32x16_bf16 a[144:159], v[108:111], v[112:115], a[144:159]" :  :  : "memory"); \
  asm volatile("v_mfma_f32_32x32x16_bf16 a[176:191], v[108:111], v[116:119], a[176:191]" :  :  : "memory", ACL64); \
  asm volatile("v_mfma_f32_32x32x16_bf16 a[208:223], v[108:111], v[120:123], a[208:223]" :  :  : "memory"); \
  asm volatile("v_mfma_f32_32x32x16_bf16 a[240:255], v[108:111], v[124:127], a[240:255]" :  :  : "memory"); \
} while (0)
DI void kv_pack_lo() {
  asm volatile("v_accvgpr_read_b32 v64, a0\n\tv_accvgpr_read_b32 v65, a1\n\tv_accvgpr_read_b32 v66, a2\n\tv_accvgpr_read_b32 v67, a3\n\tv_accvgpr_read_b32 v68, a4\n\tv_accvgpr_read_b32 v69, a5\n\tv_accvgpr_read_b32 v70, a6\n\tv_accvgpr_read_b32 v71, a7\n\tv_cvt_pk_bf16_f32 v184, v64, v65\n\tv_cvt_pk_bf16_f32 v185, v66, v67\n\tv_cvt_pk_bf16_f32 v186, v68, v69\n\tv_cvt_pk_bf16_f32 v187, v70, v71" :  :  : "memory");
  asm volatile("v_accvgpr_read_b32 v72, a8\n\tv_accvgpr_read_b32 v73, a9\n\tv_accvgpr_read_b32 v74, a10\n\tv_accvgpr_read_b32 v75, a11\n\tv_accvgpr_read_b32 v76, a12\n\tv_accvgpr_read_b32 v77, a13\n\tv_accvgpr_read_b32 v78, a14\n\tv_accvgpr_read_b32 v79, a15\n\tv_cvt_pk_bf16_f32 v188, v72, v73\n\tv_cvt_pk_bf16_f32 v189, v74, v75\n\tv_cvt_pk_bf16_f32 v190, v76, v77\n\tv_cvt_pk_bf16_f32 v191, v78, v79" :  :  : "memory");
  asm volatile("v_accvgpr_read_b32 v80, a16\n\tv_accvgpr_read_b32 v81, a17\n\tv_accvgpr_read_b32 v82, a18\n\tv_accvgpr_read_b32 v83, a19\n\tv_accvgpr_read_b32 v84, a20\n\tv_accvgpr_read_b32 v85, a21\n\tv_accvgpr_read_b32 v86, a22\n\tv_accvgpr_read_b32 v87, a23\n\tv_cvt_pk_bf16_f32 v192, v80, v81\n\tv_cvt_pk_bf16_f32 v193, v82, v83\n\tv_cvt_pk_bf16_f32 v194, v84, v85\n\tv_cvt_pk_bf16_f32 v195, v86, v87" :  :  : "memory");
  asm volatile("v_accvgpr_read_b32 v88, a24\n\tv_accvgpr_read_b32 v89, a25\n\tv_accvgpr_read_b32 v90, a26\n\tv_accvgpr_read_b32 v91, a27\n\tv_accvgpr_read_b32 v92, a28\n\tv_accvgpr_read_b32 v93, a29\n\tv_accvgpr_read_b32 v94, a30\n\tv_accvgpr_read_b32 v95, a31\n\tv_cvt_pk_bf16_f32 v196, v88, v89\n\tv_cvt_pk_bf16_f32 v197, v90, v91\n\tv_cvt_pk_bf16_f32 v198, v92, v93\n\tv_cvt_pk_bf16_f32 v199, v94, v95" :  :  : "memory");
  asm volatile("v_accvgpr_read_b32 v64, a32\n\tv_accvgpr_read_b32 v65, a33\n\tv_accvgpr_read_b32 v66, a34\n\tv_accvgpr_read_b32 v67, a35\n\tv_accvgpr_read_b32 v68, a36\n\tv_accvgpr_read_b32 v69, a37\n\tv_accvgpr_read_b32 v70, a38\n\tv_accvgpr_read_b32 v71, a39\n\tv_cvt_pk_bf16_f32 v200, v64, v65\n\tv_cvt_pk_bf16_f32 v201, v66, v67\n\tv_cvt_pk_bf16_f32 v202, v68, v69\n\tv_cvt_pk_bf16_f32 v203, v70, v71" :  :  : "memory");
  asm volatile("v_accvgpr_read_b32 v72, a40\n\tv_accvgpr_read_b32 v73, a41\n\tv_accvgpr_read_b32 v74, a42\n\tv_accvgpr_read_b32 v75, a43\n\tv_accvgpr_read_b32 v76, a44\n\tv_accvgpr_read_b32 v77, a45\n\tv_accvgpr_read_b32 v78, a46\n\tv_accvgpr_read_b32 v79, a47\n\tv_cvt_pk_bf16_f32 v204, v72, v73\n\tv_cvt_pk_bf16_f32 v205, v74, v75\n\tv_cvt_pk_bf16_f32 v206, v76, v77\n\tv_cvt_pk_bf16_f32 v207, v78, v79" :  :  : "memory", ACL64);
  asm volatile("v_accvgpr_read_b32 v80, a48\n\tv_accvgpr_read_b32 v81, a49\n\tv_accvgpr_read_b32 v82, a50\n\tv_accvgpr_read_b32 v83, a51\n\tv_accvgpr_read_b32 v84, a52\n\tv_accvgpr_read_b32 v85, a53\n\tv_accvgpr_read_b32 v86, a54\n\tv_accvgpr_read_b32 v87, a55\n\tv_cvt_pk_bf16_f32 v208, v80, v81\n\tv_cvt_pk_bf16_f32 v209, v82, v83\n\tv_cvt_pk_bf16_f32 v210, v84, v85\n\tv_cvt_pk_bf16_f32 v211, v86, v87" :  :  : "memory");
  asm volatile("v_accvgpr_read_b32 v88, a56\n\tv_accvgpr_read_b32 v89, a57\n\tv_accvgpr_read_b32 v90, a58\n\tv_accvgpr_read_b32 v91, a59\n\tv_accvgpr_read_b32 v92, a60\n\tv_accvgpr_read_b32 v93, a61\n\tv_accvgpr_read_b32 v94, a62\n\tv_accvgpr_read_b32 v95, a63\n\tv_cvt_pk_bf16_f32 v212, v88, v89\n\tv_cvt_pk_bf16_f32 v213, v90, v91\n\tv_cvt_pk_bf16_f32 v214, v92, v93\n\tv_cvt_pk_bf16_f32 v215, v94, v95" :  :  : "memory");
  asm volatile("v_accvgpr_read_b32 v64, a64\n\tv_accvgpr_read_b32 v65, a65\n\tv_accvgpr_read_b32 v66, a66\n\tv_accvgpr_read_b32 v67, a67\n\tv_accvgpr_read_b32 v68, a68\n\tv_accvgpr_read_b32 v69, a69\n\tv_accvgpr_read_b32 v70, a70\n\tv_accvgpr_read_b32 v71, a71\n\tv_cvt_pk_bf16_f32 v216, v64, v65\n\tv_cvt_pk_bf16_f32 v217, v66, v67\n\tv_cvt_pk_bf16_f32 v218, v68, v69\n\tv_cvt_pk_bf16_f32 v219, v70, v71" :  :  : "memory");
  asm volatile("v_accvgpr_read_b32 v72, a72\n\tv_accvgpr_read_b32 v73, a73\n\tv_accvgpr_read_b32 v74, a74\n\tv_accvgpr_read_b32 v75, a75\n\tv_accvgpr_read_b32 v76, a76\n\tv_accvgpr_read_b32 v77, a77\n\tv_accvgpr_read_b32 v78, a78\n\tv_accvgpr_read_b32 v79, a79\n\tv_cvt_pk_bf16_f32 v220, v72, v73\n\tv_cvt_pk_bf16_f32 v221, v74, v75\n\tv_cvt_pk_bf16_f32 v222, v76, v77\n\tv_cvt_pk_bf16_f32 v223, v78, v79" :  :  : "memory");
  asm volatile("v_accvgpr_read_b32 v80, a80\n\tv_accvgpr_read_b32 v81, a81\n\tv_accvgpr_read_b32 v82, a82\n\tv_accvgpr_read_b32 v83, a83\n\tv_accvgpr_read_b32 v84, a84\n\tv_accvgpr_read_b32 v85, a85\n\tv_accvgpr_read_b32 v86, a86\n\tv_accvgpr_read_b32 v87, a87\n\tv_cvt_pk_bf16_f32 v224, v80, v81\n\tv_cvt_pk_bf16_f32 v225, v82, v83\n\tv_cvt_pk_bf16_f32 v226, v84, v85\n\tv_cvt_pk_bf16_f32 v227, v86, v87" :  :  : "memory");
  asm volatile("v_accvgpr_read_b32 v88, a88\n\tv_accvgpr_read_b32 v89, a89\n\tv_accvgpr_read_b32 v90, a90\n\tv_accvgpr_read_b32 v91, a91\n\tv_accvgpr_read_b32 v92, a92\n\tv_accvgpr_read_b32 v93, a93\n\tv_accvgpr_read_b32 v94, a94\n\tv_accvgpr_read_b32 v95, a95\n\tv_cvt_pk_bf16_f32 v228, v88, v89\n\tv_cvt_pk_bf16_f32 v229, v90, v91\n\tv_cvt_pk_bf16_f32 v230, v92, v93\n\tv_cvt_pk_bf16_f32 v231, v94, v95" :  :  : "memory");
  asm volatile("v_accvgpr_read_b32 v64, a96\n\tv_accvgpr_read_b32 v65, a97\n\tv_accvgpr_read_b32 v66, a98\n\tv_accvgpr_read_b32 v67, a99\n\tv_accvgpr_read_b32 v68, a100\n\tv_accvgpr_read_b32 v69, a101\n\tv_accvgpr_read_b32 v70, a102\n\tv_accvgpr_read_b32 v71, a103\n\tv_cvt_pk_bf16_f32 v232, v64, v65\n\tv_cvt_pk_bf16_f32 v233, v66, v67\n\tv_cvt_pk_bf16_f32 v234, v68, v69\n\tv_cvt_pk_bf16_f32 v235, v70, v71" :  :  : "memory");
  asm volatile("v_accvgpr_read_b32 v72, a104\n\tv_accvgpr_read_b32 v73, a105\n\tv_accvgpr_read_b32 v74, a106\n\tv_accvgpr_read_b32 v75, a107\n\tv_accvgpr_read_b32 v76, a108\n\tv_accvgpr_read_b32 v77, a109\n\tv_accvgpr_read_b32 v78, a110\n\tv_accvgpr_read_b32 v79, a111\n\tv_cvt_pk_bf16_f32 v236, v72, v73\n\tv_cvt_pk_bf16_f32 v237, v74, v75\n\tv_cvt_pk_bf16_f32 v238, v76, v77\n\tv_cvt_pk_bf16_f32 v239, v78, v79" :  :  : "memory", ACL64);
  asm volatile("v_accvgpr_read_b32 v80, a112\n\tv_accvgpr_read_b32 v81, a113\n\tv_accvgpr_read_b32 v82, a114\n\tv_accvgpr_read_b32 v83, a115\n\tv_accvgpr_read_b32 v84, a116\n\tv_accvgpr_read_b32 v85, a117\n\tv_accvgpr_read_b32 v86, a118\n\tv_accvgpr_read_b32 v87, a119\n\tv_cvt_pk_bf16_f32 v240, v80, v81\n\tv_cvt_pk_bf16_f32 v241, v82, v83\n\tv_cvt_pk_bf16_f32 v242, v84, v85\n\tv_cvt_pk_bf16_f32 v243, v86, v87" :  :  : "memory");
  asm volatile("v_accvgpr_read_b32 v88, a120\n\tv_accvgpr_read_b32 v89, a121\n\tv_accvgpr_read_b32 v90, a122\n\tv_accvgpr_read_b32 v91, a123\n\tv_accvgpr_read_b32 v92, a124\n\tv_accvgpr_read_b32 v93, a125\n\tv_accvgpr_read_b32 v94, a126\n\tv_accvgpr_read_b32 v95, a127\n\tv_cvt_pk_bf16_f32 v244, v88, v89\n\tv_cvt_pk_bf16_f32 v245, v90, v91\n\tv_cvt_pk_bf16_f32 v246, v92, v93\n\tv_cvt_pk_bf16_f32 v247, v94, v95" :  :  : "memory");
}
DI void pj_pro(PC& p) {
  unsigned so284;
  asm volatile("s_add_u32 m0, %1, 77824\n\ts_add_u32 %0, %4, 128\n\tbuffer_load_dwordx4 %2, %3, %0 offen offset:0 sc1 lds" : "=&s"(so284) : "s"(p.m0x), "v"(p.vx), "s"(p.sx), "s"(p.sox) : "memory", "m0", "scc");
  unsigned so285;
  asm volatile("s_add_u32 m0, %1, 78848\n\ts_add_u32 %0, %4, 16512\n\tbuffer_load_dwordx4 %2, %3, %0 offen offset:0 sc1 lds" : "=&s"(so285) : "s"(p.m0x), "v"(p.vx), "s"(p.sx), "s"(p.sox) : "memory", "m0", "scc");
  unsigned so286;
  asm volatile("s_add_u32 m0, %1, 79872\n\ts_add_u32 %0, %4, 32896\n\tbuffer_load_dwordx4 %2, %3, %0 offen offset:0 sc1 lds" : "=&s"(so286) : "s"(p.m0x), "v"(p.vx), "s"(p.sx), "s"(p.sox) : "memory", "m0", "scc");
  unsigned so287;
  asm volatile("s_add_u32 m0, %1, 80896\n\ts_add_u32 %0, %4, 49280\n\tbuffer_load_dwordx4 %2, %3, %0 offen offset:0 sc1 lds" : "=&s"(so287) : "s"(p.m0x), "v"(p.vx), "s"(p.sx), "s"(p.sox) : "memory", "m0", "scc");
  unsigned so288;
  asm volatile("s_add_u32 m0, %1, 77824\n\ts_add_u32 %0, %4, 524288\n\tbuffer_load_dwordx4 %2, %3, %0 offen offset:0 lds" : "=&s"(so288) : "s"(p.m0q), "v"(p.vq), "s"(p.sq), "s"(p.soq) : "memory", "m0", "scc");
  unsigned so289;
  asm volatile("s_add_u32 m0, %1, 78848\n\ts_add_u32 %0, %4, 557056\n\tbuffer_load_dwordx4 %2, %3, %0 offen offset:0 lds" : "=&s"(so289) : "s"(p.m0q), "v"(p.vq), "s"(p.sq), "s"(p.soq) : "memory", "m0", "scc", ACL64);
  unsigned so290;
  asm volatile("s_add_u32 m0, %1, 77824\n\ts_add_u32 %0, %4, 176128\n\tbuffer_load_dwordx4 %2, %3, %0 offen offset:0 lds" : "=&s"(so290) : "s"(p.m01), "v"(p.v1), "s"(p.s1), "s"(p.so1) : "memory", "m0", "scc");
}
#define PJ_BODY(p) do { \
  asm volatile("ds_read_b64_tr_b16 v[76:77], %0 offset:28672\n\tds_read_b64_tr_b16 v[78:79], %0 offset:29696\n\tds_read_b128 v[64:67], %1 offset:28672\n\tds_read_b128 v[68:71], %1 offset:30720\n\tds_read_b64_tr_b16 v[80:81], %2 offset:28672\n\tds_read_b64_tr_b16 v[82:83], %2 offset:29696\n\tds_read_b64_tr_b16 v[84:85], %3 offset:28672\n\tds_read_b64_tr_b16 v[86:87], %3 offset:29696\n\tds_read_b64_tr_b16 v[88:89], %4 offset:28672\n\tds_read_b64_tr_b16 v[90:91], %4 offset:29696\n\tds_read_b64_tr_b16 v[92:93], %5 offset:28672\n\tds_read_b64_tr_b16 v[94:95], %5 offset:28928\n\tds_read_b128 v[72:75], %6 offset:28672\n\tds_read_b64_tr_b16 v[96:97], %5 offset:30720\n\tds_read_b64_tr_b16 v[98:99], %5 offset:30976" :  : "v"(p.q_rd[1][0]), "v"(p.x_rd[1][0]), "v"(p.q_rd[1][1]), "v"(p.q_rd[1][2]), "v"(p.q_rd[1][3]), "v"(p.f_rd[1]), "v"(p.xs_rd[1][0]) : "memory"); \
  asm volatile("s_waitcnt vmcnt(7)\n\ts_barrier" ::: "memory"); \
  PJ_HOOK(0); \
  asm volatile("s_waitcnt lgkmcnt(12)\n\tv_mfma_f32_32x32x16_bf16 a[0:15], v[76:79], v[64:67], 0\n\tds_read_b64_tr_b16 v[112:113], %0 offset:32768" :  : "v"(p.q_rd[1][0]) : "memory"); \
  unsigned so291; \
  asm volatile("s_waitcnt lgkmcnt(12)\n\tv_mfma_f32_32x32x16_bf16 a[16:31], v[76:79], v[68:71], 0\n\tds_read_b64_tr_b16 v[114:115], %1 offset:33792\n\ts_add_u32 m0, %2, 106496\n\ts_add_u32 %0, %5, 192\n\tbuffer_load_dwordx4 %3, %4, %0 offen offset:0 sc1 lds" : "=&s"(so291) : "v"(p.q_rd[1][0]), "s"(p.m0x), "v"(p.vx), "s"(p.sx), "s"(p.sox) : "memory", "m0", "scc"); \
  asm volatile("s_waitcnt lgkmcnt(11)\n\tv_mfma_f32_32x32x16_bf16 a[32:47], v[80:83], v[64:67], 0\n\tds_read_b128 v[100:103], %0 offset:28672\n\tds_read_b128 v[104:107], %0 offset:30720" :  : "v"(p.x_rd[1][1]) : "memory"); \
  asm volatile("v_mfma_f32_32x32x16_bf16 a[48:63], v[80:83], v[68:71], 0\n\tds_read_b64_tr_b16 v[116:117], %0 offset:32768" :  : "v"(p.q_rd[1][1]) : "memory"); \
  unsigned so292; \
  asm volatile("s_waitcnt lgkmcnt(12)\n\tv_mfma_f32_32x32x16_bf16 a[64:79], v[84:87], v[64:67], 0\n\tds_read_b64_tr_b16 v[118:119], %1 offset:33792\n\ts_add_u32 m0, %2, 107520\n\ts_add_u32 %0, %5, 16576\n\tbuffer_load_dwordx4 %3, %4, %0 offen offset:0 sc1 lds" : "=&s"(so292) : "v"(p.q_rd[1][1]), "s"(p.m0x), "v"(p.vx), "s"(p.sx), "s"(p.sox) : "memory", "m0", "scc"); \
  asm volatile("v_mfma_f32_32x32x16_bf16 a[80:95], v[84:87], v[68:71], 0\n\tds_read_b64_tr_b16 v[120:121], %0 offset:32768\n\tds_read_b64_tr_b16 v[122:123], %0 offset:33792" :  : "v"(p.q_rd[1][2]) : "memory", ACL64); \
  asm volatile("s_waitcnt lgkmcnt(13)\n\tv_mfma_f32_32x32x16_bf16 a[96:111], v[88:91], v[64:67], 0\n\tds_read_b64_tr_b16 v[124:125], %0 offset:32768" :  : "v"(p.q_rd[1][3]) : "memory"); \
  unsigned so293; \
  asm volatile("v_mfma_f32_32x32x16_bf16 a[112:127], v[88:91], v[68:71], 0\n\tds_read_b64_tr_b16 v[126:127], %1 offset:33792\n\ts_add_u32 m0, %2, 108544\n\ts_add_u32 %0, %5, 32960\n\tbuffer_load_dwordx4 %3, %4, %0 offen offset:0 sc1 lds" : "=&s"(so293) : "v"(p.q_rd[1][3]), "s"(p.m0x), "v"(p.vx), "s"(p.sx), "s"(p.sox) : "memory", "m0", "scc"); \
  asm volatile("s_waitcnt lgkmcnt(13)\n\tv_mfma_f32_32x32x16_bf16 v[136:151], v[92:95], v[64:67], 0\n\tds_read_b64_tr_b16 v[128:129], %0 offset:29696\n\tds_read_b64_tr_b16 v[130:131], %0 offset:29952" :  : "v"(p.f_rd[1]) : "memory"); \
  asm volatile("v_mfma_f32_32x32x16_bf16 v[152:167], v[92:95], v[68:71], 0\n\tds_read_b128 v[108:111], %0 offset:28672" :  : "v"(p.xs_rd[1][1]) : "memory"); \
  unsigned so294; \
  asm volatile("s_waitcnt lgkmcnt(13)\n\tv_mfma_f32_32x32x16_bf16 v[168:183], v[96:99], v[72:75], 0\n\tds_read_b64_tr_b16 v[132:133], %1 offset:31744\n\tds_read_b64_tr_b16 v[134:135], %1 offset:32000\n\ts_add_u32 m0, %2, 109568\n\ts_add_u32 %0, %5, 49344\n\tbuffer_load_dwordx4 %3, %4, %0 offen offset:0 sc1 lds" : "=&s"(so294) : "v"(p.f_rd[1]), "s"(p.m0x), "v"(p.vx), "s"(p.sx), "s"(p.sox) : "memory", "m0", "scc"); \
  asm volatile("s_waitcnt lgkmcnt(12)\n\tv_mfma_f32_32x32x16_bf16 a[0:15], v[112:115], v[100:103], a[0:15]\n\tds_read_b64_tr_b16 v[76:77], %0 offset:0" :  : "v"(p.q_rd[0][0]) : "memory"); \
  asm volatile("s_waitcnt lgkmcnt(12)\n\tv_mfma_f32_32x32x16_bf16 a[16:31], v[112:115], v[104:107], a[16:31]\n\tds_read_b64_tr_b16 v[78:79], %0 offset:1024" :  : "v"(p.q_rd[0][0]) : "memory"); \
  unsigned so295; \
  asm volatile("s_waitcnt lgkmcnt(11)\n\tv_mfma_f32_32x32x16_bf16 a[32:47], v[116:119], v[100:103], a[32:47]\n\tds_read_b128 v[64:67], %1 offset:0\n\tds_read_b128 v[68:71], %1 offset:2048\n\ts_add_u32 m0, %2, 106496\n\ts_add_u32 %0, %5, 786432\n\tbuffer_load_dwordx4 %3, %4, %0 offen offset:0 lds" : "=&s"(so295) : "v"(p.x_rd[0][0]), "s"(p.m0q), "v"(p.vq), "s"(p.sq), "s"(p.soq) : "memory", "m0", "scc", ACL64); \
  asm volatile("v_mfma_f32_32x32x16_bf16 a[48:63], v[116:119], v[104:107], a[48:63]\n\tds_read_b64_tr_b16 v[80:81], %0 offset:0" :  : "v"(p.q_rd[0][1]) : "memory"); \
  asm volatile("s_waitcnt lgkmcnt(12)\n\tv_mfma_f32_32x32x16_bf16 a[64:79], v[120:123], v[100:103], a[64:79]\n\tds_read_b64_tr_b16 v[82:83], %0 offset:1024" :  : "v"(p.q_rd[0][1]) : "memory"); \
  unsigned so296; \
  asm volatile("v_mfma_f32_32x32x16_bf16 a[80:95], v[120:123], v[104:107], a[80:95]\n\tds_read_b64_tr_b16 v[84:85], %1 offset:0\n\tds_read_b64_tr_b16 v[86:87], %1 offset:1024\n\ts_add_u32 m0, %2, 107520\n\ts_add_u32 %0, %5, 819200\n\tbuffer_load_dwordx4 %3, %4, %0 offen offset:0 lds" : "=&s"(so296) : "v"(p.q_rd[0][2]), "s"(p.m0q), "v"(p.vq), "s"(p.sq), "s"(p.soq) : "memory", "m0", "scc"); \
  asm volatile("s_waitcnt lgkmcnt(13)\n\tv_mfma_f32_32x32x16_bf16 a[96:111], v[124:127], v[100:103], a[96:111]\n\tds_read_b64_tr_b16 v[88:89], %0 offset:0" :  : "v"(p.q_rd[0][3]) : "memory"); \
  asm volatile("v_mfma_f32_32x32x16_bf16 a[112:127], v[124:127], v[104:107], a[112:127]\n\tds_read_b64_tr_b16 v[90:91], %0 offset:1024" :  : "v"(p.q_rd[0][3]) : "memory"); \
  unsigned so297; \
  asm volatile("s_waitcnt lgkmcnt(13)\n\tv_mfma_f32_32x32x16_bf16 v[136:151], v[128:131], v[100:103], v[136:151]\n\tds_read_b64_tr_b16 v[92:93], %1 offset:0\n\tds_read_b64_tr_b16 v[94:95], %1 offset:256\n\ts_add_u32 m0, %2, 106496\n\ts_add_u32 %0, %5, 264192\n\tbuffer_load_dwordx4 %3, %4, %0 offen offset:0 lds" : "=&s"(so297) : "v"(p.f_rd[0]), "s"(p.m01), "v"(p.v1), "s"(p.s1), "s"(p.so1) : "memory", "m0", "scc"); \
  asm volatile("v_mfma_f32_32x32x16_bf16 v[152:167], v[128:131], v[104:107], v[152:167]\n\tds_read_b128 v[72:75], %0 offset:0" :  : "v"(p.xs_rd[0][0]) : "memory"); \
  asm volatile("s_waitcnt lgkmcnt(13)\n\tv_mfma_f32_32x32x16_bf16 v[168:183], v[132:135], v[108:111], v[168:183]\n\tds_read_b64_tr_b16 v[96:97], %0 offset:2048\n\tds_read_b64_tr_b16 v[98:99], %0 offset:2304" :  : "v"(p.f_rd[0]) : "memory", ACL64); \
  asm volatile("s_waitcnt vmcnt(7)\n\ts_barrier" ::: "memory"); \
  PJ_HOOK(1); \
  asm volatile("s_waitcnt lgkmcnt(12)\n\tv_mfma_f32_32x32x16_bf16 a[0:15], v[76:79], v[64:67], a[0:15]\n\tds_read_b64_tr_b16 v[112:113], %0 offset:4096" :  : "v"(p.q_rd[0][0]) : "memory"); \
  unsigned so298; \
  asm volatile("s_waitcnt lgkmcnt(12)\n\tv_mfma_f32_32x32x16_bf16 a[16:31], v[76:79], v[68:71], a[16:31]\n\tds_read_b64_tr_b16 v[114:115], %1 offset:5120\n\ts_add_u32 m0, %2, 135168\n\ts_add_u32 %0, %5, 256\n\tbuffer_load_dwordx4 %3, %4, %0 offen offset:0 sc1 lds" : "=&s"(so298) : "v"(p.q_rd[0][0]), "s"(p.m0x), "v"(p.vx), "s"(p.sx), "s"(p.sox) : "memory", "m0", "scc"); \
  asm volatile("s_waitcnt lgkmcnt(11)\n\tv_mfma_f32_32x32x16_bf16 a[32:47], v[80:83], v[64:67], a[32:47]\n\tds_read_b128 v[100:103], %0 offset:0\n\tds_read_b128 v[104:107], %0 offset:2048" :  : "v"(p.x_rd[0][1]) : "memory"); \
  asm volatile("v_mfma_f32_32x32x16_bf16 a[48:63], v[80:83], v[68:71], a[48:63]\n\tds_read_b64_tr_b16 v[116:117], %0 offset:4096" :  : "v"(p.q_rd[0][1]) : "memory"); \
  unsigned so299; \
  asm volatile("s_waitcnt lgkmcnt(12)\n\tv_mfma_f32_32x32x16_bf16 a[64:79], v[84:87], v[64:67], a[64:79]\n\tds_read_b64_tr_b16 v[118:119], %1 offset:5120\n\ts_add_u32 m0, %2, 136192\n\ts_add_u32 %0, %5, 16640\n\tbuffer_load_dwordx4 %3, %4, %0 offen offset:0 sc1 lds" : "=&s"(so299) : "v"(p.q_rd[0][1]), "s"(p.m0x), "v"(p.vx), "s"(p.sx), "s"(p.sox) : "memory", "m0", "scc"); \
  asm volatile("v_mfma_f32_32x32x16_bf16 a[80:95], v[84:87], v[68:71], a[80:95]\n\tds_read_b64_tr_b16 v[120:121], %0 offset:4096\n\tds_read_b64_tr_b16 v[122:123], %0 offset:5120\n\tds_write_b128 %1, v[184:187]" :  : "v"(p.q_rd[0][2]), "v"(p.te[0]) : "memory"); \
  asm volatile("s_waitcnt lgkmcnt(14)\n\tv_mfma_f32_32x32x16_bf16 a[96:111], v[88:91], v[64:67], a[96:111]\n\tds_read_b64_tr_b16 v[124:125], %0 offset:4096" :  : "v"(p.q_rd[0][3]) : "memory"); \
  unsigned so300; \
  asm volatile("v_mfma_f32_32x32x16_bf16 a[112:127], v[88:91], v[68:71], a[112:127]\n\tds_read_b64_tr_b16 v[126:127], %1 offset:5120\n\ts_add_u32 m0, %2, 137216\n\ts_add_u32 %0, %5, 33024\n\tbuffer_load_dwordx4 %3, %4, %0 offen offset:0 sc1 lds\n\tds_write_b128 %6, v[188:191]" : "=&s"(so300) : "v"(p.q_rd[0][3]), "s"(p.m0x), "v"(p.vx), "s"(p.sx), "s"(p.sox), "v"(p.te[1]) : "memory", "m0", "scc", ACL64); \
  asm volatile("s_waitcnt lgkmcnt(15)\n\tv_mfma_f32_32x32x16_bf16 v[136:151], v[92:95], v[64:67], v[136:151]\n\tds_read_b64_tr_b16 v[128:129], %0 offset:1024\n\tds_read_b64_tr_b16 v[130:131], %0 offset:1280" :  : "v"(p.f_rd[0]) : "memory"); \
  asm volatile("v_mfma_f32_32x32x16_bf16 v[152:167], v[92:95], v[68:71], v[152:167]\n\tds_read_b128 v[108:111], %0 offset:0\n\tds_write_b128 %1, v[192:195]" :  : "v"(p.xs_rd[0][1]), "v"(p.te[2]) : "memory"); \
  unsigned so301; \
  asm volatile("s_waitcnt lgkmcnt(15)\n\tv_mfma_f32_32x32x16_bf16 v[168:183], v[96:99], v[72:75], v[168:183]\n\tds_read_b64_tr_b16 v[132:133], %1 offset:3072\n\tds_read_b64_tr_b16 v[134:135], %1 offset:3328\n\ts_add_u32 m0, %2, 138240\n\ts_add_u32 %0, %5, 49408\n\tbuffer_load_dwordx4 %3, %4, %0 offen offset:0 sc1 lds" : "=&s"(so301) : "v"(p.f_rd[0]), "s"(p.m0x), "v"(p.vx), "s"(p.sx), "s"(p.sox) : "memory", "m0", "scc"); \
  asm volatile("s_waitcnt lgkmcnt(15)\n\tv_mfma_f32_32x32x16_bf16 a[0:15], v[112:115], v[100:103], a[0:15]\n\tds_read_b64_tr_b16 v[76:77], %0 offset:28672\n\tds_write_b128 %1, v[196:199]" :  : "v"(p.q_rd[0][0]), "v"(p.te[3]) : "memory"); \
  asm volatile("s_waitcnt lgkmcnt(15)\n\tv_mfma_f32_32x32x16_bf16 a[16:31], v[112:115], v[104:107], a[16:31]\n\tds_read_b64_tr_b16 v[78:79], %0 offset:29696" :  : "v"(p.q_rd[0][0]) : "memory"); \
  unsigned so302; \
  asm volatile("s_waitcnt lgkmcnt(15)\n\tv_mfma_f32_32x32x16_bf16 a[32:47], v[116:119], v[100:103], a[32:47]\n\tds_read_b128 v[64:67], %1 offset:28672\n\tds_read_b128 v[68:71], %1 offset:30720\n\ts_add_u32 m0, %2, 135168\n\ts_add_u32 %0, %5, 1048576\n\tbuffer_load_dwordx4 %3, %4, %0 offen offset:0 lds\n\tds_write_b128 %6, v[200:203]" : "=&s"(so302) : "v"(p.x_rd[0][0]), "s"(p.m0q), "v"(p.vq), "s"(p.sq), "s"(p.soq), "v"(p.te[4]) : "memory", "m0", "scc"); \
  asm volatile("v_mfma_f32_32x32x16_bf16 a[48:63], v[116:119], v[104:107], a[48:63]\n\tds_read_b64_tr_b16 v[80:81], %0 offset:28672" :  : "v"(p.q_rd[0][1]) : "memory"); \
  asm volatile("s_waitcnt lgkmcnt(15)\n\tv_mfma_f32_32x32x16_bf16 a[64:79], v[120:123], v[100:103], a[64:79]\n\tds_read_b64_tr_b16 v[82:83], %0 offset:29696\n\tds_write_b128 %1, v[204:207]" :  : "v"(p.q_rd[0][1]), "v"(p.te[5]) : "memory", ACL64); \
  unsigned so303; \
  asm volatile("v_mfma_f32_32x32x16_bf16 a[80:95], v[120:123], v[104:107], a[80:95]\n\tds_read_b64_tr_b16 v[84:85], %1 offset:28672\n\tds_read_b64_tr_b16 v[86:87], %1 offset:29696\n\ts_add_u32 m0, %2, 136192\n\ts_add_u32 %0, %5, 1081344\n\tbuffer_load_dwordx4 %3, %4, %0 offen offset:0 lds" : "=&s"(so303) : "v"(p.q_rd[0][2]), "s"(p.m0q), "v"(p.vq), "s"(p.sq), "s"(p.soq) : "memory", "m0", "scc"); \
  asm volatile("s_waitcnt lgkmcnt(15)\n\tv_mfma_f32_32x32x16_bf16 a[96:111], v[124:127], v[100:103], a[96:111]\n\tds_read_b64_tr_b16 v[88:89], %0 offset:28672\n\tds_write_b128 %1, v[208:211]" :  : "v"(p.q_rd[0][3]), "v"(p.te[6]) : "memory"); \
  asm volatile("v_mfma_f32_32x32x16_bf16 a[112:127], v[124:127], v[104:107], a[112:127]\n\tds_read_b64_tr_b16 v[90:91], %0 offset:29696" :  : "v"(p.q_rd[0][3]) : "memory"); \
  unsigned so304; \
  asm volatile("v_mfma_f32_32x32x16_bf16 v[136:151], v[128:131], v[100:103], v[136:151]\n\tds_read_b64_tr_b16 v[92:93], %1 offset:28672\n\tds_read_b64_tr_b16 v[94:95], %1 offset:28928\n\ts_add_u32 m0, %2, 135168\n\ts_add_u32 %0, %5, 352256\n\tbuffer_load_dwordx4 %3, %4, %0 offen offset:0 lds\n\tds_write_b128 %6, v[212:215]" : "=&s"(so304) : "v"(p.f_rd[0]), "s"(p.m01), "v"(p.v1), "s"(p.s1), "s"(p.so1), "v"(p.te[7]) : "memory", "m0", "scc"); \
  asm volatile("v_mfma_f32_32x32x16_bf16 v[152:167], v[128:131], v[104:107], v[152:167]\n\tds_read_b128 v[72:75], %0 offset:28672" :  : "v"(p.xs_rd[0][0]) : "memory"); \
  asm volatile("s_waitcnt lgkmcnt(15)\n\tv_mfma_f32_32x32x16_bf16 v[168:183], v[132:135], v[108:111], v[168:183]\n\tds_read_b64_tr_b16 v[96:97], %0 offset:30720\n\tds_read_b64_tr_b16 v[98:99], %0 offset:30976" :  : "v"(p.f_rd[0]) : "memory"); \
  asm volatile("s_waitcnt vmcnt(7)\n\ts_barrier" ::: "memory"); \
  PJ_HOOK(2); \
  asm volatile("s_waitcnt lgkmcnt(15)\n\tv_mfma_f32_32x32x16_bf16 a[0:15], v[76:79], v[64:67], a[0:15]\n\tds_read_b64_tr_b16 v[112:113], %0 offset:32768\n\tds_read_b128 v[184:187], %1 offset:0" :  : "v"(p.q_rd[0][0]), "v"(p.tl) : "memory"); \
  unsigned so305; \
  asm volatile("v_mfma_f32_32x32x16_bf16 a[16:31], v[76:79], v[68:71], a[16:31]\n\tds_read_b64_tr_b16 v[114:115], %1 offset:33792\n\ts_add_u32 m0, %2, 49152\n\ts_add_u32 %0, %5, 320\n\tbuffer_load_dwordx4 %3, %4, %0 offen offset:0 sc1 lds\n\tds_read_b128 v[188:191], %6 offset:1024" : "=&s"(so305) : "v"(p.q_rd[0][0]), "s"(p.m0x), "v"(p.vx), "s"(p.sx), "s"(p.sox), "v"(p.tl) : "memory", "m0", "scc", ACL64); \
  asm volatile("s_waitcnt lgkmcnt(15)\n\tv_mfma_f32_32x32x16_bf16 a[32:47], v[80:83], v[64:67], a[32:47]\n\tds_read_b128 v[100:103], %0 offset:28672\n\tds_read_b128 v[104:107], %0 offset:30720\n\tds_read_b128 v[192:195], %1 offset:2048" :  : "v"(p.x_rd[0][1]), "v"(p.tl) : "memory"); \
  asm volatile("v_mfma_f32_32x32x16_bf16 a[48:63], v[80:83], v[68:71], a[48:63]\n\tds_read_b64_tr_b16 v[116:117], %0 offset:32768\n\tds_read_b128 v[196:199], %1 offset:3072" :  : "v"(p.q_rd[0][1]), "v"(p.tl) : "memory"); \
  unsigned so306; \
  asm volatile("s_waitcnt lgkmcnt(15)\n\tv_mfma_f32_32x32x16_bf16 a[64:79], v[84:87], v[64:67], a[64:79]\n\tds_read_b64_tr_b16 v[118:119], %1 offset:33792\n\ts_add_u32 m0, %2, 50176\n\ts_add_u32 %0, %5, 16704\n\tbuffer_load_dwordx4 %3, %4, %0 offen offset:0 sc1 lds" : "=&s"(so306) : "v"(p.q_rd[0][1]), "s"(p.m0x), "v"(p.vx), "s"(p.sx), "s"(p.sox) : "memory", "m0", "scc"); \
  asm volatile("v_mfma_f32_32x32x16_bf16 a[80:95], v[84:87], v[68:71], a[80:95]\n\tds_read_b64_tr_b16 v[120:121], %0 offset:32768\n\tds_read_b64_tr_b16 v[122:123], %0 offset:33792" :  : "v"(p.q_rd[0][2]) : "memory"); \
  unsigned so307; \
  asm volatile("v_mfma_f32_32x32x16_bf16 a[96:111], v[88:91], v[64:67], a[96:111]\n\tds_read_b64_tr_b16 v[124:125], %1 offset:32768\n\ts_waitcnt lgkmcnt(11)\n\ts_add_u32 %0, %2, 0\n\tbuffer_store_dwordx4 v[184:187], %3, %4, %0 offen offset:0 sc1" : "=&s"(so307) : "v"(p.q_rd[0][3]), "s"(p.tso), "v"(p.tvo), "s"(p.tsrd) : "memory", "scc"); \
  unsigned so308; \
  asm volatile("v_mfma_f32_32x32x16_bf16 a[112:127], v[88:91], v[68:71], a[112:127]\n\tds_read_b64_tr_b16 v[126:127], %1 offset:33792\n\ts_add_u32 m0, %2, 51200\n\ts_add_u32 %0, %5, 33088\n\tbuffer_load_dwordx4 %3, %4, %0 offen offset:0 sc1 lds" : "=&s"(so308) : "v"(p.q_rd[0][3]), "s"(p.m0x), "v"(p.vx), "s"(p.sx), "s"(p.sox) : "memory", "m0", "scc"); \
  asm volatile("v_mfma_f32_32x32x16_bf16 v[136:151], v[92:95], v[64:67], v[136:151]\n\tds_read_b64_tr_b16 v[128:129], %0 offset:29696\n\tds_read_b64_tr_b16 v[130:131], %0 offset:29952" :  : "v"(p.f_rd[0]) : "memory"); \
  asm volatile("v_mfma_f32_32x32x16_bf16 v[152:167], v[92:95], v[68:71], v[152:167]\n\tds_read_b128 v[108:111], %0 offset:28672" :  : "v"(p.xs_rd[0][1]) : "memory", ACL64); \
  unsigned so309; \
  unsigned so310; \
  asm volatile("v_mfma_f32_32x32x16_bf16 v[168:183], v[96:99], v[72:75], v[168:183]\n\tds_read_b64_tr_b16 v[132:133], %2 offset:31744\n\tds_read_b64_tr_b16 v[134:135], %2 offset:32000\n\ts_add_u32 m0, %3, 52224\n\ts_add_u32 %0, %6, 49472\n\tbuffer_load_dwordx4 %4, %5, %0 offen offset:0 sc1 lds\n\ts_waitcnt lgkmcnt(15)\n\ts_add_u32 %1, %7, 0\n\tbuffer_store_dwordx4 v[188:191], %8, %9, %1 offen offset:1024 sc1" : "=&s"(so309), "=&s"(so310) : "v"(p.f_rd[0]), "s"(p.m0x), "v"(p.vx), "s"(p.sx), "s"(p.sox), "s"(p.tso), "v"(p.tvo), "s"(p.tsrd) : "memory", "m0", "scc"); \
  asm volatile("s_waitcnt lgkmcnt(14)\n\tv_mfma_f32_32x32x16_bf16 a[0:15], v[112:115], v[100:103], a[0:15]\n\tds_read_b64_tr_b16 v[76:77], %0 offset:0" :  : "v"(p.q_rd[1][0]) : "memory"); \
  asm volatile("s_waitcnt lgkmcnt(14)\n\tv_mfma_f32_32x32x16_bf16 a[16:31], v[112:115], v[104:107], a[16:31]\n\tds_read_b64_tr_b16 v[78:79], %0 offset:1024" :  : "v"(p.q_rd[1][0]) : "memory"); \
  unsigned so311; \
  asm volatile("s_waitcnt lgkmcnt(11)\n\tv_mfma_f32_32x32x16_bf16 a[32:47], v[116:119], v[100:103], a[32:47]\n\tds_read_b128 v[64:67], %1 offset:0\n\tds_read_b128 v[68:71], %1 offset:2048\n\ts_add_u32 m0, %2, 49152\n\ts_add_u32 %0, %5, 1310720\n\tbuffer_load_dwordx4 %3, %4, %0 offen offset:0 lds" : "=&s"(so311) : "v"(p.x_rd[1][0]), "s"(p.m0q), "v"(p.vq), "s"(p.sq), "s"(p.soq) : "memory", "m0", "scc"); \
  unsigned so312; \
  asm volatile("v_mfma_f32_32x32x16_bf16 a[48:63], v[116:119], v[104:107], a[48:63]\n\tds_read_b64_tr_b16 v[80:81], %1 offset:0\n\ts_add_u32 %0, %2, 0\n\tbuffer_store_dwordx4 v[192:195], %3, %4, %0 offen offset:2048 sc1" : "=&s"(so312) : "v"(p.q_rd[1][1]), "s"(p.tso), "v"(p.tvo), "s"(p.tsrd) : "memory", "scc"); \
  asm volatile("s_waitcnt lgkmcnt(12)\n\tv_mfma_f32_32x32x16_bf16 a[64:79], v[120:123], v[100:103], a[64:79]\n\tds_read_b64_tr_b16 v[82:83], %0 offset:1024" :  : "v"(p.q_rd[1][1]) : "memory"); \
  unsigned so313; \
  asm volatile("v_mfma_f32_32x32x16_bf16 a[80:95], v[120:123], v[104:107], a[80:95]\n\tds_read_b64_tr_b16 v[84:85], %1 offset:0\n\tds_read_b64_tr_b16 v[86:87], %1 offset:1024\n\ts_add_u32 m0, %2, 50176\n\ts_add_u32 %0, %5, 1343488\n\tbuffer_load_dwordx4 %3, %4, %0 offen offset:0 lds" : "=&s"(so313) : "v"(p.q_rd[1][2]), "s"(p.m0q), "v"(p.vq), "s"(p.sq), "s"(p.soq) : "memory", "m0", "scc"); \
  asm volatile("s_waitcnt lgkmcnt(13)\n\tv_mfma_f32_32x32x16_bf16 a[96:111], v[124:127], v[100:103], a[96:111]\n\tds_read_b64_tr_b16 v[88:89], %0 offset:0" :  : "v"(p.q_rd[1][3]) : "memory", ACL64); \
  unsigned so314; \
  asm volatile("v_mfma_f32_32x32x16_bf16 a[112:127], v[124:127], v[104:107], a[112:127]\n\tds_read_b64_tr_b16 v[90:91], %1 offset:1024\n\ts_add_u32 %0, %2, 0\n\tbuffer_store_dwordx4 v[196:199], %3, %4, %0 offen offset:3072 sc1" : "=&s"(so314) : "v"(p.q_rd[1][3]), "s"(p.tso), "v"(p.tvo), "s"(p.tsrd) : "memory", "scc"); \
  unsigned so315; \
  asm volatile("s_waitcnt lgkmcnt(13)\n\tv_mfma_f32_32x32x16_bf16 v[136:151], v[128:131], v[100:103], v[136:151]\n\tds_read_b64_tr_b16 v[92:93], %1 offset:0\n\tds_read_b64_tr_b16 v[94:95], %1 offset:256\n\ts_add_u32 m0, %2, 49152\n\ts_add_u32 %0, %5, 440320\n\tbuffer_load_dwordx4 %3, %4, %0 offen offset:0 lds" : "=&s"(so315) : "v"(p.f_rd[1]), "s"(p.m01), "v"(p.v1), "s"(p.s1), "s"(p.so1) : "memory", "m0", "scc"); \
  asm volatile("v_mfma_f32_32x32x16_bf16 v[152:167], v[128:131], v[104:107], v[152:167]\n\tds_read_b128 v[72:75], %0 offset:0" :  : "v"(p.xs_rd[1][0]) : "memory"); \
  asm volatile("s_waitcnt lgkmcnt(13)\n\tv_mfma_f32_32x32x16_bf16 v[168:183], v[132:135], v[108:111], v[168:183]\n\tds_read_b64_tr_b16 v[96:97], %0 offset:2048\n\tds_read_b64_tr_b16 v[98:99], %0 offset:2304" :  : "v"(p.f_rd[1]) : "memory"); \
  asm volatile("s_waitcnt vmcnt(11)\n\ts_barrier" ::: "memory"); \
  PJ_HOOK(3); \
  asm volatile("s_waitcnt lgkmcnt(12)\n\tv_mfma_f32_32x32x16_bf16 a[0:15], v[76:79], v[64:67], a[0:15]\n\tds_read_b64_tr_b16 v[112:113], %0 offset:4096\n\tds_read_b128 v[200:203], %1 offset:4096" :  : "v"(p.q_rd[1][0]), "v"(p.tl) : "memory"); \
  unsigned so316; \
  asm volatile("s_waitcnt lgkmcnt(13)\n\tv_mfma_f32_32x32x16_bf16 a[16:31], v[76:79], v[68:71], a[16:31]\n\tds_read_b64_tr_b16 v[114:115], %1 offset:5120\n\ts_add_u32 m0, %2, 77824\n\ts_add_u32 %0, %5, 384\n\tbuffer_load_dwordx4 %3, %4, %0 offen offset:0 sc1 lds\n\tds_read_b128 v[204:207], %6 offset:5120" : "=&s"(so316) : "v"(p.q_rd[1][0]), "s"(p.m0x), "v"(p.vx), "s"(p.sx), "s"(p.sox), "v"(p.tl) : "memory", "m0", "scc"); \
  asm volatile("s_waitcnt lgkmcnt(13)\n\tv_mfma_f32_32x32x16_bf16 a[32:47], v[80:83], v[64:67], a[32:47]\n\tds_read_b128 v[100:103], %0 offset:0\n\tds_read_b128 v[104:107], %0 offset:2048\n\tds_read_b128 v[208:211], %1 offset:6144" :  : "v"(p.x_rd[1][1]), "v"(p.tl) : "memory"); \
  asm volatile("v_mfma_f32_32x32x16_bf16 a[48:63], v[80:83], v[68:71], a[48:63]\n\tds_read_b64_tr_b16 v[116:117], %0 offset:4096\n\tds_read_b128 v[212:215], %1 offset:7168" :  : "v"(p.q_rd[1][1]), "v"(p.tl) : "memory", ACL64); \
  unsigned so317; \
  asm volatile("s_waitcnt lgkmcnt(15)\n\tv_mfma_f32_32x32x16_bf16 a[64:79], v[84:87], v[64:67], a[64:79]\n\tds_read_b64_tr_b16 v[118:119], %1 offset:5120\n\ts_add_u32 m0, %2, 78848\n\ts_add_u32 %0, %5, 16768\n\tbuffer_load_dwordx4 %3, %4, %0 offen offset:0 sc1 lds" : "=&s"(so317) : "v"(p.q_rd[1][1]), "s"(p.m0x), "v"(p.vx), "s"(p.sx), "s"(p.sox) : "memory", "m0", "scc"); \
  asm volatile("v_mfma_f32_32x32x16_bf16 a[80:95], v[84:87], v[68:71], a[80:95]\n\tds_read_b64_tr_b16 v[120:121], %0 offset:4096\n\tds_read_b64_tr_b16 v[122:123], %0 offset:5120\n\tds_write_b128 %1, v[216:219]" :  : "v"(p.q_rd[1][2]), "v"(p.te[0]) : "memory"); \
  unsigned so318; \
  asm volatile("s_waitcnt lgkmcnt(15)\n\tv_mfma_f32_32x32x16_bf16 a[96:111], v[88:91], v[64:67], a[96:111]\n\tds_read_b64_tr_b16 v[124:125], %1 offset:4096\n\ts_waitcnt lgkmcnt(12)\n\ts_add_u32 %0, %2, 4096\n\tbuffer_store_dwordx4 v[200:203], %3, %4, %0 offen offset:0 sc1" : "=&s"(so318) : "v"(p.q_rd[1][3]), "s"(p.tso), "v"(p.tvo), "s"(p.tsrd) : "memory", "scc"); \
  unsigned so319; \
  asm volatile("v_mfma_f32_32x32x16_bf16 a[112:127], v[88:91], v[68:71], a[112:127]\n\tds_read_b64_tr_b16 v[126:127], %1 offset:5120\n\ts_add_u32 m0, %2, 79872\n\ts_add_u32 %0, %5, 33152\n\tbuffer_load_dwordx4 %3, %4, %0 offen offset:0 sc1 lds\n\tds_write_b128 %6, v[220:223]" : "=&s"(so319) : "v"(p.q_rd[1][3]), "s"(p.m0x), "v"(p.vx), "s"(p.sx), "s"(p.sox), "v"(p.te[1]) : "memory", "m0", "scc"); \
  asm volatile("v_mfma_f32_32x32x16_bf16 v[136:151], v[92:95], v[64:67], v[136:151]\n\tds_read_b64_tr_b16 v[128:129], %0 offset:1024\n\tds_read_b64_tr_b16 v[130:131], %0 offset:1280" :  : "v"(p.f_rd[1]) : "memory"); \
  asm volatile("v_mfma_f32_32x32x16_bf16 v[152:167], v[92:95], v[68:71], v[152:167]\n\tds_read_b128 v[108:111], %0 offset:0\n\tds_write_b128 %1, v[224:227]" :  : "v"(p.xs_rd[1][1]), "v"(p.te[2]) : "memory"); \
  unsigned so320; \
  unsigned so321; \
  asm volatile("v_mfma_f32_32x32x16_bf16 v[168:183], v[96:99], v[72:75], v[168:183]\n\tds_read_b64_tr_b16 v[132:133], %2 offset:3072\n\tds_read_b64_tr_b16 v[134:135], %2 offset:3328\n\ts_add_u32 m0, %3, 80896\n\ts_add_u32 %0, %6, 49536\n\tbuffer_load_dwordx4 %4, %5, %0 offen offset:0 sc1 lds\n\ts_waitcnt lgkmcnt(15)\n\ts_add_u32 %1, %7, 4096\n\tbuffer_store_dwordx4 v[204:207], %8, %9, %1 offen offset:1024 sc1" : "=&s"(so320), "=&s"(so321) : "v"(p.f_rd[1]), "s"(p.m0x), "v"(p.vx), "s"(p.sx), "s"(p.sox), "s"(p.tso), "v"(p.tvo), "s"(p.tsrd) : "memory", "m0", "scc"); \
  asm volatile("v_mfma_f32_32x32x16_bf16 a[0:15], v[112:115], v[100:103], a[0:15]\n\tds_read_b64_tr_b16 v[76:77], %0 offset:28672\n\tds_write_b128 %1, v[228:231]" :  : "v"(p.q_rd[1][0]), "v"(p.te[3]) : "memory", ACL64); \
  asm volatile("v_mfma_f32_32x32x16_bf16 a[16:31], v[112:115], v[104:107], a[16:31]\n\tds_read_b64_tr_b16 v[78:79], %0 offset:29696" :  : "v"(p.q_rd[1][0]) : "memory"); \
  unsigned so322; \
  asm volatile("s_waitcnt lgkmcnt(15)\n\tv_mfma_f32_32x32x16_bf16 a[32:47], v[116:119], v[100:103], a[32:47]\n\tds_read_b128 v[64:67], %1 offset:28672\n\tds_read_b128 v[68:71], %1 offset:30720\n\ts_add_u32 m0, %2, 77824\n\ts_add_u32 %0, %5, 1572864\n\tbuffer_load_dwordx4 %3, %4, %0 offen offset:0 lds\n\tds_write_b128 %6, v[232:235]" : "=&s"(so322) : "v"(p.x_rd[1][0]), "s"(p.m0q), "v"(p.vq), "s"(p.sq), "s"(p.soq), "v"(p.te[4]) : "memory", "m0", "scc"); \
  unsigned so323; \
  asm volatile("v_mfma_f32_32x32x16_bf16 a[48:63], v[116:119], v[104:107], a[48:63]\n\tds_read_b64_tr_b16 v[80:81], %1 offset:28672\n\ts_add_u32 %0, %2, 4096\n\tbuffer_store_dwordx4 v[208:211], %3, %4, %0 offen offset:2048 sc1" : "=&s"(so323) : "v"(p.q_rd[1][1]), "s"(p.tso), "v"(p.tvo), "s"(p.tsrd) : "memory", "scc"); \
  asm volatile("s_waitcnt lgkmcnt(15)\n\tv_mfma_f32_32x32x16_bf16 a[64:79], v[120:123], v[100:103], a[64:79]\n\tds_read_b64_tr_b16 v[82:83], %0 offset:29696\n\tds_write_b128 %1, v[236:239]" :  : "v"(p.q_rd[1][1]), "v"(p.te[5]) : "memory"); \
  unsigned so324; \
  asm volatile("v_mfma_f32_32x32x16_bf16 a[80:95], v[120:123], v[104:107], a[80:95]\n\tds_read_b64_tr_b16 v[84:85], %1 offset:28672\n\tds_read_b64_tr_b16 v[86:87], %1 offset:29696\n\ts_add_u32 m0, %2, 78848\n\ts_add_u32 %0, %5, 1605632\n\tbuffer_load_dwordx4 %3, %4, %0 offen offset:0 lds" : "=&s"(so324) : "v"(p.q_rd[1][2]), "s"(p.m0q), "v"(p.vq), "s"(p.sq), "s"(p.soq) : "memory", "m0", "scc"); \
  asm volatile("s_waitcnt lgkmcnt(15)\n\tv_mfma_f32_32x32x16_bf16 a[96:111], v[124:127], v[100:103], a[96:111]\n\tds_read_b64_tr_b16 v[88:89], %0 offset:28672\n\tds_write_b128 %1, v[240:243]" :  : "v"(p.q_rd[1][3]), "v"(p.te[6]) : "memory"); \
  unsigned so325; \
  asm volatile("v_mfma_f32_32x32x16_bf16 a[112:127], v[124:127], v[104:107], a[112:127]\n\tds_read_b64_tr_b16 v[90:91], %1 offset:29696\n\ts_add_u32 %0, %2, 4096\n\tbuffer_store_dwordx4 v[212:215], %3, %4, %0 offen offset:3072 sc1" : "=&s"(so325) : "v"(p.q_rd[1][3]), "s"(p.tso), "v"(p.tvo), "s"(p.tsrd) : "memory", "scc"); \
  unsigned so326; \
  asm volatile("v_mfma_f32_32x32x16_bf16 v[136:151], v[128:131], v[100:103], v[136:151]\n\tds_read_b64_tr_b16 v[92:93], %1 offset:28672\n\tds_read_b64_tr_b16 v[94:95], %1 offset:28928\n\ts_add_u32 m0, %2, 77824\n\ts_add_u32 %0, %5, 528384\n\tbuffer_load_dwordx4 %3, %4, %0 offen offset:0 lds\n\tds_write_b128 %6, v[244:247]" : "=&s"(so326) : "v"(p.f_rd[1]), "s"(p.m01), "v"(p.v1), "s"(p.s1), "s"(p.so1), "v"(p.te[7]) : "memory", "m0", "scc", ACL64); \
  asm volatile("v_mfma_f32_32x32x16_bf16 v[152:167], v[128:131], v[104:107], v[152:167]\n\tds_read_b128 v[72:75], %0 offset:28672" :  : "v"(p.xs_rd[1][0]) : "memory"); \
  asm volatile("s_waitcnt lgkmcnt(15)\n\tv_mfma_f32_32x32x16_bf16 v[168:183], v[132:135], v[108:111], v[168:183]\n\tds_read_b64_tr_b16 v[96:97], %0 offset:30720\n\tds_read_b64_tr_b16 v[98:99], %0 offset:30976" :  : "v"(p.f_rd[1]) : "memory"); \
  asm volatile("s_waitcnt vmcnt(11)\n\ts_barrier" ::: "memory"); \
  PJ_HOOK(4); \
  asm volatile("s_waitcnt lgkmcnt(15)\n\tv_mfma_f32_32x32x16_bf16 a[0:15], v[76:79], v[64:67], a[0:15]\n\tds_read_b64_tr_b16 v[112:113], %0 offset:32768\n\tds_read_b128 v[184:187], %1 offset:0" :  : "v"(p.q_rd[1][0]), "v"(p.tl) : "memory"); \
  unsigned so327; \
  asm volatile("v_mfma_f32_32x32x16_bf16 a[16:31], v[76:79], v[68:71], a[16:31]\n\tds_read_b64_tr_b16 v[114:115], %1 offset:33792\n\ts_add_u32 m0, %2, 106496\n\ts_add_u32 %0, %5, 448\n\tbuffer_load_dwordx4 %3, %4, %0 offen offset:0 sc1 lds\n\tds_read_b128 v[188:191], %6 offset:1024" : "=&s"(so327) : "v"(p.q_rd[1][0]), "s"(p.m0x), "v"(p.vx), "s"(p.sx), "s"(p.sox), "v"(p.tl) : "memory", "m0", "scc"); \
  asm volatile("s_waitcnt lgkmcnt(15)\n\tv_mfma_f32_32x32x16_bf16 a[32:47], v[80:83], v[64:67], a[32:47]\n\tds_read_b128 v[100:103], %0 offset:28672\n\tds_read_b128 v[104:107], %0 offset:30720\n\tds_read_b128 v[192:195], %1 offset:2048" :  : "v"(p.x_rd[1][1]), "v"(p.tl) : "memory"); \
  asm volatile("v_mfma_f32_32x32x16_bf16 a[48:63], v[80:83], v[68:71], a[48:63]\n\tds_read_b64_tr_b16 v[116:117], %0 offset:32768\n\tds_read_b128 v[196:199], %1 offset:3072" :  : "v"(p.q_rd[1][1]), "v"(p.tl) : "memory"); \
  unsigned so328; \
  asm volatile("s_waitcnt lgkmcnt(15)\n\tv_mfma_f32_32x32x16_bf16 a[64:79], v[84:87], v[64:67], a[64:79]\n\tds_read_b64_tr_b16 v[118:119], %1 offset:33792\n\ts_add_u32 m0, %2, 107520\n\ts_add_u32 %0, %5, 16832\n\tbuffer_load_dwordx4 %3, %4, %0 offen offset:0 sc1 lds" : "=&s"(so328) : "v"(p.q_rd[1][1]), "s"(p.m0x), "v"(p.vx), "s"(p.sx), "s"(p.sox) : "memory", "m0", "scc"); \
  asm volatile("v_mfma_f32_32x32x16_bf16 a[80:95], v[84:87], v[68:71], a[80:95]\n\tds_read_b64_tr_b16 v[120:121], %0 offset:32768\n\tds_read_b64_tr_b16 v[122:123], %0 offset:33792" :  : "v"(p.q_rd[1][2]) : "memory", ACL64); \
  unsigned so329; \
  asm volatile("v_mfma_f32_32x32x16_bf16 a[96:111], v[88:91], v[64:67], a[96:111]\n\tds_read_b64_tr_b16 v[124:125], %1 offset:32768\n\ts_waitcnt lgkmcnt(11)\n\ts_add_u32 %0, %2, 8192\n\tbuffer_store_dwordx4 v[184:187], %3, %4, %0 offen offset:0 sc1" : "=&s"(so329) : "v"(p.q_rd[1][3]), "s"(p.tso), "v"(p.tvo), "s"(p.tsrd) : "memory", "scc"); \
  unsigned so330; \
  asm volatile("v_mfma_f32_32x32x16_bf16 a[112:127], v[88:91], v[68:71], a[112:127]\n\tds_read_b64_tr_b16 v[126:127], %1 offset:33792\n\ts_add_u32 m0, %2, 108544\n\ts_add_u32 %0, %5, 33216\n\tbuffer_load_dwordx4 %3, %4, %0 offen offset:0 sc1 lds" : "=&s"(so330) : "v"(p.q_rd[1][3]), "s"(p.m0x), "v"(p.vx), "s"(p.sx), "s"(p.sox) : "memory", "m0", "scc"); \
  asm volatile("v_mfma_f32_32x32x16_bf16 v[136:151], v[92:95], v[64:67], v[136:151]\n\tds_read_b64_tr_b16 v[128:129], %0 offset:29696\n\tds_read_b64_tr_b16 v[130:131], %0 offset:29952" :  : "v"(p.f_rd[1]) : "memory"); \
  asm volatile("v_mfma_f32_32x32x16_bf16 v[152:167], v[92:95], v[68:71], v[152:167]\n\tds_read_b128 v[108:111], %0 offset:28672" :  : "v"(p.xs_rd[1][1]) : "memory"); \
  unsigned so331; \
  unsigned so332; \
  asm volatile("v_mfma_f32_32x32x16_bf16 v[168:183], v[96:99], v[72:75], v[168:183]\n\tds_read_b64_tr_b16 v[132:133], %2 offset:31744\n\tds_read_b64_tr_b16 v[134:135], %2 offset:32000\n\ts_add_u32 m0, %3, 109568\n\ts_add_u32 %0, %6, 49600\n\tbuffer_load_dwordx4 %4, %5, %0 offen offset:0 sc1 lds\n\ts_waitcnt lgkmcnt(15)\n\ts_add_u32 %1, %7, 8192\n\tbuffer_store_dwordx4 v[188:191], %8, %9, %1 offen offset:1024 sc1" : "=&s"(so331), "=&s"(so332) : "v"(p.f_rd[1]), "s"(p.m0x), "v"(p.vx), "s"(p.sx), "s"(p.sox), "s"(p.tso), "v"(p.tvo), "s"(p.tsrd) : "memory", "m0", "scc"); \
  asm volatile("s_waitcnt lgkmcnt(14)\n\tv_mfma_f32_32x32x16_bf16 a[0:15], v[112:115], v[100:103], a[0:15]\n\tds_read_b64_tr_b16 v[76:77], %0 offset:0" :  : "v"(p.q_rd[0][0]) : "memory"); \
  asm volatile("s_waitcnt lgkmcnt(14)\n\tv_mfma_f32_32x32x16_bf16 a[16:31], v[112:115], v[104:107], a[16:31]\n\tds_read_b64_tr_b16 v[78:79], %0 offset:1024" :  : "v"(p.q_rd[0][0]) : "memory"); \
  unsigned so333; \
  asm volatile("s_waitcnt lgkmcnt(11)\n\tv_mfma_f32_32x32x16_bf16 a[32:47], v[116:119], v[100:103], a[32:47]\n\tds_read_b128 v[64:67], %1 offset:0\n\tds_read_b128 v[68:71], %1 offset:2048\n\ts_add_u32 m0, %2, 106496\n\ts_add_u32 %0, %5, 1835008\n\tbuffer_load_dwordx4 %3, %4, %0 offen offset:0 lds" : "=&s"(so333) : "v"(p.x_rd[0][0]), "s"(p.m0q), "v"(p.vq), "s"(p.sq), "s"(p.soq) : "memory", "m0", "scc", ACL64); \
  unsigned so334; \
  asm volatile("v_mfma_f32_32x32x16_bf16 a[48:63], v[116:119], v[104:107], a[48:63]\n\tds_read_b64_tr_b16 v[80:81], %1 offset:0\n\ts_add_u32 %0, %2, 8192\n\tbuffer_store_dwordx4 v[192:195], %3, %4, %0 offen offset:2048 sc1" : "=&s"(so334) : "v"(p.q_rd[0][1]), "s"(p.tso), "v"(p.tvo), "s"(p.tsrd) : "memory", "scc"); \
  asm volatile("s_waitcnt lgkmcnt(12)\n\tv_mfma_f32_32x32x16_bf16 a[64:79], v[120:123], v[100:103], a[64:79]\n\tds_read_b64_tr_b16 v[82:83], %0 offset:1024" :  : "v"(p.q_rd[0][1]) : "memory"); \
  unsigned so335; \
  asm volatile("v_mfma_f32_32x32x16_bf16 a[80:95], v[120:123], v[104:107], a[80:95]\n\tds_read_b64_tr_b16 v[84:85], %1 offset:0\n\tds_read_b64_tr_b16 v[86:87], %1 offset:1024\n\ts_add_u32 m0, %2, 107520\n\ts_add_u32 %0, %5, 1867776\n\tbuffer_load_dwordx4 %3, %4, %0 offen offset:0 lds" : "=&s"(so335) : "v"(p.q_rd[0][2]), "s"(p.m0q), "v"(p.vq), "s"(p.sq), "s"(p.soq) : "memory", "m0", "scc"); \
  asm volatile("s_waitcnt lgkmcnt(13)\n\tv_mfma_f32_32x32x16_bf16 a[96:111], v[124:127], v[100:103], a[96:111]\n\tds_read_b64_tr_b16 v[88:89], %0 offset:0" :  : "v"(p.q_rd[0][3]) : "memory"); \
  unsigned so336; \
  asm volatile("v_mfma_f32_32x32x16_bf16 a[112:127], v[124:127], v[104:107], a[112:127]\n\tds_read_b64_tr_b16 v[90:91], %1 offset:1024\n\ts_add_u32 %0, %2, 8192\n\tbuffer_store_dwordx4 v[196:199], %3, %4, %0 offen offset:3072 sc1" : "=&s"(so336) : "v"(p.q_rd[0][3]), "s"(p.tso), "v"(p.tvo), "s"(p.tsrd) : "memory", "scc"); \
  unsigned so337; \
  asm volatile("s_waitcnt lgkmcnt(13)\n\tv_mfma_f32_32x32x16_bf16 v[136:151], v[128:131], v[100:103], v[136:151]\n\tds_read_b64_tr_b16 v[92:93], %1 offset:0\n\tds_read_b64_tr_b16 v[94:95], %1 offset:256\n\ts_add_u32 m0, %2, 106496\n\ts_add_u32 %0, %5, 616448\n\tbuffer_load_dwordx4 %3, %4, %0 offen offset:0 lds" : "=&s"(so337) : "v"(p.f_rd[0]), "s"(p.m01), "v"(p.v1), "s"(p.s1), "s"(p.so1) : "memory", "m0", "scc"); \
  asm volatile("v_mfma_f32_32x32x16_bf16 v[152:167], v[128:131], v[104:107], v[152:167]\n\tds_read_b128 v[72:75], %0 offset:0" :  : "v"(p.xs_rd[0][0]) : "memory"); \
  asm volatile("s_waitcnt lgkmcnt(13)\n\tv_mfma_f32_32x32x16_bf16 v[168:183], v[132:135], v[108:111], v[168:183]\n\tds_read_b64_tr_b16 v[96:97], %0 offset:2048\n\tds_read_b64_tr_b16 v[98:99], %0 offset:2304" :  : "v"(p.f_rd[0]) : "memory", ACL64); \
  asm volatile("s_waitcnt vmcnt(11)\n\ts_barrier" ::: "memory"); \
  PJ_HOOK(5); \
  asm volatile("s_waitcnt lgkmcnt(12)\n\tv_mfma_f32_32x32x16_bf16 a[0:15], v[76:79], v[64:67], a[0:15]\n\tds_read_b64_tr_b16 v[112:113], %0 offset:4096\n\tds_read_b128 v[200:203], %1 offset:4096" :  : "v"(p.q_rd[0][0]), "v"(p.tl) : "memory"); \
  unsigned so338; \
  asm volatile("s_waitcnt lgkmcnt(13)\n\tv_mfma_f32_32x32x16_bf16 a[16:31], v[76:79], v[68:71], a[16:31]\n\tds_read_b64_tr_b16 v[114:115], %1 offset:5120\n\ts_add_u32 m0, %2, 135168\n\ts_add_u32 %0, %5, 512\n\tbuffer_load_dwordx4 %3, %4, %0 offen offset:0 sc1 lds\n\tds_read_b128 v[204:207], %6 offset:5120" : "=&s"(so338) : "v"(p.q_rd[0][0]), "s"(p.m0x), "v"(p.vx), "s"(p.sx), "s"(p.sox), "v"(p.tl) : "memory", "m0", "scc"); \
  asm volatile("s_waitcnt lgkmcnt(13)\n\tv_mfma_f32_32x32x16_bf16 a[32:47], v[80:83], v[64:67], a[32:47]\n\tds_read_b128 v[100:103], %0 offset:0\n\tds_read_b128 v[104:107], %0 offset:2048\n\tds_read_b128 v[208:211], %1 offset:6144" :  : "v"(p.x_rd[0][1]), "v"(p.tl) : "memory"); \
  asm volatile("v_mfma_f32_32x32x16_bf16 a[48:63], v[80:83], v[68:71], a[48:63]\n\tds_read_b64_tr_b16 v[116:117], %0 offset:4096\n\tds_read_b128 v[212:215], %1 offset:7168" :  : "v"(p.q_rd[0][1]), "v"(p.tl) : "memory"); \
  unsigned so339; \
  asm volatile("s_waitcnt lgkmcnt(15)\n\tv_mfma_f32_32x32x16_bf16 a[64:79], v[84:87], v[64:67], a[64:79]\n\tds_read_b64_tr_b16 v[118:119], %1 offset:5120\n\ts_add_u32 m0, %2, 136192\n\ts_add_u32 %0, %5, 16896\n\tbuffer_load_dwordx4 %3, %4, %0 offen offset:0 sc1 lds" : "=&s"(so339) : "v"(p.q_rd[0][1]), "s"(p.m0x), "v"(p.vx), "s"(p.sx), "s"(p.sox) : "memory", "m0", "scc"); \
  asm volatile("v_mfma_f32_32x32x16_bf16 a[80:95], v[84:87], v[68:71], a[80:95]\n\tds_read_b64_tr_b16 v[120:121], %0 offset:4096\n\tds_read_b64_tr_b16 v[122:123], %0 offset:5120\n\tv_accvgpr_read_b32 v216, a128\n\tv_accvgpr_read_b32 v217, a129\n\tv_accvgpr_read_b32 v218, a130\n\tv_accvgpr_read_b32 v219, a131\n\tv_accvgpr_read_b32 v220, a132\n\tv_accvgpr_read_b32 v221, a133\n\tv_accvgpr_read_b32 v222, a134\n\tv_accvgpr_read_b32 v223, a135\n\tv_cvt_pk_bf16_f32 v216, v216, v217\n\tv_cvt_pk_bf16_f32 v217, v218, v219\n\tv_cvt_pk_bf16_f32 v218, v220, v221\n\tv_cvt_pk_bf16_f32 v219, v222, v223\n\tds_write_b128 %1, v[216:219]" :  : "v"(p.q_rd[0][2]), "v"(p.te[0]) : "memory"); \
  unsigned so340; \
  asm volatile("s_waitcnt lgkmcnt(15)\n\tv_mfma_f32_32x32x16_bf16 a[96:111], v[88:91], v[64:67], a[96:111]\n\tds_read_b64_tr_b16 v[124:125], %1 offset:4096\n\ts_waitcnt lgkmcnt(12)\n\ts_add_u32 %0, %2, 12288\n\tbuffer_store_dwordx4 v[200:203], %3, %4, %0 offen offset:0 sc1" : "=&s"(so340) : "v"(p.q_rd[0][3]), "s"(p.tso), "v"(p.tvo), "s"(p.tsrd) : "memory", "scc"); \
  unsigned so341; \
  asm volatile("v_mfma_f32_32x32x16_bf16 a[112:127], v[88:91], v[68:71], a[112:127]\n\tds_read_b64_tr_b16 v[126:127], %1 offset:5120\n\ts_add_u32 m0, %2, 137216\n\ts_add_u32 %0, %5, 33280\n\tbuffer_load_dwordx4 %3, %4, %0 offen offset:0 sc1 lds\n\tv_accvgpr_read_b32 v224, a136\n\tv_accvgpr_read_b32 v225, a137\n\tv_accvgpr_read_b32 v226, a138\n\tv_accvgpr_read_b32 v227, a139\n\tv_accvgpr_read_b32 v228, a140\n\tv_accvgpr_read_b32 v229, a141\n\tv_accvgpr_read_b32 v230, a142\n\tv_accvgpr_read_b32 v231, a143\n\tv_cvt_pk_bf16_f32 v224, v224, v225\n\tv_cvt_pk_bf16_f32 v225, v226, v227\n\tv_cvt_pk_bf16_f32 v226, v228, v229\n\tv_cvt_pk_bf16_f32 v227, v230, v231\n\tds_write_b128 %6, v[224:227]" : "=&s"(so341) : "v"(p.q_rd[0][3]), "s"(p.m0x), "v"(p.vx), "s"(p.sx), "s"(p.sox), "v"(p.te[1]) : "memory", "m0", "scc", ACL64); \
  asm volatile("v_mfma_f32_32x32x16_bf16 v[136:151], v[92:95], v[64:67], v[136:151]\n\tds_read_b64_tr_b16 v[128:129], %0 offset:1024\n\tds_read_b64_tr_b16 v[130:131], %0 offset:1280" :  : "v"(p.f_rd[0]) : "memory"); \
  asm volatile("v_mfma_f32_32x32x16_bf16 v[152:167], v[92:95], v[68:71], v[152:167]\n\tds_read_b128 v[108:111], %0 offset:0\n\tv_accvgpr_read_b32 v232, a144\n\tv_accvgpr_read_b32 v233, a145\n\tv_accvgpr_read_b32 v234, a146\n\tv_accvgpr_read_b32 v235, a147\n\tv_accvgpr_read_b32 v236, a148\n\tv_accvgpr_read_b32 v237, a149\n\tv_accvgpr_read_b32 v238, a150\n\tv_accvgpr_read_b32 v239, a151\n\tv_cvt_pk_bf16_f32 v232, v232, v233\n\tv_cvt_pk_bf16_f32 v233, v234, v235\n\tv_cvt_pk_bf16_f32 v234, v236, v237\n\tv_cvt_pk_bf16_f32 v235, v238, v239\n\tds_write_b128 %1, v[232:235]" :  : "v"(p.xs_rd[0][1]), "v"(p.te[2]) : "memory"); \
  unsigned so342; \
  unsigned so343; \
  asm volatile("v_mfma_f32_32x32x16_bf16 v[168:183], v[96:99], v[72:75], v[168:183]\n\tds_read_b64_tr_b16 v[132:133], %2 offset:3072\n\tds_read_b64_tr_b16 v[134:135], %2 offset:3328\n\ts_add_u32 m0, %3, 138240\n\ts_add_u32 %0, %6, 49664\n\tbuffer_load_dwordx4 %4, %5, %0 offen offset:0 sc1 lds\n\ts_waitcnt lgkmcnt(15)\n\ts_add_u32 %1, %7, 12288\n\tbuffer_store_dwordx4 v[204:207], %8, %9, %1 offen offset:1024 sc1" : "=&s"(so342), "=&s"(so343) : "v"(p.f_rd[0]), "s"(p.m0x), "v"(p.vx), "s"(p.sx), "s"(p.sox), "s"(p.tso), "v"(p.tvo), "s"(p.tsrd) : "memory", "m0", "scc"); \
  asm volatile("v_mfma_f32_32x32x16_bf16 a[0:15], v[112:115], v[100:103], a[0:15]\n\tds_read_b64_tr_b16 v[76:77], %0 offset:28672\n\tv_accvgpr_read_b32 v240, a152\n\tv_accvgpr_read_b32 v241, a153\n\tv_accvgpr_read_b32 v242, a154\n\tv_accvgpr_read_b32 v243, a155\n\tv_accvgpr_read_b32 v244, a156\n\tv_accvgpr_read_b32 v245, a157\n\tv_accvgpr_read_b32 v246, a158\n\tv_accvgpr_read_b32 v247, a159\n\tv_cvt_pk_bf16_f32 v240, v240, v241\n\tv_cvt_pk_bf16_f32 v241, v242, v243\n\tv_cvt_pk_bf16_f32 v242, v244, v245\n\tv_cvt_pk_bf16_f32 v243, v246, v247\n\tds_write_b128 %1, v[240:243]" :  : "v"(p.q_rd[0][0]), "v"(p.te[3]) : "memory"); \
  asm volatile("v_mfma_f32_32x32x16_bf16 a[16:31], v[112:115], v[104:107], a[16:31]\n\tds_read_b64_tr_b16 v[78:79], %0 offset:29696" :  : "v"(p.q_rd[0][0]) : "memory"); \
  unsigned so344; \
  asm volatile("s_waitcnt lgkmcnt(15)\n\tv_mfma_f32_32x32x16_bf16 a[32:47], v[116:119], v[100:103], a[32:47]\n\tds_read_b128 v[64:67], %1 offset:28672\n\tds_read_b128 v[68:71], %1 offset:30720\n\ts_add_u32 m0, %2, 135168\n\ts_add_u32 %0, %5, 2097152\n\tbuffer_load_dwordx4 %3, %4, %0 offen offset:0 lds\n\tv_accvgpr_read_b32 v216, a160\n\tv_accvgpr_read_b32 v217, a161\n\tv_accvgpr_read_b32 v218, a162\n\tv_accvgpr_read_b32 v219, a163\n\tv_accvgpr_read_b32 v220, a164\n\tv_accvgpr_read_b32 v221, a165\n\tv_accvgpr_read_b32 v222, a166\n\tv_accvgpr_read_b32 v223, a167\n\tv_cvt_pk_bf16_f32 v216, v216, v217\n\tv_cvt_pk_bf16_f32 v217, v218, v219\n\tv_cvt_pk_bf16_f32 v218, v220, v221\n\tv_cvt_pk_bf16_f32 v219, v222, v223\n\tds_write_b128 %6, v[216:219]" : "=&s"(so344) : "v"(p.x_rd[0][0]), "s"(p.m0q), "v"(p.vq), "s"(p.sq), "s"(p.soq), "v"(p.te[4]) : "memory", "m0", "scc"); \
  unsigned so345; \
  asm volatile("v_mfma_f32_32x32x16_bf16 a[48:63], v[116:119], v[104:107], a[48:63]\n\tds_read_b64_tr_b16 v[80:81], %1 offset:28672\n\ts_add_u32 %0, %2, 12288\n\tbuffer_store_dwordx4 v[208:211], %3, %4, %0 offen offset:2048 sc1" : "=&s"(so345) : "v"(p.q_rd[0][1]), "s"(p.tso), "v"(p.tvo), "s"(p.tsrd) : "memory", "scc"); \
  asm volatile("s_waitcnt lgkmcnt(15)\n\tv_mfma_f32_32x32x16_bf16 a[64:79], v[120:123], v[100:103], a[64:79]\n\tds_read_b64_tr_b16 v[82:83], %0 offset:29696\n\tv_accvgpr_read_b32 v224, a168\n\tv_accvgpr_read_b32 v225, a169\n\tv_accvgpr_read_b32 v226, a170\n\tv_accvgpr_read_b32 v227, a171\n\tv_accvgpr_read_b32 v228, a172\n\tv_accvgpr_read_b32 v229, a173\n\tv_accvgpr_read_b32 v230, a174\n\tv_accvgpr_read_b32 v231, a175\n\tv_cvt_pk_bf16_f32 v224, v224, v225\n\tv_cvt_pk_bf16_f32 v225, v226, v227\n\tv_cvt_pk_bf16_f32 v226, v228, v229\n\tv_cvt_pk_bf16_f32 v227, v230, v231\n\tds_write_b128 %1, v[224:227]" :  : "v"(p.q_rd[0][1]), "v"(p.te[5]) : "memory", ACL64); \
  unsigned so346; \
  asm volatile("v_mfma_f32_32x32x16_bf16 a[80:95], v[120:123], v[104:107], a[80:95]\n\tds_read_b64_tr_b16 v[84:85], %1 offset:28672\n\tds_read_b64_tr_b16 v[86:87], %1 offset:29696\n\ts_add_u32 m0, %2, 136192\n\ts_add_u32 %0, %5, 2129920\n\tbuffer_load_dwordx4 %3, %4, %0 offen offset:0 lds" : "=&s"(so346) : "v"(p.q_rd[0][2]), "s"(p.m0q), "v"(p.vq), "s"(p.sq), "s"(p.soq) : "memory", "m0", "scc"); \
  asm volatile("s_waitcnt lgkmcnt(15)\n\tv_mfma_f32_32x32x16_bf16 a[96:111], v[124:127], v[100:103], a[96:111]\n\tds_read_b64_tr_b16 v[88:89], %0 offset:28672\n\tv_accvgpr_read_b32 v232, a176\n\tv_accvgpr_read_b32 v233, a177\n\tv_accvgpr_read_b32 v234, a178\n\tv_accvgpr_read_b32 v235, a179\n\tv_accvgpr_read_b32 v236, a180\n\tv_accvgpr_read_b32 v237, a181\n\tv_accvgpr_read_b32 v238, a182\n\tv_accvgpr_read_b32 v239, a183\n\tv_cvt_pk_bf16_f32 v232, v232, v233\n\tv_cvt_pk_bf16_f32 v233, v234, v235\n\tv_cvt_pk_bf16_f32 v234, v236, v237\n\tv_cvt_pk_bf16_f32 v235, v238, v239\n\tds_write_b128 %1, v[232:235]" :  : "v"(p.q_rd[0][3]), "v"(p.te[6]) : "memory"); \
  unsigned so347; \
  asm volatile("v_mfma_f32_32x32x16_bf16 a[112:127], v[124:127], v[104:107], a[112:127]\n\tds_read_b64_tr_b16 v[90:91], %1 offset:29696\n\ts_add_u32 %0, %2, 12288\n\tbuffer_store_dwordx4 v[212:215], %3, %4, %0 offen offset:3072 sc1" : "=&s"(so347) : "v"(p.q_rd[0][3]), "s"(p.tso), "v"(p.tvo), "s"(p.tsrd) : "memory", "scc"); \
  unsigned so348; \
  asm volatile("v_mfma_f32_32x32x16_bf16 v[136:151], v[128:131], v[100:103], v[136:151]\n\tds_read_b64_tr_b16 v[92:93], %1 offset:28672\n\tds_read_b64_tr_b16 v[94:95], %1 offset:28928\n\ts_add_u32 m0, %2, 135168\n\ts_add_u32 %0, %5, 704512\n\tbuffer_load_dwordx4 %3, %4, %0 offen offset:0 lds\n\tv_accvgpr_read_b32 v240, a184\n\tv_accvgpr_read_b32 v241, a185\n\tv_accvgpr_read_b32 v242, a186\n\tv_accvgpr_read_b32 v243, a187\n\tv_accvgpr_read_b32 v244, a188\n\tv_accvgpr_read_b32 v245, a189\n\tv_accvgpr_read_b32 v246, a190\n\tv_accvgpr_read_b32 v247, a191\n\tv_cvt_pk_bf16_f32 v240, v240, v241\n\tv_cvt_pk_bf16_f32 v241, v242, v243\n\tv_cvt_pk_bf16_f32 v242, v244, v245\n\tv_cvt_pk_bf16_f32 v243, v246, v247\n\tds_write_b128 %6, v[240:243]" : "=&s"(so348) : "v"(p.f_rd[0]), "s"(p.m01), "v"(p.v1), "s"(p.s1), "s"(p.so1), "v"(p.te[7]) : "memory", "m0", "scc"); \
  asm volatile("v_mfma_f32_32x32x16_bf16 v[152:167], v[128:131], v[104:107], v[152:167]\n\tds_read_b128 v[72:75], %0 offset:28672" :  : "v"(p.xs_rd[0][0]) : "memory"); \
  asm volatile("s_waitcnt lgkmcnt(15)\n\tv_mfma_f32_32x32x16_bf16 v[168:183], v[132:135], v[108:111], v[168:183]\n\tds_read_b64_tr_b16 v[96:97], %0 offset:30720\n\tds_read_b64_tr_b16 v[98:99], %0 offset:30976" :  : "v"(p.f_rd[0]) : "memory"); \
  asm volatile("s_waitcnt vmcnt(11)\n\ts_barrier" ::: "memory"); \
  PJ_HOOK(6); \
  asm volatile("s_waitcnt lgkmcnt(15)\n\tv_mfma_f32_32x32x16_bf16 a[0:15], v[76:79], v[64:67], a[0:15]\n\tds_read_b64_tr_b16 v[112:113], %0 offset:32768\n\tds_read_b128 v[184:187], %1 offset:0" :  : "v"(p.q_rd[0][0]), "v"(p.tl) : "memory"); \
  unsigned so349; \
  asm volatile("v_mfma_f32_32x32x16_bf16 a[16:31], v[76:79], v[68:71], a[16:31]\n\tds_read_b64_tr_b16 v[114:115], %1 offset:33792\n\ts_add_u32 m0, %2, 49152\n\ts_add_u32 %0, %5, 576\n\tbuffer_load_dwordx4 %3, %4, %0 offen offset:0 sc1 lds\n\tds_read_b128 v[188:191], %6 offset:1024" : "=&s"(so349) : "v"(p.q_rd[0][0]), "s"(p.m0x), "v"(p.vx), "s"(p.sx), "s"(p.sox), "v"(p.tl) : "memory", "m0", "scc", ACL64); \
  asm volatile("s_waitcnt lgkmcnt(15)\n\tv_mfma_f32_32x32x16_bf16 a[32:47], v[80:83], v[64:67], a[32:47]\n\tds_read_b128 v[100:103], %0 offset:28672\n\tds_read_b128 v[104:107], %0 offset:30720\n\tds_read_b128 v[192:195], %1 offset:2048" :  : "v"(p.x_rd[0][1]), "v"(p.tl) : "memory"); \
  asm volatile("v_mfma_f32_32x32x16_bf16 a[48:63], v[80:83], v[68:71], a[48:63]\n\tds_read_b64_tr_b16 v[116:117], %0 offset:32768\n\tds_read_b128 v[196:199], %1 offset:3072" :  : "v"(p.q_rd[0][1]), "v"(p.tl) : "memory"); \
  unsigned so350; \
  asm volatile("s_waitcnt lgkmcnt(15)\n\tv_mfma_f32_32x32x16_bf16 a[64:79], v[84:87], v[64:67], a[64:79]\n\tds_read_b64_tr_b16 v[118:119], %1 offset:33792\n\ts_add_u32 m0, %2, 50176\n\ts_add_u32 %0, %5, 16960\n\tbuffer_load_dwordx4 %3, %4, %0 offen offset:0 sc1 lds" : "=&s"(so350) : "v"(p.q_rd[0][1]), "s"(p.m0x), "v"(p.vx), "s"(p.sx), "s"(p.sox) : "memory", "m0", "scc"); \
  asm volatile("v_mfma_f32_32x32x16_bf16 a[80:95], v[84:87], v[68:71], a[80:95]\n\tds_read_b64_tr_b16 v[120:121], %0 offset:32768\n\tds_read_b64_tr_b16 v[122:123], %0 offset:33792" :  : "v"(p.q_rd[0][2]) : "memory"); \
  unsigned so351; \
  asm volatile("v_mfma_f32_32x32x16_bf16 a[96:111], v[88:91], v[64:67], a[96:111]\n\tds_read_b64_tr_b16 v[124:125], %1 offset:32768\n\ts_waitcnt lgkmcnt(11)\n\ts_add_u32 %0, %2, 16384\n\tbuffer_store_dwordx4 v[184:187], %3, %4, %0 offen offset:0 sc1" : "=&s"(so351) : "v"(p.q_rd[0][3]), "s"(p.tso), "v"(p.tvo), "s"(p.tsrd) : "memory", "scc"); \
  unsigned so352; \
  asm volatile("v_mfma_f32_32x32x16_bf16 a[112:127], v[88:91], v[68:71], a[112:127]\n\tds_read_b64_tr_b16 v[126:127], %1 offset:33792\n\ts_add_u32 m0, %2, 51200\n\ts_add_u32 %0, %5, 33344\n\tbuffer_load_dwordx4 %3, %4, %0 offen offset:0 sc1 lds" : "=&s"(so352) : "v"(p.q_rd[0][3]), "s"(p.m0x), "v"(p.vx), "s"(p.sx), "s"(p.sox) : "memory", "m0", "scc"); \
  asm volatile("v_mfma_f32_32x32x16_bf16 v[136:151], v[92:95], v[64:67], v[136:151]\n\tds_read_b64_tr_b16 v[128:129], %0 offset:29696\n\tds_read_b64_tr_b16 v[130:131], %0 offset:29952" :  : "v"(p.f_rd[0]) : "memory"); \
  asm volatile("v_mfma_f32_32x32x16_bf16 v[152:167], v[92:95], v[68:71], v[152:167]\n\tds_read_b128 v[108:111], %0 offset:28672" :  : "v"(p.xs_rd[0][1]) : "memory", ACL64); \
  unsigned so353; \
  unsigned so354; \
  asm volatile("v_mfma_f32_32x32x16_bf16 v[168:183], v[96:99], v[72:75], v[168:183]\n\tds_read_b64_tr_b16 v[132:133], %2 offset:31744\n\tds_read_b64_tr_b16 v[134:135], %2 offset:32000\n\ts_add_u32 m0, %3, 52224\n\ts_add_u32 %0, %6, 49728\n\tbuffer_load_dwordx4 %4, %5, %0 offen offset:0 sc1 lds\n\ts_waitcnt lgkmcnt(15)\n\ts_add_u32 %1, %7, 16384\n\tbuffer_store_dwordx4 v[188:191], %8, %9, %1 offen offset:1024 sc1" : "=&s"(so353), "=&s"(so354) : "v"(p.f_rd[0]), "s"(p.m0x), "v"(p.vx), "s"(p.sx), "s"(p.sox), "s"(p.tso), "v"(p.tvo), "s"(p.tsrd) : "memory", "m0", "scc"); \
  asm volatile("s_waitcnt lgkmcnt(14)\n\tv_mfma_f32_32x32x16_bf16 a[0:15], v[112:115], v[100:103], a[0:15]\n\tds_read_b64_tr_b16 v[76:77], %0 offset:0" :  : "v"(p.q_rd[1][0]) : "memory"); \
  asm volatile("s_waitcnt lgkmcnt(14)\n\tv_mfma_f32_32x32x16_bf16 a[16:31], v[112:115], v[104:107], a[16:31]\n\tds_read_b64_tr_b16 v[78:79], %0 offset:1024" :  : "v"(p.q_rd[1][0]) : "memory"); \
  unsigned so355; \
  asm volatile("s_waitcnt lgkmcnt(11)\n\tv_mfma_f32_32x32x16_bf16 a[32:47], v[116:119], v[100:103], a[32:47]\n\tds_read_b128 v[64:67], %1 offset:0\n\tds_read_b128 v[68:71], %1 offset:2048\n\ts_add_u32 m0, %2, 49152\n\ts_add_u32 %0, %5, 2359296\n\tbuffer_load_dwordx4 %3, %4, %0 offen offset:0 lds" : "=&s"(so355) : "v"(p.x_rd[1][0]), "s"(p.m0q), "v"(p.vq), "s"(p.sq), "s"(p.soq) : "memory", "m0", "scc"); \
  unsigned so356; \
  asm volatile("v_mfma_f32_32x32x16_bf16 a[48:63], v[116:119], v[104:107], a[48:63]\n\tds_read_b64_tr_b16 v[80:81], %1 offset:0\n\ts_add_u32 %0, %2, 16384\n\tbuffer_store_dwordx4 v[192:195], %3, %4, %0 offen offset:2048 sc1" : "=&s"(so356) : "v"(p.q_rd[1][1]), "s"(p.tso), "v"(p.tvo), "s"(p.tsrd) : "memory", "scc"); \
  asm volatile("s_waitcnt lgkmcnt(12)\n\tv_mfma_f32_32x32x16_bf16 a[64:79], v[120:123], v[100:103], a[64:79]\n\tds_read_b64_tr_b16 v[82:83], %0 offset:1024" :  : "v"(p.q_rd[1][1]) : "memory"); \
  unsigned so357; \
  asm volatile("v_mfma_f32_32x32x16_bf16 a[80:95], v[120:123], v[104:107], a[80:95]\n\tds_read_b64_tr_b16 v[84:85], %1 offset:0\n\tds_read_b64_tr_b16 v[86:87], %1 offset:1024\n\ts_add_u32 m0, %2, 50176\n\ts_add_u32 %0, %5, 2392064\n\tbuffer_load_dwordx4 %3, %4, %0 offen offset:0 lds" : "=&s"(so357) : "v"(p.q_rd[1][2]), "s"(p.m0q), "v"(p.vq), "s"(p.sq), "s"(p.soq) : "memory", "m0", "scc"); \
  asm volatile("s_waitcnt lgkmcnt(13)\n\tv_mfma_f32_32x32x16_bf16 a[96:111], v[124:127], v[100:103], a[96:111]\n\tds_read_b64_tr_b16 v[88:89], %0 offset:0" :  : "v"(p.q_rd[1][3]) : "memory", ACL64); \
  unsigned so358; \
  asm volatile("v_mfma_f32_32x32x16_bf16 a[112:127], v[124:127], v[104:107], a[112:127]\n\tds_read_b64_tr_b16 v[90:91], %1 offset:1024\n\ts_add_u32 %0, %2, 16384\n\tbuffer_store_dwordx4 v[196:199], %3, %4, %0 offen offset:3072 sc1" : "=&s"(so358) : "v"(p.q_rd[1][3]), "s"(p.tso), "v"(p.tvo), "s"(p.tsrd) : "memory", "scc"); \
  unsigned so359; \
  asm volatile("s_waitcnt lgkmcnt(13)\n\tv_mfma_f32_32x32x16_bf16 v[136:151], v[128:131], v[100:103], v[136:151]\n\tds_read_b64_tr_b16 v[92:93], %1 offset:0\n\tds_read_b64_tr_b16 v[94:95], %1 offset:256\n\ts_add_u32 m0, %2, 49152\n\ts_add_u32 %0, %5, 792576\n\tbuffer_load_dwordx4 %3, %4, %0 offen offset:0 lds" : "=&s"(so359) : "v"(p.f_rd[1]), "s"(p.m01), "v"(p.v1), "s"(p.s1), "s"(p.so1) : "memory", "m0", "scc"); \
  asm volatile("v_mfma_f32_32x32x16_bf16 v[152:167], v[128:131], v[104:107], v[152:167]\n\tds_read_b128 v[72:75], %0 offset:0" :  : "v"(p.xs_rd[1][0]) : "memory"); \
  asm volatile("s_waitcnt lgkmcnt(13)\n\tv_mfma_f32_32x32x16_bf16 v[168:183], v[132:135], v[108:111], v[168:183]\n\tds_read_b64_tr_b16 v[96:97], %0 offset:2048\n\tds_read_b64_tr_b16 v[98:99], %0 offset:2304" :  : "v"(p.f_rd[1]) : "memory"); \
  asm volatile("s_waitcnt vmcnt(11)\n\ts_barrier" ::: "memory"); \
  PJ_HOOK(7); \
  asm volatile("s_waitcnt lgkmcnt(12)\n\tv_mfma_f32_32x32x16_bf16 a[0:15], v[76:79], v[64:67], a[0:15]\n\tds_read_b64_tr_b16 v[112:113], %0 offset:4096\n\tds_read_b128 v[200:203], %1 offset:4096" :  : "v"(p.q_rd[1][0]), "v"(p.tl) : "memory"); \
  unsigned so360; \
  asm volatile("s_waitcnt lgkmcnt(13)\n\tv_mfma_f32_32x32x16_bf16 a[16:31], v[76:79], v[68:71], a[16:31]\n\tds_read_b64_tr_b16 v[114:115], %1 offset:5120\n\ts_add_u32 m0, %2, 77824\n\ts_add_u32 %0, %5, 640\n\tbuffer_load_dwordx4 %3, %4, %0 offen offset:0 sc1 lds\n\tds_read_b128 v[204:207], %6 offset:5120" : "=&s"(so360) : "v"(p.q_rd[1][0]), "s"(p.m0x), "v"(p.vx), "s"(p.sx), "s"(p.sox), "v"(p.tl) : "memory", "m0", "scc"); \
  asm volatile("s_waitcnt lgkmcnt(13)\n\tv_mfma_f32_32x32x16_bf16 a[32:47], v[80:83], v[64:67], a[32:47]\n\tds_read_b128 v[100:103], %0 offset:0\n\tds_read_b128 v[104:107], %0 offset:2048\n\tds_read_b128 v[208:211], %1 offset:6144" :  : "v"(p.x_rd[1][1]), "v"(p.tl) : "memory"); \
  asm volatile("v_mfma_f32_32x32x16_bf16 a[48:63], v[80:83], v[68:71], a[48:63]\n\tds_read_b64_tr_b16 v[116:117], %0 offset:4096\n\tds_read_b128 v[212:215], %1 offset:7168" :  : "v"(p.q_rd[1][1]), "v"(p.tl) : "memory", ACL64); \
  unsigned so361; \
  asm volatile("s_waitcnt lgkmcnt(15)\n\tv_mfma_f32_32x32x16_bf16 a[64:79], v[84:87], v[64:67], a[64:79]\n\tds_read_b64_tr_b16 v[118:119], %1 offset:5120\n\ts_add_u32 m0, %2, 78848\n\ts_add_u32 %0, %5, 17024\n\tbuffer_load_dwordx4 %3, %4, %0 offen offset:0 sc1 lds" : "=&s"(so361) : "v"(p.q_rd[1][1]), "s"(p.m0x), "v"(p.vx), "s"(p.sx), "s"(p.sox) : "memory", "m0", "scc"); \
  asm volatile("v_mfma_f32_32x32x16_bf16 a[80:95], v[84:87], v[68:71], a[80:95]\n\tds_read_b64_tr_b16 v[120:121], %0 offset:4096\n\tds_read_b64_tr_b16 v[122:123], %0 offset:5120\n\tv_accvgpr_read_b32 v216, a192\n\tv_accvgpr_read_b32 v217, a193\n\tv_accvgpr_read_b32 v218, a194\n\tv_accvgpr_read_b32 v219, a195\n\tv_accvgpr_read_b32 v220, a196\n\tv_accvgpr_read_b32 v221, a197\n\tv_accvgpr_read_b32 v222, a198\n\tv_accvgpr_read_b32 v223, a199\n\tv_cvt_pk_bf16_f32 v216, v216, v217\n\tv_cvt_pk_bf16_f32 v217, v218, v219\n\tv_cvt_pk_bf16_f32 v218, v220, v221\n\tv_cvt_pk_bf16_f32 v219, v222, v223\n\tds_write_b128 %1, v[216:219]" :  : "v"(p.q_rd[1][2]), "v"(p.te[0]) : "memory"); \
  unsigned so362; \
  asm volatile("s_waitcnt lgkmcnt(15)\n\tv_mfma_f32_32x32x16_bf16 a[96:111], v[88:91], v[64:67], a[96:111]\n\tds_read_b64_tr_b16 v[124:125], %1 offset:4096\n\ts_waitcnt lgkmcnt(12)\n\ts_add_u32 %0, %2, 20480\n\tbuffer_store_dwordx4 v[200:203], %3, %4, %0 offen offset:0 sc1" : "=&s"(so362) : "v"(p.q_rd[1][3]), "s"(p.tso), "v"(p.tvo), "s"(p.tsrd) : "memory", "scc"); \
  unsigned so363; \
  asm volatile("v_mfma_f32_32x32x16_bf16 a[112:127], v[88:91], v[68:71], a[112:127]\n\tds_read_b64_tr_b16 v[126:127], %1 offset:5120\n\ts_add_u32 m0, %2, 79872\n\ts_add_u32 %0, %5, 33408\n\tbuffer_load_dwordx4 %3, %4, %0 offen offset:0 sc1 lds\n\tv_accvgpr_read_b32 v224, a200\n\tv_accvgpr_read_b32 v225, a201\n\tv_accvgpr_read_b32 v226, a202\n\tv_accvgpr_read_b32 v227, a203\n\tv_accvgpr_read_b32 v228, a204\n\tv_accvgpr_read_b32 v229, a205\n\tv_accvgpr_read_b32 v230, a206\n\tv_accvgpr_read_b32 v231, a207\n\tv_cvt_pk_bf16_f32 v224, v224, v225\n\tv_cvt_pk_bf16_f32 v225, v226, v227\n\tv_cvt_pk_bf16_f32 v226, v228, v229\n\tv_cvt_pk_bf16_f32 v227, v230, v231\n\tds_write_b128 %6, v[224:227]" : "=&s"(so363) : "v"(p.q_rd[1][3]), "s"(p.m0x), "v"(p.vx), "s"(p.sx), "s"(p.sox), "v"(p.te[1]) : "memory", "m0", "scc"); \
  asm volatile("v_mfma_f32_32x32x16_bf16 v[136:151], v[92:95], v[64:67], v[136:151]\n\tds_read_b64_tr_b16 v[128:129], %0 offset:1024\n\tds_read_b64_tr_b16 v[130:131], %0 offset:1280" :  : "v"(p.f_rd[1]) : "memory"); \
  asm volatile("v_mfma_f32_32x32x16_bf16 v[152:167], v[92:95], v[68:71], v[152:167]\n\tds_read_b128 v[108:111], %0 offset:0\n\tv_accvgpr_read_b32 v232, a208\n\tv_accvgpr_read_b32 v233, a209\n\tv_accvgpr_read_b32 v234, a210\n\tv_accvgpr_read_b32 v235, a211\n\tv_accvgpr_read_b32 v236, a212\n\tv_accvgpr_read_b32 v237, a213\n\tv_accvgpr_read_b32 v238, a214\n\tv_accvgpr_read_b32 v239, a215\n\tv_cvt_pk_bf16_f32 v232, v232, v233\n\tv_cvt_pk_bf16_f32 v233, v234, v235\n\tv_cvt_pk_bf16_f32 v234, v236, v237\n\tv_cvt_pk_bf16_f32 v235, v238, v239\n\tds_write_b128 %1, v[232:235]" :  : "v"(p.xs_rd[1][1]), "v"(p.te[2]) : "memory"); \
  unsigned so364; \
  unsigned so365; \
  asm volatile("v_mfma_f32_32x32x16_bf16 v[168:183], v[96:99], v[72:75], v[168:183]\n\tds_read_b64_tr_b16 v[132:133], %2 offset:3072\n\tds_read_b64_tr_b16 v[134:135], %2 offset:3328\n\ts_add_u32 m0, %3, 80896\n\ts_add_u32 %0, %6, 49792\n\tbuffer_load_dwordx4 %4, %5, %0 offen offset:0 sc1 lds\n\ts_waitcnt lgkmcnt(15)\n\ts_add_u32 %1, %7, 20480\n\tbuffer_store_dwordx4 v[204:207], %8, %9, %1 offen offset:1024 sc1" : "=&s"(so364), "=&s"(so365) : "v"(p.f_rd[1]), "s"(p.m0x), "v"(p.vx), "s"(p.sx), "s"(p.sox), "s"(p.tso), "v"(p.tvo), "s"(p.tsrd) : "memory", "m0", "scc"); \
  asm volatile("v_mfma_f32_32x32x16_bf16 a[0:15], v[112:115], v[100:103], a[0:15]\n\tds_read_b64_tr_b16 v[76:77], %0 offset:28672\n\tv_accvgpr_read_b32 v240, a216\n\tv_accvgpr_read_b32 v241, a217\n\tv_accvgpr_read_b32 v242, a218\n\tv_accvgpr_read_b32 v243, a219\n\tv_accvgpr_read_b32 v244, a220\n\tv_accvgpr_read_b32 v245, a221\n\tv_accvgpr_read_b32 v246, a222\n\tv_accvgpr_read_b32 v247, a223\n\tv_cvt_pk_bf16_f32 v240, v240, v241\n\tv_cvt_pk_bf16_f32 v241, v242, v243\n\tv_cvt_pk_bf16_f32 v242, v244, v245\n\tv_cvt_pk_bf16_f32 v243, v246, v247\n\tds_write_b128 %1, v[240:243]" :  : "v"(p.q_rd[1][0]), "v"(p.te[3]) : "memory", ACL64); \
  asm volatile("v_mfma_f32_32x32x16_bf16 a[16:31], v[112:115], v[104:107], a[16:31]\n\tds_read_b64_tr_b16 v[78:79], %0 offset:29696" :  : "v"(p.q_rd[1][0]) : "memory"); \
  unsigned so366; \
  asm volatile("s_waitcnt lgkmcnt(15)\n\tv_mfma_f32_32x32x16_bf16 a[32:47], v[116:119], v[100:103], a[32:47]\n\tds_read_b128 v[64:67], %1 offset:28672\n\tds_read_b128 v[68:71], %1 offset:30720\n\ts_add_u32 m0, %2, 77824\n\ts_add_u32 %0, %5, 2621440\n\tbuffer_load_dwordx4 %3, %4, %0 offen offset:0 lds\n\tv_accvgpr_read_b32 v216, a224\n\tv_accvgpr_read_b32 v217, a225\n\tv_accvgpr_read_b32 v218, a226\n\tv_accvgpr_read_b32 v219, a227\n\tv_accvgpr_read_b32 v220, a228\n\tv_accvgpr_read_b32 v221, a229\n\tv_accvgpr_read_b32 v222, a230\n\tv_accvgpr_read_b32 v223, a231\n\tv_cvt_pk_bf16_f32 v216, v216, v217\n\tv_cvt_pk_bf16_f32 v217, v218, v219\n\tv_cvt_pk_bf16_f32 v218, v220, v221\n\tv_cvt_pk_bf16_f32 v219, v222, v223\n\tds_write_b128 %6, v[216:219]" : "=&s"(so366) : "v"(p.x_rd[1][0]), "s"(p.m0q), "v"(p.vq), "s"(p.sq), "s"(p.soq), "v"(p.te[4]) : "memory", "m0", "scc"); \
  unsigned so367; \
  asm volatile("v_mfma_f32_32x32x16_bf16 a[48:63], v[116:119], v[104:107], a[48:63]\n\tds_read_b64_tr_b16 v[80:81], %1 offset:28672\n\ts_add_u32 %0, %2, 20480\n\tbuffer_store_dwordx4 v[208:211], %3, %4, %0 offen offset:2048 sc1" : "=&s"(so367) : "v"(p.q_rd[1][1]), "s"(p.tso), "v"(p.tvo), "s"(p.tsrd) : "memory", "scc"); \
  asm volatile("s_waitcnt lgkmcnt(15)\n\tv_mfma_f32_32x32x16_bf16 a[64:79], v[120:123], v[100:103], a[64:79]\n\tds_read_b64_tr_b16 v[82:83], %0 offset:29696\n\tv_accvgpr_read_b32 v224, a232\n\tv_accvgpr_read_b32 v225, a233\n\tv_accvgpr_read_b32 v226, a234\n\tv_accvgpr_read_b32 v227, a235\n\tv_accvgpr_read_b32 v228, a236\n\tv_accvgpr_read_b32 v229, a237\n\tv_accvgpr_read_b32 v230, a238\n\tv_accvgpr_read_b32 v231, a239\n\tv_cvt_pk_bf16_f32 v224, v224, v225\n\tv_cvt_pk_bf16_f32 v225, v226, v227\n\tv_cvt_pk_bf16_f32 v226, v228, v229\n\tv_cvt_pk_bf16_f32 v227, v230, v231\n\tds_write_b128 %1, v[224:227]" :  : "v"(p.q_rd[1][1]), "v"(p.te[5]) : "memory"); \
  unsigned so368; \
  asm volatile("v_mfma_f32_32x32x16_bf16 a[80:95], v[120:123], v[104:107], a[80:95]\n\tds_read_b64_tr_b16 v[84:85], %1 offset:28672\n\tds_read_b64_tr_b16 v[86:87], %1 offset:29696\n\ts_add_u32 m0, %2, 78848\n\ts_add_u32 %0, %5, 2654208\n\tbuffer_load_dwordx4 %3, %4, %0 offen offset:0 lds" : "=&s"(so368) : "v"(p.q_rd[1][2]), "s"(p.m0q), "v"(p.vq), "s"(p.sq), "s"(p.soq) : "memory", "m0", "scc"); \
  asm volatile("s_waitcnt lgkmcnt(15)\n\tv_mfma_f32_32x32x16_bf16 a[96:111], v[124:127], v[100:103], a[96:111]\n\tds_read_b64_tr_b16 v[88:89], %0 offset:28672\n\tv_accvgpr_read_b32 v232, a240\n\tv_accvgpr_read_b32 v233, a241\n\tv_accvgpr_read_b32 v234, a242\n\tv_accvgpr_read_b32 v235, a243\n\tv_accvgpr_read_b32 v236, a244\n\tv_accvgpr_read_b32 v237, a245\n\tv_accvgpr_read_b32 v238, a246\n\tv_accvgpr_read_b32 v239, a247\n\tv_cvt_pk_bf16_f32 v232, v232, v233\n\tv_cvt_pk_bf16_f32 v233, v234, v235\n\tv_cvt_pk_bf16_f32 v234, v236, v237\n\tv_cvt_pk_bf16_f32 v235, v238, v239\n\tds_write_b128 %1, v[232:235]" :  : "v"(p.q_rd[1][3]), "v"(p.te[6]) : "memory"); \
  unsigned so369; \
  asm volatile("v_mfma_f32_32x32x16_bf16 a[112:127], v[124:127], v[104:107], a[112:127]\n\tds_read_b64_tr_b16 v[90:91], %1 offset:29696\n\ts_add_u32 %0, %2, 20480\n\tbuffer_store_dwordx4 v[212:215], %3, %4, %0 offen offset:3072 sc1" : "=&s"(so369) : "v"(p.q_rd[1][3]), "s"(p.tso), "v"(p.tvo), "s"(p.tsrd) : "memory", "scc"); \
  unsigned so370; \
  asm volatile("v_mfma_f32_32x32x16_bf16 v[136:151], v[128:131], v[100:103], v[136:151]\n\tds_read_b64_tr_b16 v[92:93], %1 offset:28672\n\tds_read_b64_tr_b16 v[94:95], %1 offset:28928\n\ts_add_u32 m0, %2, 77824\n\ts_add_u32 %0, %5, 880640\n\tbuffer_load_dwordx4 %3, %4, %0 offen offset:0 lds\n\tv_accvgpr_read_b32 v240, a248\n\tv_accvgpr_read_b32 v241, a249\n\tv_accvgpr_read_b32 v242, a250\n\tv_accvgpr_read_b32 v243, a251\n\tv_accvgpr_read_b32 v244, a252\n\tv_accvgpr_read_b32 v245, a253\n\tv_accvgpr_read_b32 v246, a254\n\tv_accvgpr_read_b32 v247, a255\n\tv_cvt_pk_bf16_f32 v240, v240, v241\n\tv_cvt_pk_bf16_f32 v241, v242, v243\n\tv_cvt_pk_bf16_f32 v242, v244, v245\n\tv_cvt_pk_bf16_f32 v243, v246, v247\n\tds_write_b128 %6, v[240:243]" : "=&s"(so370) : "v"(p.f_rd[1]), "s"(p.m01), "v"(p.v1), "s"(p.s1), "s"(p.so1), "v"(p.te[7]) : "memory", "m0", "scc", ACL64); \
  asm volatile("v_mfma_f32_32x32x16_bf16 v[152:167], v[128:131], v[104:107], v[152:167]\n\tds_read_b128 v[72:75], %0 offset:28672" :  : "v"(p.xs_rd[1][0]) : "memory"); \
  asm volatile("s_waitcnt lgkmcnt(15)\n\tv_mfma_f32_32x32x16_bf16 v[168:183], v[132:135], v[108:111], v[168:183]\n\tds_read_b64_tr_b16 v[96:97], %0 offset:30720\n\tds_read_b64_tr_b16 v[98:99], %0 offset:30976" :  : "v"(p.f_rd[1]) : "memory"); \
  asm volatile("s_waitcnt vmcnt(11)\n\ts_barrier" ::: "memory"); \
  PJ_HOOK(8); \
  asm volatile("s_waitcnt lgkmcnt(15)\n\tv_mfma_f32_32x32x16_bf16 a[0:15], v[76:79], v[64:67], a[0:15]\n\tds_read_b64_tr_b16 v[112:113], %0 offset:32768\n\tds_read_b128 v[184:187], %1 offset:0" :  : "v"(p.q_rd[1][0]), "v"(p.tl) : "memory"); \
  unsigned so371; \
  asm volatile("v_mfma_f32_32x32x16_bf16 a[16:31], v[76:79], v[68:71], a[16:31]\n\tds_read_b64_tr_b16 v[114:115], %1 offset:33792\n\ts_add_u32 m0, %2, 106496\n\ts_add_u32 %0, %5, 704\n\tbuffer_load_dwordx4 %3, %4, %0 offen offset:0 sc1 lds\n\tds_read_b128 v[188:191], %6 offset:1024" : "=&s"(so371) : "v"(p.q_rd[1][0]), "s"(p.m0x), "v"(p.vx), "s"(p.sx), "s"(p.sox), "v"(p.tl) : "memory", "m0", "scc"); \
  asm volatile("s_waitcnt lgkmcnt(15)\n\tv_mfma_f32_32x32x16_bf16 a[32:47], v[80:83], v[64:67], a[32:47]\n\tds_read_b128 v[100:103], %0 offset:28672\n\tds_read_b128 v[104:107], %0 offset:30720\n\tds_read_b128 v[192:195], %1 offset:2048" :  : "v"(p.x_rd[1][1]), "v"(p.tl) : "memory"); \
  asm volatile("v_mfma_f32_32x32x16_bf16 a[48:63], v[80:83], v[68:71], a[48:63]\n\tds_read_b64_tr_b16 v[116:117], %0 offset:32768\n\tds_read_b128 v[196:199], %1 offset:3072" :  : "v"(p.q_rd[1][1]), "v"(p.tl) : "memory"); \
  unsigned so372; \
  asm volatile("s_waitcnt lgkmcnt(15)\n\tv_mfma_f32_32x32x16_bf16 a[64:79], v[84:87], v[64:67], a[64:79]\n\tds_read_b64_tr_b16 v[118:119], %1 offset:33792\n\ts_add_u32 m0, %2, 107520\n\ts_add_u32 %0, %5, 17088\n\tbuffer_load_dwordx4 %3, %4, %0 offen offset:0 sc1 lds" : "=&s"(so372) : "v"(p.q_rd[1][1]), "s"(p.m0x), "v"(p.vx), "s"(p.sx), "s"(p.sox) : "memory", "m0", "scc"); \
  asm volatile("v_mfma_f32_32x32x16_bf16 a[80:95], v[84:87], v[68:71], a[80:95]\n\tds_read_b64_tr_b16 v[120:121], %0 offset:32768\n\tds_read_b64_tr_b16 v[122:123], %0 offset:33792" :  : "v"(p.q_rd[1][2]) : "memory", ACL64); \
  unsigned so373; \
  asm volatile("v_mfma_f32_32x32x16_bf16 a[96:111], v[88:91], v[64:67], a[96:111]\n\tds_read_b64_tr_b16 v[124:125], %1 offset:32768\n\ts_waitcnt lgkmcnt(11)\n\ts_add_u32 %0, %2, 24576\n\tbuffer_store_dwordx4 v[184:187], %3, %4, %0 offen offset:0 sc1" : "=&s"(so373) : "v"(p.q_rd[1][3]), "s"(p.tso), "v"(p.tvo), "s"(p.tsrd) : "memory", "scc"); \
  unsigned so374; \
  asm volatile("v_mfma_f32_32x32x16_bf16 a[112:127], v[88:91], v[68:71], a[112:127]\n\tds_read_b64_tr_b16 v[126:127], %1 offset:33792\n\ts_add_u32 m0, %2, 108544\n\ts_add_u32 %0, %5, 33472\n\tbuffer_load_dwordx4 %3, %4, %0 offen offset:0 sc1 lds" : "=&s"(so374) : "v"(p.q_rd[1][3]), "s"(p.m0x), "v"(p.vx), "s"(p.sx), "s"(p.sox) : "memory", "m0", "scc"); \
  asm volatile("v_mfma_f32_32x32x16_bf16 v[136:151], v[92:95], v[64:67], v[136:151]\n\tds_read_b64_tr_b16 v[128:129], %0 offset:29696\n\tds_read_b64_tr_b16 v[130:131], %0 offset:29952" :  : "v"(p.f_rd[1]) : "memory"); \
  asm volatile("v_mfma_f32_32x32x16_bf16 v[152:167], v[92:95], v[68:71], v[152:167]\n\tds_read_b128 v[108:111], %0 offset:28672" :  : "v"(p.xs_rd[1][1]) : "memory"); \
  unsigned so375; \
  unsigned so376; \
  asm volatile("v_mfma_f32_32x32x16_bf16 v[168:183], v[96:99], v[72:75], v[168:183]\n\tds_read_b64_tr_b16 v[132:133], %2 offset:31744\n\tds_read_b64_tr_b16 v[134:135], %2 offset:32000\n\ts_add_u32 m0, %3, 109568\n\ts_add_u32 %0, %6, 49856\n\tbuffer_load_dwordx4 %4, %5, %0 offen offset:0 sc1 lds\n\ts_waitcnt lgkmcnt(15)\n\ts_add_u32 %1, %7, 24576\n\tbuffer_store_dwordx4 v[188:191], %8, %9, %1 offen offset:1024 sc1" : "=&s"(so375), "=&s"(so376) : "v"(p.f_rd[1]), "s"(p.m0x), "v"(p.vx), "s"(p.sx), "s"(p.sox), "s"(p.tso), "v"(p.tvo), "s"(p.tsrd) : "memory", "m0", "scc"); \
  asm volatile("s_waitcnt lgkmcnt(14)\n\tv_mfma_f32_32x32x16_bf16 a[0:15], v[112:115], v[100:103], a[0:15]\n\tds_read_b64_tr_b16 v[76:77], %0 offset:0" :  : "v"(p.q_rd[0][0]) : "memory"); \
  asm volatile("s_waitcnt lgkmcnt(14)\n\tv_mfma_f32_32x32x16_bf16 a[16:31], v[112:115], v[104:107], a[16:31]\n\tds_read_b64_tr_b16 v[78:79], %0 offset:1024" :  : "v"(p.q_rd[0][0]) : "memory"); \
  unsigned so377; \
  asm volatile("s_waitcnt lgkmcnt(11)\n\tv_mfma_f32_32x32x16_bf16 a[32:47], v[116:119], v[100:103], a[32:47]\n\tds_read_b128 v[64:67], %1 offset:0\n\tds_read_b128 v[68:71], %1 offset:2048\n\ts_add_u32 m0, %2, 106496\n\ts_add_u32 %0, %5, 2883584\n\tbuffer_load_dwordx4 %3, %4, %0 offen offset:0 lds" : "=&s"(so377) : "v"(p.x_rd[0][0]), "s"(p.m0q), "v"(p.vq), "s"(p.sq), "s"(p.soq) : "memory", "m0", "scc", ACL64); \
  unsigned so378; \
  asm volatile("v_mfma_f32_32x32x16_bf16 a[48:63], v[116:119], v[104:107], a[48:63]\n\tds_read_b64_tr_b16 v[80:81], %1 offset:0\n\ts_add_u32 %0, %2, 24576\n\tbuffer_store_dwordx4 v[192:195], %3, %4, %0 offen offset:2048 sc1" : "=&s"(so378) : "v"(p.q_rd[0][1]), "s"(p.tso), "v"(p.tvo), "s"(p.tsrd) : "memory", "scc"); \
  asm volatile("s_waitcnt lgkmcnt(12)\n\tv_mfma_f32_32x32x16_bf16 a[64:79], v[120:123], v[100:103], a[64:79]\n\tds_read_b64_tr_b16 v[82:83], %0 offset:1024" :  : "v"(p.q_rd[0][1]) : "memory"); \
  unsigned so379; \
  asm volatile("v_mfma_f32_32x32x16_bf16 a[80:95], v[120:123], v[104:107], a[80:95]\n\tds_read_b64_tr_b16 v[84:85], %1 offset:0\n\tds_read_b64_tr_b16 v[86:87], %1 offset:1024\n\ts_add_u32 m0, %2, 107520\n\ts_add_u32 %0, %5, 2916352\n\tbuffer_load_dwordx4 %3, %4, %0 offen offset:0 lds" : "=&s"(so379) : "v"(p.q_rd[0][2]), "s"(p.m0q), "v"(p.vq), "s"(p.sq), "s"(p.soq) : "memory", "m0", "scc"); \
  asm volatile("s_waitcnt lgkmcnt(13)\n\tv_mfma_f32_32x32x16_bf16 a[96:111], v[124:127], v[100:103], a[96:111]\n\tds_read_b64_tr_b16 v[88:89], %0 offset:0" :  : "v"(p.q_rd[0][3]) : "memory"); \
  unsigned so380; \
  asm volatile("v_mfma_f32_32x32x16_bf16 a[112:127], v[124:127], v[104:107], a[112:127]\n\tds_read_b64_tr_b16 v[90:91], %1 offset:1024\n\ts_add_u32 %0, %2, 24576\n\tbuffer_store_dwordx4 v[196:199], %3, %4, %0 offen offset:3072 sc1" : "=&s"(so380) : "v"(p.q_rd[0][3]), "s"(p.tso), "v"(p.tvo), "s"(p.tsrd) : "memory", "scc"); \
  unsigned so381; \
  asm volatile("s_waitcnt lgkmcnt(13)\n\tv_mfma_f32_32x32x16_bf16 v[136:151], v[128:131], v[100:103], v[136:151]\n\tds_read_b64_tr_b16 v[92:93], %1 offset:0\n\tds_read_b64_tr_b16 v[94:95], %1 offset:256\n\ts_add_u32 m0, %2, 106496\n\ts_add_u32 %0, %5, 968704\n\tbuffer_load_dwordx4 %3, %4, %0 offen offset:0 lds" : "=&s"(so381) : "v"(p.f_rd[0]), "s"(p.m01), "v"(p.v1), "s"(p.s1), "s"(p.so1) : "memory", "m0", "scc"); \
  asm volatile("v_mfma_f32_32x32x16_bf16 v[152:167], v[128:131], v[104:107], v[152:167]\n\tds_read_b128 v[72:75], %0 offset:0" :  : "v"(p.xs_rd[0][0]) : "memory"); \
  asm volatile("s_waitcnt lgkmcnt(13)\n\tv_mfma_f32_32x32x16_bf16 v[168:183], v[132:135], v[108:111], v[168:183]\n\tds_read_b64_tr_b16 v[96:97], %0 offset:2048\n\tds_read_b64_tr_b16 v[98:99], %0 offset:2304" :  : "v"(p.f_rd[0]) : "memory", ACL64); \
  asm volatile("s_waitcnt vmcnt(11)\n\ts_barrier" ::: "memory"); \
  PJ_HOOK(9); \
  asm volatile("s_waitcnt lgkmcnt(12)\n\tv_mfma_f32_32x32x16_bf16 a[0:15], v[76:79], v[64:67], a[0:15]\n\tds_read_b64_tr_b16 v[112:113], %0 offset:4096\n\tds_read_b128 v[200:203], %1 offset:4096" :  : "v"(p.q_rd[0][0]), "v"(p.tl) : "memory"); \
  unsigned so382; \
  asm volatile("s_waitcnt lgkmcnt(13)\n\tv_mfma_f32_32x32x16_bf16 a[16:31], v[76:79], v[68:71], a[16:31]\n\tds_read_b64_tr_b16 v[114:115], %1 offset:5120\n\ts_add_u32 m0, %2, 135168\n\ts_add_u32 %0, %5, 768\n\tbuffer_load_dwordx4 %3, %4, %0 offen offset:0 sc1 lds\n\tds_read_b128 v[204:207], %6 offset:5120" : "=&s"(so382) : "v"(p.q_rd[0][0]), "s"(p.m0x), "v"(p.vx), "s"(p.sx), "s"(p.sox), "v"(p.tl) : "memory", "m0", "scc"); \
  asm volatile("s_waitcnt lgkmcnt(13)\n\tv_mfma_f32_32x32x16_bf16 a[32:47], v[80:83], v[64:67], a[32:47]\n\tds_read_b128 v[100:103], %0 offset:0\n\tds_read_b128 v[104:107], %0 offset:2048\n\tds_read_b128 v[208:211], %1 offset:6144" :  : "v"(p.x_rd[0][1]), "v"(p.tl) : "memory"); \
  asm volatile("v_mfma_f32_32x32x16_bf16 a[48:63], v[80:83], v[68:71], a[48:63]\n\tds_read_b64_tr_b16 v[116:117], %0 offset:4096\n\tds_read_b128 v[212:215], %1 offset:7168" :  : "v"(p.q_rd[0][1]), "v"(p.tl) : "memory"); \
  unsigned so383; \
  asm volatile("s_waitcnt lgkmcnt(15)\n\tv_mfma_f32_32x32x16_bf16 a[64:79], v[84:87], v[64:67], a[64:79]\n\tds_read_b64_tr_b16 v[118:119], %1 offset:5120\n\ts_add_u32 m0, %2, 136192\n\ts_add_u32 %0, %5, 17152\n\tbuffer_load_dwordx4 %3, %4, %0 offen offset:0 sc1 lds" : "=&s"(so383) : "v"(p.q_rd[0][1]), "s"(p.m0x), "v"(p.vx), "s"(p.sx), "s"(p.sox) : "memory", "m0", "scc"); \
  asm volatile("v_mfma_f32_32x32x16_bf16 a[80:95], v[84:87], v[68:71], a[80:95]\n\tds_read_b64_tr_b16 v[120:121], %0 offset:4096\n\tds_read_b64_tr_b16 v[122:123], %0 offset:5120" :  : "v"(p.q_rd[0][2]) : "memory"); \
  unsigned so384; \
  asm volatile("s_waitcnt lgkmcnt(15)\n\tv_mfma_f32_32x32x16_bf16 a[96:111], v[88:91], v[64:67], a[96:111]\n\tds_read_b64_tr_b16 v[124:125], %1 offset:4096\n\ts_waitcnt lgkmcnt(11)\n\ts_add_u32 %0, %2, 28672\n\tbuffer_store_dwordx4 v[200:203], %3, %4, %0 offen offset:0 sc1" : "=&s"(so384) : "v"(p.q_rd[0][3]), "s"(p.tso), "v"(p.tvo), "s"(p.tsrd) : "memory", "scc"); \
  unsigned so385; \
  asm volatile("v_mfma_f32_32x32x16_bf16 a[112:127], v[88:91], v[68:71], a[112:127]\n\tds_read_b64_tr_b16 v[126:127], %1 offset:5120\n\ts_add_u32 m0, %2, 137216\n\ts_add_u32 %0, %5, 33536\n\tbuffer_load_dwordx4 %3, %4, %0 offen offset:0 sc1 lds" : "=&s"(so385) : "v"(p.q_rd[0][3]), "s"(p.m0x), "v"(p.vx), "s"(p.sx), "s"(p.sox) : "memory", "m0", "scc", ACL64); \
  asm volatile("v_mfma_f32_32x32x16_bf16 v[136:151], v[92:95], v[64:67], v[136:151]\n\tds_read_b64_tr_b16 v[128:129], %0 offset:1024\n\tds_read_b64_tr_b16 v[130:131], %0 offset:1280" :  : "v"(p.f_rd[0]) : "memory"); \
  asm volatile("v_mfma_f32_32x32x16_bf16 v[152:167], v[92:95], v[68:71], v[152:167]\n\tds_read_b128 v[108:111], %0 offset:0" :  : "v"(p.xs_rd[0][1]) : "memory"); \
  unsigned so386; \
  unsigned so387; \
  asm volatile("v_mfma_f32_32x32x16_bf16 v[168:183], v[96:99], v[72:75], v[168:183]\n\tds_read_b64_tr_b16 v[132:133], %2 offset:3072\n\tds_read_b64_tr_b16 v[134:135], %2 offset:3328\n\ts_add_u32 m0, %3, 138240\n\ts_add_u32 %0, %6, 49920\n\tbuffer_load_dwordx4 %4, %5, %0 offen offset:0 sc1 lds\n\ts_waitcnt lgkmcnt(15)\n\ts_add_u32 %1, %7, 28672\n\tbuffer_store_dwordx4 v[204:207], %8, %9, %1 offen offset:1024 sc1" : "=&s"(so386), "=&s"(so387) : "v"(p.f_rd[0]), "s"(p.m0x), "v"(p.vx), "s"(p.sx), "s"(p.sox), "s"(p.tso), "v"(p.tvo), "s"(p.tsrd) : "memory", "m0", "scc"); \
  asm volatile("s_waitcnt lgkmcnt(14)\n\tv_mfma_f32_32x32x16_bf16 a[0:15], v[112:115], v[100:103], a[0:15]\n\tds_read_b64_tr_b16 v[76:77], %0 offset:28672" :  : "v"(p.q_rd[0][0]) : "memory"); \
  asm volatile("s_waitcnt lgkmcnt(14)\n\tv_mfma_f32_32x32x16_bf16 a[16:31], v[112:115], v[104:107], a[16:31]\n\tds_read_b64_tr_b16 v[78:79], %0 offset:29696" :  : "v"(p.q_rd[0][0]) : "memory"); \
  unsigned so388; \
  asm volatile("s_waitcnt lgkmcnt(11)\n\tv_mfma_f32_32x32x16_bf16 a[32:47], v[116:119], v[100:103], a[32:47]\n\tds_read_b128 v[64:67], %1 offset:28672\n\tds_read_b128 v[68:71], %1 offset:30720\n\ts_add_u32 m0, %2, 135168\n\ts_add_u32 %0, %5, 3145728\n\tbuffer_load_dwordx4 %3, %4, %0 offen offset:0 lds" : "=&s"(so388) : "v"(p.x_rd[0][0]), "s"(p.m0q), "v"(p.vq), "s"(p.sq), "s"(p.soq) : "memory", "m0", "scc"); \
  unsigned so389; \
  asm volatile("v_mfma_f32_32x32x16_bf16 a[48:63], v[116:119], v[104:107], a[48:63]\n\tds_read_b64_tr_b16 v[80:81], %1 offset:28672\n\ts_add_u32 %0, %2, 28672\n\tbuffer_store_dwordx4 v[208:211], %3, %4, %0 offen offset:2048 sc1" : "=&s"(so389) : "v"(p.q_rd[0][1]), "s"(p.tso), "v"(p.tvo), "s"(p.tsrd) : "memory", "scc"); \
  asm volatile("s_waitcnt lgkmcnt(12)\n\tv_mfma_f32_32x32x16_bf16 a[64:79], v[120:123], v[100:103], a[64:79]\n\tds_read_b64_tr_b16 v[82:83], %0 offset:29696" :  : "v"(p.q_rd[0][1]) : "memory", ACL64); \
  unsigned so390; \
  asm volatile("v_mfma_f32_32x32x16_bf16 a[80:95], v[120:123], v[104:107], a[80:95]\n\tds_read_b64_tr_b16 v[84:85], %1 offset:28672\n\tds_read_b64_tr_b16 v[86:87], %1 offset:29696\n\ts_add_u32 m0, %2, 136192\n\ts_add_u32 %0, %5, 3178496\n\tbuffer_load_dwordx4 %3, %4, %0 offen offset:0 lds" : "=&s"(so390) : "v"(p.q_rd[0][2]), "s"(p.m0q), "v"(p.vq), "s"(p.sq), "s"(p.soq) : "memory", "m0", "scc"); \
  asm volatile("s_waitcnt lgkmcnt(13)\n\tv_mfma_f32_32x32x16_bf16 a[96:111], v[124:127], v[100:103], a[96:111]\n\tds_read_b64_tr_b16 v[88:89], %0 offset:28672" :  : "v"(p.q_rd[0][3]) : "memory"); \
  unsigned so391; \
  asm volatile("v_mfma_f32_32x32x16_bf16 a[112:127], v[124:127], v[104:107], a[112:127]\n\tds_read_b64_tr_b16 v[90:91], %1 offset:29696\n\ts_add_u32 %0, %2, 28672\n\tbuffer_store_dwordx4 v[212:215], %3, %4, %0 offen offset:3072 sc1" : "=&s"(so391) : "v"(p.q_rd[0][3]), "s"(p.tso), "v"(p.tvo), "s"(p.tsrd) : "memory", "scc"); \
  unsigned so392; \
  asm volatile("s_waitcnt lgkmcnt(13)\n\tv_mfma_f32_32x32x16_bf16 v[136:151], v[128:131], v[100:103], v[136:151]\n\tds_read_b64_tr_b16 v[92:93], %1 offset:28672\n\tds_read_b64_tr_b16 v[94:95], %1 offset:28928\n\ts_add_u32 m0, %2, 135168\n\ts_add_u32 %0, %5, 1056768\n\tbuffer_load_dwordx4 %3, %4, %0 offen offset:0 lds" : "=&s"(so392) : "v"(p.f_rd[0]), "s"(p.m01), "v"(p.v1), "s"(p.s1), "s"(p.so1) : "memory", "m0", "scc"); \
  asm volatile("v_mfma_f32_32x32x16_bf16 v[152:167], v[128:131], v[104:107], v[152:167]\n\tds_read_b128 v[72:75], %0 offset:28672" :  : "v"(p.xs_rd[0][0]) : "memory"); \
  asm volatile("s_waitcnt lgkmcnt(13)\n\tv_mfma_f32_32x32x16_bf16 v[168:183], v[132:135], v[108:111], v[168:183]\n\tds_read_b64_tr_b16 v[96:97], %0 offset:30720\n\tds_read_b64_tr_b16 v[98:99], %0 offset:30976" :  : "v"(p.f_rd[0]) : "memory"); \
  asm volatile("s_waitcnt vmcnt(11)\n\ts_barrier" ::: "memory"); \
  PJ_HOOK(10); \
  asm volatile("s_waitcnt lgkmcnt(12)\n\tv_mfma_f32_32x32x16_bf16 a[0:15], v[76:79], v[64:67], a[0:15]\n\tds_read_b64_tr_b16 v[112:113], %0 offset:32768" :  : "v"(p.q_rd[0][0]) : "memory"); \
  unsigned so393; \
  asm volatile("s_waitcnt lgkmcnt(12)\n\tv_mfma_f32_32x32x16_bf16 a[16:31], v[76:79], v[68:71], a[16:31]\n\tds_read_b64_tr_b16 v[114:115], %1 offset:33792\n\ts_add_u32 m0, %2, 49152\n\ts_add_u32 %0, %5, 832\n\tbuffer_load_dwordx4 %3, %4, %0 offen offset:0 sc1 lds" : "=&s"(so393) : "v"(p.q_rd[0][0]), "s"(p.m0x), "v"(p.vx), "s"(p.sx), "s"(p.sox) : "memory", "m0", "scc", ACL64); \
  asm volatile("s_waitcnt lgkmcnt(11)\n\tv_mfma_f32_32x32x16_bf16 a[32:47], v[80:83], v[64:67], a[32:47]\n\tds_read_b128 v[100:103], %0 offset:28672\n\tds_read_b128 v[104:107], %0 offset:30720" :  : "v"(p.x_rd[0][1]) : "memory"); \
  asm volatile("v_mfma_f32_32x32x16_bf16 a[48:63], v[80:83], v[68:71], a[48:63]\n\tds_read_b64_tr_b16 v[116:117], %0 offset:32768" :  : "v"(p.q_rd[0][1]) : "memory"); \
  unsigned so394; \
  asm volatile("s_waitcnt lgkmcnt(12)\n\tv_mfma_f32_32x32x16_bf16 a[64:79], v[84:87], v[64:67], a[64:79]\n\tds_read_b64_tr_b16 v[118:119], %1 offset:33792\n\ts_add_u32 m0, %2, 50176\n\ts_add_u32 %0, %5, 17216\n\tbuffer_load_dwordx4 %3, %4, %0 offen offset:0 sc1 lds" : "=&s"(so394) : "v"(p.q_rd[0][1]), "s"(p.m0x), "v"(p.vx), "s"(p.sx), "s"(p.sox) : "memory", "m0", "scc"); \
  asm volatile("v_mfma_f32_32x32x16_bf16 a[80:95], v[84:87], v[68:71], a[80:95]\n\tds_read_b64_tr_b16 v[120:121], %0 offset:32768\n\tds_read_b64_tr_b16 v[122:123], %0 offset:33792" :  : "v"(p.q_rd[0][2]) : "memory"); \
  asm volatile("s_waitcnt lgkmcnt(13)\n\tv_mfma_f32_32x32x16_bf16 a[96:111], v[88:91], v[64:67], a[96:111]\n\tds_read_b64_tr_b16 v[124:125], %0 offset:32768" :  : "v"(p.q_rd[0][3]) : "memory"); \
  unsigned so395; \
  asm volatile("v_mfma_f32_32x32x16_bf16 a[112:127], v[88:91], v[68:71], a[112:127]\n\tds_read_b64_tr_b16 v[126:127], %1 offset:33792\n\ts_add_u32 m0, %2, 51200\n\ts_add_u32 %0, %5, 33600\n\tbuffer_load_dwordx4 %3, %4, %0 offen offset:0 sc1 lds" : "=&s"(so395) : "v"(p.q_rd[0][3]), "s"(p.m0x), "v"(p.vx), "s"(p.sx), "s"(p.sox) : "memory", "m0", "scc"); \
  asm volatile("s_waitcnt lgkmcnt(13)\n\tv_mfma_f32_32x32x16_bf16 v[136:151], v[92:95], v[64:67], v[136:151]\n\tds_read_b64_tr_b16 v[128:129], %0 offset:29696\n\tds_read_b64_tr_b16 v[130:131], %0 offset:29952" :  : "v"(p.f_rd[0]) : "memory"); \
  asm volatile("v_mfma_f32_32x32x16_bf16 v[152:167], v[92:95], v[68:71], v[152:167]\n\tds_read_b128 v[108:111], %0 offset:28672" :  : "v"(p.xs_rd[0][1]) : "memory", ACL64); \
  unsigned so396; \
  asm volatile("s_waitcnt lgkmcnt(13)\n\tv_mfma_f32_32x32x16_bf16 v[168:183], v[96:99], v[72:75], v[168:183]\n\tds_read_b64_tr_b16 v[132:133], %1 offset:31744\n\tds_read_b64_tr_b16 v[134:135], %1 offset:32000\n\ts_add_u32 m0, %2, 52224\n\ts_add_u32 %0, %5, 49984\n\tbuffer_load_dwordx4 %3, %4, %0 offen offset:0 sc1 lds" : "=&s"(so396) : "v"(p.f_rd[0]), "s"(p.m0x), "v"(p.vx), "s"(p.sx), "s"(p.sox) : "memory", "m0", "scc"); \
  asm volatile("s_waitcnt lgkmcnt(12)\n\tv_mfma_f32_32x32x16_bf16 a[0:15], v[112:115], v[100:103], a[0:15]\n\tds_read_b64_tr_b16 v[76:77], %0 offset:0" :  : "v"(p.q_rd[1][0]) : "memory"); \
  asm volatile("s_waitcnt lgkmcnt(12)\n\tv_mfma_f32_32x32x16_bf16 a[16:31], v[112:115], v[104:107], a[16:31]\n\tds_read_b64_tr_b16 v[78:79], %0 offset:1024" :  : "v"(p.q_rd[1][0]) : "memory"); \
  unsigned so397; \
  asm volatile("s_waitcnt lgkmcnt(11)\n\tv_mfma_f32_32x32x16_bf16 a[32:47], v[116:119], v[100:103], a[32:47]\n\tds_read_b128 v[64:67], %1 offset:0\n\tds_read_b128 v[68:71], %1 offset:2048\n\ts_add_u32 m0, %2, 49152\n\ts_add_u32 %0, %5, 3407872\n\tbuffer_load_dwordx4 %3, %4, %0 offen offset:0 lds" : "=&s"(so397) : "v"(p.x_rd[1][0]), "s"(p.m0q), "v"(p.vq), "s"(p.sq), "s"(p.soq) : "memory", "m0", "scc"); \
  asm volatile("v_mfma_f32_32x32x16_bf16 a[48:63], v[116:119], v[104:107], a[48:63]\n\tds_read_b64_tr_b16 v[80:81], %0 offset:0" :  : "v"(p.q_rd[1][1]) : "memory"); \
  asm volatile("s_waitcnt lgkmcnt(12)\n\tv_mfma_f32_32x32x16_bf16 a[64:79], v[120:123], v[100:103], a[64:79]\n\tds_read_b64_tr_b16 v[82:83], %0 offset:1024" :  : "v"(p.q_rd[1][1]) : "memory"); \
  unsigned so398; \
  asm volatile("v_mfma_f32_32x32x16_bf16 a[80:95], v[120:123], v[104:107], a[80:95]\n\tds_read_b64_tr_b16 v[84:85], %1 offset:0\n\tds_read_b64_tr_b16 v[86:87], %1 offset:1024\n\ts_add_u32 m0, %2, 50176\n\ts_add_u32 %0, %5, 3440640\n\tbuffer_load_dwordx4 %3, %4, %0 offen offset:0 lds" : "=&s"(so398) : "v"(p.q_rd[1][2]), "s"(p.m0q), "v"(p.vq), "s"(p.sq), "s"(p.soq) : "memory", "m0", "scc"); \
  asm volatile("s_waitcnt lgkmcnt(13)\n\tv_mfma_f32_32x32x16_bf16 a[96:111], v[124:127], v[100:103], a[96:111]\n\tds_read_b64_tr_b16 v[88:89], %0 offset:0" :  : "v"(p.q_rd[1][3]) : "memory", ACL64); \
  asm volatile("v_mfma_f32_32x32x16_bf16 a[112:127], v[124:127], v[104:107], a[112:127]\n\tds_read_b64_tr_b16 v[90:91], %0 offset:1024" :  : "v"(p.q_rd[1][3]) : "memory"); \
  unsigned so399; \
  asm volatile("s_waitcnt lgkmcnt(13)\n\tv_mfma_f32_32x32x16_bf16 v[136:151], v[128:131], v[100:103], v[136:151]\n\tds_read_b64_tr_b16 v[92:93], %1 offset:0\n\tds_read_b64_tr_b16 v[94:95], %1 offset:256\n\ts_add_u32 m0, %2, 49152\n\ts_add_u32 %0, %5, 1144832\n\tbuffer_load_dwordx4 %3, %4, %0 offen offset:0 lds" : "=&s"(so399) : "v"(p.f_rd[1]), "s"(p.m01), "v"(p.v1), "s"(p.s1), "s"(p.so1) : "memory", "m0", "scc"); \
  asm volatile("v_mfma_f32_32x32x16_bf16 v[152:167], v[128:131], v[104:107], v[152:167]\n\tds_read_b128 v[72:75], %0 offset:0" :  : "v"(p.xs_rd[1][0]) : "memory"); \
  asm volatile("s_waitcnt lgkmcnt(13)\n\tv_mfma_f32_32x32x16_bf16 v[168:183], v[132:135], v[108:111], v[168:183]\n\tds_read_b64_tr_b16 v[96:97], %0 offset:2048\n\tds_read_b64_tr_b16 v[98:99], %0 offset:2304" :  : "v"(p.f_rd[1]) : "memory"); \
  asm volatile("s_waitcnt vmcnt(7)\n\ts_barrier" ::: "memory"); \
  PJ_HOOK(11); \
  asm volatile("s_waitcnt lgkmcnt(12)\n\tv_mfma_f32_32x32x16_bf16 a[0:15], v[76:79], v[64:67], a[0:15]\n\tds_read_b64_tr_b16 v[112:113], %0 offset:4096" :  : "v"(p.q_rd[1][0]) : "memory"); \
  unsigned so400; \
  asm volatile("s_waitcnt lgkmcnt(12)\n\tv_mfma_f32_32x32x16_bf16 a[16:31], v[76:79], v[68:71], a[16:31]\n\tds_read_b64_tr_b16 v[114:115], %1 offset:5120\n\ts_add_u32 m0, %2, 77824\n\ts_add_u32 %0, %5, 896\n\tbuffer_load_dwordx4 %3, %4, %0 offen offset:0 sc1 lds" : "=&s"(so400) : "v"(p.q_rd[1][0]), "s"(p.m0x), "v"(p.vx), "s"(p.sx), "s"(p.sox) : "memory", "m0", "scc"); \
  asm volatile("s_waitcnt lgkmcnt(11)\n\tv_mfma_f32_32x32x16_bf16 a[32:47], v[80:83], v[64:67], a[32:47]\n\tds_read_b128 v[100:103], %0 offset:0\n\tds_read_b128 v[104:107], %0 offset:2048" :  : "v"(p.x_rd[1][1]) : "memory"); \
  asm volatile("v_mfma_f32_32x32x16_bf16 a[48:63], v[80:83], v[68:71], a[48:63]\n\tds_read_b64_tr_b16 v[116:117], %0 offset:4096" :  : "v"(p.q_rd[1][1]) : "memory", ACL64); \
  unsigned so401; \
  asm volatile("s_waitcnt lgkmcnt(12)\n\tv_mfma_f32_32x32x16_bf16 a[64:79], v[84:87], v[64:67], a[64:79]\n\tds_read_b64_tr_b16 v[118:119], %1 offset:5120\n\ts_add_u32 m0, %2, 78848\n\ts_add_u32 %0, %5, 17280\n\tbuffer_load_dwordx4 %3, %4, %0 offen offset:0 sc1 lds" : "=&s"(so401) : "v"(p.q_rd[1][1]), "s"(p.m0x), "v"(p.vx), "s"(p.sx), "s"(p.sox) : "memory", "m0", "scc"); \
  asm volatile("v_mfma_f32_32x32x16_bf16 a[80:95], v[84:87], v[68:71], a[80:95]\n\tds_read_b64_tr_b16 v[120:121], %0 offset:4096\n\tds_read_b64_tr_b16 v[122:123], %0 offset:5120" :  : "v"(p.q_rd[1][2]) : "memory"); \
  asm volatile("s_waitcnt lgkmcnt(13)\n\tv_mfma_f32_32x32x16_bf16 a[96:111], v[88:91], v[64:67], a[96:111]\n\tds_read_b64_tr_b16 v[124:125], %0 offset:4096" :  : "v"(p.q_rd[1][3]) : "memory"); \
  unsigned so402; \
  asm volatile("v_mfma_f32_32x32x16_bf16 a[112:127], v[88:91], v[68:71], a[112:127]\n\tds_read_b64_tr_b16 v[126:127], %1 offset:5120\n\ts_add_u32 m0, %2, 79872\n\ts_add_u32 %0, %5, 33664\n\tbuffer_load_dwordx4 %3, %4, %0 offen offset:0 sc1 lds" : "=&s"(so402) : "v"(p.q_rd[1][3]), "s"(p.m0x), "v"(p.vx), "s"(p.sx), "s"(p.sox) : "memory", "m0", "scc"); \
  asm volatile("s_waitcnt lgkmcnt(13)\n\tv_mfma_f32_32x32x16_bf16 v[136:151], v[92:95], v[64:67], v[136:151]\n\tds_read_b64_tr_b16 v[128:129], %0 offset:1024\n\tds_read_b64_tr_b16 v[130:131], %0 offset:1280" :  : "v"(p.f_rd[1]) : "memory"); \
  asm volatile("v_mfma_f32_32x32x16_bf16 v[152:167], v[92:95], v[68:71], v[152:167]\n\tds_read_b128 v[108:111], %0 offset:0" :  : "v"(p.xs_rd[1][1]) : "memory"); \
  unsigned so403; \
  asm volatile("s_waitcnt lgkmcnt(13)\n\tv_mfma_f32_32x32x16_bf16 v[168:183], v[96:99], v[72:75], v[168:183]\n\tds_read_b64_tr_b16 v[132:133], %1 offset:3072\n\tds_read_b64_tr_b16 v[134:135], %1 offset:3328\n\ts_add_u32 m0, %2, 80896\n\ts_add_u32 %0, %5, 50048\n\tbuffer_load_dwordx4 %3, %4, %0 offen offset:0 sc1 lds" : "=&s"(so403) : "v"(p.f_rd[1]), "s"(p.m0x), "v"(p.vx), "s"(p.sx), "s"(p.sox) : "memory", "m0", "scc"); \
  asm volatile("s_waitcnt lgkmcnt(12)\n\tv_mfma_f32_32x32x16_bf16 a[0:15], v[112:115], v[100:103], a[0:15]\n\tds_read_b64_tr_b16 v[76:77], %0 offset:28672" :  : "v"(p.q_rd[1][0]) : "memory", ACL64); \
  asm volatile("s_waitcnt lgkmcnt(12)\n\tv_mfma_f32_32x32x16_bf16 a[16:31], v[112:115], v[104:107], a[16:31]\n\tds_read_b64_tr_b16 v[78:79], %0 offset:29696" :  : "v"(p.q_rd[1][0]) : "memory"); \
  unsigned so404; \
  asm volatile("s_waitcnt lgkmcnt(11)\n\tv_mfma_f32_32x32x16_bf16 a[32:47], v[116:119], v[100:103], a[32:47]\n\tds_read_b128 v[64:67], %1 offset:28672\n\tds_read_b128 v[68:71], %1 offset:30720\n\ts_add_u32 m0, %2, 77824\n\ts_add_u32 %0, %5, 3670016\n\tbuffer_load_dwordx4 %3, %4, %0 offen offset:0 lds" : "=&s"(so404) : "v"(p.x_rd[1][0]), "s"(p.m0q), "v"(p.vq), "s"(p.sq), "s"(p.soq) : "memory", "m0", "scc"); \
  asm volatile("v_mfma_f32_32x32x16_bf16 a[48:63], v[116:119], v[104:107], a[48:63]\n\tds_read_b64_tr_b16 v[80:81], %0 offset:28672" :  : "v"(p.q_rd[1][1]) : "memory"); \
  asm volatile("s_waitcnt lgkmcnt(12)\n\tv_mfma_f32_32x32x16_bf16 a[64:79], v[120:123], v[100:103], a[64:79]\n\tds_read_b64_tr_b16 v[82:83], %0 offset:29696" :  : "v"(p.q_rd[1][1]) : "memory"); \
  unsigned so405; \
  asm volatile("v_mfma_f32_32x32x16_bf16 a[80:95], v[120:123], v[104:107], a[80:95]\n\tds_read_b64_tr_b16 v[84:85], %1 offset:28672\n\tds_read_b64_tr_b16 v[86:87], %1 offset:29696\n\ts_add_u32 m0, %2, 78848\n\ts_add_u32 %0, %5, 3702784\n\tbuffer_load_dwordx4 %3, %4, %0 offen offset:0 lds" : "=&s"(so405) : "v"(p.q_rd[1][2]), "s"(p.m0q), "v"(p.vq), "s"(p.sq), "s"(p.soq) : "memory", "m0", "scc"); \
  asm volatile("s_waitcnt lgkmcnt(13)\n\tv_mfma_f32_32x32x16_bf16 a[96:111], v[124:127], v[100:103], a[96:111]\n\tds_read_b64_tr_b16 v[88:89], %0 offset:28672" :  : "v"(p.q_rd[1][3]) : "memory"); \
  asm volatile("v_mfma_f32_32x32x16_bf16 a[112:127], v[124:127], v[104:107], a[112:127]\n\tds_read_b64_tr_b16 v[90:91], %0 offset:29696" :  : "v"(p.q_rd[1][3]) : "memory"); \
  unsigned so406; \
  asm volatile("s_waitcnt lgkmcnt(13)\n\tv_mfma_f32_32x32x16_bf16 v[136:151], v[128:131], v[100:103], v[136:151]\n\tds_read_b64_tr_b16 v[92:93], %1 offset:28672\n\tds_read_b64_tr_b16 v[94:95], %1 offset:28928\n\ts_add_u32 m0, %2, 77824\n\ts_add_u32 %0, %5, 1232896\n\tbuffer_load_dwordx4 %3, %4, %0 offen offset:0 lds" : "=&s"(so406) : "v"(p.f_rd[1]), "s"(p.m01), "v"(p.v1), "s"(p.s1), "s"(p.so1) : "memory", "m0", "scc", ACL64); \
  asm volatile("v_mfma_f32_32x32x16_bf16 v[152:167], v[128:131], v[104:107], v[152:167]\n\tds_read_b128 v[72:75], %0 offset:28672" :  : "v"(p.xs_rd[1][0]) : "memory"); \
  asm volatile("s_waitcnt lgkmcnt(13)\n\tv_mfma_f32_32x32x16_bf16 v[168:183], v[132:135], v[108:111], v[168:183]\n\tds_read_b64_tr_b16 v[96:97], %0 offset:30720\n\tds_read_b64_tr_b16 v[98:99], %0 offset:30976" :  : "v"(p.f_rd[1]) : "memory"); \
  asm volatile("s_waitcnt vmcnt(7)\n\ts_barrier" ::: "memory"); \
  PJ_HOOK(12); \
  asm volatile("s_waitcnt lgkmcnt(12)\n\tv_mfma_f32_32x32x16_bf16 a[0:15], v[76:79], v[64:67], a[0:15]\n\tds_read_b64_tr_b16 v[112:113], %0 offset:32768" :  : "v"(p.q_rd[1][0]) : "memory"); \
  unsigned so407; \
  asm volatile("s_waitcnt lgkmcnt(12)\n\tv_mfma_f32_32x32x16_bf16 a[16:31], v[76:79], v[68:71], a[16:31]\n\tds_read_b64_tr_b16 v[114:115], %1 offset:33792\n\ts_add_u32 m0, %2, 106496\n\ts_add_u32 %0, %5, 960\n\tbuffer_load_dwordx4 %3, %4, %0 offen offset:0 sc1 lds" : "=&s"(so407) : "v"(p.q_rd[1][0]), "s"(p.m0x), "v"(p.vx), "s"(p.sx), "s"(p.sox) : "memory", "m0", "scc"); \
  asm volatile("s_waitcnt lgkmcnt(11)\n\tv_mfma_f32_32x32x16_bf16 a[32:47], v[80:83], v[64:67], a[32:47]\n\tds_read_b128 v[100:103], %0 offset:28672\n\tds_read_b128 v[104:107], %0 offset:30720" :  : "v"(p.x_rd[1][1]) : "memory"); \
  asm volatile("v_mfma_f32_32x32x16_bf16 a[48:63], v[80:83], v[68:71], a[48:63]\n\tds_read_b64_tr_b16 v[116:117], %0 offset:32768" :  : "v"(p.q_rd[1][1]) : "memory"); \
  unsigned so408; \
  asm volatile("s_waitcnt lgkmcnt(12)\n\tv_mfma_f32_32x32x16_bf16 a[64:79], v[84:87], v[64:67], a[64:79]\n\tds_read_b64_tr_b16 v[118:119], %1 offset:33792\n\ts_add_u32 m0, %2, 107520\n\ts_add_u32 %0, %5, 17344\n\tbuffer_load_dwordx4 %3, %4, %0 offen offset:0 sc1 lds" : "=&s"(so408) : "v"(p.q_rd[1][1]), "s"(p.m0x), "v"(p.vx), "s"(p.sx), "s"(p.sox) : "memory", "m0", "scc"); \
  asm volatile("v_mfma_f32_32x32x16_bf16 a[80:95], v[84:87], v[68:71], a[80:95]\n\tds_read_b64_tr_b16 v[120:121], %0 offset:32768\n\tds_read_b64_tr_b16 v[122:123], %0 offset:33792" :  : "v"(p.q_rd[1][2]) : "memory", ACL64); \
  asm volatile("s_waitcnt lgkmcnt(13)\n\tv_mfma_f32_32x32x16_bf16 a[96:111], v[88:91], v[64:67], a[96:111]\n\tds_read_b64_tr_b16 v[124:125], %0 offset:32768" :  : "v"(p.q_rd[1][3]) : "memory"); \
  unsigned so409; \
  asm volatile("v_mfma_f32_32x32x16_bf16 a[112:127], v[88:91], v[68:71], a[112:127]\n\tds_read_b64_tr_b16 v[126:127], %1 offset:33792\n\ts_add_u32 m0, %2, 108544\n\ts_add_u32 %0, %5, 33728\n\tbuffer_load_dwordx4 %3, %4, %0 offen offset:0 sc1 lds" : "=&s"(so409) : "v"(p.q_rd[1][3]), "s"(p.m0x), "v"(p.vx), "s"(p.sx), "s"(p.sox) : "memory", "m0", "scc"); \
  asm volatile("s_waitcnt lgkmcnt(13)\n\tv_mfma_f32_32x32x16_bf16 v[136:151], v[92:95], v[64:67], v[136:151]\n\tds_read_b64_tr_b16 v[128:129], %0 offset:29696\n\tds_read_b64_tr_b16 v[130:131], %0 offset:29952" :  : "v"(p.f_rd[1]) : "memory"); \
  asm volatile("v_mfma_f32_32x32x16_bf16 v[152:167], v[92:95], v[68:71], v[152:167]\n\tds_read_b128 v[108:111], %0 offset:28672" :  : "v"(p.xs_rd[1][1]) : "memory"); \
  unsigned so410; \
  asm volatile("s_waitcnt lgkmcnt(13)\n\tv_mfma_f32_32x32x16_bf16 v[168:183], v[96:99], v[72:75], v[168:183]\n\tds_read_b64_tr_b16 v[132:133], %1 offset:31744\n\tds_read_b64_tr_b16 v[134:135], %1 offset:32000\n\ts_add_u32 m0, %2, 109568\n\ts_add_u32 %0, %5, 50112\n\tbuffer_load_dwordx4 %3, %4, %0 offen offset:0 sc1 lds" : "=&s"(so410) : "v"(p.f_rd[1]), "s"(p.m0x), "v"(p.vx), "s"(p.sx), "s"(p.sox) : "memory", "m0", "scc"); \
  asm volatile("s_waitcnt lgkmcnt(12)\n\tv_mfma_f32_32x32x16_bf16 a[0:15], v[112:115], v[100:103], a[0:15]\n\tds_read_b64_tr_b16 v[76:77], %0 offset:0" :  : "v"(p.q_rd[0][0]) : "memory"); \
  asm volatile("s_waitcnt lgkmcnt(12)\n\tv_mfma_f32_32x32x16_bf16 a[16:31], v[112:115], v[104:107], a[16:31]\n\tds_read_b64_tr_b16 v[78:79], %0 offset:1024" :  : "v"(p.q_rd[0][0]) : "memory"); \
  unsigned so411; \
  asm volatile("s_waitcnt lgkmcnt(11)\n\tv_mfma_f32_32x32x16_bf16 a[32:47], v[116:119], v[100:103], a[32:47]\n\tds_read_b128 v[64:67], %1 offset:0\n\tds_read_b128 v[68:71], %1 offset:2048\n\ts_add_u32 m0, %2, 106496\n\ts_add_u32 %0, %5, 3932160\n\tbuffer_load_dwordx4 %3, %4, %0 offen offset:0 lds" : "=&s"(so411) : "v"(p.x_rd[0][0]), "s"(p.m0q), "v"(p.vq), "s"(p.sq), "s"(p.soq) : "memory", "m0", "scc", ACL64); \
  asm volatile("v_mfma_f32_32x32x16_bf16 a[48:63], v[116:119], v[104:107], a[48:63]\n\tds_read_b64_tr_b16 v[80:81], %0 offset:0" :  : "v"(p.q_rd[0][1]) : "memory"); \
  asm volatile("s_waitcnt lgkmcnt(12)\n\tv_mfma_f32_32x32x16_bf16 a[64:79], v[120:123], v[100:103], a[64:79]\n\tds_read_b64_tr_b16 v[82:83], %0 offset:1024" :  : "v"(p.q_rd[0][1]) : "memory"); \
  unsigned so412; \
  asm volatile("v_mfma_f32_32x32x16_bf16 a[80:95], v[120:123], v[104:107], a[80:95]\n\tds_read_b64_tr_b16 v[84:85], %1 offset:0\n\tds_read_b64_tr_b16 v[86:87], %1 offset:1024\n\ts_add_u32 m0, %2, 107520\n\ts_add_u32 %0, %5, 3964928\n\tbuffer_load_dwordx4 %3, %4, %0 offen offset:0 lds" : "=&s"(so412) : "v"(p.q_rd[0][2]), "s"(p.m0q), "v"(p.vq), "s"(p.sq), "s"(p.soq) : "memory", "m0", "scc"); \
  asm volatile("s_waitcnt lgkmcnt(13)\n\tv_mfma_f32_32x32x16_bf16 a[96:111], v[124:127], v[100:103], a[96:111]\n\tds_read_b64_tr_b16 v[88:89], %0 offset:0" :  : "v"(p.q_rd[0][3]) : "memory"); \
  asm volatile("v_mfma_f32_32x32x16_bf16 a[112:127], v[124:127], v[104:107], a[112:127]\n\tds_read_b64_tr_b16 v[90:91], %0 offset:1024" :  : "v"(p.q_rd[0][3]) : "memory"); \
  unsigned so413; \
  asm volatile("s_waitcnt lgkmcnt(13)\n\tv_mfma_f32_32x32x16_bf16 v[136:151], v[128:131], v[100:103], v[136:151]\n\tds_read_b64_tr_b16 v[92:93], %1 offset:0\n\tds_read_b64_tr_b16 v[94:95], %1 offset:256\n\ts_add_u32 m0, %2, 106496\n\ts_add_u32 %0, %5, 1320960\n\tbuffer_load_dwordx4 %3, %4, %0 offen offset:0 lds" : "=&s"(so413) : "v"(p.f_rd[0]), "s"(p.m01), "v"(p.v1), "s"(p.s1), "s"(p.so1) : "memory", "m0", "scc"); \
  asm volatile("v_mfma_f32_32x32x16_bf16 v[152:167], v[128:131], v[104:107], v[152:167]\n\tds_read_b128 v[72:75], %0 offset:0" :  : "v"(p.xs_rd[0][0]) : "memory"); \
  asm volatile("s_waitcnt lgkmcnt(13)\n\tv_mfma_f32_32x32x16_bf16 v[168:183], v[132:135], v[108:111], v[168:183]\n\tds_read_b64_tr_b16 v[96:97], %0 offset:2048\n\tds_read_b64_tr_b16 v[98:99], %0 offset:2304" :  : "v"(p.f_rd[0]) : "memory", ACL64); \
  asm volatile("s_waitcnt vmcnt(7)\n\ts_barrier" ::: "memory"); \
  PJ_HOOK(13); \
  asm volatile("s_waitcnt lgkmcnt(12)\n\tv_mfma_f32_32x32x16_bf16 a[0:15], v[76:79], v[64:67], a[0:15]\n\tds_read_b64_tr_b16 v[112:113], %0 offset:4096" :  : "v"(p.q_rd[0][0]) : "memory"); \
  asm volatile("s_waitcnt lgkmcnt(12)\n\tv_mfma_f32_32x32x16_bf16 a[16:31], v[76:79], v[68:71], a[16:31]\n\tds_read_b64_tr_b16 v[114:115], %0 offset:5120" :  : "v"(p.q_rd[0][0]) : "memory"); \
  asm volatile("s_waitcnt lgkmcnt(11)\n\tv_mfma_f32_32x32x16_bf16 a[32:47], v[80:83], v[64:67], a[32:47]\n\tds_read_b128 v[100:103], %0 offset:0\n\tds_read_b128 v[104:107], %0 offset:2048" :  : "v"(p.x_rd[0][1]) : "memory"); \
  asm volatile("v_mfma_f32_32x32x16_bf16 a[48:63], v[80:83], v[68:71], a[48:63]\n\tds_read_b64_tr_b16 v[116:117], %0 offset:4096" :  : "v"(p.q_rd[0][1]) : "memory"); \
  asm volatile("s_waitcnt lgkmcnt(12)\n\tv_mfma_f32_32x32x16_bf16 a[64:79], v[84:87], v[64:67], a[64:79]\n\tds_read_b64_tr_b16 v[118:119], %0 offset:5120" :  : "v"(p.q_rd[0][1]) : "memory"); \
  asm volatile("v_mfma_f32_32x32x16_bf16 a[80:95], v[84:87], v[68:71], a[80:95]\n\tds_read_b64_tr_b16 v[120:121], %0 offset:4096\n\tds_read_b64_tr_b16 v[122:123], %0 offset:5120" :  : "v"(p.q_rd[0][2]) : "memory"); \
  asm volatile("s_waitcnt lgkmcnt(13)\n\tv_mfma_f32_32x32x16_bf16 a[96:111], v[88:91], v[64:67], a[96:111]\n\tds_read_b64_tr_b16 v[124:125], %0 offset:4096" :  : "v"(p.q_rd[0][3]) : "memory"); \
  asm volatile("v_mfma_f32_32x32x16_bf16 a[112:127], v[88:91], v[68:71], a[112:127]\n\tds_read_b64_tr_b16 v[126:127], %0 offset:5120" :  : "v"(p.q_rd[0][3]) : "memory", ACL64); \
  asm volatile("s_waitcnt lgkmcnt(13)\n\tv_mfma_f32_32x32x16_bf16 v[136:151], v[92:95], v[64:67], v[136:151]\n\tds_read_b64_tr_b16 v[128:129], %0 offset:1024\n\tds_read_b64_tr_b16 v[130:131], %0 offset:1280" :  : "v"(p.f_rd[0]) : "memory"); \
  asm volatile("v_mfma_f32_32x32x16_bf16 v[152:167], v[92:95], v[68:71], v[152:167]\n\tds_read_b128 v[108:111], %0 offset:0" :  : "v"(p.xs_rd[0][1]) : "memory"); \
  asm volatile("s_waitcnt lgkmcnt(13)\n\tv_mfma_f32_32x32x16_bf16 v[168:183], v[96:99], v[72:75], v[168:183]\n\tds_read_b64_tr_b16 v[132:133], %0 offset:3072\n\tds_read_b64_tr_b16 v[134:135], %0 offset:3328" :  : "v"(p.f_rd[0]) : "memory"); \
  asm volatile("s_waitcnt lgkmcnt(12)\n\tv_mfma_f32_32x32x16_bf16 a[0:15], v[112:115], v[100:103], a[0:15]\n\tds_read_b64_tr_b16 v[76:77], %0 offset:28672" :  : "v"(p.q_rd[0][0]) : "memory"); \
  asm volatile("s_waitcnt lgkmcnt(12)\n\tv_mfma_f32_32x32x16_bf16 a[16:31], v[112:115], v[104:107], a[16:31]\n\tds_read_b64_tr_b16 v[78:79], %0 offset:29696" :  : "v"(p.q_rd[0][0]) : "memory"); \
  asm volatile("s_waitcnt lgkmcnt(11)\n\tv_mfma_f32_32x32x16_bf16 a[32:47], v[116:119], v[100:103], a[32:47]\n\tds_read_b128 v[64:67], %0 offset:28672\n\tds_read_b128 v[68:71], %0 offset:30720" :  : "v"(p.x_rd[0][0]) : "memory"); \
  asm volatile("v_mfma_f32_32x32x16_bf16 a[48:63], v[116:119], v[104:107], a[48:63]\n\tds_read_b64_tr_b16 v[80:81], %0 offset:28672" :  : "v"(p.q_rd[0][1]) : "memory"); \
  asm volatile("s_waitcnt lgkmcnt(12)\n\tv_mfma_f32_32x32x16_bf16 a[64:79], v[120:123], v[100:103], a[64:79]\n\tds_read_b64_tr_b16 v[82:83], %0 offset:29696" :  : "v"(p.q_rd[0][1]) : "memory", ACL64); \
  asm volatile("v_mfma_f32_32x32x16_bf16 a[80:95], v[120:123], v[104:107], a[80:95]\n\tds_read_b64_tr_b16 v[84:85], %0 offset:28672\n\tds_read_b64_tr_b16 v[86:87], %0 offset:29696" :  : "v"(p.q_rd[0][2]) : "memory"); \
  asm volatile("s_waitcnt lgkmcnt(13)\n\tv_mfma_f32_32x32x16_bf16 a[96:111], v[124:127], v[100:103], a[96:111]\n\tds_read_b64_tr_b16 v[88:89], %0 offset:28672" :  : "v"(p.q_rd[0][3]) : "memory"); \
  asm volatile("v_mfma_f32_32x32x16_bf16 a[112:127], v[124:127], v[104:107], a[112:127]\n\tds_read_b64_tr_b16 v[90:91], %0 offset:29696" :  : "v"(p.q_rd[0][3]) : "memory"); \
  asm volatile("s_waitcnt lgkmcnt(13)\n\tv_mfma_f32_32x32x16_bf16 v[136:151], v[128:131], v[100:103], v[136:151]\n\tds_read_b64_tr_b16 v[92:93], %0 offset:28672\n\tds_read_b64_tr_b16 v[94:95], %0 offset:28928" :  : "v"(p.f_rd[0]) : "memory"); \
  asm volatile("v_mfma_f32_32x32x16_bf16 v[152:167], v[128:131], v[104:107], v[152:167]\n\tds_read_b128 v[72:75], %0 offset:28672" :  : "v"(p.xs_rd[0][0]) : "memory"); \
  asm volatile("s_waitcnt lgkmcnt(13)\n\tv_mfma_f32_32x32x16_bf16 v[168:183], v[132:135], v[108:111], v[168:183]\n\tds_read_b64_tr_b16 v[96:97], %0 offset:30720\n\tds_read_b64_tr_b16 v[98:99], %0 offset:30976" :  : "v"(p.f_rd[0]) : "memory"); \
  asm volatile("s_waitcnt vmcnt(0)\n\ts_barrier" ::: "memory"); \
  PJ_HOOK(14); \
  asm volatile("s_waitcnt lgkmcnt(12)\n\tv_mfma_f32_32x32x16_bf16 a[0:15], v[76:79], v[64:67], a[0:15]\n\tds_read_b64_tr_b16 v[112:113], %0 offset:32768" :  : "v"(p.q_rd[0][0]) : "memory"); \
  asm volatile("s_waitcnt lgkmcnt(12)\n\tv_mfma_f32_32x32x16_bf16 a[16:31], v[76:79], v[68:71], a[16:31]\n\tds_read_b64_tr_b16 v[114:115], %0 offset:33792" :  : "v"(p.q_rd[0][0]) : "memory", ACL64); \
  asm volatile("s_waitcnt lgkmcnt(11)\n\tv_mfma_f32_32x32x16_bf16 a[32:47], v[80:83], v[64:67], a[32:47]\n\tds_read_b128 v[100:103], %0 offset:28672\n\tds_read_b128 v[104:107], %0 offset:30720" :  : "v"(p.x_rd[0][1]) : "memory"); \
  asm volatile("v_mfma_f32_32x32x16_bf16 a[48:63], v[80:83], v[68:71], a[48:63]\n\tds_read_b64_tr_b16 v[116:117], %0 offset:32768" :  : "v"(p.q_rd[0][1]) : "memory"); \
  asm volatile("s_waitcnt lgkmcnt(12)\n\tv_mfma_f32_32x32x16_bf16 a[64:79], v[84:87], v[64:67], a[64:79]\n\tds_read_b64_tr_b16 v[118:119], %0 offset:33792" :  : "v"(p.q_rd[0][1]) : "memory"); \
  asm volatile("v_mfma_f32_32x32x16_bf16 a[80:95], v[84:87], v[68:71], a[80:95]\n\tds_read_b64_tr_b16 v[120:121], %0 offset:32768\n\tds_read_b64_tr_b16 v[122:123], %0 offset:33792" :  : "v"(p.q_rd[0][2]) : "memory"); \
  asm volatile("s_waitcnt lgkmcnt(13)\n\tv_mfma_f32_32x32x16_bf16 a[96:111], v[88:91], v[64:67], a[96:111]\n\tds_read_b64_tr_b16 v[124:125], %0 offset:32768" :  : "v"(p.q_rd[0][3]) : "memory"); \
  asm volatile("v_mfma_f32_32x32x16_bf16 a[112:127], v[88:91], v[68:71], a[112:127]\n\tds_read_b64_tr_b16 v[126:127], %0 offset:33792" :  : "v"(p.q_rd[0][3]) : "memory"); \
  asm volatile("s_waitcnt lgkmcnt(13)\n\tv_mfma_f32_32x32x16_bf16 v[136:151], v[92:95], v[64:67], v[136:151]\n\tds_read_b64_tr_b16 v[128:129], %0 offset:29696\n\tds_read_b64_tr_b16 v[130:131], %0 offset:29952" :  : "v"(p.f_rd[0]) : "memory"); \
  asm volatile("v_mfma_f32_32x32x16_bf16 v[152:167], v[92:95], v[68:71], v[152:167]\n\tds_read_b128 v[108:111], %0 offset:28672" :  : "v"(p.xs_rd[0][1]) : "memory", ACL64); \
  asm volatile("s_waitcnt lgkmcnt(13)\n\tv_mfma_f32_32x32x16_bf16 v[168:183], v[96:99], v[72:75], v[168:183]\n\tds_read_b64_tr_b16 v[132:133], %0 offset:31744\n\tds_read_b64_tr_b16 v[134:135], %0 offset:32000" :  : "v"(p.f_rd[0]) : "memory"); \
  asm volatile("s_waitcnt lgkmcnt(12)\n\tv_mfma_f32_32x32x16_bf16 a[0:15], v[112:115], v[100:103], a[0:15]\n\tds_read_b64_tr_b16 v[76:77], %0 offset:0" :  : "v"(p.q_rd[1][0]) : "memory"); \
  asm volatile("s_waitcnt lgkmcnt(12)\n\tv_mfma_f32_32x32x16_bf16 a[16:31], v[112:115], v[104:107], a[16:31]\n\tds_read_b64_tr_b16 v[78:79], %0 offset:1024" :  : "v"(p.q_rd[1][0]) : "memory"); \
  asm volatile("s_waitcnt lgkmcnt(11)\n\tv_mfma_f32_32x32x16_bf16 a[32:47], v[116:119], v[100:103], a[32:47]\n\tds_read_b128 v[64:67], %0 offset:0\n\tds_read_b128 v[68:71], %0 offset:2048" :  : "v"(p.x_rd[1][0]) : "memory"); \
  asm volatile("v_mfma_f32_32x32x16_bf16 a[48:63], v[116:119], v[104:107], a[48:63]\n\tds_read_b64_tr_b16 v[80:81], %0 offset:0" :  : "v"(p.q_rd[1][1]) : "memory"); \
  asm volatile("s_waitcnt lgkmcnt(12)\n\tv_mfma_f32_32x32x16_bf16 a[64:79], v[120:123], v[100:103], a[64:79]\n\tds_read_b64_tr_b16 v[82:83], %0 offset:1024" :  : "v"(p.q_rd[1][1]) : "memory"); \
  asm volatile("v_mfma_f32_32x32x16_bf16 a[80:95], v[120:123], v[104:107], a[80:95]\n\tds_read_b64_tr_b16 v[84:85], %0 offset:0\n\tds_read_b64_tr_b16 v[86:87], %0 offset:1024" :  : "v"(p.q_rd[1][2]) : "memory"); \
  asm volatile("s_waitcnt lgkmcnt(13)\n\tv_mfma_f32_32x32x16_bf16 a[96:111], v[124:127], v[100:103], a[96:111]\n\tds_read_b64_tr_b16 v[88:89], %0 offset:0" :  : "v"(p.q_rd[1][3]) : "memory", ACL64); \
  asm volatile("v_mfma_f32_32x32x16_bf16 a[112:127], v[124:127], v[104:107], a[112:127]\n\tds_read_b64_tr_b16 v[90:91], %0 offset:1024" :  : "v"(p.q_rd[1][3]) : "memory"); \
  asm volatile("s_waitcnt lgkmcnt(13)\n\tv_mfma_f32_32x32x16_bf16 v[136:151], v[128:131], v[100:103], v[136:151]\n\tds_read_b64_tr_b16 v[92:93], %0 offset:0\n\tds_read_b64_tr_b16 v[94:95], %0 offset:256" :  : "v"(p.f_rd[1]) : "memory"); \
  asm volatile("v_mfma_f32_32x32x16_bf16 v[152:167], v[128:131], v[104:107], v[152:167]\n\tds_read_b128 v[72:75], %0 offset:0" :  : "v"(p.xs_rd[1][0]) : "memory"); \
  asm volatile("s_waitcnt lgkmcnt(13)\n\tv_mfma_f32_32x32x16_bf16 v[168:183], v[132:135], v[108:111], v[168:183]\n\tds_read_b64_tr_b16 v[96:97], %0 offset:2048\n\tds_read_b64_tr_b16 v[98:99], %0 offset:2304" :  : "v"(p.f_rd[1]) : "memory"); \
  asm volatile("s_waitcnt vmcnt(0)\n\ts_barrier" ::: "memory"); \
  PJ_HOOK(15); \
  asm volatile("s_waitcnt lgkmcnt(12)\n\tv_mfma_f32_32x32x16_bf16 a[0:15], v[76:79], v[64:67], a[0:15]\n\tds_read_b64_tr_b16 v[112:113], %0 offset:4096" :  : "v"(p.q_rd[1][0]) : "memory"); \
  asm volatile("s_waitcnt lgkmcnt(12)\n\tv_mfma_f32_32x32x16_bf16 a[16:31], v[76:79], v[68:71], a[16:31]\n\tds_read_b64_tr_b16 v[114:115], %0 offset:5120" :  : "v"(p.q_rd[1][0]) : "memory"); \
  asm volatile("s_waitcnt lgkmcnt(11)\n\tv_mfma_f32_32x32x16_bf16 a[32:47], v[80:83], v[64:67], a[32:47]\n\tds_read_b128 v[100:103], %0 offset:0\n\tds_read_b128 v[104:107], %0 offset:2048" :  : "v"(p.x_rd[1][1]) : "memory"); \
  asm volatile("v_mfma_f32_32x32x16_bf16 a[48:63], v[80:83], v[68:71], a[48:63]\n\tds_read_b64_tr_b16 v[116:117], %0 offset:4096" :  : "v"(p.q_rd[1][1]) : "memory", ACL64); \
  asm volatile("s_waitcnt lgkmcnt(12)\n\tv_mfma_f32_32x32x16_bf16 a[64:79], v[84:87], v[64:67], a[64:79]\n\tds_read_b64_tr_b16 v[118:119], %0 offset:5120" :  : "v"(p.q_rd[1][1]) : "memory"); \
  asm volatile("v_mfma_f32_32x32x16_bf16 a[80:95], v[84:87], v[68:71], a[80:95]\n\tds_read_b64_tr_b16 v[120:121], %0 offset:4096\n\tds_read_b64_tr_b16 v[122:123], %0 offset:5120" :  : "v"(p.q_rd[1][2]) : "memory"); \
  asm volatile("s_waitcnt lgkmcnt(13)\n\tv_mfma_f32_32x32x16_bf16 a[96:111], v[88:91], v[64:67], a[96:111]\n\tds_read_b64_tr_b16 v[124:125], %0 offset:4096" :  : "v"(p.q_rd[1][3]) : "memory"); \
  asm volatile("v_mfma_f32_32x32x16_bf16 a[112:127], v[88:91], v[68:71], a[112:127]\n\tds_read_b64_tr_b16 v[126:127], %0 offset:5120" :  : "v"(p.q_rd[1][3]) : "memory"); \
  asm volatile("s_waitcnt lgkmcnt(13)\n\tv_mfma_f32_32x32x16_bf16 v[136:151], v[92:95], v[64:67], v[136:151]\n\tds_read_b64_tr_b16 v[128:129], %0 offset:1024\n\tds_read_b64_tr_b16 v[130:131], %0 offset:1280" :  : "v"(p.f_rd[1]) : "memory"); \
  asm volatile("v_mfma_f32_32x32x16_bf16 v[152:167], v[92:95], v[68:71], v[152:167]\n\tds_read_b128 v[108:111], %0 offset:0" :  : "v"(p.xs_rd[1][1]) : "memory"); \
  asm volatile("s_waitcnt lgkmcnt(13)\n\tv_mfma_f32_32x32x16_bf16 v[168:183], v[96:99], v[72:75], v[168:183]\n\tds_read_b64_tr_b16 v[132:133], %0 offset:3072\n\tds_read_b64_tr_b16 v[134:135], %0 offset:3328" :  : "v"(p.f_rd[1]) : "memory"); \
  asm volatile("s_waitcnt lgkmcnt(12)\n\tv_mfma_f32_32x32x16_bf16 a[0:15], v[112:115], v[100:103], a[0:15]" :  :  : "memory", ACL64); \
  asm volatile("s_waitcnt lgkmcnt(11)\n\tv_mfma_f32_32x32x16_bf16 a[16:31], v[112:115], v[104:107], a[16:31]" :  :  : "memory"); \
  asm volatile("s_waitcnt lgkmcnt(9)\n\tv_mfma_f32_32x32x16_bf16 a[32:47], v[116:119], v[100:103], a[32:47]" :  :  : "memory"); \
  asm volatile("v_mfma_f32_32x32x16_bf16 a[48:63], v[116:119], v[104:107], a[48:63]" :  :  : "memory"); \
  asm volatile("s_waitcnt lgkmcnt(7)\n\tv_mfma_f32_32x32x16_bf16 a[64:79], v[120:123], v[100:103], a[64:79]" :  :  : "memory"); \
  asm volatile("v_mfma_f32_32x32x16_bf16 a[80:95], v[120:123], v[104:107], a[80:95]" :  :  : "memory"); \
  asm volatile("s_waitcnt lgkmcnt(5)\n\tv_mfma_f32_32x32x16_bf16 a[96:111], v[124:127], v[100:103], a[96:111]" :  :  : "memory"); \
  asm volatile("v_mfma_f32_32x32x16_bf16 a[112:127], v[124:127], v[104:107], a[112:127]" :  :  : "memory"); \
  asm volatile("s_waitcnt lgkmcnt(3)\n\tv_mfma_f32_32x32x16_bf16 v[136:151], v[128:131], v[100:103], v[136:151]" :  :  : "memory", ACL64); \
  asm volatile("v_mfma_f32_32x32x16_bf16 v[152:167], v[128:131], v[104:107], v[152:167]" :  :  : "memory"); \
  asm volatile("s_waitcnt lgkmcnt(0)\n\tv_mfma_f32_32x32x16_bf16 v[168:183], v[132:135], v[108:111], v[168:183]" :  :  : "memory"); \
} while (0)
DI void q_pack(const f32x2 sc2) {
  asm volatile("v_mov_b32 v252, 0\n\tv_mov_b32 v253, 0\n\tv_mov_b32 v254, 0\n\tv_mov_b32 v255, 0" :  :  : "memory");
  asm volatile("v_accvgpr_read_b32 v64, a0\n\tv_accvgpr_read_b32 v65, a1\n\tv_accvgpr_read_b32 v66, a2\n\tv_accvgpr_read_b32 v67, a3\n\tv_accvgpr_read_b32 v68, a4\n\tv_accvgpr_read_b32 v69, a5\n\tv_accvgpr_read_b32 v70, a6\n\tv_accvgpr_read_b32 v71, a7\n\tv_accvgpr_read_b32 v72, a8\n\tv_accvgpr_read_b32 v73, a9\n\tv_accvgpr_read_b32 v74, a10\n\tv_accvgpr_read_b32 v75, a11\n\tv_accvgpr_read_b32 v76, a12\n\tv_accvgpr_read_b32 v77, a13\n\tv_accvgpr_read_b32 v78, a14\n\tv_accvgpr_read_b32 v79, a15\n\tv_mfma_f32_32x32x16_bf16 a[0:15], v[252:255], v[252:255], 0\n\tv_pk_mul_f32 v[64:65], v[64:65], %0\n\tv_pk_mul_f32 v[66:67], v[66:67], %0\n\tv_pk_mul_f32 v[68:69], v[68:69], %0\n\tv_pk_mul_f32 v[70:71], v[70:71], %0\n\tv_pk_mul_f32 v[72:73], v[72:73], %0\n\tv_pk_mul_f32 v[74:75], v[74:75], %0\n\tv_pk_mul_f32 v[76:77], v[76:77], %0\n\tv_pk_mul_f32 v[78:79], v[78:79], %0\n\tv_cvt_pk_bf16_f32 v64, v64, v65\n\tv_cvt_pk_bf16_f32 v65, v66, v67\n\tv_cvt_pk_bf16_f32 v66, v68, v69\n\tv_cvt_pk_bf16_f32 v67, v70, v71\n\tv_cvt_pk_bf16_f32 v68, v72, v73\n\tv_cvt_pk_bf16_f32 v69, v74, v75\n\tv_cvt_pk_bf16_f32 v70, v76, v77\n\tv_cvt_pk_bf16_f32 v71, v78, v79\n\tv_accvgpr_write_b32 a128, v64\n\tv_accvgpr_write_b32 a129, v65\n\tv_accvgpr_write_b32 a130, v66\n\tv_accvgpr_write_b32 a131, v67\n\tv_accvgpr_write_b32 a132, v68\n\tv_accvgpr_write_b32 a133, v69\n\tv_accvgpr_write_b32 a134, v70\n\tv_accvgpr_write_b32 a135, v71" :  : "v"(sc2) : "memory");
  asm volatile("v_accvgpr_read_b32 v80, a16\n\tv_accvgpr_read_b32 v81, a17\n\tv_accvgpr_read_b32 v82, a18\n\tv_accvgpr_read_b32 v83, a19\n\tv_accvgpr_read_b32 v84, a20\n\tv_accvgpr_read_b32 v85, a21\n\tv_accvgpr_read_b32 v86, a22\n\tv_accvgpr_read_b32 v87, a23\n\tv_accvgpr_read_b32 v88, a24\n\tv_accvgpr_read_b32 v89, a25\n\tv_accvgpr_read_b32 v90, a26\n\tv_accvgpr_read_b32 v91, a27\n\tv_accvgpr_read_b32 v92, a28\n\tv_accvgpr_read_b32 v93, a29\n\tv_accvgpr_read_b32 v94, a30\n\tv_accvgpr_read_b32 v95, a31\n\tv_mfma_f32_32x32x16_bf16 a[16:31], v[252:255], v[252:255], 0\n\tv_pk_mul_f32 v[80:81], v[80:81], %0\n\tv_pk_mul_f32 v[82:83], v[82:83], %0\n\tv_pk_mul_f32 v[84:85], v[84:85], %0\n\tv_pk_mul_f32 v[86:87], v[86:87], %0\n\tv_pk_mul_f32 v[88:89], v[88:89], %0\n\tv_pk_mul_f32 v[90:91], v[90:91], %0\n\tv_pk_mul_f32 v[92:93], v[92:93], %0\n\tv_pk_mul_f32 v[94:95], v[94:95], %0\n\tv_cvt_pk_bf16_f32 v80, v80, v81\n\tv_cvt_pk_bf16_f32 v81, v82, v83\n\tv_cvt_pk_bf16_f32 v82, v84, v85\n\tv_cvt_pk_bf16_f32 v83, v86, v87\n\tv_cvt_pk_bf16_f32 v84, v88, v89\n\tv_cvt_pk_bf16_f32 v85, v90, v91\n\tv_cvt_pk_bf16_f32 v86, v92, v93\n\tv_cvt_pk_bf16_f32 v87, v94, v95\n\tv_accvgpr_write_b32 a160, v80\n\tv_accvgpr_write_b32 a161, v81\n\tv_accvgpr_write_b32 a162, v82\n\tv_accvgpr_write_b32 a163, v83\n\tv_accvgpr_write_b32 a164, v84\n\tv_accvgpr_write_b32 a165, v85\n\tv_accvgpr_write_b32 a166, v86\n\tv_accvgpr_write_b32 a167, v87" :  : "v"(sc2) : "memory");
  asm volatile("v_accvgpr_read_b32 v64, a32\n\tv_accvgpr_read_b32 v65, a33\n\tv_accvgpr_read_b32 v66, a34\n\tv_accvgpr_read_b32 v67, a35\n\tv_accvgpr_read_b32 v68, a36\n\tv_accvgpr_read_b32 v69, a37\n\tv_accvgpr_read_b32 v70, a38\n\tv_accvgpr_read_b32 v71, a39\n\tv_accvgpr_read_b32 v72, a40\n\tv_accvgpr_read_b32 v73, a41\n\tv_accvgpr_read_b32 v74, a42\n\tv_accvgpr_read_b32 v75, a43\n\tv_accvgpr_read_b32 v76, a44\n\tv_accvgpr_read_b32 v77, a45\n\tv_accvgpr_read_b32 v78, a46\n\tv_accvgpr_read_b32 v79, a47\n\tv_mfma_f32_32x32x16_bf16 a[32:47], v[252:255], v[252:255], 0\n\tv_pk_mul_f32 v[64:65], v[64:65], %0\n\tv_pk_mul_f32 v[66:67], v[66:67], %0\n\tv_pk_mul_f32 v[68:69], v[68:69], %0\n\tv_pk_mul_f32 v[70:71], v[70:71], %0\n\tv_pk_mul_f32 v[72:73], v[72:73], %0\n\tv_pk_mul_f32 v[74:75], v[74:75], %0\n\tv_pk_mul_f32 v[76:77], v[76:77], %0\n\tv_pk_mul_f32 v[78:79], v[78:79], %0\n\tv_cvt_pk_bf16_f32 v64, v64, v65\n\tv_cvt_pk_bf16_f32 v65, v66, v67\n\tv_cvt_pk_bf16_f32 v66, v68, v69\n\tv_cvt_pk_bf16_f32 v67, v70, v71\n\tv_cvt_pk_bf16_f32 v68, v72, v73\n\tv_cvt_pk_bf16_f32 v69, v74, v75\n\tv_cvt_pk_bf16_f32 v70, v76, v77\n\tv_cvt_pk_bf16_f32 v71, v78, v79\n\tv_accvgpr_write_b32 a136, v64\n\tv_accvgpr_write_b32 a137, v65\n\tv_accvgpr_write_b32 a138, v66\n\tv_accvgpr_write_b32 a139, v67\n\tv_accvgpr_write_b32 a140, v68\n\tv_accvgpr_write_b32 a141, v69\n\tv_accvgpr_write_b32 a142, v70\n\tv_accvgpr_write_b32 a143, v71" :  : "v"(sc2) : "memory");
  asm volatile("v_accvgpr_read_b32 v80, a48\n\tv_accvgpr_read_b32 v81, a49\n\tv_accvgpr_read_b32 v82, a50\n\tv_accvgpr_read_b32 v83, a51\n\tv_accvgpr_read_b32 v84, a52\n\tv_accvgpr_read_b32 v85, a53\n\tv_accvgpr_read_b32 v86, a54\n\tv_accvgpr_read_b32 v87, a55\n\tv_accvgpr_read_b32 v88, a56\n\tv_accvgpr_read_b32 v89, a57\n\tv_accvgpr_read_b32 v90, a58\n\tv_accvgpr_read_b32 v91, a59\n\tv_accvgpr_read_b32 v92, a60\n\tv_accvgpr_read_b32 v93, a61\n\tv_accvgpr_read_b32 v94, a62\n\tv_accvgpr_read_b32 v95, a63\n\tv_mfma_f32_32x32x16_bf16 a[48:63], v[252:255], v[252:255], 0\n\tv_pk_mul_f32 v[80:81], v[80:81], %0\n\tv_pk_mul_f32 v[82:83], v[82:83], %0\n\tv_pk_mul_f32 v[84:85], v[84:85], %0\n\tv_pk_mul_f32 v[86:87], v[86:87], %0\n\tv_pk_mul_f32 v[88:89], v[88:89], %0\n\tv_pk_mul_f32 v[90:91], v[90:91], %0\n\tv_pk_mul_f32 v[92:93], v[92:93], %0\n\tv_pk_mul_f32 v[94:95], v[94:95], %0\n\tv_cvt_pk_bf16_f32 v80, v80, v81\n\tv_cvt_pk_bf16_f32 v81, v82, v83\n\tv_cvt_pk_bf16_f32 v82, v84, v85\n\tv_cvt_pk_bf16_f32 v83, v86, v87\n\tv_cvt_pk_bf16_f32 v84, v88, v89\n\tv_cvt_pk_bf16_f32 v85, v90, v91\n\tv_cvt_pk_bf16_f32 v86, v92, v93\n\tv_cvt_pk_bf16_f32 v87, v94, v95\n\tv_accvgpr_write_b32 a168, v80\n\tv_accvgpr_write_b32 a169, v81\n\tv_accvgpr_write_b32 a170, v82\n\tv_accvgpr_write_b32 a171, v83\n\tv_accvgpr_write_b32 a172, v84\n\tv_accvgpr_write_b32 a173, v85\n\tv_accvgpr_write_b32 a174, v86\n\tv_accvgpr_write_b32 a175, v87" :  : "v"(sc2) : "memory");
  asm volatile("v_accvgpr_read_b32 v64, a64\n\tv_accvgpr_read_b32 v65, a65\n\tv_accvgpr_read_b32 v66, a66\n\tv_accvgpr_read_b32 v67, a67\n\tv_accvgpr_read_b32 v68, a68\n\tv_accvgpr_read_b32 v69, a69\n\tv_accvgpr_read_b32 v70, a70\n\tv_accvgpr_read_b32 v71, a71\n\tv_accvgpr_read_b32 v72, a72\n\tv_accvgpr_read_b32 v73, a73\n\tv_accvgpr_read_b32 v74, a74\n\tv_accvgpr_read_b32 v75, a75\n\tv_accvgpr_read_b32 v76, a76\n\tv_accvgpr_read_b32 v77, a77\n\tv_accvgpr_read_b32 v78, a78\n\tv_accvgpr_read_b32 v79, a79\n\tv_mfma_f32_32x32x16_bf16 a[64:79], v[252:255], v[252:255], 0\n\tv_pk_mul_f32 v[64:65], v[64:65], %0\n\tv_pk_mul_f32 v[66:67], v[66:67], %0\n\tv_pk_mul_f32 v[68:69], v[68:69], %0\n\tv_pk_mul_f32 v[70:71], v[70:71], %0\n\tv_pk_mul_f32 v[72:73], v[72:73], %0\n\tv_pk_mul_f32 v[74:75], v[74:75], %0\n\tv_pk_mul_f32 v[76:77], v[76:77], %0\n\tv_pk_mul_f32 v[78:79], v[78:79], %0\n\tv_cvt_pk_bf16_f32 v64, v64, v65\n\tv_cvt_pk_bf16_f32 v65, v66, v67\n\tv_cvt_pk_bf16_f32 v66, v68, v69\n\tv_cvt_pk_bf16_f32 v67, v70, v71\n\tv_cvt_pk_bf16_f32 v68, v72, v73\n\tv_cvt_pk_bf16_f32 v69, v74, v75\n\tv_cvt_pk_bf16_f32 v70, v76, v77\n\tv_cvt_pk_bf16_f32 v71, v78, v79\n\tv_accvgpr_write_b32 a144, v64\n\tv_accvgpr_write_b32 a145, v65\n\tv_accvgpr_write_b32 a146, v66\n\tv_accvgpr_write_b32 a147, v67\n\tv_accvgpr_write_b32 a148, v68\n\tv_accvgpr_write_b32 a149, v69\n\tv_accvgpr_write_b32 a150, v70\n\tv_accvgpr_write_b32 a151, v71" :  : "v"(sc2) : "memory", ACL64);
  asm volatile("v_accvgpr_read_b32 v80, a80\n\tv_accvgpr_read_b32 v81, a81\n\tv_accvgpr_read_b32 v82, a82\n\tv_accvgpr_read_b32 v83, a83\n\tv_accvgpr_read_b32 v84, a84\n\tv_accvgpr_read_b32 v85, a85\n\tv_accvgpr_read_b32 v86, a86\n\tv_accvgpr_read_b32 v87, a87\n\tv_accvgpr_read_b32 v88, a88\n\tv_accvgpr_read_b32 v89, a89\n\tv_accvgpr_read_b32 v90, a90\n\tv_accvgpr_read_b32 v91, a91\n\tv_accvgpr_read_b32 v92, a92\n\tv_accvgpr_read_b32 v93, a93\n\tv_accvgpr_read_b32 v94, a94\n\tv_accvgpr_read_b32 v95, a95\n\tv_mfma_f32_32x32x16_bf16 a[80:95], v[252:255], v[252:255], 0\n\tv_pk_mul_f32 v[80:81], v[80:81], %0\n\tv_pk_mul_f32 v[82:83], v[82:83], %0\n\tv_pk_mul_f32 v[84:85], v[84:85], %0\n\tv_pk_mul_f32 v[86:87], v[86:87], %0\n\tv_pk_mul_f32 v[88:89], v[88:89], %0\n\tv_pk_mul_f32 v[90:91], v[90:91], %0\n\tv_pk_mul_f32 v[92:93], v[92:93], %0\n\tv_pk_mul_f32 v[94:95], v[94:95], %0\n\tv_cvt_pk_bf16_f32 v80, v80, v81\n\tv_cvt_pk_bf16_f32 v81, v82, v83\n\tv_cvt_pk_bf16_f32 v82, v84, v85\n\tv_cvt_pk_bf16_f32 v83, v86, v87\n\tv_cvt_pk_bf16_f32 v84, v88, v89\n\tv_cvt_pk_bf16_f32 v85, v90, v91\n\tv_cvt_pk_bf16_f32 v86, v92, v93\n\tv_cvt_pk_bf16_f32 v87, v94, v95\n\tv_accvgpr_write_b32 a176, v80\n\tv_accvgpr_write_b32 a177, v81\n\tv_accvgpr_write_b32 a178, v82\n\tv_accvgpr_write_b32 a179, v83\n\tv_accvgpr_write_b32 a180, v84\n\tv_accvgpr_write_b32 a181, v85\n\tv_accvgpr_write_b32 a182, v86\n\tv_accvgpr_write_b32 a183, v87" :  : "v"(sc2) : "memory");
  asm volatile("v_accvgpr_read_b32 v64, a96\n\tv_accvgpr_read_b32 v65, a97\n\tv_accvgpr_read_b32 v66, a98\n\tv_accvgpr_read_b32 v67, a99\n\tv_accvgpr_read_b32 v68, a100\n\tv_accvgpr_read_b32 v69, a101\n\tv_accvgpr_read_b32 v70, a102\n\tv_accvgpr_read_b32 v71, a103\n\tv_accvgpr_read_b32 v72, a104\n\tv_accvgpr_read_b32 v73, a105\n\tv_accvgpr_read_b32 v74, a106\n\tv_accvgpr_read_b32 v75, a107\n\tv_accvgpr_read_b32 v76, a108\n\tv_accvgpr_read_b32 v77, a109\n\tv_accvgpr_read_b32 v78, a110\n\tv_accvgpr_read_b32 v79, a111\n\tv_mfma_f32_32x32x16_bf16 a[96:111], v[252:255], v[252:255], 0\n\tv_pk_mul_f32 v[64:65], v[64:65], %0\n\tv_pk_mul_f32 v[66:67], v[66:67], %0\n\tv_pk_mul_f32 v[68:69], v[68:69], %0\n\tv_pk_mul_f32 v[70:71], v[70:71], %0\n\tv_pk_mul_f32 v[72:73], v[72:73], %0\n\tv_pk_mul_f32 v[74:75], v[74:75], %0\n\tv_pk_mul_f32 v[76:77], v[76:77], %0\n\tv_pk_mul_f32 v[78:79], v[78:79], %0\n\tv_cvt_pk_bf16_f32 v64, v64, v65\n\tv_cvt_pk_bf16_f32 v65, v66, v67\n\tv_cvt_pk_bf16_f32 v66, v68, v69\n\tv_cvt_pk_bf16_f32 v67, v70, v71\n\tv_cvt_pk_bf16_f32 v68, v72, v73\n\tv_cvt_pk_bf16_f32 v69, v74, v75\n\tv_cvt_pk_bf16_f32 v70, v76, v77\n\tv_cvt_pk_bf16_f32 v71, v78, v79\n\tv_accvgpr_write_b32 a152, v64\n\tv_accvgpr_write_b32 a153, v65\n\tv_accvgpr_write_b32 a154, v66\n\tv_accvgpr_write_b32 a155, v67\n\tv_accvgpr_write_b32 a156, v68\n\tv_accvgpr_write_b32 a157, v69\n\tv_accvgpr_write_b32 a158, v70\n\tv_accvgpr_write_b32 a159, v71" :  : "v"(sc2) : "memory");
  asm volatile("v_accvgpr_read_b32 v80, a112\n\tv_accvgpr_read_b32 v81, a113\n\tv_accvgpr_read_b32 v82, a114\n\tv_accvgpr_read_b32 v83, a115\n\tv_accvgpr_read_b32 v84, a116\n\tv_accvgpr_read_b32 v85, a117\n\tv_accvgpr_read_b32 v86, a118\n\tv_accvgpr_read_b32 v87, a119\n\tv_accvgpr_read_b32 v88, a120\n\tv_accvgpr_read_b32 v89, a121\n\tv_accvgpr_read_b32 v90, a122\n\tv_accvgpr_read_b32 v91, a123\n\tv_accvgpr_read_b32 v92, a124\n\tv_accvgpr_read_b32 v93, a125\n\tv_accvgpr_read_b32 v94, a126\n\tv_accvgpr_read_b32 v95, a127\n\tv_mfma_f32_32x32x16_bf16 a[112:127], v[252:255], v[252:255], 0\n\tv_pk_mul_f32 v[80:81], v[80:81], %0\n\tv_pk_mul_f32 v[82:83], v[82:83], %0\n\tv_pk_mul_f32 v[84:85], v[84:85], %0\n\tv_pk_mul_f32 v[86:87], v[86:87], %0\n\tv_pk_mul_f32 v[88:89], v[88:89], %0\n\tv_pk_mul_f32 v[90:91], v[90:91], %0\n\tv_pk_mul_f32 v[92:93], v[92:93], %0\n\tv_pk_mul_f32 v[94:95], v[94:95], %0\n\tv_cvt_pk_bf16_f32 v80, v80, v81\n\tv_cvt_pk_bf16_f32 v81, v82, v83\n\tv_cvt_pk_bf16_f32 v82, v84, v85\n\tv_cvt_pk_bf16_f32 v83, v86, v87\n\tv_cvt_pk_bf16_f32 v84, v88, v89\n\tv_cvt_pk_bf16_f32 v85, v90, v91\n\tv_cvt_pk_bf16_f32 v86, v92, v93\n\tv_cvt_pk_bf16_f32 v87, v94, v95\n\tv_accvgpr_write_b32 a184, v80\n\tv_accvgpr_write_b32 a185, v81\n\tv_accvgpr_write_b32 a186, v82\n\tv_accvgpr_write_b32 a187, v83\n\tv_accvgpr_write_b32 a188, v84\n\tv_accvgpr_write_b32 a189, v85\n\tv_accvgpr_write_b32 a190, v86\n\tv_accvgpr_write_b32 a191, v87" :  : "v"(sc2) : "memory");
}
DI void h_store0(const unsigned hoff, const srd_t hsrd, const unsigned hso) {
  asm volatile("v_max_f32_e32 v136, 0, v136\n\tv_max_f32_e32 v137, 0, v137\n\tv_max_f32_e32 v138, 0, v138\n\tv_max_f32_e32 v139, 0, v139\n\tv_max_f32_e32 v140, 0, v140\n\tv_max_f32_e32 v141, 0, v141\n\tv_max_f32_e32 v142, 0, v142\n\tv_max_f32_e32 v143, 0, v143\n\tv_max_f32_e32 v144, 0, v144\n\tv_max_f32_e32 v145, 0, v145\n\tv_max_f32_e32 v146, 0, v146\n\tv_max_f32_e32 v147, 0, v147\n\tv_max_f32_e32 v148, 0, v148\n\tv_max_f32_e32 v149, 0, v149\n\tv_max_f32_e32 v150, 0, v150\n\tv_max_f32_e32 v151, 0, v151\n\tv_cvt_pk_bf16_f32 v136, v136, v137\n\tv_cvt_pk_bf16_f32 v137, v138, v139\n\tv_cvt_pk_bf16_f32 v138, v140, v141\n\tv_cvt_pk_bf16_f32 v139, v142, v143\n\tv_cvt_pk_bf16_f32 v140, v144, v145\n\tv_cvt_pk_bf16_f32 v141, v146, v147\n\tv_cvt_pk_bf16_f32 v142, v148, v149\n\tv_cvt_pk_bf16_f32 v143, v150, v151\n\ts_nop 1\n\tv_permlane32_swap_b32_e32 v136, v138\n\tv_permlane32_swap_b32_e32 v137, v139\n\tv_permlane32_swap_b32_e32 v140, v142\n\tv_permlane32_swap_b32_e32 v141, v143\n\tbuffer_store_dwordx4 v[136:139], %0, %1, %2 offen offset:0 sc1\n\tbuffer_store_dwordx4 v[140:143], %0, %1, %2 offen offset:32 sc1" :  : "v"(hoff), "s"(hsrd), "s"(hso) : "memory");
}
DI void h_store1(const unsigned hoff, const srd_t hsrd, const unsigned hso) {
  asm volatile("v_max_f32_e32 v152, 0, v152\n\tv_max_f32_e32 v153, 0, v153\n\tv_max_f32_e32 v154, 0, v154\n\tv_max_f32_e32 v155, 0, v155\n\tv_max_f32_e32 v156, 0, v156\n\tv_max_f32_e32 v157, 0, v157\n\tv_max_f32_e32 v158, 0, v158\n\tv_max_f32_e32 v159, 0, v159\n\tv_max_f32_e32 v160, 0, v160\n\tv_max_f32_e32 v161, 0, v161\n\tv_max_f32_e32 v162, 0, v162\n\tv_max_f32_e32 v163, 0, v163\n\tv_max_f32_e32 v164, 0, v164\n\tv_max_f32_e32 v165, 0, v165\n\tv_max_f32_e32 v166, 0, v166\n\tv_max_f32_e32 v167, 0, v167\n\tv_cvt_pk_bf16_f32 v152, v152, v153\n\tv_cvt_pk_bf16_f32 v153, v154, v155\n\tv_cvt_pk_bf16_f32 v154, v156, v157\n\tv_cvt_pk_bf16_f32 v155, v158, v159\n\tv_cvt_pk_bf16_f32 v156, v160, v161\n\tv_cvt_pk_bf16_f32 v157, v162, v163\n\tv_cvt_pk_bf16_f32 v158, v164, v165\n\tv_cvt_pk_bf16_f32 v159, v166, v167\n\ts_nop 1\n\tv_permlane32_swap_b32_e32 v152, v154\n\tv_permlane32_swap_b32_e32 v153, v155\n\tv_permlane32_swap_b32_e32 v156, v158\n\tv_permlane32_swap_b32_e32 v157, v159\n\tbuffer_store_dwordx4 v[152:155], %0, %1, %2 offen offset:0 sc1\n\tbuffer_store_dwordx4 v[156:159], %0, %1, %2 offen offset:32 sc1" :  : "v"(hoff), "s"(hsrd), "s"(hso) : "memory");
}
DI void h_store2(const unsigned hoff, const srd_t hsrd, const unsigned hso) {
  asm volatile("v_max_f32_e32 v168, 0, v168\n\tv_max_f32_e32 v169, 0, v169\n\tv_max_f32_e32 v170, 0, v170\n\tv_max_f32_e32 v171, 0, v171\n\tv_max_f32_e32 v172, 0, v172\n\tv_max_f32_e32 v173, 0, v173\n\tv_max_f32_e32 v174, 0, v174\n\tv_max_f32_e32 v175, 0, v175\n\tv_max_f32_e32 v176, 0, v176\n\tv_max_f32_e32 v177, 0, v177\n\tv_max_f32_e32 v178, 0, v178\n\tv_max_f32_e32 v179, 0, v179\n\tv_max_f32_e32 v180, 0, v180\n\tv_max_f32_e32 v181, 0, v181\n\tv_max_f32_e32 v182, 0, v182\n\tv_max_f32_e32 v183, 0, v183\n\tv_cvt_pk_bf16_f32 v168, v168, v169\n\tv_cvt_pk_bf16_f32 v169, v170, v171\n\tv_cvt_pk_bf16_f32 v170, v172, v173\n\tv_cvt_pk_bf16_f32 v171, v174, v175\n\tv_cvt_pk_bf16_f32 v172, v176, v177\n\tv_cvt_pk_bf16_f32 v173, v178, v179\n\tv_cvt_pk_bf16_f32 v174, v180, v181\n\tv_cvt_pk_bf16_f32 v175, v182, v183\n\ts_nop 1\n\tv_permlane32_swap_b32_e32 v168, v170\n\tv_permlane32_swap_b32_e32 v169, v171\n\tv_permlane32_swap_b32_e32 v172, v174\n\tv_permlane32_swap_b32_e32 v173, v175\n\tbuffer_store_dwordx4 v[168:171], %0, %1, %2 offen offset:0 sc1\n\tbuffer_store_dwordx4 v[172:175], %0, %1, %2 offen offset:32 sc1" :  : "v"(hoff), "s"(hsrd), "s"(hso) : "memory");
}
DI void gA0f(AC& c) {
  asm volatile("s_waitcnt lgkmcnt(4)\n\tv_mfma_f32_32x32x16_bf16 v[128:143], a[192:195], a[128:131], 0\n\tds_read_b128 a[216:219], %0 offset:0" :  : "v"(c.kb_[3]) : "memory");
  asm volatile("v_mfma_f32_32x32x16_bf16 v[144:159], a[192:195], a[160:163], 0" :  :  : "memory", ACL64);
  asm volatile("v_mfma_f32_32x32x16_bf16 v[160:175], a[196:199], a[128:131], 0\n\tds_read_b128 a[220:223], %0 offset:8192" :  : "v"(c.kb_[3]) : "memory");
  asm volatile("v_mfma_f32_32x32x16_bf16 v[176:191], a[196:199], a[160:163], 0" :  :  : "memory");
  asm volatile("s_waitcnt lgkmcnt(4)\n\tv_mfma_f32_32x32x16_bf16 v[128:143], a[200:203], a[132:135], v[128:143]\n\tds_read_b128 a[224:227], %0 offset:0" :  : "v"(c.kb_[4]) : "memory");
  asm volatile("v_mfma_f32_32x32x16_bf16 v[144:159], a[200:203], a[164:167], v[144:159]" :  :  : "memory");
  asm volatile("v_mfma_f32_32x32x16_bf16 v[160:175], a[204:207], a[132:135], v[160:175]\n\tds_read_b128 a[228:231], %0 offset:8192" :  : "v"(c.kb_[4]) : "memory");
  asm volatile("v_mfma_f32_32x32x16_bf16 v[176:191], a[204:207], a[164:167], v[176:191]" :  :  : "memory");
  asm volatile("s_waitcnt lgkmcnt(4)\n\tv_mfma_f32_32x32x16_bf16 v[128:143], a[208:211], a[136:139], v[128:143]\n\tds_read_b128 a[232:235], %0 offset:0" :  : "v"(c.kb_[5]) : "memory");
  asm volatile("v_mfma_f32_32x32x16_bf16 v[144:159], a[208:211], a[168:171], v[144:159]" :  :  : "memory", ACL64);
  asm volatile("v_mfma_f32_32x32x16_bf16 v[160:175], a[212:215], a[136:139], v[160:175]\n\tds_read_b128 a[236:239], %0 offset:8192" :  : "v"(c.kb_[5]) : "memory");
  asm volatile("v_mfma_f32_32x32x16_bf16 v[176:191], a[212:215], a[168:171], v[176:191]" :  :  : "memory");
  asm volatile("s_waitcnt lgkmcnt(4)\n\tv_mfma_f32_32x32x16_bf16 v[128:143], a[216:219], a[140:143], v[128:143]\n\tds_read_b128 a[240:243], %0 offset:0" :  : "v"(c.kb_[6]) : "memory");
  asm volatile("v_mfma_f32_32x32x16_bf16 v[144:159], a[216:219], a[172:175], v[144:159]" :  :  : "memory");
  asm volatile("v_mfma_f32_32x32x16_bf16 v[160:175], a[220:223], a[140:143], v[160:175]\n\tds_read_b128 a[244:247], %0 offset:8192" :  : "v"(c.kb_[6]) : "memory");
  asm volatile("v_mfma_f32_32x32x16_bf16 v[176:191], a[220:223], a[172:175], v[176:191]" :  :  : "memory");
  asm volatile("s_waitcnt lgkmcnt(4)\n\tv_mfma_f32_32x32x16_bf16 v[128:143], a[224:227], a[144:147], v[128:143]\n\tds_read_b128 a[248:251], %0 offset:0" :  : "v"(c.kb_[7]) : "memory");
  asm volatile("v_mfma_f32_32x32x16_bf16 v[144:159], a[224:227], a[176:179], v[144:159]" :  :  : "memory", ACL64);
  asm volatile("v_mfma_f32_32x32x16_bf16 v[160:175], a[228:231], a[144:147], v[160:175]\n\tds_read_b128 a[252:255], %0 offset:8192" :  : "v"(c.kb_[7]) : "memory");
  asm volatile("v_mfma_f32_32x32x16_bf16 v[176:191], a[228:231], a[176:179], v[176:191]" :  :  : "memory");
  asm volatile("s_waitcnt lgkmcnt(4)\n\tv_mfma_f32_32x32x16_bf16 v[128:143], a[232:235], a[148:151], v[128:143]" :  :  : "memory");
  asm volatile("v_mfma_f32_32x32x16_bf16 v[144:159], a[232:235], a[180:183], v[144:159]" :  :  : "memory");
  asm volatile("v_mfma_f32_32x32x16_bf16 v[160:175], a[236:239], a[148:151], v[160:175]" :  :  : "memory");
  asm volatile("v_mfma_f32_32x32x16_bf16 v[176:191], a[236:239], a[180:183], v[176:191]" :  :  : "memory");
  asm volatile("s_waitcnt vmcnt(16) lgkmcnt(0)\n\ts_barrier\n\ts_waitcnt lgkmcnt(2)\n\tv_mfma_f32_32x32x16_bf16 v[128:143], a[240:243], a[152:155], v[128:143]" :  :  : "memory");
  asm volatile("v_mfma_f32_32x32x16_bf16 v[144:159], a[240:243], a[184:187], v[144:159]" :  :  : "memory", ACL64);
  asm volatile("v_mfma_f32_32x32x16_bf16 v[160:175], a[244:247], a[152:155], v[160:175]\n\ts_add_u32 m0, %0, 32768\n\ts_nop 0\n\tbuffer_load_dwordx4 %1, %2, %3 offen offset:0 sc1 lds\n\tbuffer_load_dwordx4 %1, %2, %3 offen offset:1024 sc1 lds" :  : "s"(c.m0w), "v"(c.vlin), "s"(c.srdV), "s"(c.soVA) : "memory", "m0", "scc");
  asm volatile("v_mfma_f32_32x32x16_bf16 v[176:191], a[244:247], a[184:187], v[176:191]" :  :  : "memory");
  asm volatile("s_waitcnt lgkmcnt(0)\n\tv_mfma_f32_32x32x16_bf16 v[128:143], a[248:251], a[156:159], v[128:143]\n\ts_add_u32 m0, %0, 32768\n\ts_nop 0\n\tbuffer_load_dwordx4 %1, %2, %3 offen offset:2048 sc1 lds\n\tbuffer_load_dwordx4 %1, %2, %3 offen offset:3072 sc1 lds" :  : "s"(c.m0w), "v"(c.vlin), "s"(c.srdV), "s"(c.soVA) : "memory", "m0", "scc");
  asm volatile("v_mfma_f32_32x32x16_bf16 v[144:159], a[248:251], a[188:191], v[144:159]" :  :  : "memory");
  asm volatile("v_mfma_f32_32x32x16_bf16 v[160:175], a[252:255], a[156:159], v[160:175]\n\ts_cmp_le_i32 %0, 14\n\ts_cbranch_scc0 .Ldma414_%=\n\ts_add_u32 m0, %1, 0\n\ts_nop 0\n\tbuffer_load_dwordx4 %2, %3, %4 offen offset:0 sc1 lds\n\tbuffer_load_dwordx4 %2, %3, %4 offen offset:1024 sc1 lds\n\t.Ldma414_%=:" :  : "s"(c.t), "s"(c.m0w), "v"(c.vlin), "s"(c.srdK), "s"(c.soKA) : "memory", "scc", "m0");
  asm volatile("v_mfma_f32_32x32x16_bf16 v[176:191], a[252:255], a[188:191], v[176:191]\n\ts_cmp_le_i32 %0, 14\n\ts_cbranch_scc0 .Ldma415_%=\n\ts_add_u32 m0, %1, 0\n\ts_nop 0\n\tbuffer_load_dwordx4 %2, %3, %4 offen offset:2048 sc1 lds\n\tbuffer_load_dwordx4 %2, %3, %4 offen offset:3072 sc1 lds\n\t.Ldma415_%=:" :  : "s"(c.t), "s"(c.m0w), "v"(c.vlin), "s"(c.srdK), "s"(c.soKA) : "memory", "scc", "m0");
}
DI void gB0fa(AC& c) {
  asm volatile("s_cmp_ge_i32 %0, %1\n\ts_cbranch_scc0 .Ldma416_%=\n\ts_add_u32 m0, %2, 98304\n\ts_nop 0\n\tbuffer_load_dwordx4 %3, %4, %5 offen offset:0 sc1 lds\n\tbuffer_load_dwordx4 %3, %4, %5 offen offset:1024 sc1 lds\n\tbuffer_load_dwordx4 %3, %4, %5 offen offset:2048 sc1 lds\n\tbuffer_load_dwordx4 %3, %4, %5 offen offset:3072 sc1 lds\n\t.Ldma416_%=:" :  : "s"(c.t), "s"(c.n1), "s"(c.m0w), "v"(c.vlin), "s"(c.srdV), "s"(c.soVB) : "memory", "scc", "m0");
  asm volatile("s_cmp_lt_u32 %0, %1\n\ts_cbranch_scc0 .Ldma417_%=\n\ts_add_u32 m0, %2, 65536\n\ts_nop 0\n\tbuffer_load_dwordx4 %3, %4, %5 offen offset:0 sc1 lds\n\tbuffer_load_dwordx4 %3, %4, %5 offen offset:1024 sc1 lds\n\tbuffer_load_dwordx4 %3, %4, %5 offen offset:2048 sc1 lds\n\tbuffer_load_dwordx4 %3, %4, %5 offen offset:3072 sc1 lds\n\t.Ldma417_%=:" :  : "s"(c.u3), "s"(c.lim3), "s"(c.m0w), "v"(c.vlin), "s"(c.srdK), "s"(c.soKB) : "memory", "scc", "m0", ACL64);
  asm volatile("s_nop 7\n\ts_nop 3\n\tv_max3_f32 %0, v128, v129, v160\n\tv_max3_f32 %1, v130, v131, v161\n\tv_max3_f32 %0, %0, v162, v163" : "=&v"(c.pmx[0][0]), "=&v"(c.pmx[0][1]) :  : "memory");
  asm volatile("v_max3_f32 %0, %0, v132, v133\n\tv_max3_f32 %1, %1, v134, v135\n\tv_max3_f32 %0, %0, v164, v165\n\tv_max3_f32 %1, %1, v166, v167" : "+v"(c.pmx[0][0]), "+v"(c.pmx[0][1]) :  : "memory");
  asm volatile("v_max3_f32 %0, %0, v136, v137\n\tv_max3_f32 %1, %1, v138, v139\n\tv_max3_f32 %0, %0, v168, v169\n\tv_max3_f32 %1, %1, v170, v171" : "+v"(c.pmx[0][0]), "+v"(c.pmx[0][1]) :  : "memory");
  asm volatile("v_max3_f32 %0, %0, v140, v141\n\tv_max3_f32 %1, %1, v142, v143\n\tv_max3_f32 %0, %0, v172, v173\n\tv_max3_f32 %1, %1, v174, v175" : "+v"(c.pmx[0][0]), "+v"(c.pmx[0][1]) :  : "memory");
  asm volatile("v_max3_f32 %0, v144, v145, v176\n\tv_max3_f32 %1, v146, v147, v177\n\tv_max3_f32 %0, %0, v178, v179" : "=&v"(c.pmx[1][0]), "=&v"(c.pmx[1][1]) :  : "memory");
  asm volatile("v_max3_f32 %0, %0, v148, v149\n\tv_max3_f32 %1, %1, v150, v151\n\tv_max3_f32 %0, %0, v180, v181\n\tv_max3_f32 %1, %1, v182, v183" : "+v"(c.pmx[1][0]), "+v"(c.pmx[1][1]) :  : "memory");
  asm volatile("v_max3_f32 %0, %0, v152, v153\n\tv_max3_f32 %1, %1, v154, v155\n\tv_max3_f32 %0, %0, v184, v185\n\tv_max3_f32 %1, %1, v186, v187" : "+v"(c.pmx[1][0]), "+v"(c.pmx[1][1]) :  : "memory");
  asm volatile("v_max3_f32 %0, %0, v156, v157\n\tv_max3_f32 %1, %1, v158, v159\n\tv_max3_f32 %0, %0, v188, v189\n\tv_max3_f32 %1, %1, v190, v191" : "+v"(c.pmx[1][0]), "+v"(c.pmx[1][1]) :  : "memory", ACL64);
  asm volatile("v_max_f32_e32 %0, %0, %1\n\tv_mov_b32_e32 %1, %0\n\ts_nop 1\n\tv_permlane32_swap_b32_e32 %0, %1\n\tv_max_f32_e32 %0, %0, %1" : "+v"(c.pmx[0][0]), "+v"(c.pmx[0][1]) :  : "memory");
  asm volatile("v_max_f32_e32 %0, %0, %1\n\tv_mov_b32_e32 %1, %0\n\ts_nop 1\n\tv_permlane32_swap_b32_e32 %0, %1\n\tv_max_f32_e32 %0, %0, %1" : "+v"(c.pmx[1][0]), "+v"(c.pmx[1][1]) :  : "memory");
}
DI void gB0fb(AC& c) {
  asm volatile("v_exp_f32_e32 v128, v128\n\tv_exp_f32_e32 v129, v129" :  :  : "memory");
  asm volatile("v_exp_f32_e32 v130, v130\n\tv_exp_f32_e32 v131, v131" :  :  : "memory");
  asm volatile("v_exp_f32_e32 v132, v132\n\tv_exp_f32_e32 v133, v133" :  :  : "memory");
  asm volatile("v_exp_f32_e32 v134, v134\n\tv_exp_f32_e32 v135, v135\n\tv_add_f32_e32 %0, %0, v128" : "+v"(c.rs[0][0]) :  : "memory");
  asm volatile("v_exp_f32_e32 v136, v136\n\tv_exp_f32_e32 v137, v137\n\tv_add_f32_e32 %0, %0, v129" : "+v"(c.rs[0][1]) :  : "memory");
  asm volatile("v_exp_f32_e32 v138, v138\n\tv_exp_f32_e32 v139, v139\n\tv_add_f32_e32 %0, %0, v130" : "+v"(c.rs[0][0]) :  : "memory", ACL64);
  asm volatile("v_exp_f32_e32 v140, v140\n\tv_exp_f32_e32 v141, v141\n\tv_add_f32_e32 %0, %0, v131" : "+v"(c.rs[0][1]) :  : "memory");
  asm volatile("ds_read_b128 a[192:195], %1 offset:16384\n\tv_exp_f32_e32 v142, v142\n\tv_exp_f32_e32 v143, v143\n\tv_add_f32_e32 %0, %0, v132" : "+v"(c.rs[0][0]) : "v"(c.kb_[0]) : "memory");
  asm volatile("v_exp_f32_e32 v144, v144\n\tv_exp_f32_e32 v145, v145\n\tv_add_f32_e32 %0, %0, v133" : "+v"(c.rs[0][1]) :  : "memory");
  asm volatile("ds_read_b128 a[196:199], %1 offset:24576\n\tv_exp_f32_e32 v146, v146\n\tv_exp_f32_e32 v147, v147\n\tv_add_f32_e32 %0, %0, v134" : "+v"(c.rs[0][0]) : "v"(c.kb_[0]) : "memory");
  asm volatile("v_exp_f32_e32 v148, v148\n\tv_exp_f32_e32 v149, v149\n\tv_add_f32_e32 %0, %0, v135" : "+v"(c.rs[0][1]) :  : "memory");
  asm volatile("ds_read_b128 a[200:203], %1 offset:16384\n\tv_exp_f32_e32 v150, v150\n\tv_exp_f32_e32 v151, v151\n\tv_add_f32_e32 %0, %0, v136" : "+v"(c.rs[0][0]) : "v"(c.kb_[1]) : "memory");
  asm volatile("v_exp_f32_e32 v152, v152\n\tv_exp_f32_e32 v153, v153\n\tv_add_f32_e32 %0, %0, v137" : "+v"(c.rs[0][1]) :  : "memory");
  asm volatile("ds_read_b128 a[204:207], %1 offset:24576\n\tv_exp_f32_e32 v154, v154\n\tv_add_f32_e32 %0, %0, v138" : "+v"(c.rs[0][0]) : "v"(c.kb_[1]) : "memory", ACL64);
  asm volatile("v_exp_f32_e32 v155, v155\n\tv_add_f32_e32 %0, %0, v139" : "+v"(c.rs[0][1]) :  : "memory");
  asm volatile("ds_read_b128 a[208:211], %1 offset:16384\n\tv_exp_f32_e32 v156, v156\n\tv_add_f32_e32 %0, %0, v140" : "+v"(c.rs[0][0]) : "v"(c.kb_[2]) : "memory");
  asm volatile("v_exp_f32_e32 v157, v157\n\tv_add_f32_e32 %0, %0, v141" : "+v"(c.rs[0][1]) :  : "memory");
  asm volatile("ds_read_b128 a[212:215], %1 offset:24576\n\tv_exp_f32_e32 v158, v158\n\tv_add_f32_e32 %0, %0, v142" : "+v"(c.rs[0][0]) : "v"(c.kb_[2]) : "memory");
  asm volatile("v_exp_f32_e32 v159, v159\n\tv_add_f32_e32 %0, %0, v143" : "+v"(c.rs[0][1]) :  : "memory");
}
DI void gA0(AC& c) {
  asm volatile("s_waitcnt lgkmcnt(4)\n\tv_mfma_f32_32x32x16_bf16 v[128:143], a[192:195], a[128:131], v[96:111]\n\tds_read_b128 a[216:219], %0 offset:0\n\tv_exp_f32_e32 v224, v224\n\tv_exp_f32_e32 v225, v225\n\tv_cvt_pk_bf16_f32 v64, v192, v193" :  : "v"(c.kb_[3]) : "memory");
  asm volatile("v_mfma_f32_32x32x16_bf16 v[144:159], a[192:195], a[160:163], v[112:127]\n\tv_exp_f32_e32 v226, v226\n\tv_exp_f32_e32 v227, v227\n\tv_cvt_pk_bf16_f32 v65, v194, v195\n\tv_add_f32_e32 %0, %0, v224" : "+v"(c.rs[0][1]) :  : "memory");
  asm volatile("v_mfma_f32_32x32x16_bf16 v[160:175], a[196:199], a[128:131], v[96:111]\n\tds_read_b128 a[220:223], %1 offset:8192\n\tv_exp_f32_e32 v228, v228\n\tv_cvt_pk_bf16_f32 v66, v196, v197\n\tv_add_f32_e32 %0, %0, v225" : "+v"(c.rs[0][0]) : "v"(c.kb_[3]) : "memory", ACL64);
  asm volatile("v_mfma_f32_32x32x16_bf16 v[176:191], a[196:199], a[160:163], v[112:127]\n\tv_exp_f32_e32 v229, v229\n\tv_cvt_pk_bf16_f32 v67, v198, v199\n\tv_add_f32_e32 %0, %0, v226" : "+v"(c.rs[0][1]) :  : "memory");
  asm volatile("s_waitcnt lgkmcnt(4)\n\tv_mfma_f32_32x32x16_bf16 v[128:143], a[200:203], a[132:135], v[128:143]\n\tds_read_b128 a[224:227], %1 offset:0\n\tv_exp_f32_e32 v230, v230\n\tv_cvt_pk_bf16_f32 v80, v208, v209\n\tv_add_f32_e32 %0, %0, v227" : "+v"(c.rs[0][0]) : "v"(c.kb_[4]) : "memory");
  asm volatile("v_mfma_f32_32x32x16_bf16 v[144:159], a[200:203], a[164:167], v[144:159]\n\tv_exp_f32_e32 v231, v231\n\tv_cvt_pk_bf16_f32 v81, v210, v211\n\tv_add_f32_e32 %0, %0, v228" : "+v"(c.rs[0][1]) :  : "memory");
  asm volatile("v_mfma_f32_32x32x16_bf16 v[160:175], a[204:207], a[132:135], v[160:175]\n\tds_read_b128 a[228:231], %1 offset:8192\n\tv_exp_f32_e32 v232, v232\n\tv_cvt_pk_bf16_f32 v82, v212, v213\n\tv_add_f32_e32 %0, %0, v229" : "+v"(c.rs[0][0]) : "v"(c.kb_[4]) : "memory");
  asm volatile("v_mfma_f32_32x32x16_bf16 v[176:191], a[204:207], a[164:167], v[176:191]\n\tv_exp_f32_e32 v233, v233\n\tv_cvt_pk_bf16_f32 v83, v214, v215\n\tv_add_f32_e32 %0, %0, v230" : "+v"(c.rs[0][1]) :  : "memory");
  asm volatile("s_waitcnt lgkmcnt(4)\n\tv_mfma_f32_32x32x16_bf16 v[128:143], a[208:211], a[136:139], v[128:143]\n\tds_read_b128 a[232:235], %1 offset:0\n\tv_exp_f32_e32 v234, v234\n\tv_cvt_pk_bf16_f32 v68, v200, v201\n\tv_add_f32_e32 %0, %0, v231" : "+v"(c.rs[0][0]) : "v"(c.kb_[5]) : "memory");
  asm volatile("v_mfma_f32_32x32x16_bf16 v[144:159], a[208:211], a[168:171], v[144:159]\n\tv_exp_f32_e32 v235, v235\n\tv_cvt_pk_bf16_f32 v69, v202, v203\n\tv_add_f32_e32 %0, %0, v232" : "+v"(c.rs[0][1]) :  : "memory");
  asm volatile("v_mfma_f32_32x32x16_bf16 v[160:175], a[212:215], a[136:139], v[160:175]\n\tds_read_b128 a[236:239], %1 offset:8192\n\tv_exp_f32_e32 v236, v236\n\tv_cvt_pk_bf16_f32 v70, v204, v205\n\tv_add_f32_e32 %0, %0, v233" : "+v"(c.rs[0][0]) : "v"(c.kb_[5]) : "memory", ACL64);
  asm volatile("v_mfma_f32_32x32x16_bf16 v[176:191], a[212:215], a[168:171], v[176:191]\n\tv_exp_f32_e32 v237, v237\n\tv_cvt_pk_bf16_f32 v71, v206, v207\n\tv_add_f32_e32 %0, %0, v234" : "+v"(c.rs[0][1]) :  : "memory");
  asm volatile("s_waitcnt lgkmcnt(4)\n\tv_mfma_f32_32x32x16_bf16 v[128:143], a[216:219], a[140:143], v[128:143]\n\tds_read_b128 a[240:243], %1 offset:0\n\tv_exp_f32_e32 v238, v238\n\tv_cvt_pk_bf16_f32 v84, v216, v217\n\tv_add_f32_e32 %0, %0, v235" : "+v"(c.rs[0][0]) : "v"(c.kb_[6]) : "memory");
  asm volatile("v_mfma_f32_32x32x16_bf16 v[144:159], a[216:219], a[172:175], v[144:159]\n\tv_exp_f32_e32 v239, v239\n\tv_cvt_pk_bf16_f32 v85, v218, v219\n\tv_add_f32_e32 %0, %0, v236" : "+v"(c.rs[0][1]) :  : "memory");
  asm volatile("v_mfma_f32_32x32x16_bf16 v[160:175], a[220:223], a[140:143], v[160:175]\n\tds_read_b128 a[244:247], %1 offset:8192\n\tv_exp_f32_e32 v240, v240\n\tv_cvt_pk_bf16_f32 v86, v220, v221\n\tv_add_f32_e32 %0, %0, v237" : "+v"(c.rs[0][0]) : "v"(c.kb_[6]) : "memory");
  asm volatile("v_mfma_f32_32x32x16_bf16 v[176:191], a[220:223], a[172:175], v[176:191]\n\tv_exp_f32_e32 v241, v241\n\tv_cvt_pk_bf16_f32 v87, v222, v223\n\tv_add_f32_e32 %0, %0, v238" : "+v"(c.rs[0][1]) :  : "memory");
  asm volatile("s_waitcnt lgkmcnt(4)\n\tv_mfma_f32_32x32x16_bf16 v[128:143], a[224:227], a[144:147], v[128:143]\n\tds_read_b128 a[248:251], %2 offset:0\n\tv_exp_f32_e32 v242, v242\n\tv_cvt_pk_bf16_f32 v72, v224, v225\n\tv_add_f32_e32 %0, %0, v239\n\tv_add_f32_e32 %1, %1, v208" : "+v"(c.rs[0][0]), "+v"(c.rs[1][2]) : "v"(c.kb_[7]) : "memory");
  asm volatile("v_mfma_f32_32x32x16_bf16 v[144:159], a[224:227], a[176:179], v[144:159]\n\tv_exp_f32_e32 v243, v243\n\tv_cvt_pk_bf16_f32 v73, v226, v227\n\tv_add_f32_e32 %0, %0, v240\n\tv_add_f32_e32 %1, %1, v209" : "+v"(c.rs[1][1]), "+v"(c.rs[1][3]) :  : "memory");
  asm volatile("v_mfma_f32_32x32x16_bf16 v[160:175], a[228:231], a[144:147], v[160:175]\n\tds_read_b128 a[252:255], %2 offset:8192\n\tv_exp_f32_e32 v244, v244\n\tv_cvt_pk_bf16_f32 v74, v228, v229\n\tv_add_f32_e32 %0, %0, v241\n\tv_add_f32_e32 %1, %1, v210" : "+v"(c.rs[1][0]), "+v"(c.rs[1][2]) : "v"(c.kb_[7]) : "memory", ACL64);
  asm volatile("v_mfma_f32_32x32x16_bf16 v[176:191], a[228:231], a[176:179], v[176:191]\n\tv_exp_f32_e32 v245, v245\n\tv_cvt_pk_bf16_f32 v75, v230, v231\n\tv_add_f32_e32 %0, %0, v242\n\tv_add_f32_e32 %1, %1, v211" : "+v"(c.rs[1][1]), "+v"(c.rs[1][3]) :  : "memory");
  asm volatile("s_waitcnt lgkmcnt(4)\n\tv_mfma_f32_32x32x16_bf16 v[128:143], a[232:235], a[148:151], v[128:143]\n\tv_exp_f32_e32 v246, v246\n\tv_cvt_pk_bf16_f32 v76, v232, v233\n\tv_add_f32_e32 %0, %0, v243\n\tv_add_f32_e32 %1, %1, v212" : "+v"(c.rs[1][0]), "+v"(c.rs[1][2]) :  : "memory");
  asm volatile("v_mfma_f32_32x32x16_bf16 v[144:159], a[232:235], a[180:183], v[144:159]\n\tv_exp_f32_e32 v247, v247\n\tv_cvt_pk_bf16_f32 v77, v234, v235\n\tv_add_f32_e32 %0, %0, v244\n\tv_add_f32_e32 %1, %1, v213" : "+v"(c.rs[1][1]), "+v"(c.rs[1][3]) :  : "memory");
  asm volatile("v_mfma_f32_32x32x16_bf16 v[160:175], a[236:239], a[148:151], v[160:175]\n\tv_exp_f32_e32 v248, v248\n\tv_cvt_pk_bf16_f32 v78, v236, v237\n\tv_add_f32_e32 %0, %0, v245\n\tv_add_f32_e32 %1, %1, v214" : "+v"(c.rs[1][0]), "+v"(c.rs[1][2]) :  : "memory");
  asm volatile("v_mfma_f32_32x32x16_bf16 v[176:191], a[236:239], a[180:183], v[176:191]\n\tv_exp_f32_e32 v249, v249\n\tv_cvt_pk_bf16_f32 v79, v238, v239\n\tv_add_f32_e32 %0, %0, v246\n\tv_add_f32_e32 %1, %1, v215" : "+v"(c.rs[1][1]), "+v"(c.rs[1][3]) :  : "memory");
  asm volatile("s_waitcnt vmcnt(0) lgkmcnt(0)\n\ts_barrier\n\ts_waitcnt lgkmcnt(2)\n\tv_mfma_f32_32x32x16_bf16 v[128:143], a[240:243], a[152:155], v[128:143]\n\tds_read_b128 a[192:195], %2 offset:49152\n\tv_exp_f32_e32 v250, v250\n\tv_cvt_pk_bf16_f32 v88, v240, v241\n\tv_add_f32_e32 %0, %0, v247\n\tv_add_f32_e32 %1, %1, v216" : "+v"(c.rs[1][0]), "+v"(c.rs[1][2]) : "v"(c.vb_[0]) : "memory");
  asm volatile("v_mfma_f32_32x32x16_bf16 v[144:159], a[240:243], a[184:187], v[144:159]\n\tds_read_b128 a[196:199], %2 offset:53248\n\tv_exp_f32_e32 v251, v251\n\tv_cvt_pk_bf16_f32 v89, v242, v243\n\tv_add_f32_e32 %0, %0, v248\n\tv_add_f32_e32 %1, %1, v217" : "+v"(c.rs[1][1]), "+v"(c.rs[1][3]) : "v"(c.vb_[0]) : "memory");
  asm volatile("v_mfma_f32_32x32x16_bf16 v[160:175], a[244:247], a[152:155], v[160:175]\n\tds_read_b128 a[200:203], %2 offset:57344\n\tv_exp_f32_e32 v252, v252\n\tv_cvt_pk_bf16_f32 v90, v244, v245\n\tv_add_f32_e32 %0, %0, v249\n\tv_add_f32_e32 %1, %1, v218\n\ts_add_u32 m0, %3, 32768\n\ts_nop 0\n\tbuffer_load_dwordx4 %4, %5, %6 offen offset:0 sc1 lds\n\tbuffer_load_dwordx4 %4, %5, %6 offen offset:1024 sc1 lds" : "+v"(c.rs[1][0]), "+v"(c.rs[1][2]) : "v"(c.vb_[0]), "s"(c.m0w), "v"(c.vlin), "s"(c.srdV), "s"(c.soVA) : "memory", "m0", "scc", ACL64);
  asm volatile("v_mfma_f32_32x32x16_bf16 v[176:191], a[244:247], a[184:187], v[176:191]\n\tds_read_b128 a[204:207], %2 offset:61440\n\tv_exp_f32_e32 v253, v253\n\tv_cvt_pk_bf16_f32 v91, v246, v247\n\tv_add_f32_e32 %0, %0, v250\n\tv_add_f32_e32 %1, %1, v219" : "+v"(c.rs[1][1]), "+v"(c.rs[1][3]) : "v"(c.vb_[0]) : "memory");
  asm volatile("s_waitcnt lgkmcnt(4)\n\tv_mfma_f32_32x32x16_bf16 v[128:143], a[248:251], a[156:159], v[128:143]\n\tds_read_b128 a[208:211], %2 offset:49152\n\tv_exp_f32_e32 v254, v254\n\tv_cvt_pk_bf16_f32 v92, v248, v249\n\tv_add_f32_e32 %0, %0, v251\n\tv_add_f32_e32 %1, %1, v220\n\ts_add_u32 m0, %3, 32768\n\ts_nop 0\n\tbuffer_load_dwordx4 %4, %5, %6 offen offset:2048 sc1 lds\n\tbuffer_load_dwordx4 %4, %5, %6 offen offset:3072 sc1 lds" : "+v"(c.rs[1][0]), "+v"(c.rs[1][2]) : "v"(c.vb_[1]), "s"(c.m0w), "v"(c.vlin), "s"(c.srdV), "s"(c.soVA) : "memory", "m0", "scc");
  asm volatile("v_mfma_f32_32x32x16_bf16 v[144:159], a[248:251], a[188:191], v[144:159]\n\tv_exp_f32_e32 v255, v255\n\tv_cvt_pk_bf16_f32 v93, v250, v251\n\tv_add_f32_e32 %0, %0, v252\n\tv_add_f32_e32 %1, %1, v221" : "+v"(c.rs[1][1]), "+v"(c.rs[1][3]) :  : "memory");
  asm volatile("v_mfma_f32_32x32x16_bf16 v[160:175], a[252:255], a[156:159], v[160:175]\n\tds_read_b128 a[212:215], %2 offset:53248\n\tv_cvt_pk_bf16_f32 v94, v252, v253\n\tv_add_f32_e32 %0, %0, v253\n\tv_add_f32_e32 %1, %1, v222\n\ts_cmp_le_i32 %3, 14\n\ts_cbranch_scc0 .Ldma420_%=\n\ts_add_u32 m0, %4, 0\n\ts_nop 0\n\tbuffer_load_dwordx4 %5, %6, %7 offen offset:0 sc1 lds\n\tbuffer_load_dwordx4 %5, %6, %7 offen offset:1024 sc1 lds\n\t.Ldma420_%=:" : "+v"(c.rs[1][0]), "+v"(c.rs[1][2]) : "v"(c.vb_[1]), "s"(c.t), "s"(c.m0w), "v"(c.vlin), "s"(c.srdK), "s"(c.soKA) : "memory", "scc", "m0");
  asm volatile("v_mfma_f32_32x32x16_bf16 v[176:191], a[252:255], a[188:191], v[176:191]\n\tv_cvt_pk_bf16_f32 v95, v254, v255\n\tv_add_f32_e32 %0, %0, v254\n\tv_add_f32_e32 %1, %1, v255\n\tv_add_f32_e32 %2, %2, v223\n\ts_cmp_le_i32 %3, 14\n\ts_cbranch_scc0 .Ldma421_%=\n\ts_add_u32 m0, %4, 0\n\ts_nop 0\n\tbuffer_load_dwordx4 %5, %6, %7 offen offset:2048 sc1 lds\n\tbuffer_load_dwordx4 %5, %6, %7 offen offset:3072 sc1 lds\n\t.Ldma421_%=:" : "+v"(c.rs[1][1]), "+v"(c.rs[1][0]), "+v"(c.rs[1][3]) : "s"(c.t), "s"(c.m0w), "v"(c.vlin), "s"(c.srdK), "s"(c.soKA) : "memory", "scc", "m0");
}
DI void gB0a(AC& c) {
  asm volatile("s_waitcnt lgkmcnt(4)\n\tv_mfma_f32_32x32x16_bf16 a[0:15], a[192:195], v[64:67], a[0:15]\n\tds_read_b128 a[216:219], %0 offset:57344\n\ts_cmp_ge_i32 %1, %2\n\ts_cbranch_scc0 .Ldma422_%=\n\ts_add_u32 m0, %3, 98304\n\ts_nop 0\n\tbuffer_load_dwordx4 %4, %5, %6 offen offset:0 sc1 lds\n\tbuffer_load_dwordx4 %4, %5, %6 offen offset:1024 sc1 lds\n\tbuffer_load_dwordx4 %4, %5, %6 offen offset:2048 sc1 lds\n\tbuffer_load_dwordx4 %4, %5, %6 offen offset:3072 sc1 lds\n\t.Ldma422_%=:" :  : "v"(c.vb_[1]), "s"(c.t), "s"(c.n1), "s"(c.m0w), "v"(c.vlin), "s"(c.srdV), "s"(c.soVB) : "memory", "scc", "m0");
  asm volatile("v_mfma_f32_32x32x16_bf16 a[16:31], a[192:195], v[80:83], a[16:31]\n\ts_cmp_lt_u32 %0, %1\n\ts_cbranch_scc0 .Ldma423_%=\n\ts_add_u32 m0, %2, 65536\n\ts_nop 0\n\tbuffer_load_dwordx4 %3, %4, %5 offen offset:0 sc1 lds\n\tbuffer_load_dwordx4 %3, %4, %5 offen offset:1024 sc1 lds\n\tbuffer_load_dwordx4 %3, %4, %5 offen offset:2048 sc1 lds\n\tbuffer_load_dwordx4 %3, %4, %5 offen offset:3072 sc1 lds\n\t.Ldma423_%=:" :  : "s"(c.u3), "s"(c.lim3), "s"(c.m0w), "v"(c.vlin), "s"(c.srdK), "s"(c.soKB) : "memory", "scc", "m0");
  asm volatile("v_mfma_f32_32x32x16_bf16 a[32:47], a[196:199], v[64:67], a[32:47]\n\tds_read_b128 a[220:223], %2 offset:61440\n\tv_max3_f32 %0, v128, v129, v160\n\tv_max3_f32 %1, v130, v131, v161\n\tv_max3_f32 %0, %0, v162, v163" : "=&v"(c.pmx[0][0]), "=&v"(c.pmx[0][1]) : "v"(c.vb_[1]) : "memory", ACL64);
  asm volatile("v_mfma_f32_32x32x16_bf16 a[48:63], a[196:199], v[80:83], a[48:63]\n\tv_max3_f32 %0, %0, v132, v133\n\tv_max3_f32 %1, %1, v134, v135\n\tv_max3_f32 %0, %0, v164, v165\n\tv_max3_f32 %1, %1, v166, v167" : "+v"(c.pmx[0][0]), "+v"(c.pmx[0][1]) :  : "memory");
  asm volatile("s_waitcnt lgkmcnt(4)\n\tv_mfma_f32_32x32x16_bf16 a[64:79], a[200:203], v[64:67], a[64:79]\n\tds_read_b128 a[224:227], %2 offset:49152\n\tv_max3_f32 %0, %0, v136, v137\n\tv_max3_f32 %1, %1, v138, v139\n\tv_max3_f32 %0, %0, v168, v169\n\tv_max3_f32 %1, %1, v170, v171" : "+v"(c.pmx[0][0]), "+v"(c.pmx[0][1]) : "v"(c.vb_[2]) : "memory");
  asm volatile("v_mfma_f32_32x32x16_bf16 a[80:95], a[200:203], v[80:83], a[80:95]\n\tv_max3_f32 %0, %0, v140, v141\n\tv_max3_f32 %1, %1, v142, v143\n\tv_max3_f32 %0, %0, v172, v173\n\tv_max3_f32 %1, %1, v174, v175" : "+v"(c.pmx[0][0]), "+v"(c.pmx[0][1]) :  : "memory");
  asm volatile("v_mfma_f32_32x32x16_bf16 a[96:111], a[204:207], v[64:67], a[96:111]\n\tds_read_b128 a[228:231], %2 offset:53248\n\tv_max3_f32 %0, v144, v145, v176\n\tv_max3_f32 %1, v146, v147, v177\n\tv_max3_f32 %0, %0, v178, v179" : "=&v"(c.pmx[1][0]), "=&v"(c.pmx[1][1]) : "v"(c.vb_[2]) : "memory");
  asm volatile("v_mfma_f32_32x32x16_bf16 a[112:127], a[204:207], v[80:83], a[112:127]\n\tv_max3_f32 %0, %0, v148, v149\n\tv_max3_f32 %1, %1, v150, v151\n\tv_max3_f32 %0, %0, v180, v181\n\tv_max3_f32 %1, %1, v182, v183" : "+v"(c.pmx[1][0]), "+v"(c.pmx[1][1]) :  : "memory");
  asm volatile("s_waitcnt lgkmcnt(4)\n\tv_mfma_f32_32x32x16_bf16 a[0:15], a[208:211], v[68:71], a[0:15]\n\tds_read_b128 a[232:235], %2 offset:57344\n\tv_max3_f32 %0, %0, v152, v153\n\tv_max3_f32 %1, %1, v154, v155\n\tv_max3_f32 %0, %0, v184, v185\n\tv_max3_f32 %1, %1, v186, v187" : "+v"(c.pmx[1][0]), "+v"(c.pmx[1][1]) : "v"(c.vb_[2]) : "memory");
  asm volatile("v_mfma_f32_32x32x16_bf16 a[16:31], a[208:211], v[84:87], a[16:31]\n\tv_max3_f32 %0, %0, v156, v157\n\tv_max3_f32 %1, %1, v158, v159\n\tv_max3_f32 %0, %0, v188, v189\n\tv_max3_f32 %1, %1, v190, v191" : "+v"(c.pmx[1][0]), "+v"(c.pmx[1][1]) :  : "memory");
  asm volatile("v_mfma_f32_32x32x16_bf16 a[32:47], a[212:215], v[68:71], a[32:47]\n\tds_read_b128 a[236:239], %2 offset:61440\n\tv_max_f32_e32 %0, %0, %1\n\tv_mov_b32_e32 %1, %0\n\ts_nop 1\n\tv_permlane32_swap_b32_e32 %0, %1\n\tv_max_f32_e32 %0, %0, %1" : "+v"(c.pmx[0][0]), "+v"(c.pmx[0][1]) : "v"(c.vb_[2]) : "memory", ACL64);
  asm volatile("v_mfma_f32_32x32x16_bf16 a[48:63], a[212:215], v[84:87], a[48:63]\n\tv_max_f32_e32 %0, %0, %1\n\tv_mov_b32_e32 %1, %0\n\ts_nop 1\n\tv_permlane32_swap_b32_e32 %0, %1\n\tv_max_f32_e32 %0, %0, %1" : "+v"(c.pmx[1][0]), "+v"(c.pmx[1][1]) :  : "memory");
}
DI void gB0b(AC& c) {
  asm volatile("s_waitcnt lgkmcnt(4)\n\tv_mfma_f32_32x32x16_bf16 a[64:79], a[216:219], v[68:71], a[64:79]\n\tds_read_b128 a[240:243], %0 offset:49152" :  : "v"(c.vb_[3]) : "memory");
  asm volatile("v_mfma_f32_32x32x16_bf16 a[80:95], a[216:219], v[84:87], a[80:95]\n\tv_exp_f32_e32 v128, v128\n\tv_exp_f32_e32 v129, v129" :  :  : "memory");
  asm volatile("v_mfma_f32_32x32x16_bf16 a[96:111], a[220:223], v[68:71], a[96:111]\n\tds_read_b128 a[244:247], %0 offset:53248\n\tv_exp_f32_e32 v130, v130\n\tv_exp_f32_e32 v131, v131" :  : "v"(c.vb_[3]) : "memory");
  asm volatile("v_mfma_f32_32x32x16_bf16 a[112:127], a[220:223], v[84:87], a[112:127]\n\tv_exp_f32_e32 v132, v132\n\tv_exp_f32_e32 v133, v133" :  :  : "memory");
  asm volatile("s_waitcnt lgkmcnt(4)\n\tv_mfma_f32_32x32x16_bf16 a[0:15], a[224:227], v[72:75], a[0:15]\n\tds_read_b128 a[248:251], %1 offset:57344\n\tv_exp_f32_e32 v134, v134\n\tv_exp_f32_e32 v135, v135\n\tv_add_f32_e32 %0, %0, v128" : "+v"(c.rs[0][0]) : "v"(c.vb_[3]) : "memory");
  asm volatile("v_mfma_f32_32x32x16_bf16 a[16:31], a[224:227], v[88:91], a[16:31]\n\tv_exp_f32_e32 v136, v136\n\tv_exp_f32_e32 v137, v137\n\tv_add_f32_e32 %0, %0, v129" : "+v"(c.rs[0][1]) :  : "memory");
  asm volatile("v_mfma_f32_32x32x16_bf16 a[32:47], a[228:231], v[72:75], a[32:47]\n\tds_read_b128 a[252:255], %1 offset:61440\n\tv_exp_f32_e32 v138, v138\n\tv_exp_f32_e32 v139, v139\n\tv_add_f32_e32 %0, %0, v130" : "+v"(c.rs[0][0]) : "v"(c.vb_[3]) : "memory", ACL64);
  asm volatile("v_mfma_f32_32x32x16_bf16 a[48:63], a[228:231], v[88:91], a[48:63]\n\tv_exp_f32_e32 v140, v140\n\tv_exp_f32_e32 v141, v141\n\tv_add_f32_e32 %0, %0, v131" : "+v"(c.rs[0][1]) :  : "memory");
  asm volatile("s_waitcnt lgkmcnt(4)\n\tv_mfma_f32_32x32x16_bf16 a[64:79], a[232:235], v[72:75], a[64:79]\n\tds_read_b128 a[192:195], %1 offset:16384\n\tv_exp_f32_e32 v142, v142\n\tv_exp_f32_e32 v143, v143\n\tv_add_f32_e32 %0, %0, v132" : "+v"(c.rs[0][0]) : "v"(c.kb_[0]) : "memory");
  asm volatile("v_mfma_f32_32x32x16_bf16 a[80:95], a[232:235], v[88:91], a[80:95]\n\tv_exp_f32_e32 v144, v144\n\tv_exp_f32_e32 v145, v145\n\tv_add_f32_e32 %0, %0, v133" : "+v"(c.rs[0][1]) :  : "memory");
  asm volatile("v_mfma_f32_32x32x16_bf16 a[96:111], a[236:239], v[72:75], a[96:111]\n\tds_read_b128 a[196:199], %1 offset:24576\n\tv_exp_f32_e32 v146, v146\n\tv_exp_f32_e32 v147, v147\n\tv_add_f32_e32 %0, %0, v134" : "+v"(c.rs[0][0]) : "v"(c.kb_[0]) : "memory");
  asm volatile("v_mfma_f32_32x32x16_bf16 a[112:127], a[236:239], v[88:91], a[112:127]\n\tv_exp_f32_e32 v148, v148\n\tv_exp_f32_e32 v149, v149\n\tv_add_f32_e32 %0, %0, v135" : "+v"(c.rs[0][1]) :  : "memory");
  asm volatile("s_waitcnt lgkmcnt(4)\n\tv_mfma_f32_32x32x16_bf16 a[0:15], a[240:243], v[76:79], a[0:15]\n\tds_read_b128 a[200:203], %1 offset:16384\n\tv_exp_f32_e32 v150, v150\n\tv_exp_f32_e32 v151, v151\n\tv_add_f32_e32 %0, %0, v136" : "+v"(c.rs[0][0]) : "v"(c.kb_[1]) : "memory");
  asm volatile("v_mfma_f32_32x32x16_bf16 a[16:31], a[240:243], v[92:95], a[16:31]\n\tv_exp_f32_e32 v152, v152\n\tv_exp_f32_e32 v153, v153\n\tv_add_f32_e32 %0, %0, v137" : "+v"(c.rs[0][1]) :  : "memory");
  asm volatile("v_mfma_f32_32x32x16_bf16 a[32:47], a[244:247], v[76:79], a[32:47]\n\tds_read_b128 a[204:207], %1 offset:24576\n\tv_exp_f32_e32 v154, v154\n\tv_add_f32_e32 %0, %0, v138" : "+v"(c.rs[0][0]) : "v"(c.kb_[1]) : "memory", ACL64);
  asm volatile("v_mfma_f32_32x32x16_bf16 a[48:63], a[244:247], v[92:95], a[48:63]\n\tv_exp_f32_e32 v155, v155\n\tv_add_f32_e32 %0, %0, v139" : "+v"(c.rs[0][1]) :  : "memory");
  asm volatile("s_waitcnt lgkmcnt(4)\n\tv_mfma_f32_32x32x16_bf16 a[64:79], a[248:251], v[76:79], a[64:79]\n\tds_read_b128 a[208:211], %1 offset:16384\n\tv_exp_f32_e32 v156, v156\n\tv_add_f32_e32 %0, %0, v140" : "+v"(c.rs[0][0]) : "v"(c.kb_[2]) : "memory");
  asm volatile("v_mfma_f32_32x32x16_bf16 a[80:95], a[248:251], v[92:95], a[80:95]\n\tv_exp_f32_e32 v157, v157\n\tv_add_f32_e32 %0, %0, v141" : "+v"(c.rs[0][1]) :  : "memory");
  asm volatile("v_mfma_f32_32x32x16_bf16 a[96:111], a[252:255], v[76:79], a[96:111]\n\tds_read_b128 a[212:215], %1 offset:24576\n\tv_exp_f32_e32 v158, v158\n\tv_add_f32_e32 %0, %0, v142" : "+v"(c.rs[0][0]) : "v"(c.kb_[2]) : "memory");
  asm volatile("v_mfma_f32_32x32x16_bf16 a[112:127], a[252:255], v[92:95], a[112:127]\n\tv_exp_f32_e32 v159, v159\n\tv_add_f32_e32 %0, %0, v143" : "+v"(c.rs[0][1]) :  : "memory");
}
DI void s_sub0(const float (&dl)[2]) {
  asm volatile("v_sub_f32_e32 v128, v128, %0\n\tv_sub_f32_e32 v129, v129, %0\n\tv_sub_f32_e32 v130, v130, %0\n\tv_sub_f32_e32 v131, v131, %0\n\tv_sub_f32_e32 v132, v132, %0\n\tv_sub_f32_e32 v133, v133, %0\n\tv_sub_f32_e32 v134, v134, %0\n\tv_sub_f32_e32 v135, v135, %0\n\tv_sub_f32_e32 v136, v136, %0\n\tv_sub_f32_e32 v137, v137, %0\n\tv_sub_f32_e32 v138, v138, %0\n\tv_sub_f32_e32 v139, v139, %0\n\tv_sub_f32_e32 v140, v140, %0\n\tv_sub_f32_e32 v141, v141, %0\n\tv_sub_f32_e32 v142, v142, %0\n\tv_sub_f32_e32 v143, v143, %0\n\tv_sub_f32_e32 v160, v160, %0\n\tv_sub_f32_e32 v161, v161, %0\n\tv_sub_f32_e32 v162, v162, %0\n\tv_sub_f32_e32 v163, v163, %0\n\tv_sub_f32_e32 v164, v164, %0\n\tv_sub_f32_e32 v165, v165, %0\n\tv_sub_f32_e32 v166, v166, %0\n\tv_sub_f32_e32 v167, v167, %0\n\tv_sub_f32_e32 v168, v168, %0\n\tv_sub_f32_e32 v169, v169, %0\n\tv_sub_f32_e32 v170, v170, %0\n\tv_sub_f32_e32 v171, v171, %0\n\tv_sub_f32_e32 v172, v172, %0\n\tv_sub_f32_e32 v173, v173, %0\n\tv_sub_f32_e32 v174, v174, %0\n\tv_sub_f32_e32 v175, v175, %0\n\tv_sub_f32_e32 v144, v144, %1\n\tv_sub_f32_e32 v145, v145, %1\n\tv_sub_f32_e32 v146, v146, %1\n\tv_sub_f32_e32 v147, v147, %1\n\tv_sub_f32_e32 v148, v148, %1\n\tv_sub_f32_e32 v149, v149, %1\n\tv_sub_f32_e32 v150, v150, %1\n\tv_sub_f32_e32 v151, v151, %1\n\tv_sub_f32_e32 v152, v152, %1\n\tv_sub_f32_e32 v153, v153, %1\n\tv_sub_f32_e32 v154, v154, %1\n\tv_sub_f32_e32 v155, v155, %1\n\tv_sub_f32_e32 v156, v156, %1\n\tv_sub_f32_e32 v157, v157, %1\n\tv_sub_f32_e32 v158, v158, %1\n\tv_sub_f32_e32 v159, v159, %1\n\tv_sub_f32_e32 v176, v176, %1\n\tv_sub_f32_e32 v177, v177, %1\n\tv_sub_f32_e32 v178, v178, %1\n\tv_sub_f32_e32 v179, v179, %1\n\tv_sub_f32_e32 v180, v180, %1\n\tv_sub_f32_e32 v181, v181, %1\n\tv_sub_f32_e32 v182, v182, %1\n\tv_sub_f32_e32 v183, v183, %1\n\tv_sub_f32_e32 v184, v184, %1\n\tv_sub_f32_e32 v185, v185, %1\n\tv_sub_f32_e32 v186, v186, %1\n\tv_sub_f32_e32 v187, v187, %1\n\tv_sub_f32_e32 v188, v188, %1\n\tv_sub_f32_e32 v189, v189, %1\n\tv_sub_f32_e32 v190, v190, %1\n\tv_sub_f32_e32 v191, v191, %1" :  : "v"(dl[0]), "v"(dl[1]) : "memory");
}
DI void s_mask0_full(const float neg) {
  asm volatile("s_nop 7\n\ts_nop 7\n\tv_mov_b32_e32 v160, %0\n\tv_mov_b32_e32 v161, %0\n\tv_mov_b32_e32 v162, %0\n\tv_mov_b32_e32 v163, %0\n\tv_mov_b32_e32 v164, %0\n\tv_mov_b32_e32 v165, %0\n\tv_mov_b32_e32 v166, %0\n\tv_mov_b32_e32 v167, %0\n\tv_mov_b32_e32 v168, %0\n\tv_mov_b32_e32 v169, %0\n\tv_mov_b32_e32 v170, %0\n\tv_mov_b32_e32 v171, %0\n\tv_mov_b32_e32 v172, %0\n\tv_mov_b32_e32 v173, %0\n\tv_mov_b32_e32 v174, %0\n\tv_mov_b32_e32 v175, %0\n\tv_mov_b32_e32 v128, %0\n\tv_mov_b32_e32 v144, %0\n\tv_mov_b32_e32 v176, %0\n\tv_mov_b32_e32 v129, %0\n\tv_mov_b32_e32 v145, %0\n\tv_mov_b32_e32 v177, %0\n\tv_mov_b32_e32 v130, %0\n\tv_mov_b32_e32 v146, %0\n\tv_mov_b32_e32 v178, %0\n\tv_mov_b32_e32 v131, %0\n\tv_mov_b32_e32 v147, %0\n\tv_mov_b32_e32 v179, %0\n\tv_mov_b32_e32 v132, %0\n\tv_mov_b32_e32 v148, %0\n\tv_mov_b32_e32 v180, %0\n\tv_mov_b32_e32 v133, %0\n\tv_mov_b32_e32 v149, %0\n\tv_mov_b32_e32 v181, %0\n\tv_mov_b32_e32 v134, %0\n\tv_mov_b32_e32 v150, %0\n\tv_mov_b32_e32 v182, %0\n\tv_mov_b32_e32 v135, %0\n\tv_mov_b32_e32 v151, %0\n\tv_mov_b32_e32 v183, %0\n\tv_mov_b32_e32 v136, %0\n\tv_mov_b32_e32 v152, %0\n\tv_mov_b32_e32 v184, %0\n\tv_mov_b32_e32 v137, %0\n\tv_mov_b32_e32 v153, %0\n\tv_mov_b32_e32 v185, %0\n\tv_mov_b32_e32 v138, %0\n\tv_mov_b32_e32 v154, %0\n\tv_mov_b32_e32 v186, %0\n\tv_mov_b32_e32 v139, %0\n\tv_mov_b32_e32 v155, %0\n\tv_mov_b32_e32 v187, %0\n\tv_mov_b32_e32 v140, %0\n\tv_mov_b32_e32 v156, %0\n\tv_mov_b32_e32 v188, %0\n\tv_mov_b32_e32 v141, %0\n\tv_mov_b32_e32 v157, %0\n\tv_mov_b32_e32 v189, %0\n\tv_mov_b32_e32 v142, %0\n\tv_mov_b32_e32 v158, %0\n\tv_mov_b32_e32 v190, %0\n\tv_mov_b32_e32 v143, %0\n\tv_mov_b32_e32 v159, %0\n\tv_mov_b32_e32 v191, %0" :  : "v"(neg) : "memory");
}
DI void s_mask0_tri(const float neg) {
  asm volatile("s_nop 7\n\ts_nop 7\n\tv_mov_b32_e32 v160, %0\n\tv_mov_b32_e32 v161, %0\n\tv_mov_b32_e32 v162, %0\n\tv_mov_b32_e32 v163, %0\n\tv_mov_b32_e32 v164, %0\n\tv_mov_b32_e32 v165, %0\n\tv_mov_b32_e32 v166, %0\n\tv_mov_b32_e32 v167, %0\n\tv_mov_b32_e32 v168, %0\n\tv_mov_b32_e32 v169, %0\n\tv_mov_b32_e32 v170, %0\n\tv_mov_b32_e32 v171, %0\n\tv_mov_b32_e32 v172, %0\n\tv_mov_b32_e32 v173, %0\n\tv_mov_b32_e32 v174, %0\n\tv_mov_b32_e32 v175, %0\n\ts_mov_b32 vcc_lo, 0x0\n\ts_mov_b32 vcc_hi, 0xf\n\tv_cndmask_b32_e32 v128, v128, %0, vcc\n\tv_cndmask_b32_e32 v176, v176, %0, vcc\n\ts_mov_b32 vcc_lo, 0x1\n\ts_mov_b32 vcc_hi, 0x1f\n\tv_cndmask_b32_e32 v129, v129, %0, vcc\n\tv_cndmask_b32_e32 v177, v177, %0, vcc\n\ts_mov_b32 vcc_lo, 0x3\n\ts_mov_b32 vcc_hi, 0x3f\n\tv_cndmask_b32_e32 v130, v130, %0, vcc\n\tv_cndmask_b32_e32 v178, v178, %0, vcc\n\ts_mov_b32 vcc_lo, 0x7\n\ts_mov_b32 vcc_hi, 0x7f\n\tv_cndmask_b32_e32 v131, v131, %0, vcc\n\tv_cndmask_b32_e32 v179, v179, %0, vcc\n\ts_mov_b32 vcc_lo, 0xff\n\ts_mov_b32 vcc_hi, 0xfff\n\tv_cndmask_b32_e32 v132, v132, %0, vcc\n\tv_cndmask_b32_e32 v180, v180, %0, vcc\n\ts_mov_b32 vcc_lo, 0x1ff\n\ts_mov_b32 vcc_hi, 0x1fff\n\tv_cndmask_b32_e32 v133, v133, %0, vcc\n\tv_cndmask_b32_e32 v181, v181, %0, vcc\n\ts_mov_b32 vcc_lo, 0x3ff\n\ts_mov_b32 vcc_hi, 0x3fff\n\tv_cndmask_b32_e32 v134, v134, %0, vcc\n\tv_cndmask_b32_e32 v182, v182, %0, vcc\n\ts_mov_b32 vcc_lo, 0x7ff\n\ts_mov_b32 vcc_hi, 0x7fff\n\tv_cndmask_b32_e32 v135, v135, %0, vcc\n\tv_cndmask_b32_e32 v183, v183, %0, vcc\n\ts_mov_b32 vcc_lo, 0xffff\n\ts_mov_b32 vcc_hi, 0xfffff\n\tv_cndmask_b32_e32 v136, v136, %0, vcc\n\tv_cndmask_b32_e32 v184, v184, %0, vcc\n\ts_mov_b32 vcc_lo, 0x1ffff\n\ts_mov_b32 vcc_hi, 0x1fffff\n\tv_cndmask_b32_e32 v137, v137, %0, vcc\n\tv_cndmask_b32_e32 v185, v185, %0, vcc\n\ts_mov_b32 vcc_lo, 0x3ffff\n\ts_mov_b32 vcc_hi, 0x3fffff\n\tv_cndmask_b32_e32 v138, v138, %0, vcc\n\tv_cndmask_b32_e32 v186, v186, %0, vcc\n\ts_mov_b32 vcc_lo, 0x7ffff\n\ts_mov_b32 vcc_hi, 0x7fffff\n\tv_cndmask_b32_e32 v139, v139, %0, vcc\n\tv_cndmask_b32_e32 v187, v187, %0, vcc\n\ts_mov_b32 vcc_lo, 0xffffff\n\ts_mov_b32 vcc_hi, 0xfffffff\n\tv_cndmask_b32_e32 v140, v140, %0, vcc\n\tv_cndmask_b32_e32 v188, v188, %0, vcc\n\ts_mov_b32 vcc_lo, 0x1ffffff\n\ts_mov_b32 vcc_hi, 0x1fffffff\n\tv_cndmask_b32_e32 v141, v141, %0, vcc\n\tv_cndmask_b32_e32 v189, v189, %0, vcc\n\ts_mov_b32 vcc_lo, 0x3ffffff\n\ts_mov_b32 vcc_hi, 0x3fffffff\n\tv_cndmask_b32_e32 v142, v142, %0, vcc\n\tv_cndmask_b32_e32 v190, v190, %0, vcc\n\ts_mov_b32 vcc_lo, 0x7ffffff\n\ts_mov_b32 vcc_hi, 0x7fffffff\n\tv_cndmask_b32_e32 v143, v143, %0, vcc\n\tv_cndmask_b32_e32 v191, v191, %0, vcc" :  : "v"(neg) : "memory", "vcc", ACL64);
}
DI void gA1(AC& c) {
  asm volatile("s_waitcnt lgkmcnt(4)\n\tv_mfma_f32_32x32x16_bf16 v[192:207], a[192:195], a[128:131], v[96:111]\n\tds_read_b128 a[216:219], %0 offset:16384\n\tv_exp_f32_e32 v160, v160\n\tv_exp_f32_e32 v161, v161\n\tv_cvt_pk_bf16_f32 v64, v128, v129" :  : "v"(c.kb_[3]) : "memory");
  asm volatile("v_mfma_f32_32x32x16_bf16 v[208:223], a[192:195], a[160:163], v[112:127]\n\tv_exp_f32_e32 v162, v162\n\tv_exp_f32_e32 v163, v163\n\tv_cvt_pk_bf16_f32 v65, v130, v131\n\tv_add_f32_e32 %0, %0, v160" : "+v"(c.rs[0][1]) :  : "memory");
  asm volatile("v_mfma_f32_32x32x16_bf16 v[224:239], a[196:199], a[128:131], v[96:111]\n\tds_read_b128 a[220:223], %1 offset:24576\n\tv_exp_f32_e32 v164, v164\n\tv_cvt_pk_bf16_f32 v66, v132, v133\n\tv_add_f32_e32 %0, %0, v161" : "+v"(c.rs[0][0]) : "v"(c.kb_[3]) : "memory");
  asm volatile("v_mfma_f32_32x32x16_bf16 v[240:255], a[196:199], a[160:163], v[112:127]\n\tv_exp_f32_e32 v165, v165\n\tv_cvt_pk_bf16_f32 v67, v134, v135\n\tv_add_f32_e32 %0, %0, v162" : "+v"(c.rs[0][1]) :  : "memory");
  asm volatile("s_waitcnt lgkmcnt(4)\n\tv_mfma_f32_32x32x16_bf16 v[192:207], a[200:203], a[132:135], v[192:207]\n\tds_read_b128 a[224:227], %1 offset:16384\n\tv_exp_f32_e32 v166, v166\n\tv_cvt_pk_bf16_f32 v80, v144, v145\n\tv_add_f32_e32 %0, %0, v163" : "+v"(c.rs[0][0]) : "v"(c.kb_[4]) : "memory");
  asm volatile("v_mfma_f32_32x32x16_bf16 v[208:223], a[200:203], a[164:167], v[208:223]\n\tv_exp_f32_e32 v167, v167\n\tv_cvt_pk_bf16_f32 v81, v146, v147\n\tv_add_f32_e32 %0, %0, v164" : "+v"(c.rs[0][1]) :  : "memory");
  asm volatile("v_mfma_f32_32x32x16_bf16 v[224:239], a[204:207], a[132:135], v[224:239]\n\tds_read_b128 a[228:231], %1 offset:24576\n\tv_exp_f32_e32 v168, v168\n\tv_cvt_pk_bf16_f32 v82, v148, v149\n\tv_add_f32_e32 %0, %0, v165" : "+v"(c.rs[0][0]) : "v"(c.kb_[4]) : "memory");
  asm volatile("v_mfma_f32_32x32x16_bf16 v[240:255], a[204:207], a[164:167], v[240:255]\n\tv_exp_f32_e32 v169, v169\n\tv_cvt_pk_bf16_f32 v83, v150, v151\n\tv_add_f32_e32 %0, %0, v166" : "+v"(c.rs[0][1]) :  : "memory", ACL64);
  asm volatile("s_waitcnt lgkmcnt(4)\n\tv_mfma_f32_32x32x16_bf16 v[192:207], a[208:211], a[136:139], v[192:207]\n\tds_read_b128 a[232:235], %1 offset:16384\n\tv_exp_f32_e32 v170, v170\n\tv_cvt_pk_bf16_f32 v68, v136, v137\n\tv_add_f32_e32 %0, %0, v167" : "+v"(c.rs[0][0]) : "v"(c.kb_[5]) : "memory");
  asm volatile("v_mfma_f32_32x32x16_bf16 v[208:223], a[208:211], a[168:171], v[208:223]\n\tv_exp_f32_e32 v171, v171\n\tv_cvt_pk_bf16_f32 v69, v138, v139\n\tv_add_f32_e32 %0, %0, v168" : "+v"(c.rs[0][1]) :  : "memory");
  asm volatile("v_mfma_f32_32x32x16_bf16 v[224:239], a[212:215], a[136:139], v[224:239]\n\tds_read_b128 a[236:239], %1 offset:24576\n\tv_exp_f32_e32 v172, v172\n\tv_cvt_pk_bf16_f32 v70, v140, v141\n\tv_add_f32_e32 %0, %0, v169" : "+v"(c.rs[0][0]) : "v"(c.kb_[5]) : "memory");
  asm volatile("v_mfma_f32_32x32x16_bf16 v[240:255], a[212:215], a[168:171], v[240:255]\n\tv_exp_f32_e32 v173, v173\n\tv_cvt_pk_bf16_f32 v71, v142, v143\n\tv_add_f32_e32 %0, %0, v170" : "+v"(c.rs[0][1]) :  : "memory");
  asm volatile("s_waitcnt lgkmcnt(4)\n\tv_mfma_f32_32x32x16_bf16 v[192:207], a[216:219], a[140:143], v[192:207]\n\tds_read_b128 a[240:243], %1 offset:16384\n\tv_exp_f32_e32 v174, v174\n\tv_cvt_pk_bf16_f32 v84, v152, v153\n\tv_add_f32_e32 %0, %0, v171" : "+v"(c.rs[0][0]) : "v"(c.kb_[6]) : "memory");
  asm volatile("v_mfma_f32_32x32x16_bf16 v[208:223], a[216:219], a[172:175], v[208:223]\n\tv_exp_f32_e32 v175, v175\n\tv_cvt_pk_bf16_f32 v85, v154, v155\n\tv_add_f32_e32 %0, %0, v172" : "+v"(c.rs[0][1]) :  : "memory");
  asm volatile("v_mfma_f32_32x32x16_bf16 v[224:239], a[220:223], a[140:143], v[224:239]\n\tds_read_b128 a[244:247], %1 offset:24576\n\tv_exp_f32_e32 v176, v176\n\tv_cvt_pk_bf16_f32 v86, v156, v157\n\tv_add_f32_e32 %0, %0, v173" : "+v"(c.rs[0][0]) : "v"(c.kb_[6]) : "memory");
  asm volatile("v_mfma_f32_32x32x16_bf16 v[240:255], a[220:223], a[172:175], v[240:255]\n\tv_exp_f32_e32 v177, v177\n\tv_cvt_pk_bf16_f32 v87, v158, v159\n\tv_add_f32_e32 %0, %0, v174" : "+v"(c.rs[0][1]) :  : "memory", ACL64);
  asm volatile("s_waitcnt lgkmcnt(4)\n\tv_mfma_f32_32x32x16_bf16 v[192:207], a[224:227], a[144:147], v[192:207]\n\tds_read_b128 a[248:251], %2 offset:16384\n\tv_exp_f32_e32 v178, v178\n\tv_cvt_pk_bf16_f32 v72, v160, v161\n\tv_add_f32_e32 %0, %0, v175\n\tv_add_f32_e32 %1, %1, v144" : "+v"(c.rs[0][0]), "+v"(c.rs[1][2]) : "v"(c.kb_[7]) : "memory");
  asm volatile("v_mfma_f32_32x32x16_bf16 v[208:223], a[224:227], a[176:179], v[208:223]\n\tv_exp_f32_e32 v179, v179\n\tv_cvt_pk_bf16_f32 v73, v162, v163\n\tv_add_f32_e32 %0, %0, v176\n\tv_add_f32_e32 %1, %1, v145" : "+v"(c.rs[1][1]), "+v"(c.rs[1][3]) :  : "memory");
  asm volatile("v_mfma_f32_32x32x16_bf16 v[224:239], a[228:231], a[144:147], v[224:239]\n\tds_read_b128 a[252:255], %2 offset:24576\n\tv_exp_f32_e32 v180, v180\n\tv_cvt_pk_bf16_f32 v74, v164, v165\n\tv_add_f32_e32 %0, %0, v177\n\tv_add_f32_e32 %1, %1, v146" : "+v"(c.rs[1][0]), "+v"(c.rs[1][2]) : "v"(c.kb_[7]) : "memory");
  asm volatile("v_mfma_f32_32x32x16_bf16 v[240:255], a[228:231], a[176:179], v[240:255]\n\tv_exp_f32_e32 v181, v181\n\tv_cvt_pk_bf16_f32 v75, v166, v167\n\tv_add_f32_e32 %0, %0, v178\n\tv_add_f32_e32 %1, %1, v147" : "+v"(c.rs[1][1]), "+v"(c.rs[1][3]) :  : "memory");
  asm volatile("s_waitcnt lgkmcnt(4)\n\tv_mfma_f32_32x32x16_bf16 v[192:207], a[232:235], a[148:151], v[192:207]\n\tv_exp_f32_e32 v182, v182\n\tv_cvt_pk_bf16_f32 v76, v168, v169\n\tv_add_f32_e32 %0, %0, v179\n\tv_add_f32_e32 %1, %1, v148" : "+v"(c.rs[1][0]), "+v"(c.rs[1][2]) :  : "memory");
  asm volatile("v_mfma_f32_32x32x16_bf16 v[208:223], a[232:235], a[180:183], v[208:223]\n\tv_exp_f32_e32 v183, v183\n\tv_cvt_pk_bf16_f32 v77, v170, v171\n\tv_add_f32_e32 %0, %0, v180\n\tv_add_f32_e32 %1, %1, v149" : "+v"(c.rs[1][1]), "+v"(c.rs[1][3]) :  : "memory");
  asm volatile("v_mfma_f32_32x32x16_bf16 v[224:239], a[236:239], a[148:151], v[224:239]\n\tv_exp_f32_e32 v184, v184\n\tv_cvt_pk_bf16_f32 v78, v172, v173\n\tv_add_f32_e32 %0, %0, v181\n\tv_add_f32_e32 %1, %1, v150" : "+v"(c.rs[1][0]), "+v"(c.rs[1][2]) :  : "memory");
  asm volatile("v_mfma_f32_32x32x16_bf16 v[240:255], a[236:239], a[180:183], v[240:255]\n\tv_exp_f32_e32 v185, v185\n\tv_cvt_pk_bf16_f32 v79, v174, v175\n\tv_add_f32_e32 %0, %0, v182\n\tv_add_f32_e32 %1, %1, v151" : "+v"(c.rs[1][1]), "+v"(c.rs[1][3]) :  : "memory", ACL64);
  asm volatile("s_waitcnt vmcnt(0) lgkmcnt(0)\n\ts_barrier\n\ts_waitcnt lgkmcnt(2)\n\tv_mfma_f32_32x32x16_bf16 v[192:207], a[240:243], a[152:155], v[192:207]\n\tds_read_b128 a[192:195], %2 offset:32768\n\tv_exp_f32_e32 v186, v186\n\tv_cvt_pk_bf16_f32 v88, v176, v177\n\tv_add_f32_e32 %0, %0, v183\n\tv_add_f32_e32 %1, %1, v152" : "+v"(c.rs[1][0]), "+v"(c.rs[1][2]) : "v"(c.vb_[0]) : "memory");
  asm volatile("v_mfma_f32_32x32x16_bf16 v[208:223], a[240:243], a[184:187], v[208:223]\n\tds_read_b128 a[196:199], %2 offset:36864\n\tv_exp_f32_e32 v187, v187\n\tv_cvt_pk_bf16_f32 v89, v178, v179\n\tv_add_f32_e32 %0, %0, v184\n\tv_add_f32_e32 %1, %1, v153" : "+v"(c.rs[1][1]), "+v"(c.rs[1][3]) : "v"(c.vb_[0]) : "memory");
  asm volatile("v_mfma_f32_32x32x16_bf16 v[224:239], a[244:247], a[152:155], v[224:239]\n\tds_read_b128 a[200:203], %2 offset:40960\n\tv_exp_f32_e32 v188, v188\n\tv_cvt_pk_bf16_f32 v90, v180, v181\n\tv_add_f32_e32 %0, %0, v185\n\tv_add_f32_e32 %1, %1, v154\n\ts_add_u32 m0, %3, 49152\n\ts_nop 0\n\tbuffer_load_dwordx4 %4, %5, %6 offen offset:0 sc1 lds\n\tbuffer_load_dwordx4 %4, %5, %6 offen offset:1024 sc1 lds" : "+v"(c.rs[1][0]), "+v"(c.rs[1][2]) : "v"(c.vb_[0]), "s"(c.m0w), "v"(c.vlin), "s"(c.srdV), "s"(c.soVA) : "memory", "m0", "scc");
  asm volatile("v_mfma_f32_32x32x16_bf16 v[240:255], a[244:247], a[184:187], v[240:255]\n\tds_read_b128 a[204:207], %2 offset:45056\n\tv_exp_f32_e32 v189, v189\n\tv_cvt_pk_bf16_f32 v91, v182, v183\n\tv_add_f32_e32 %0, %0, v186\n\tv_add_f32_e32 %1, %1, v155" : "+v"(c.rs[1][1]), "+v"(c.rs[1][3]) : "v"(c.vb_[0]) : "memory");
  asm volatile("s_waitcnt lgkmcnt(4)\n\tv_mfma_f32_32x32x16_bf16 v[192:207], a[248:251], a[156:159], v[192:207]\n\tds_read_b128 a[208:211], %2 offset:32768\n\tv_exp_f32_e32 v190, v190\n\tv_cvt_pk_bf16_f32 v92, v184, v185\n\tv_add_f32_e32 %0, %0, v187\n\tv_add_f32_e32 %1, %1, v156\n\ts_add_u32 m0, %3, 49152\n\ts_nop 0\n\tbuffer_load_dwordx4 %4, %5, %6 offen offset:2048 sc1 lds\n\tbuffer_load_dwordx4 %4, %5, %6 offen offset:3072 sc1 lds" : "+v"(c.rs[1][0]), "+v"(c.rs[1][2]) : "v"(c.vb_[1]), "s"(c.m0w), "v"(c.vlin), "s"(c.srdV), "s"(c.soVA) : "memory", "m0", "scc");
  asm volatile("v_mfma_f32_32x32x16_bf16 v[208:223], a[248:251], a[188:191], v[208:223]\n\tv_exp_f32_e32 v191, v191\n\tv_cvt_pk_bf16_f32 v93, v186, v187\n\tv_add_f32_e32 %0, %0, v188\n\tv_add_f32_e32 %1, %1, v157" : "+v"(c.rs[1][1]), "+v"(c.rs[1][3]) :  : "memory");
  asm volatile("v_mfma_f32_32x32x16_bf16 v[224:239], a[252:255], a[156:159], v[224:239]\n\tds_read_b128 a[212:215], %2 offset:36864\n\tv_cvt_pk_bf16_f32 v94, v188, v189\n\tv_add_f32_e32 %0, %0, v189\n\tv_add_f32_e32 %1, %1, v158\n\ts_cmp_le_i32 %3, 14\n\ts_cbranch_scc0 .Ldma426_%=\n\ts_add_u32 m0, %4, 16384\n\ts_nop 0\n\tbuffer_load_dwordx4 %5, %6, %7 offen offset:0 sc1 lds\n\tbuffer_load_dwordx4 %5, %6, %7 offen offset:1024 sc1 lds\n\t.Ldma426_%=:" : "+v"(c.rs[1][0]), "+v"(c.rs[1][2]) : "v"(c.vb_[1]), "s"(c.t), "s"(c.m0w), "v"(c.vlin), "s"(c.srdK), "s"(c.soKA) : "memory", "scc", "m0");
  asm volatile("v_mfma_f32_32x32x16_bf16 v[240:255], a[252:255], a[188:191], v[240:255]\n\tv_cvt_pk_bf16_f32 v95, v190, v191\n\tv_add_f32_e32 %0, %0, v190\n\tv_add_f32_e32 %1, %1, v191\n\tv_add_f32_e32 %2, %2, v159\n\ts_cmp_le_i32 %3, 14\n\ts_cbranch_scc0 .Ldma427_%=\n\ts_add_u32 m0, %4, 16384\n\ts_nop 0\n\tbuffer_load_dwordx4 %5, %6, %7 offen offset:2048 sc1 lds\n\tbuffer_load_dwordx4 %5, %6, %7 offen offset:3072 sc1 lds\n\t.Ldma427_%=:" : "+v"(c.rs[1][1]), "+v"(c.rs[1][0]), "+v"(c.rs[1][3]) : "s"(c.t), "s"(c.m0w), "v"(c.vlin), "s"(c.srdK), "s"(c.soKA) : "memory", "scc", "m0", ACL64);
}
DI void gB1a(AC& c) {
  asm volatile("s_waitcnt lgkmcnt(4)\n\tv_mfma_f32_32x32x16_bf16 a[0:15], a[192:195], v[64:67], a[0:15]\n\tds_read_b128 a[216:219], %0 offset:40960\n\ts_cmp_ge_i32 %1, %2\n\ts_cbranch_scc0 .Ldma428_%=\n\ts_add_u32 m0, %3, 114688\n\ts_nop 0\n\tbuffer_load_dwordx4 %4, %5, %6 offen offset:0 sc1 lds\n\tbuffer_load_dwordx4 %4, %5, %6 offen offset:1024 sc1 lds\n\tbuffer_load_dwordx4 %4, %5, %6 offen offset:2048 sc1 lds\n\tbuffer_load_dwordx4 %4, %5, %6 offen offset:3072 sc1 lds\n\t.Ldma428_%=:" :  : "v"(c.vb_[1]), "s"(c.t), "s"(c.n1), "s"(c.m0w), "v"(c.vlin), "s"(c.srdV), "s"(c.soVB) : "memory", "scc", "m0");
  asm volatile("v_mfma_f32_32x32x16_bf16 a[16:31], a[192:195], v[80:83], a[16:31]\n\ts_cmp_lt_u32 %0, %1\n\ts_cbranch_scc0 .Ldma429_%=\n\ts_add_u32 m0, %2, 81920\n\ts_nop 0\n\tbuffer_load_dwordx4 %3, %4, %5 offen offset:0 sc1 lds\n\tbuffer_load_dwordx4 %3, %4, %5 offen offset:1024 sc1 lds\n\tbuffer_load_dwordx4 %3, %4, %5 offen offset:2048 sc1 lds\n\tbuffer_load_dwordx4 %3, %4, %5 offen offset:3072 sc1 lds\n\t.Ldma429_%=:" :  : "s"(c.u3), "s"(c.lim3), "s"(c.m0w), "v"(c.vlin), "s"(c.srdK), "s"(c.soKB) : "memory", "scc", "m0");
  asm volatile("v_mfma_f32_32x32x16_bf16 a[32:47], a[196:199], v[64:67], a[32:47]\n\tds_read_b128 a[220:223], %2 offset:45056\n\tv_max3_f32 %0, v192, v193, v224\n\tv_max3_f32 %1, v194, v195, v225\n\tv_max3_f32 %0, %0, v226, v227" : "=&v"(c.pmx[0][0]), "=&v"(c.pmx[0][1]) : "v"(c.vb_[1]) : "memory");
  asm volatile("v_mfma_f32_32x32x16_bf16 a[48:63], a[196:199], v[80:83], a[48:63]\n\tv_max3_f32 %0, %0, v196, v197\n\tv_max3_f32 %1, %1, v198, v199\n\tv_max3_f32 %0, %0, v228, v229\n\tv_max3_f32 %1, %1, v230, v231" : "+v"(c.pmx[0][0]), "+v"(c.pmx[0][1]) :  : "memory");
  asm volatile("s_waitcnt lgkmcnt(4)\n\tv_mfma_f32_32x32x16_bf16 a[64:79], a[200:203], v[64:67], a[64:79]\n\tds_read_b128 a[224:227], %2 offset:32768\n\tv_max3_f32 %0, %0, v200, v201\n\tv_max3_f32 %1, %1, v202, v203\n\tv_max3_f32 %0, %0, v232, v233\n\tv_max3_f32 %1, %1, v234, v235" : "+v"(c.pmx[0][0]), "+v"(c.pmx[0][1]) : "v"(c.vb_[2]) : "memory");
  asm volatile("v_mfma_f32_32x32x16_bf16 a[80:95], a[200:203], v[80:83], a[80:95]\n\tv_max3_f32 %0, %0, v204, v205\n\tv_max3_f32 %1, %1, v206, v207\n\tv_max3_f32 %0, %0, v236, v237\n\tv_max3_f32 %1, %1, v238, v239" : "+v"(c.pmx[0][0]), "+v"(c.pmx[0][1]) :  : "memory");
  asm volatile("v_mfma_f32_32x32x16_bf16 a[96:111], a[204:207], v[64:67], a[96:111]\n\tds_read_b128 a[228:231], %2 offset:36864\n\tv_max3_f32 %0, v208, v209, v240\n\tv_max3_f32 %1, v210, v211, v241\n\tv_max3_f32 %0, %0, v242, v243" : "=&v"(c.pmx[1][0]), "=&v"(c.pmx[1][1]) : "v"(c.vb_[2]) : "memory");
  asm volatile("v_mfma_f32_32x32x16_bf16 a[112:127], a[204:207], v[80:83], a[112:127]\n\tv_max3_f32 %0, %0, v212, v213\n\tv_max3_f32 %1, %1, v214, v215\n\tv_max3_f32 %0, %0, v244, v245\n\tv_max3_f32 %1, %1, v246, v247" : "+v"(c.pmx[1][0]), "+v"(c.pmx[1][1]) :  : "memory", ACL64);
  asm volatile("s_waitcnt lgkmcnt(4)\n\tv_mfma_f32_32x32x16_bf16 a[0:15], a[208:211], v[68:71], a[0:15]\n\tds_read_b128 a[232:235], %2 offset:40960\n\tv_max3_f32 %0, %0, v216, v217\n\tv_max3_f32 %1, %1, v218, v219\n\tv_max3_f32 %0, %0, v248, v249\n\tv_max3_f32 %1, %1, v250, v251" : "+v"(c.pmx[1][0]), "+v"(c.pmx[1][1]) : "v"(c.vb_[2]) : "memory");
  asm volatile("v_mfma_f32_32x32x16_bf16 a[16:31], a[208:211], v[84:87], a[16:31]\n\tv_max3_f32 %0, %0, v220, v221\n\tv_max3_f32 %1, %1, v222, v223\n\tv_max3_f32 %0, %0, v252, v253\n\tv_max3_f32 %1, %1, v254, v255" : "+v"(c.pmx[1][0]), "+v"(c.pmx[1][1]) :  : "memory");
  asm volatile("v_mfma_f32_32x32x16_bf16 a[32:47], a[212:215], v[68:71], a[32:47]\n\tds_read_b128 a[236:239], %2 offset:45056\n\tv_max_f32_e32 %0, %0, %1\n\tv_mov_b32_e32 %1, %0\n\ts_nop 1\n\tv_permlane32_swap_b32_e32 %0, %1\n\tv_max_f32_e32 %0, %0, %1" : "+v"(c.pmx[0][0]), "+v"(c.pmx[0][1]) : "v"(c.vb_[2]) : "memory");
  asm volatile("v_mfma_f32_32x32x16_bf16 a[48:63], a[212:215], v[84:87], a[48:63]\n\tv_max_f32_e32 %0, %0, %1\n\tv_mov_b32_e32 %1, %0\n\ts_nop 1\n\tv_permlane32_swap_b32_e32 %0, %1\n\tv_max_f32_e32 %0, %0, %1" : "+v"(c.pmx[1][0]), "+v"(c.pmx[1][1]) :  : "memory");
}
DI void gB1b(AC& c) {
  asm volatile("s_waitcnt lgkmcnt(4)\n\tv_mfma_f32_32x32x16_bf16 a[64:79], a[216:219], v[68:71], a[64:79]\n\tds_read_b128 a[240:243], %0 offset:32768" :  : "v"(c.vb_[3]) : "memory");
  asm volatile("v_mfma_f32_32x32x16_bf16 a[80:95], a[216:219], v[84:87], a[80:95]\n\tv_exp_f32_e32 v192, v192\n\tv_exp_f32_e32 v193, v193" :  :  : "memory");
  asm volatile("v_mfma_f32_32x32x16_bf16 a[96:111], a[220:223], v[68:71], a[96:111]\n\tds_read_b128 a[244:247], %0 offset:36864\n\tv_exp_f32_e32 v194, v194\n\tv_exp_f32_e32 v195, v195" :  : "v"(c.vb_[3]) : "memory");
  asm volatile("v_mfma_f32_32x32x16_bf16 a[112:127], a[220:223], v[84:87], a[112:127]\n\tv_exp_f32_e32 v196, v196\n\tv_exp_f32_e32 v197, v197" :  :  : "memory", ACL64);
  asm volatile("s_waitcnt lgkmcnt(4)\n\tv_mfma_f32_32x32x16_bf16 a[0:15], a[224:227], v[72:75], a[0:15]\n\tds_read_b128 a[248:251], %1 offset:40960\n\tv_exp_f32_e32 v198, v198\n\tv_exp_f32_e32 v199, v199\n\tv_add_f32_e32 %0, %0, v192" : "+v"(c.rs[0][0]) : "v"(c.vb_[3]) : "memory");
  asm volatile("v_mfma_f32_32x32x16_bf16 a[16:31], a[224:227], v[88:91], a[16:31]\n\tv_exp_f32_e32 v200, v200\n\tv_exp_f32_e32 v201, v201\n\tv_add_f32_e32 %0, %0, v193" : "+v"(c.rs[0][1]) :  : "memory");
  asm volatile("v_mfma_f32_32x32x16_bf16 a[32:47], a[228:231], v[72:75], a[32:47]\n\tds_read_b128 a[252:255], %1 offset:45056\n\tv_exp_f32_e32 v202, v202\n\tv_exp_f32_e32 v203, v203\n\tv_add_f32_e32 %0, %0, v194" : "+v"(c.rs[0][0]) : "v"(c.vb_[3]) : "memory");
  asm volatile("v_mfma_f32_32x32x16_bf16 a[48:63], a[228:231], v[88:91], a[48:63]\n\tv_exp_f32_e32 v204, v204\n\tv_exp_f32_e32 v205, v205\n\tv_add_f32_e32 %0, %0, v195" : "+v"(c.rs[0][1]) :  : "memory");
  asm volatile("s_waitcnt lgkmcnt(4)\n\tv_mfma_f32_32x32x16_bf16 a[64:79], a[232:235], v[72:75], a[64:79]\n\tds_read_b128 a[192:195], %1 offset:0\n\tv_exp_f32_e32 v206, v206\n\tv_exp_f32_e32 v207, v207\n\tv_add_f32_e32 %0, %0, v196" : "+v"(c.rs[0][0]) : "v"(c.kb_[0]) : "memory");
  asm volatile("v_mfma_f32_32x32x16_bf16 a[80:95], a[232:235], v[88:91], a[80:95]\n\tv_exp_f32_e32 v208, v208\n\tv_exp_f32_e32 v209, v209\n\tv_add_f32_e32 %0, %0, v197" : "+v"(c.rs[0][1]) :  : "memory");
  asm volatile("v_mfma_f32_32x32x16_bf16 a[96:111], a[236:239], v[72:75], a[96:111]\n\tds_read_b128 a[196:199], %1 offset:8192\n\tv_exp_f32_e32 v210, v210\n\tv_exp_f32_e32 v211, v211\n\tv_add_f32_e32 %0, %0, v198" : "+v"(c.rs[0][0]) : "v"(c.kb_[0]) : "memory");
  asm volatile("v_mfma_f32_32x32x16_bf16 a[112:127], a[236:239], v[88:91], a[112:127]\n\tv_exp_f32_e32 v212, v212\n\tv_exp_f32_e32 v213, v213\n\tv_add_f32_e32 %0, %0, v199" : "+v"(c.rs[0][1]) :  : "memory", ACL64);
  asm volatile("s_waitcnt lgkmcnt(4)\n\tv_mfma_f32_32x32x16_bf16 a[0:15], a[240:243], v[76:79], a[0:15]\n\tds_read_b128 a[200:203], %1 offset:0\n\tv_exp_f32_e32 v214, v214\n\tv_exp_f32_e32 v215, v215\n\tv_add_f32_e32 %0, %0, v200" : "+v"(c.rs[0][0]) : "v"(c.kb_[1]) : "memory");
  asm volatile("v_mfma_f32_32x32x16_bf16 a[16:31], a[240:243], v[92:95], a[16:31]\n\tv_exp_f32_e32 v216, v216\n\tv_exp_f32_e32 v217, v217\n\tv_add_f32_e32 %0, %0, v201" : "+v"(c.rs[0][1]) :  : "memory");
  asm volatile("v_mfma_f32_32x32x16_bf16 a[32:47], a[244:247], v[76:79], a[32:47]\n\tds_read_b128 a[204:207], %1 offset:8192\n\tv_exp_f32_e32 v218, v218\n\tv_add_f32_e32 %0, %0, v202" : "+v"(c.rs[0][0]) : "v"(c.kb_[1]) : "memory");
  asm volatile("v_mfma_f32_32x32x16_bf16 a[48:63], a[244:247], v[92:95], a[48:63]\n\tv_exp_f32_e32 v219, v219\n\tv_add_f32_e32 %0, %0, v203" : "+v"(c.rs[0][1]) :  : "memory");
  asm volatile("s_waitcnt lgkmcnt(4)\n\tv_mfma_f32_32x32x16_bf16 a[64:79], a[248:251], v[76:79], a[64:79]\n\tds_read_b128 a[208:211], %1 offset:0\n\tv_exp_f32_e32 v220, v220\n\tv_add_f32_e32 %0, %0, v204" : "+v"(c.rs[0][0]) : "v"(c.kb_[2]) : "memory");
  asm volatile("v_mfma_f32_32x32x16_bf16 a[80:95], a[248:251], v[92:95], a[80:95]\n\tv_exp_f32_e32 v221, v221\n\tv_add_f32_e32 %0, %0, v205" : "+v"(c.rs[0][1]) :  : "memory");
  asm volatile("v_mfma_f32_32x32x16_bf16 a[96:111], a[252:255], v[76:79], a[96:111]\n\tds_read_b128 a[212:215], %1 offset:8192\n\tv_exp_f32_e32 v222, v222\n\tv_add_f32_e32 %0, %0, v206" : "+v"(c.rs[0][0]) : "v"(c.kb_[2]) : "memory");
  asm volatile("v_mfma_f32_32x32x16_bf16 a[112:127], a[252:255], v[92:95], a[112:127]\n\tv_exp_f32_e32 v223, v223\n\tv_add_f32_e32 %0, %0, v207" : "+v"(c.rs[0][1]) :  : "memory", ACL64);
}
DI void s_sub1(const float (&dl)[2]) {
  asm volatile("v_sub_f32_e32 v192, v192, %0\n\tv_sub_f32_e32 v193, v193, %0\n\tv_sub_f32_e32 v194, v194, %0\n\tv_sub_f32_e32 v195, v195, %0\n\tv_sub_f32_e32 v196, v196, %0\n\tv_sub_f32_e32 v197, v197, %0\n\tv_sub_f32_e32 v198, v198, %0\n\tv_sub_f32_e32 v199, v199, %0\n\tv_sub_f32_e32 v200, v200, %0\n\tv_sub_f32_e32 v201, v201, %0\n\tv_sub_f32_e32 v202, v202, %0\n\tv_sub_f32_e32 v203, v203, %0\n\tv_sub_f32_e32 v204, v204, %0\n\tv_sub_f32_e32 v205, v205, %0\n\tv_sub_f32_e32 v206, v206, %0\n\tv_sub_f32_e32 v207, v207, %0\n\tv_sub_f32_e32 v224, v224, %0\n\tv_sub_f32_e32 v225, v225, %0\n\tv_sub_f32_e32 v226, v226, %0\n\tv_sub_f32_e32 v227, v227, %0\n\tv_sub_f32_e32 v228, v228, %0\n\tv_sub_f32_e32 v229, v229, %0\n\tv_sub_f32_e32 v230, v230, %0\n\tv_sub_f32_e32 v231, v231, %0\n\tv_sub_f32_e32 v232, v232, %0\n\tv_sub_f32_e32 v233, v233, %0\n\tv_sub_f32_e32 v234, v234, %0\n\tv_sub_f32_e32 v235, v235, %0\n\tv_sub_f32_e32 v236, v236, %0\n\tv_sub_f32_e32 v237, v237, %0\n\tv_sub_f32_e32 v238, v238, %0\n\tv_sub_f32_e32 v239, v239, %0\n\tv_sub_f32_e32 v208, v208, %1\n\tv_sub_f32_e32 v209, v209, %1\n\tv_sub_f32_e32 v210, v210, %1\n\tv_sub_f32_e32 v211, v211, %1\n\tv_sub_f32_e32 v212, v212, %1\n\tv_sub_f32_e32 v213, v213, %1\n\tv_sub_f32_e32 v214, v214, %1\n\tv_sub_f32_e32 v215, v215, %1\n\tv_sub_f32_e32 v216, v216, %1\n\tv_sub_f32_e32 v217, v217, %1\n\tv_sub_f32_e32 v218, v218, %1\n\tv_sub_f32_e32 v219, v219, %1\n\tv_sub_f32_e32 v220, v220, %1\n\tv_sub_f32_e32 v221, v221, %1\n\tv_sub_f32_e32 v222, v222, %1\n\tv_sub_f32_e32 v223, v223, %1\n\tv_sub_f32_e32 v240, v240, %1\n\tv_sub_f32_e32 v241, v241, %1\n\tv_sub_f32_e32 v242, v242, %1\n\tv_sub_f32_e32 v243, v243, %1\n\tv_sub_f32_e32 v244, v244, %1\n\tv_sub_f32_e32 v245, v245, %1\n\tv_sub_f32_e32 v246, v246, %1\n\tv_sub_f32_e32 v247, v247, %1\n\tv_sub_f32_e32 v248, v248, %1\n\tv_sub_f32_e32 v249, v249, %1\n\tv_sub_f32_e32 v250, v250, %1\n\tv_sub_f32_e32 v251, v251, %1\n\tv_sub_f32_e32 v252, v252, %1\n\tv_sub_f32_e32 v253, v253, %1\n\tv_sub_f32_e32 v254, v254, %1\n\tv_sub_f32_e32 v255, v255, %1" :  : "v"(dl[0]), "v"(dl[1]) : "memory");
}
DI void s_mask1_full(const float neg) {
  asm volatile("s_nop 7\n\ts_nop 7\n\tv_mov_b32_e32 v224, %0\n\tv_mov_b32_e32 v225, %0\n\tv_mov_b32_e32 v226, %0\n\tv_mov_b32_e32 v227, %0\n\tv_mov_b32_e32 v228, %0\n\tv_mov_b32_e32 v229, %0\n\tv_mov_b32_e32 v230, %0\n\tv_mov_b32_e32 v231, %0\n\tv_mov_b32_e32 v232, %0\n\tv_mov_b32_e32 v233, %0\n\tv_mov_b32_e32 v234, %0\n\tv_mov_b32_e32 v235, %0\n\tv_mov_b32_e32 v236, %0\n\tv_mov_b32_e32 v237, %0\n\tv_mov_b32_e32 v238, %0\n\tv_mov_b32_e32 v239, %0\n\tv_mov_b32_e32 v192, %0\n\tv_mov_b32_e32 v208, %0\n\tv_mov_b32_e32 v240, %0\n\tv_mov_b32_e32 v193, %0\n\tv_mov_b32_e32 v209, %0\n\tv_mov_b32_e32 v241, %0\n\tv_mov_b32_e32 v194, %0\n\tv_mov_b32_e32 v210, %0\n\tv_mov_b32_e32 v242, %0\n\tv_mov_b32_e32 v195, %0\n\tv_mov_b32_e32 v211, %0\n\tv_mov_b32_e32 v243, %0\n\tv_mov_b32_e32 v196, %0\n\tv_mov_b32_e32 v212, %0\n\tv_mov_b32_e32 v244, %0\n\tv_mov_b32_e32 v197, %0\n\tv_mov_b32_e32 v213, %0\n\tv_mov_b32_e32 v245, %0\n\tv_mov_b32_e32 v198, %0\n\tv_mov_b32_e32 v214, %0\n\tv_mov_b32_e32 v246, %0\n\tv_mov_b32_e32 v199, %0\n\tv_mov_b32_e32 v215, %0\n\tv_mov_b32_e32 v247, %0\n\tv_mov_b32_e32 v200, %0\n\tv_mov_b32_e32 v216, %0\n\tv_mov_b32_e32 v248, %0\n\tv_mov_b32_e32 v201, %0\n\tv_mov_b32_e32 v217, %0\n\tv_mov_b32_e32 v249, %0\n\tv_mov_b32_e32 v202, %0\n\tv_mov_b32_e32 v218, %0\n\tv_mov_b32_e32 v250, %0\n\tv_mov_b32_e32 v203, %0\n\tv_mov_b32_e32 v219, %0\n\tv_mov_b32_e32 v251, %0\n\tv_mov_b32_e32 v204, %0\n\tv_mov_b32_e32 v220, %0\n\tv_mov_b32_e32 v252, %0\n\tv_mov_b32_e32 v205, %0\n\tv_mov_b32_e32 v221, %0\n\tv_mov_b32_e32 v253, %0\n\tv_mov_b32_e32 v206, %0\n\tv_mov_b32_e32 v222, %0\n\tv_mov_b32_e32 v254, %0\n\tv_mov_b32_e32 v207, %0\n\tv_mov_b32_e32 v223, %0\n\tv_mov_b32_e32 v255, %0" :  : "v"(neg) : "memory");
}
DI void s_mask1_tri(const float neg) {
  asm volatile("s_nop 7\n\ts_nop 7\n\tv_mov_b32_e32 v224, %0\n\tv_mov_b32_e32 v225, %0\n\tv_mov_b32_e32 v226, %0\n\tv_mov_b32_e32 v227, %0\n\tv_mov_b32_e32 v228, %0\n\tv_mov_b32_e32 v229, %0\n\tv_mov_b32_e32 v230, %0\n\tv_mov_b32_e32 v231, %0\n\tv_mov_b32_e32 v232, %0\n\tv_mov_b32_e32 v233, %0\n\tv_mov_b32_e32 v234, %0\n\tv_mov_b32_e32 v235, %0\n\tv_mov_b32_e32 v236, %0\n\tv_mov_b32_e32 v237, %0\n\tv_mov_b32_e32 v238, %0\n\tv_mov_b32_e32 v239, %0\n\ts_mov_b32 vcc_lo, 0x0\n\ts_mov_b32 vcc_hi, 0xf\n\tv_cndmask_b32_e32 v192, v192, %0, vcc\n\tv_cndmask_b32_e32 v240, v240, %0, vcc\n\ts_mov_b32 vcc_lo, 0x1\n\ts_mov_b32 vcc_hi, 0x1f\n\tv_cndmask_b32_e32 v193, v193, %0, vcc\n\tv_cndmask_b32_e32 v241, v241, %0, vcc\n\ts_mov_b32 vcc_lo, 0x3\n\ts_mov_b32 vcc_hi, 0x3f\n\tv_cndmask_b32_e32 v194, v194, %0, vcc\n\tv_cndmask_b32_e32 v242, v242, %0, vcc\n\ts_mov_b32 vcc_lo, 0x7\n\ts_mov_b32 vcc_hi, 0x7f\n\tv_cndmask_b32_e32 v195, v195, %0, vcc\n\tv_cndmask_b32_e32 v243, v243, %0, vcc\n\ts_mov_b32 vcc_lo, 0xff\n\ts_mov_b32 vcc_hi, 0xfff\n\tv_cndmask_b32_e32 v196, v196, %0, vcc\n\tv_cndmask_b32_e32 v244, v244, %0, vcc\n\ts_mov_b32 vcc_lo, 0x1ff\n\ts_mov_b32 vcc_hi, 0x1fff\n\tv_cndmask_b32_e32 v197, v197, %0, vcc\n\tv_cndmask_b32_e32 v245, v245, %0, vcc\n\ts_mov_b32 vcc_lo, 0x3ff\n\ts_mov_b32 vcc_hi, 0x3fff\n\tv_cndmask_b32_e32 v198, v198, %0, vcc\n\tv_cndmask_b32_e32 v246, v246, %0, vcc\n\ts_mov_b32 vcc_lo, 0x7ff\n\ts_mov_b32 vcc_hi, 0x7fff\n\tv_cndmask_b32_e32 v199, v199, %0, vcc\n\tv_cndmask_b32_e32 v247, v247, %0, vcc\n\ts_mov_b32 vcc_lo, 0xffff\n\ts_mov_b32 vcc_hi, 0xfffff\n\tv_cndmask_b32_e32 v200, v200, %0, vcc\n\tv_cndmask_b32_e32 v248, v248, %0, vcc\n\ts_mov_b32 vcc_lo, 0x1ffff\n\ts_mov_b32 vcc_hi, 0x1fffff\n\tv_cndmask_b32_e32 v201, v201, %0, vcc\n\tv_cndmask_b32_e32 v249, v249, %0, vcc\n\ts_mov_b32 vcc_lo, 0x3ffff\n\ts_mov_b32 vcc_hi, 0x3fffff\n\tv_cndmask_b32_e32 v202, v202, %0, vcc\n\tv_cndmask_b32_e32 v250, v250, %0, vcc\n\ts_mov_b32 vcc_lo, 0x7ffff\n\ts_mov_b32 vcc_hi, 0x7fffff\n\tv_cndmask_b32_e32 v203, v203, %0, vcc\n\tv_cndmask_b32_e32 v251, v251, %0, vcc\n\ts_mov_b32 vcc_lo, 0xffffff\n\ts_mov_b32 vcc_hi, 0xfffffff\n\tv_cndmask_b32_e32 v204, v204, %0, vcc\n\tv_cndmask_b32_e32 v252, v252, %0, vcc\n\ts_mov_b32 vcc_lo, 0x1ffffff\n\ts_mov_b32 vcc_hi, 0x1fffffff\n\tv_cndmask_b32_e32 v205, v205, %0, vcc\n\tv_cndmask_b32_e32 v253, v253, %0, vcc\n\ts_mov_b32 vcc_lo, 0x3ffffff\n\ts_mov_b32 vcc_hi, 0x3fffffff\n\tv_cndmask_b32_e32 v206, v206, %0, vcc\n\tv_cndmask_b32_e32 v254, v254, %0, vcc\n\ts_mov_b32 vcc_lo, 0x7ffffff\n\ts_mov_b32 vcc_hi, 0x7fffffff\n\tv_cndmask_b32_e32 v207, v207, %0, vcc\n\tv_cndmask_b32_e32 v255, v255, %0, vcc" :  : "v"(neg) : "memory", "vcc");
}
DI void gA1e(AC& c, const float (&il)[2], const unsigned (&ebase)[2]) {
  float et432_0, et432_1, et432_2, et432_3; unsigned et432_4;
  asm volatile("s_waitcnt lgkmcnt(4)\n\tv_mfma_f32_32x32x16_bf16 v[192:207], a[192:195], a[128:131], v[96:111]\n\tds_read_b128 a[216:219], %5 offset:16384\n\tv_exp_f32_e32 v160, v160\n\tv_exp_f32_e32 v161, v161\n\tv_cvt_pk_bf16_f32 v64, v128, v129\n\tv_accvgpr_read_b32 %0, a0\n\tv_accvgpr_read_b32 %1, a1\n\tv_accvgpr_read_b32 %2, a2\n\tv_accvgpr_read_b32 %3, a3\n\tv_xor_b32_e32 %4, 0, %7\n\tv_mul_f32_e32 %0, %6, %0\n\tv_mul_f32_e32 %1, %6, %1\n\tv_mul_f32_e32 %2, %6, %2\n\tv_mul_f32_e32 %3, %6, %3\n\tv_cvt_pk_bf16_f32 %0, %0, %1\n\tv_cvt_pk_bf16_f32 %1, %2, %3\n\tds_write2_b32 %4, %0, %1 offset0:0 offset1:1" : "=&v"(et432_0), "=&v"(et432_1), "=&v"(et432_2), "=&v"(et432_3), "=&v"(et432_4) : "v"(c.kb_[3]), "v"(il[0]), "v"(ebase[0]) : "memory");
  float et433_0, et433_1, et433_2, et433_3; unsigned et433_4;
  asm volatile("v_mfma_f32_32x32x16_bf16 v[208:223], a[192:195], a[160:163], v[112:127]\n\tv_exp_f32_e32 v162, v162\n\tv_exp_f32_e32 v163, v163\n\tv_cvt_pk_bf16_f32 v65, v130, v131\n\tv_add_f32_e32 %0, %0, v160\n\tv_accvgpr_read_b32 %1, a4\n\tv_accvgpr_read_b32 %2, a5\n\tv_accvgpr_read_b32 %3, a6\n\tv_accvgpr_read_b32 %4, a7\n\tv_xor_b32_e32 %5, 16, %7\n\tv_mul_f32_e32 %1, %6, %1\n\tv_mul_f32_e32 %2, %6, %2\n\tv_mul_f32_e32 %3, %6, %3\n\tv_mul_f32_e32 %4, %6, %4\n\tv_cvt_pk_bf16_f32 %1, %1, %2\n\tv_cvt_pk_bf16_f32 %2, %3, %4\n\tds_write2_b32 %5, %1, %2 offset0:0 offset1:1" : "+v"(c.rs[0][1]), "=&v"(et433_0), "=&v"(et433_1), "=&v"(et433_2), "=&v"(et433_3), "=&v"(et433_4) : "v"(il[0]), "v"(ebase[0]) : "memory");
  float et434_0, et434_1, et434_2, et434_3; unsigned et434_4;
  asm volatile("v_mfma_f32_32x32x16_bf16 v[224:239], a[196:199], a[128:131], v[96:111]\n\tds_read_b128 a[220:223], %6 offset:24576\n\tv_exp_f32_e32 v164, v164\n\tv_cvt_pk_bf16_f32 v66, v132, v133\n\tv_add_f32_e32 %0, %0, v161\n\tv_accvgpr_read_b32 %1, a8\n\tv_accvgpr_read_b32 %2, a9\n\tv_accvgpr_read_b32 %3, a10\n\tv_accvgpr_read_b32 %4, a11\n\tv_xor_b32_e32 %5, 32, %8\n\tv_mul_f32_e32 %1, %7, %1\n\tv_mul_f32_e32 %2, %7, %2\n\tv_mul_f32_e32 %3, %7, %3\n\tv_mul_f32_e32 %4, %7, %4\n\tv_cvt_pk_bf16_f32 %1, %1, %2\n\tv_cvt_pk_bf16_f32 %2, %3, %4\n\tds_write2_b32 %5, %1, %2 offset0:0 offset1:1" : "+v"(c.rs[0][0]), "=&v"(et434_0), "=&v"(et434_1), "=&v"(et434_2), "=&v"(et434_3), "=&v"(et434_4) : "v"(c.kb_[3]), "v"(il[0]), "v"(ebase[0]) : "memory");
  float et435_0, et435_1, et435_2, et435_3; unsigned et435_4;
  asm volatile("v_mfma_f32_32x32x16_bf16 v[240:255], a[196:199], a[160:163], v[112:127]\n\tv_exp_f32_e32 v165, v165\n\tv_cvt_pk_bf16_f32 v67, v134, v135\n\tv_add_f32_e32 %0, %0, v162\n\tv_accvgpr_read_b32 %1, a12\n\tv_accvgpr_read_b32 %2, a13\n\tv_accvgpr_read_b32 %3, a14\n\tv_accvgpr_read_b32 %4, a15\n\tv_xor_b32_e32 %5, 48, %7\n\tv_mul_f32_e32 %1, %6, %1\n\tv_mul_f32_e32 %2, %6, %2\n\tv_mul_f32_e32 %3, %6, %3\n\tv_mul_f32_e32 %4, %6, %4\n\tv_cvt_pk_bf16_f32 %1, %1, %2\n\tv_cvt_pk_bf16_f32 %2, %3, %4\n\tds_write2_b32 %5, %1, %2 offset0:0 offset1:1" : "+v"(c.rs[0][1]), "=&v"(et435_0), "=&v"(et435_1), "=&v"(et435_2), "=&v"(et435_3), "=&v"(et435_4) : "v"(il[0]), "v"(ebase[0]) : "memory");
  float et436_0, et436_1, et436_2, et436_3; unsigned et436_4;
  asm volatile("s_waitcnt lgkmcnt(4)\n\tv_mfma_f32_32x32x16_bf16 v[192:207], a[200:203], a[132:135], v[192:207]\n\tds_read_b128 a[224:227], %6 offset:16384\n\tv_exp_f32_e32 v166, v166\n\tv_cvt_pk_bf16_f32 v80, v144, v145\n\tv_add_f32_e32 %0, %0, v163\n\tv_accvgpr_read_b32 %1, a32\n\tv_accvgpr_read_b32 %2, a33\n\tv_accvgpr_read_b32 %3, a34\n\tv_accvgpr_read_b32 %4, a35\n\tv_xor_b32_e32 %5, 64, %8\n\tv_mul_f32_e32 %1, %7, %1\n\tv_mul_f32_e32 %2, %7, %2\n\tv_mul_f32_e32 %3, %7, %3\n\tv_mul_f32_e32 %4, %7, %4\n\tv_cvt_pk_bf16_f32 %1, %1, %2\n\tv_cvt_pk_bf16_f32 %2, %3, %4\n\tds_write2_b32 %5, %1, %2 offset0:0 offset1:1" : "+v"(c.rs[0][0]), "=&v"(et436_0), "=&v"(et436_1), "=&v"(et436_2), "=&v"(et436_3), "=&v"(et436_4) : "v"(c.kb_[4]), "v"(il[0]), "v"(ebase[0]) : "memory", ACL64);
  float et437_0, et437_1, et437_2, et437_3; unsigned et437_4;
  asm volatile("v_mfma_f32_32x32x16_bf16 v[208:223], a[200:203], a[164:167], v[208:223]\n\tv_exp_f32_e32 v167, v167\n\tv_cvt_pk_bf16_f32 v81, v146, v147\n\tv_add_f32_e32 %0, %0, v164\n\tv_accvgpr_read_b32 %1, a36\n\tv_accvgpr_read_b32 %2, a37\n\tv_accvgpr_read_b32 %3, a38\n\tv_accvgpr_read_b32 %4, a39\n\tv_xor_b32_e32 %5, 80, %7\n\tv_mul_f32_e32 %1, %6, %1\n\tv_mul_f32_e32 %2, %6, %2\n\tv_mul_f32_e32 %3, %6, %3\n\tv_mul_f32_e32 %4, %6, %4\n\tv_cvt_pk_bf16_f32 %1, %1, %2\n\tv_cvt_pk_bf16_f32 %2, %3, %4\n\tds_write2_b32 %5, %1, %2 offset0:0 offset1:1" : "+v"(c.rs[0][1]), "=&v"(et437_0), "=&v"(et437_1), "=&v"(et437_2), "=&v"(et437_3), "=&v"(et437_4) : "v"(il[0]), "v"(ebase[0]) : "memory");
  float et438_0, et438_1, et438_2, et438_3; unsigned et438_4;
  asm volatile("v_mfma_f32_32x32x16_bf16 v[224:239], a[204:207], a[132:135], v[224:239]\n\tds_read_b128 a[228:231], %6 offset:24576\n\tv_exp_f32_e32 v168, v168\n\tv_cvt_pk_bf16_f32 v82, v148, v149\n\tv_add_f32_e32 %0, %0, v165\n\tv_accvgpr_read_b32 %1, a40\n\tv_accvgpr_read_b32 %2, a41\n\tv_accvgpr_read_b32 %3, a42\n\tv_accvgpr_read_b32 %4, a43\n\tv_xor_b32_e32 %5, 96, %8\n\tv_mul_f32_e32 %1, %7, %1\n\tv_mul_f32_e32 %2, %7, %2\n\tv_mul_f32_e32 %3, %7, %3\n\tv_mul_f32_e32 %4, %7, %4\n\tv_cvt_pk_bf16_f32 %1, %1, %2\n\tv_cvt_pk_bf16_f32 %2, %3, %4\n\tds_write2_b32 %5, %1, %2 offset0:0 offset1:1" : "+v"(c.rs[0][0]), "=&v"(et438_0), "=&v"(et438_1), "=&v"(et438_2), "=&v"(et438_3), "=&v"(et438_4) : "v"(c.kb_[4]), "v"(il[0]), "v"(ebase[0]) : "memory");
  float et439_0, et439_1, et439_2, et439_3; unsigned et439_4;
  asm volatile("v_mfma_f32_32x32x16_bf16 v[240:255], a[204:207], a[164:167], v[240:255]\n\tv_exp_f32_e32 v169, v169\n\tv_cvt_pk_bf16_f32 v83, v150, v151\n\tv_add_f32_e32 %0, %0, v166\n\tv_accvgpr_read_b32 %1, a44\n\tv_accvgpr_read_b32 %2, a45\n\tv_accvgpr_read_b32 %3, a46\n\tv_accvgpr_read_b32 %4, a47\n\tv_xor_b32_e32 %5, 112, %7\n\tv_mul_f32_e32 %1, %6, %1\n\tv_mul_f32_e32 %2, %6, %2\n\tv_mul_f32_e32 %3, %6, %3\n\tv_mul_f32_e32 %4, %6, %4\n\tv_cvt_pk_bf16_f32 %1, %1, %2\n\tv_cvt_pk_bf16_f32 %2, %3, %4\n\tds_write2_b32 %5, %1, %2 offset0:0 offset1:1" : "+v"(c.rs[0][1]), "=&v"(et439_0), "=&v"(et439_1), "=&v"(et439_2), "=&v"(et439_3), "=&v"(et439_4) : "v"(il[0]), "v"(ebase[0]) : "memory");
  float et440_0, et440_1, et440_2, et440_3; unsigned et440_4;
  asm volatile("s_waitcnt lgkmcnt(4)\n\tv_mfma_f32_32x32x16_bf16 v[192:207], a[208:211], a[136:139], v[192:207]\n\tds_read_b128 a[232:235], %6 offset:16384\n\tv_exp_f32_e32 v170, v170\n\tv_cvt_pk_bf16_f32 v68, v136, v137\n\tv_add_f32_e32 %0, %0, v167\n\tv_accvgpr_read_b32 %1, a64\n\tv_accvgpr_read_b32 %2, a65\n\tv_accvgpr_read_b32 %3, a66\n\tv_accvgpr_read_b32 %4, a67\n\tv_xor_b32_e32 %5, 128, %8\n\tv_mul_f32_e32 %1, %7, %1\n\tv_mul_f32_e32 %2, %7, %2\n\tv_mul_f32_e32 %3, %7, %3\n\tv_mul_f32_e32 %4, %7, %4\n\tv_cvt_pk_bf16_f32 %1, %1, %2\n\tv_cvt_pk_bf16_f32 %2, %3, %4\n\tds_write2_b32 %5, %1, %2 offset0:0 offset1:1" : "+v"(c.rs[0][0]), "=&v"(et440_0), "=&v"(et440_1), "=&v"(et440_2), "=&v"(et440_3), "=&v"(et440_4) : "v"(c.kb_[5]), "v"(il[0]), "v"(ebase[0]) : "memory");
  float et441_0, et441_1, et441_2, et441_3; unsigned et441_4;
  asm volatile("v_mfma_f32_32x32x16_bf16 v[208:223], a[208:211], a[168:171], v[208:223]\n\tv_exp_f32_e32 v171, v171\n\tv_cvt_pk_bf16_f32 v69, v138, v139\n\tv_add_f32_e32 %0, %0, v168\n\tv_accvgpr_read_b32 %1, a68\n\tv_accvgpr_read_b32 %2, a69\n\tv_accvgpr_read_b32 %3, a70\n\tv_accvgpr_read_b32 %4, a71\n\tv_xor_b32_e32 %5, 144, %7\n\tv_mul_f32_e32 %1, %6, %1\n\tv_mul_f32_e32 %2, %6, %2\n\tv_mul_f32_e32 %3, %6, %3\n\tv_mul_f32_e32 %4, %6, %4\n\tv_cvt_pk_bf16_f32 %1, %1, %2\n\tv_cvt_pk_bf16_f32 %2, %3, %4\n\tds_write2_b32 %5, %1, %2 offset0:0 offset1:1" : "+v"(c.rs[0][1]), "=&v"(et441_0), "=&v"(et441_1), "=&v"(et441_2), "=&v"(et441_3), "=&v"(et441_4) : "v"(il[0]), "v"(ebase[0]) : "memory");
  float et442_0, et442_1, et442_2, et442_3; unsigned et442_4;
  asm volatile("v_mfma_f32_32x32x16_bf16 v[224:239], a[212:215], a[136:139], v[224:239]\n\tds_read_b128 a[236:239], %6 offset:24576\n\tv_exp_f32_e32 v172, v172\n\tv_cvt_pk_bf16_f32 v70, v140, v141\n\tv_add_f32_e32 %0, %0, v169\n\tv_accvgpr_read_b32 %1, a72\n\tv_accvgpr_read_b32 %2, a73\n\tv_accvgpr_read_b32 %3, a74\n\tv_accvgpr_read_b32 %4, a75\n\tv_xor_b32_e32 %5, 160, %8\n\tv_mul_f32_e32 %1, %7, %1\n\tv_mul_f32_e32 %2, %7, %2\n\tv_mul_f32_e32 %3, %7, %3\n\tv_mul_f32_e32 %4, %7, %4\n\tv_cvt_pk_bf16_f32 %1, %1, %2\n\tv_cvt_pk_bf16_f32 %2, %3, %4\n\tds_write2_b32 %5, %1, %2 offset0:0 offset1:1" : "+v"(c.rs[0][0]), "=&v"(et442_0), "=&v"(et442_1), "=&v"(et442_2), "=&v"(et442_3), "=&v"(et442_4) : "v"(c.kb_[5]), "v"(il[0]), "v"(ebase[0]) : "memory");
  float et443_0, et443_1, et443_2, et443_3; unsigned et443_4;
  asm volatile("v_mfma_f32_32x32x16_bf16 v[240:255], a[212:215], a[168:171], v[240:255]\n\tv_exp_f32_e32 v173, v173\n\tv_cvt_pk_bf16_f32 v71, v142, v143\n\tv_add_f32_e32 %0, %0, v170\n\tv_accvgpr_read_b32 %1, a76\n\tv_accvgpr_read_b32 %2, a77\n\tv_accvgpr_read_b32 %3, a78\n\tv_accvgpr_read_b32 %4, a79\n\tv_xor_b32_e32 %5, 176, %7\n\tv_mul_f32_e32 %1, %6, %1\n\tv_mul_f32_e32 %2, %6, %2\n\tv_mul_f32_e32 %3, %6, %3\n\tv_mul_f32_e32 %4, %6, %4\n\tv_cvt_pk_bf16_f32 %1, %1, %2\n\tv_cvt_pk_bf16_f32 %2, %3, %4\n\tds_write2_b32 %5, %1, %2 offset0:0 offset1:1" : "+v"(c.rs[0][1]), "=&v"(et443_0), "=&v"(et443_1), "=&v"(et443_2), "=&v"(et443_3), "=&v"(et443_4) : "v"(il[0]), "v"(ebase[0]) : "memory");
  float et444_0, et444_1, et444_2, et444_3; unsigned et444_4;
  asm volatile("s_waitcnt lgkmcnt(4)\n\tv_mfma_f32_32x32x16_bf16 v[192:207], a[216:219], a[140:143], v[192:207]\n\tds_read_b128 a[240:243], %6 offset:16384\n\tv_exp_f32_e32 v174, v174\n\tv_cvt_pk_bf16_f32 v84, v152, v153\n\tv_add_f32_e32 %0, %0, v171\n\tv_accvgpr_read_b32 %1, a96\n\tv_accvgpr_read_b32 %2, a97\n\tv_accvgpr_read_b32 %3, a98\n\tv_accvgpr_read_b32 %4, a99\n\tv_xor_b32_e32 %5, 192, %8\n\tv_mul_f32_e32 %1, %7, %1\n\tv_mul_f32_e32 %2, %7, %2\n\tv_mul_f32_e32 %3, %7, %3\n\tv_mul_f32_e32 %4, %7, %4\n\tv_cvt_pk_bf16_f32 %1, %1, %2\n\tv_cvt_pk_bf16_f32 %2, %3, %4\n\tds_write2_b32 %5, %1, %2 offset0:0 offset1:1" : "+v"(c.rs[0][0]), "=&v"(et444_0), "=&v"(et444_1), "=&v"(et444_2), "=&v"(et444_3), "=&v"(et444_4) : "v"(c.kb_[6]), "v"(il[0]), "v"(ebase[0]) : "memory", ACL64);
  float et445_0, et445_1, et445_2, et445_3; unsigned et445_4;
  asm volatile("v_mfma_f32_32x32x16_bf16 v[208:223], a[216:219], a[172:175], v[208:223]\n\tv_exp_f32_e32 v175, v175\n\tv_cvt_pk_bf16_f32 v85, v154, v155\n\tv_add_f32_e32 %0, %0, v172\n\tv_accvgpr_read_b32 %1, a100\n\tv_accvgpr_read_b32 %2, a101\n\tv_accvgpr_read_b32 %3, a102\n\tv_accvgpr_read_b32 %4, a103\n\tv_xor_b32_e32 %5, 208, %7\n\tv_mul_f32_e32 %1, %6, %1\n\tv_mul_f32_e32 %2, %6, %2\n\tv_mul_f32_e32 %3, %6, %3\n\tv_mul_f32_e32 %4, %6, %4\n\tv_cvt_pk_bf16_f32 %1, %1, %2\n\tv_cvt_pk_bf16_f32 %2, %3, %4\n\tds_write2_b32 %5, %1, %2 offset0:0 offset1:1" : "+v"(c.rs[0][1]), "=&v"(et445_0), "=&v"(et445_1), "=&v"(et445_2), "=&v"(et445_3), "=&v"(et445_4) : "v"(il[0]), "v"(ebase[0]) : "memory");
  float et446_0, et446_1, et446_2, et446_3; unsigned et446_4;
  asm volatile("v_mfma_f32_32x32x16_bf16 v[224:239], a[220:223], a[140:143], v[224:239]\n\tds_read_b128 a[244:247], %6 offset:24576\n\tv_exp_f32_e32 v176, v176\n\tv_cvt_pk_bf16_f32 v86, v156, v157\n\tv_add_f32_e32 %0, %0, v173\n\tv_accvgpr_read_b32 %1, a104\n\tv_accvgpr_read_b32 %2, a105\n\tv_accvgpr_read_b32 %3, a106\n\tv_accvgpr_read_b32 %4, a107\n\tv_xor_b32_e32 %5, 224, %8\n\tv_mul_f32_e32 %1, %7, %1\n\tv_mul_f32_e32 %2, %7, %2\n\tv_mul_f32_e32 %3, %7, %3\n\tv_mul_f32_e32 %4, %7, %4\n\tv_cvt_pk_bf16_f32 %1, %1, %2\n\tv_cvt_pk_bf16_f32 %2, %3, %4\n\tds_write2_b32 %5, %1, %2 offset0:0 offset1:1" : "+v"(c.rs[0][0]), "=&v"(et446_0), "=&v"(et446_1), "=&v"(et446_2), "=&v"(et446_3), "=&v"(et446_4) : "v"(c.kb_[6]), "v"(il[0]), "v"(ebase[0]) : "memory");
  float et447_0, et447_1, et447_2, et447_3; unsigned et447_4;
  asm volatile("v_mfma_f32_32x32x16_bf16 v[240:255], a[220:223], a[172:175], v[240:255]\n\tv_exp_f32_e32 v177, v177\n\tv_cvt_pk_bf16_f32 v87, v158, v159\n\tv_add_f32_e32 %0, %0, v174\n\tv_accvgpr_read_b32 %1, a108\n\tv_accvgpr_read_b32 %2, a109\n\tv_accvgpr_read_b32 %3, a110\n\tv_accvgpr_read_b32 %4, a111\n\tv_xor_b32_e32 %5, 240, %7\n\tv_mul_f32_e32 %1, %6, %1\n\tv_mul_f32_e32 %2, %6, %2\n\tv_mul_f32_e32 %3, %6, %3\n\tv_mul_f32_e32 %4, %6, %4\n\tv_cvt_pk_bf16_f32 %1, %1, %2\n\tv_cvt_pk_bf16_f32 %2, %3, %4\n\tds_write2_b32 %5, %1, %2 offset0:0 offset1:1" : "+v"(c.rs[0][1]), "=&v"(et447_0), "=&v"(et447_1), "=&v"(et447_2), "=&v"(et447_3), "=&v"(et447_4) : "v"(il[0]), "v"(ebase[0]) : "memory");
  float et448_0, et448_1, et448_2, et448_3; unsigned et448_4;
  asm volatile("s_waitcnt lgkmcnt(4)\n\tv_mfma_f32_32x32x16_bf16 v[192:207], a[224:227], a[144:147], v[192:207]\n\tds_read_b128 a[248:251], %7 offset:16384\n\tv_exp_f32_e32 v178, v178\n\tv_cvt_pk_bf16_f32 v72, v160, v161\n\tv_add_f32_e32 %0, %0, v175\n\tv_add_f32_e32 %1, %1, v144\n\tv_accvgpr_read_b32 %2, a16\n\tv_accvgpr_read_b32 %3, a17\n\tv_accvgpr_read_b32 %4, a18\n\tv_accvgpr_read_b32 %5, a19\n\tv_xor_b32_e32 %6, 0, %9\n\tv_mul_f32_e32 %2, %8, %2\n\tv_mul_f32_e32 %3, %8, %3\n\tv_mul_f32_e32 %4, %8, %4\n\tv_mul_f32_e32 %5, %8, %5\n\tv_cvt_pk_bf16_f32 %2, %2, %3\n\tv_cvt_pk_bf16_f32 %3, %4, %5\n\tds_write2_b32 %6, %2, %3 offset0:0 offset1:1" : "+v"(c.rs[0][0]), "+v"(c.rs[1][2]), "=&v"(et448_0), "=&v"(et448_1), "=&v"(et448_2), "=&v"(et448_3), "=&v"(et448_4) : "v"(c.kb_[7]), "v"(il[1]), "v"(ebase[1]) : "memory");
  float et449_0, et449_1, et449_2, et449_3; unsigned et449_4;
  asm volatile("v_mfma_f32_32x32x16_bf16 v[208:223], a[224:227], a[176:179], v[208:223]\n\tv_exp_f32_e32 v179, v179\n\tv_cvt_pk_bf16_f32 v73, v162, v163\n\tv_add_f32_e32 %0, %0, v176\n\tv_add_f32_e32 %1, %1, v145\n\tv_accvgpr_read_b32 %2, a20\n\tv_accvgpr_read_b32 %3, a21\n\tv_accvgpr_read_b32 %4, a22\n\tv_accvgpr_read_b32 %5, a23\n\tv_xor_b32_e32 %6, 16, %8\n\tv_mul_f32_e32 %2, %7, %2\n\tv_mul_f32_e32 %3, %7, %3\n\tv_mul_f32_e32 %4, %7, %4\n\tv_mul_f32_e32 %5, %7, %5\n\tv_cvt_pk_bf16_f32 %2, %2, %3\n\tv_cvt_pk_bf16_f32 %3, %4, %5\n\tds_write2_b32 %6, %2, %3 offset0:0 offset1:1" : "+v"(c.rs[1][1]), "+v"(c.rs[1][3]), "=&v"(et449_0), "=&v"(et449_1), "=&v"(et449_2), "=&v"(et449_3), "=&v"(et449_4) : "v"(il[1]), "v"(ebase[1]) : "memory");
  float et450_0, et450_1, et450_2, et450_3; unsigned et450_4;
  asm volatile("v_mfma_f32_32x32x16_bf16 v[224:239], a[228:231], a[144:147], v[224:239]\n\tds_read_b128 a[252:255], %7 offset:24576\n\tv_exp_f32_e32 v180, v180\n\tv_cvt_pk_bf16_f32 v74, v164, v165\n\tv_add_f32_e32 %0, %0, v177\n\tv_add_f32_e32 %1, %1, v146\n\tv_accvgpr_read_b32 %2, a24\n\tv_accvgpr_read_b32 %3, a25\n\tv_accvgpr_read_b32 %4, a26\n\tv_accvgpr_read_b32 %5, a27\n\tv_xor_b32_e32 %6, 32, %9\n\tv_mul_f32_e32 %2, %8, %2\n\tv_mul_f32_e32 %3, %8, %3\n\tv_mul_f32_e32 %4, %8, %4\n\tv_mul_f32_e32 %5, %8, %5\n\tv_cvt_pk_bf16_f32 %2, %2, %3\n\tv_cvt_pk_bf16_f32 %3, %4, %5\n\tds_write2_b32 %6, %2, %3 offset0:0 offset1:1" : "+v"(c.rs[1][0]), "+v"(c.rs[1][2]), "=&v"(et450_0), "=&v"(et450_1), "=&v"(et450_2), "=&v"(et450_3), "=&v"(et450_4) : "v"(c.kb_[7]), "v"(il[1]), "v"(ebase[1]) : "memory");
  float et451_0, et451_1, et451_2, et451_3; unsigned et451_4;
  asm volatile("v_mfma_f32_32x32x16_bf16 v[240:255], a[228:231], a[176:179], v[240:255]\n\tv_exp_f32_e32 v181, v181\n\tv_cvt_pk_bf16_f32 v75, v166, v167\n\tv_add_f32_e32 %0, %0, v178\n\tv_add_f32_e32 %1, %1, v147\n\tv_accvgpr_read_b32 %2, a28\n\tv_accvgpr_read_b32 %3, a29\n\tv_accvgpr_read_b32 %4, a30\n\tv_accvgpr_read_b32 %5, a31\n\tv_xor_b32_e32 %6, 48, %8\n\tv_mul_f32_e32 %2, %7, %2\n\tv_mul_f32_e32 %3, %7, %3\n\tv_mul_f32_e32 %4, %7, %4\n\tv_mul_f32_e32 %5, %7, %5\n\tv_cvt_pk_bf16_f32 %2, %2, %3\n\tv_cvt_pk_bf16_f32 %3, %4, %5\n\tds_write2_b32 %6, %2, %3 offset0:0 offset1:1" : "+v"(c.rs[1][1]), "+v"(c.rs[1][3]), "=&v"(et451_0), "=&v"(et451_1), "=&v"(et451_2), "=&v"(et451_3), "=&v"(et451_4) : "v"(il[1]), "v"(ebase[1]) : "memory");
  float et452_0, et452_1, et452_2, et452_3; unsigned et452_4;
  asm volatile("s_waitcnt lgkmcnt(4)\n\tv_mfma_f32_32x32x16_bf16 v[192:207], a[232:235], a[148:151], v[192:207]\n\tv_exp_f32_e32 v182, v182\n\tv_cvt_pk_bf16_f32 v76, v168, v169\n\tv_add_f32_e32 %0, %0, v179\n\tv_add_f32_e32 %1, %1, v148\n\tv_accvgpr_read_b32 %2, a48\n\tv_accvgpr_read_b32 %3, a49\n\tv_accvgpr_read_b32 %4, a50\n\tv_accvgpr_read_b32 %5, a51\n\tv_xor_b32_e32 %6, 64, %8\n\tv_mul_f32_e32 %2, %7, %2\n\tv_mul_f32_e32 %3, %7, %3\n\tv_mul_f32_e32 %4, %7, %4\n\tv_mul_f32_e32 %5, %7, %5\n\tv_cvt_pk_bf16_f32 %2, %2, %3\n\tv_cvt_pk_bf16_f32 %3, %4, %5\n\tds_write2_b32 %6, %2, %3 offset0:0 offset1:1" : "+v"(c.rs[1][0]), "+v"(c.rs[1][2]), "=&v"(et452_0), "=&v"(et452_1), "=&v"(et452_2), "=&v"(et452_3), "=&v"(et452_4) : "v"(il[1]), "v"(ebase[1]) : "memory", ACL64);
  float et453_0, et453_1, et453_2, et453_3; unsigned et453_4;
  asm volatile("v_mfma_f32_32x32x16_bf16 v[208:223], a[232:235], a[180:183], v[208:223]\n\tv_exp_f32_e32 v183, v183\n\tv_cvt_pk_bf16_f32 v77, v170, v171\n\tv_add_f32_e32 %0, %0, v180\n\tv_add_f32_e32 %1, %1, v149\n\tv_accvgpr_read_b32 %2, a52\n\tv_accvgpr_read_b32 %3, a53\n\tv_accvgpr_read_b32 %4, a54\n\tv_accvgpr_read_b32 %5, a55\n\tv_xor_b32_e32 %6, 80, %8\n\tv_mul_f32_e32 %2, %7, %2\n\tv_mul_f32_e32 %3, %7, %3\n\tv_mul_f32_e32 %4, %7, %4\n\tv_mul_f32_e32 %5, %7, %5\n\tv_cvt_pk_bf16_f32 %2, %2, %3\n\tv_cvt_pk_bf16_f32 %3, %4, %5\n\tds_write2_b32 %6, %2, %3 offset0:0 offset1:1" : "+v"(c.rs[1][1]), "+v"(c.rs[1][3]), "=&v"(et453_0), "=&v"(et453_1), "=&v"(et453_2), "=&v"(et453_3), "=&v"(et453_4) : "v"(il[1]), "v"(ebase[1]) : "memory");
  float et454_0, et454_1, et454_2, et454_3; unsigned et454_4;
  asm volatile("v_mfma_f32_32x32x16_bf16 v[224:239], a[236:239], a[148:151], v[224:239]\n\tv_exp_f32_e32 v184, v184\n\tv_cvt_pk_bf16_f32 v78, v172, v173\n\tv_add_f32_e32 %0, %0, v181\n\tv_add_f32_e32 %1, %1, v150\n\tv_accvgpr_read_b32 %2, a56\n\tv_accvgpr_read_b32 %3, a57\n\tv_accvgpr_read_b32 %4, a58\n\tv_accvgpr_read_b32 %5, a59\n\tv_xor_b32_e32 %6, 96, %8\n\tv_mul_f32_e32 %2, %7, %2\n\tv_mul_f32_e32 %3, %7, %3\n\tv_mul_f32_e32 %4, %7, %4\n\tv_mul_f32_e32 %5, %7, %5\n\tv_cvt_pk_bf16_f32 %2, %2, %3\n\tv_cvt_pk_bf16_f32 %3, %4, %5\n\tds_write2_b32 %6, %2, %3 offset0:0 offset1:1" : "+v"(c.rs[1][0]), "+v"(c.rs[1][2]), "=&v"(et454_0), "=&v"(et454_1), "=&v"(et454_2), "=&v"(et454_3), "=&v"(et454_4) : "v"(il[1]), "v"(ebase[1]) : "memory");
  float et455_0, et455_1, et455_2, et455_3; unsigned et455_4;
  asm volatile("v_mfma_f32_32x32x16_bf16 v[240:255], a[236:239], a[180:183], v[240:255]\n\tv_exp_f32_e32 v185, v185\n\tv_cvt_pk_bf16_f32 v79, v174, v175\n\tv_add_f32_e32 %0, %0, v182\n\tv_add_f32_e32 %1, %1, v151\n\tv_accvgpr_read_b32 %2, a60\n\tv_accvgpr_read_b32 %3, a61\n\tv_accvgpr_read_b32 %4, a62\n\tv_accvgpr_read_b32 %5, a63\n\tv_xor_b32_e32 %6, 112, %8\n\tv_mul_f32_e32 %2, %7, %2\n\tv_mul_f32_e32 %3, %7, %3\n\tv_mul_f32_e32 %4, %7, %4\n\tv_mul_f32_e32 %5, %7, %5\n\tv_cvt_pk_bf16_f32 %2, %2, %3\n\tv_cvt_pk_bf16_f32 %3, %4, %5\n\tds_write2_b32 %6, %2, %3 offset0:0 offset1:1" : "+v"(c.rs[1][1]), "+v"(c.rs[1][3]), "=&v"(et455_0), "=&v"(et455_1), "=&v"(et455_2), "=&v"(et455_3), "=&v"(et455_4) : "v"(il[1]), "v"(ebase[1]) : "memory");
  float et456_0, et456_1, et456_2, et456_3; unsigned et456_4;
  asm volatile("s_waitcnt vmcnt(0) lgkmcnt(0)\n\ts_barrier\n\ts_waitcnt lgkmcnt(2)\n\tv_mfma_f32_32x32x16_bf16 v[192:207], a[240:243], a[152:155], v[192:207]\n\tds_read_b128 a[192:195], %7 offset:32768\n\tv_exp_f32_e32 v186, v186\n\tv_cvt_pk_bf16_f32 v88, v176, v177\n\tv_add_f32_e32 %0, %0, v183\n\tv_add_f32_e32 %1, %1, v152\n\tv_accvgpr_read_b32 %2, a80\n\tv_accvgpr_read_b32 %3, a81\n\tv_accvgpr_read_b32 %4, a82\n\tv_accvgpr_read_b32 %5, a83\n\tv_xor_b32_e32 %6, 128, %9\n\tv_mul_f32_e32 %2, %8, %2\n\tv_mul_f32_e32 %3, %8, %3\n\tv_mul_f32_e32 %4, %8, %4\n\tv_mul_f32_e32 %5, %8, %5\n\tv_cvt_pk_bf16_f32 %2, %2, %3\n\tv_cvt_pk_bf16_f32 %3, %4, %5\n\tds_write2_b32 %6, %2, %3 offset0:0 offset1:1" : "+v"(c.rs[1][0]), "+v"(c.rs[1][2]), "=&v"(et456_0), "=&v"(et456_1), "=&v"(et456_2), "=&v"(et456_3), "=&v"(et456_4) : "v"(c.vb_[0]), "v"(il[1]), "v"(ebase[1]) : "memory");
  float et457_0, et457_1, et457_2, et457_3; unsigned et457_4;
  asm volatile("v_mfma_f32_32x32x16_bf16 v[208:223], a[240:243], a[184:187], v[208:223]\n\tds_read_b128 a[196:199], %7 offset:36864\n\tv_exp_f32_e32 v187, v187\n\tv_cvt_pk_bf16_f32 v89, v178, v179\n\tv_add_f32_e32 %0, %0, v184\n\tv_add_f32_e32 %1, %1, v153\n\tv_accvgpr_read_b32 %2, a84\n\tv_accvgpr_read_b32 %3, a85\n\tv_accvgpr_read_b32 %4, a86\n\tv_accvgpr_read_b32 %5, a87\n\tv_xor_b32_e32 %6, 144, %9\n\tv_mul_f32_e32 %2, %8, %2\n\tv_mul_f32_e32 %3, %8, %3\n\tv_mul_f32_e32 %4, %8, %4\n\tv_mul_f32_e32 %5, %8, %5\n\tv_cvt_pk_bf16_f32 %2, %2, %3\n\tv_cvt_pk_bf16_f32 %3, %4, %5\n\tds_write2_b32 %6, %2, %3 offset0:0 offset1:1" : "+v"(c.rs[1][1]), "+v"(c.rs[1][3]), "=&v"(et457_0), "=&v"(et457_1), "=&v"(et457_2), "=&v"(et457_3), "=&v"(et457_4) : "v"(c.vb_[0]), "v"(il[1]), "v"(ebase[1]) : "memory");
  float et458_0, et458_1, et458_2, et458_3; unsigned et458_4;
  asm volatile("v_mfma_f32_32x32x16_bf16 v[224:239], a[244:247], a[152:155], v[224:239]\n\tds_read_b128 a[200:203], %7 offset:40960\n\tv_exp_f32_e32 v188, v188\n\tv_cvt_pk_bf16_f32 v90, v180, v181\n\tv_add_f32_e32 %0, %0, v185\n\tv_add_f32_e32 %1, %1, v154\n\tv_accvgpr_read_b32 %2, a88\n\tv_accvgpr_read_b32 %3, a89\n\tv_accvgpr_read_b32 %4, a90\n\tv_accvgpr_read_b32 %5, a91\n\tv_xor_b32_e32 %6, 160, %9\n\tv_mul_f32_e32 %2, %8, %2\n\tv_mul_f32_e32 %3, %8, %3\n\tv_mul_f32_e32 %4, %8, %4\n\tv_mul_f32_e32 %5, %8, %5\n\tv_cvt_pk_bf16_f32 %2, %2, %3\n\tv_cvt_pk_bf16_f32 %3, %4, %5\n\tds_write2_b32 %6, %2, %3 offset0:0 offset1:1\n\ts_add_u32 m0, %10, 49152\n\ts_nop 0\n\tbuffer_load_dwordx4 %11, %12, %13 offen offset:0 sc1 lds\n\tbuffer_load_dwordx4 %11, %12, %13 offen offset:1024 sc1 lds" : "+v"(c.rs[1][0]), "+v"(c.rs[1][2]), "=&v"(et458_0), "=&v"(et458_1), "=&v"(et458_2), "=&v"(et458_3), "=&v"(et458_4) : "v"(c.vb_[0]), "v"(il[1]), "v"(ebase[1]), "s"(c.m0w), "v"(c.vlin), "s"(c.srdV), "s"(c.soVA) : "memory", "m0", "scc");
  float et459_0, et459_1, et459_2, et459_3; unsigned et459_4;
  asm volatile("v_mfma_f32_32x32x16_bf16 v[240:255], a[244:247], a[184:187], v[240:255]\n\tds_read_b128 a[204:207], %7 offset:45056\n\tv_exp_f32_e32 v189, v189\n\tv_cvt_pk_bf16_f32 v91, v182, v183\n\tv_add_f32_e32 %0, %0, v186\n\tv_add_f32_e32 %1, %1, v155\n\tv_accvgpr_read_b32 %2, a92\n\tv_accvgpr_read_b32 %3, a93\n\tv_accvgpr_read_b32 %4, a94\n\tv_accvgpr_read_b32 %5, a95\n\tv_xor_b32_e32 %6, 176, %9\n\tv_mul_f32_e32 %2, %8, %2\n\tv_mul_f32_e32 %3, %8, %3\n\tv_mul_f32_e32 %4, %8, %4\n\tv_mul_f32_e32 %5, %8, %5\n\tv_cvt_pk_bf16_f32 %2, %2, %3\n\tv_cvt_pk_bf16_f32 %3, %4, %5\n\tds_write2_b32 %6, %2, %3 offset0:0 offset1:1" : "+v"(c.rs[1][1]), "+v"(c.rs[1][3]), "=&v"(et459_0), "=&v"(et459_1), "=&v"(et459_2), "=&v"(et459_3), "=&v"(et459_4) : "v"(c.vb_[0]), "v"(il[1]), "v"(ebase[1]) : "memory");
  float et460_0, et460_1, et460_2, et460_3; unsigned et460_4;
  asm volatile("s_waitcnt lgkmcnt(4)\n\tv_mfma_f32_32x32x16_bf16 v[192:207], a[248:251], a[156:159], v[192:207]\n\tds_read_b128 a[208:211], %7 offset:32768\n\tv_exp_f32_e32 v190, v190\n\tv_cvt_pk_bf16_f32 v92, v184, v185\n\tv_add_f32_e32 %0, %0, v187\n\tv_add_f32_e32 %1, %1, v156\n\tv_accvgpr_read_b32 %2, a112\n\tv_accvgpr_read_b32 %3, a113\n\tv_accvgpr_read_b32 %4, a114\n\tv_accvgpr_read_b32 %5, a115\n\tv_xor_b32_e32 %6, 192, %9\n\tv_mul_f32_e32 %2, %8, %2\n\tv_mul_f32_e32 %3, %8, %3\n\tv_mul_f32_e32 %4, %8, %4\n\tv_mul_f32_e32 %5, %8, %5\n\tv_cvt_pk_bf16_f32 %2, %2, %3\n\tv_cvt_pk_bf16_f32 %3, %4, %5\n\tds_write2_b32 %6, %2, %3 offset0:0 offset1:1\n\ts_add_u32 m0, %10, 49152\n\ts_nop 0\n\tbuffer_load_dwordx4 %11, %12, %13 offen offset:2048 sc1 lds\n\tbuffer_load_dwordx4 %11, %12, %13 offen offset:3072 sc1 lds" : "+v"(c.rs[1][0]), "+v"(c.rs[1][2]), "=&v"(et460_0), "=&v"(et460_1), "=&v"(et460_2), "=&v"(et460_3), "=&v"(et460_4) : "v"(c.vb_[1]), "v"(il[1]), "v"(ebase[1]), "s"(c.m0w), "v"(c.vlin), "s"(c.srdV), "s"(c.soVA) : "memory", "m0", "scc", ACL64);
  float et461_0, et461_1, et461_2, et461_3; unsigned et461_4;
  asm volatile("v_mfma_f32_32x32x16_bf16 v[208:223], a[248:251], a[188:191], v[208:223]\n\tv_exp_f32_e32 v191, v191\n\tv_cvt_pk_bf16_f32 v93, v186, v187\n\tv_add_f32_e32 %0, %0, v188\n\tv_add_f32_e32 %1, %1, v157\n\tv_accvgpr_read_b32 %2, a116\n\tv_accvgpr_read_b32 %3, a117\n\tv_accvgpr_read_b32 %4, a118\n\tv_accvgpr_read_b32 %5, a119\n\tv_xor_b32_e32 %6, 208, %8\n\tv_mul_f32_e32 %2, %7, %2\n\tv_mul_f32_e32 %3, %7, %3\n\tv_mul_f32_e32 %4, %7, %4\n\tv_mul_f32_e32 %5, %7, %5\n\tv_cvt_pk_bf16_f32 %2, %2, %3\n\tv_cvt_pk_bf16_f32 %3, %4, %5\n\tds_write2_b32 %6, %2, %3 offset0:0 offset1:1" : "+v"(c.rs[1][1]), "+v"(c.rs[1][3]), "=&v"(et461_0), "=&v"(et461_1), "=&v"(et461_2), "=&v"(et461_3), "=&v"(et461_4) : "v"(il[1]), "v"(ebase[1]) : "memory");
  float et462_0, et462_1, et462_2, et462_3; unsigned et462_4;
  asm volatile("v_mfma_f32_32x32x16_bf16 v[224:239], a[252:255], a[156:159], v[224:239]\n\tds_read_b128 a[212:215], %7 offset:36864\n\tv_cvt_pk_bf16_f32 v94, v188, v189\n\tv_add_f32_e32 %0, %0, v189\n\tv_add_f32_e32 %1, %1, v158\n\tv_accvgpr_read_b32 %2, a120\n\tv_accvgpr_read_b32 %3, a121\n\tv_accvgpr_read_b32 %4, a122\n\tv_accvgpr_read_b32 %5, a123\n\tv_xor_b32_e32 %6, 224, %9\n\tv_mul_f32_e32 %2, %8, %2\n\tv_mul_f32_e32 %3, %8, %3\n\tv_mul_f32_e32 %4, %8, %4\n\tv_mul_f32_e32 %5, %8, %5\n\tv_cvt_pk_bf16_f32 %2, %2, %3\n\tv_cvt_pk_bf16_f32 %3, %4, %5\n\tds_write2_b32 %6, %2, %3 offset0:0 offset1:1\n\ts_cmp_le_i32 %10, 14\n\ts_cbranch_scc0 .Ldma463_%=\n\ts_add_u32 m0, %11, 16384\n\ts_nop 0\n\tbuffer_load_dwordx4 %12, %13, %14 offen offset:0 sc1 lds\n\tbuffer_load_dwordx4 %12, %13, %14 offen offset:1024 sc1 lds\n\t.Ldma463_%=:" : "+v"(c.rs[1][0]), "+v"(c.rs[1][2]), "=&v"(et462_0), "=&v"(et462_1), "=&v"(et462_2), "=&v"(et462_3), "=&v"(et462_4) : "v"(c.vb_[1]), "v"(il[1]), "v"(ebase[1]), "s"(c.t), "s"(c.m0w), "v"(c.vlin), "s"(c.srdK), "s"(c.soKA) : "memory", "scc", "m0");
  float et464_0, et464_1, et464_2, et464_3; unsigned et464_4;
  asm volatile("v_mfma_f32_32x32x16_bf16 v[240:255], a[252:255], a[188:191], v[240:255]\n\tv_cvt_pk_bf16_f32 v95, v190, v191\n\tv_add_f32_e32 %0, %0, v190\n\tv_add_f32_e32 %1, %1, v191\n\tv_add_f32_e32 %2, %2, v159\n\tv_accvgpr_read_b32 %3, a124\n\tv_accvgpr_read_b32 %4, a125\n\tv_accvgpr_read_b32 %5, a126\n\tv_accvgpr_read_b32 %6, a127\n\tv_xor_b32_e32 %7, 240, %9\n\tv_mul_f32_e32 %3, %8, %3\n\tv_mul_f32_e32 %4, %8, %4\n\tv_mul_f32_e32 %5, %8, %5\n\tv_mul_f32_e32 %6, %8, %6\n\tv_cvt_pk_bf16_f32 %3, %3, %4\n\tv_cvt_pk_bf16_f32 %4, %5, %6\n\tds_write2_b32 %7, %3, %4 offset0:0 offset1:1\n\ts_cmp_le_i32 %10, 14\n\ts_cbranch_scc0 .Ldma465_%=\n\ts_add_u32 m0, %11, 16384\n\ts_nop 0\n\tbuffer_load_dwordx4 %12, %13, %14 offen offset:2048 sc1 lds\n\tbuffer_load_dwordx4 %12, %13, %14 offen offset:3072 sc1 lds\n\t.Ldma465_%=:" : "+v"(c.rs[1][1]), "+v"(c.rs[1][0]), "+v"(c.rs[1][3]), "=&v"(et464_0), "=&v"(et464_1), "=&v"(et464_2), "=&v"(et464_3), "=&v"(et464_4) : "v"(il[1]), "v"(ebase[1]), "s"(c.t), "s"(c.m0w), "v"(c.vlin), "s"(c.srdK), "s"(c.soKA) : "memory", "scc", "m0");
}
DI void gB1az(AC& c, const unsigned (&sr)[4], const unsigned svo, const srd_t sctx, const unsigned sso) {
  asm volatile("s_waitcnt lgkmcnt(0)\n\ts_waitcnt lgkmcnt(4)\n\tv_mfma_f32_32x32x16_bf16 a[0:15], a[192:195], v[64:67], 0\n\tds_read_b128 a[216:219], %0 offset:40960\n\tds_read_b128 v[128:131], %1 offset:0\n\tds_read_b128 v[132:135], %2 offset:0\n\tds_read_b128 v[136:139], %3 offset:0\n\tds_read_b128 v[140:143], %4 offset:0\n\tds_read_b128 v[144:147], %1 offset:4096\n\tds_read_b128 v[148:151], %2 offset:4096\n\tds_read_b128 v[152:155], %3 offset:4096\n\tds_read_b128 v[156:159], %4 offset:4096\n\ts_cmp_ge_i32 %5, %6\n\ts_cbranch_scc0 .Ldma466_%=\n\ts_add_u32 m0, %7, 114688\n\ts_nop 0\n\tbuffer_load_dwordx4 %8, %9, %10 offen offset:0 sc1 lds\n\tbuffer_load_dwordx4 %8, %9, %10 offen offset:1024 sc1 lds\n\tbuffer_load_dwordx4 %8, %9, %10 offen offset:2048 sc1 lds\n\tbuffer_load_dwordx4 %8, %9, %10 offen offset:3072 sc1 lds\n\t.Ldma466_%=:" :  : "v"(c.vb_[1]), "v"(sr[0]), "v"(sr[1]), "v"(sr[2]), "v"(sr[3]), "s"(c.t), "s"(c.n1), "s"(c.m0w), "v"(c.vlin), "s"(c.srdV), "s"(c.soVB) : "memory", "scc", "m0");
  asm volatile("v_mfma_f32_32x32x16_bf16 a[16:31], a[192:195], v[80:83], 0\n\tds_read_b128 v[160:163], %0 offset:8192\n\tds_read_b128 v[164:167], %1 offset:8192\n\tds_read_b128 v[168:171], %2 offset:8192\n\tds_read_b128 v[172:175], %3 offset:8192\n\tds_read_b128 v[176:179], %0 offset:12288\n\tds_read_b128 v[180:183], %1 offset:12288\n\tds_read_b128 v[184:187], %2 offset:12288\n\tds_read_b128 v[188:191], %3 offset:12288\n\ts_cmp_lt_u32 %4, %5\n\ts_cbranch_scc0 .Ldma467_%=\n\ts_add_u32 m0, %6, 81920\n\ts_nop 0\n\tbuffer_load_dwordx4 %7, %8, %9 offen offset:0 sc1 lds\n\tbuffer_load_dwordx4 %7, %8, %9 offen offset:1024 sc1 lds\n\tbuffer_load_dwordx4 %7, %8, %9 offen offset:2048 sc1 lds\n\tbuffer_load_dwordx4 %7, %8, %9 offen offset:3072 sc1 lds\n\t.Ldma467_%=:" :  : "v"(sr[0]), "v"(sr[1]), "v"(sr[2]), "v"(sr[3]), "s"(c.u3), "s"(c.lim3), "s"(c.m0w), "v"(c.vlin), "s"(c.srdK), "s"(c.soKB) : "memory", "scc", "m0");
  asm volatile("v_mfma_f32_32x32x16_bf16 a[32:47], a[196:199], v[64:67], 0\n\tds_read_b128 a[220:223], %2 offset:45056\n\tv_max3_f32 %0, v192, v193, v224\n\tv_max3_f32 %1, v194, v195, v225\n\tv_max3_f32 %0, %0, v226, v227" : "=&v"(c.pmx[0][0]), "=&v"(c.pmx[0][1]) : "v"(c.vb_[1]) : "memory");
  asm volatile("v_mfma_f32_32x32x16_bf16 a[48:63], a[196:199], v[80:83], 0\n\tv_max3_f32 %0, %0, v196, v197\n\tv_max3_f32 %1, %1, v198, v199\n\tv_max3_f32 %0, %0, v228, v229\n\tv_max3_f32 %1, %1, v230, v231" : "+v"(c.pmx[0][0]), "+v"(c.pmx[0][1]) :  : "memory");
  unsigned so468;
  unsigned so469;
  asm volatile("s_waitcnt lgkmcnt(1)\n\ts_waitcnt lgkmcnt(4)\n\tv_mfma_f32_32x32x16_bf16 a[64:79], a[200:203], v[64:67], 0\n\tds_read_b128 a[224:227], %4 offset:32768\n\tv_max3_f32 %0, %0, v200, v201\n\tv_max3_f32 %1, %1, v202, v203\n\tv_max3_f32 %0, %0, v232, v233\n\tv_max3_f32 %1, %1, v234, v235\n\ts_add_u32 %2, %5, 0\n\tbuffer_store_dwordx4 v[128:131], %6, %7, %2 offen sc1\n\ts_add_u32 %3, %5, 32768\n\tbuffer_store_dwordx4 v[132:135], %6, %7, %3 offen sc1" : "+v"(c.pmx[0][0]), "+v"(c.pmx[0][1]), "=&s"(so468), "=&s"(so469) : "v"(c.vb_[2]), "s"(sso), "v"(svo), "s"(sctx) : "memory", "scc", ACL64);
  unsigned so470;
  unsigned so471;
  asm volatile("v_mfma_f32_32x32x16_bf16 a[80:95], a[200:203], v[80:83], 0\n\tv_max3_f32 %0, %0, v204, v205\n\tv_max3_f32 %1, %1, v206, v207\n\tv_max3_f32 %0, %0, v236, v237\n\tv_max3_f32 %1, %1, v238, v239\n\ts_add_u32 %2, %4, 65536\n\tbuffer_store_dwordx4 v[136:139], %5, %6, %2 offen sc1\n\ts_add_u32 %3, %4, 98304\n\tbuffer_store_dwordx4 v[140:143], %5, %6, %3 offen sc1" : "+v"(c.pmx[0][0]), "+v"(c.pmx[0][1]), "=&s"(so470), "=&s"(so471) : "s"(sso), "v"(svo), "s"(sctx) : "memory", "scc");
  unsigned so472;
  unsigned so473;
  asm volatile("v_mfma_f32_32x32x16_bf16 a[96:111], a[204:207], v[64:67], 0\n\tds_read_b128 a[228:231], %4 offset:36864\n\tv_max3_f32 %0, v208, v209, v240\n\tv_max3_f32 %1, v210, v211, v241\n\tv_max3_f32 %0, %0, v242, v243\n\ts_add_u32 %2, %5, 131072\n\tbuffer_store_dwordx4 v[144:147], %6, %7, %2 offen sc1\n\ts_add_u32 %3, %5, 163840\n\tbuffer_store_dwordx4 v[148:151], %6, %7, %3 offen sc1" : "=&v"(c.pmx[1][0]), "=&v"(c.pmx[1][1]), "=&s"(so472), "=&s"(so473) : "v"(c.vb_[2]), "s"(sso), "v"(svo), "s"(sctx) : "memory", "scc");
  unsigned so474;
  unsigned so475;
  asm volatile("v_mfma_f32_32x32x16_bf16 a[112:127], a[204:207], v[80:83], 0\n\tv_max3_f32 %0, %0, v212, v213\n\tv_max3_f32 %1, %1, v214, v215\n\tv_max3_f32 %0, %0, v244, v245\n\tv_max3_f32 %1, %1, v246, v247\n\ts_add_u32 %2, %4, 196608\n\tbuffer_store_dwordx4 v[152:155], %5, %6, %2 offen sc1\n\ts_add_u32 %3, %4, 229376\n\tbuffer_store_dwordx4 v[156:159], %5, %6, %3 offen sc1" : "+v"(c.pmx[1][0]), "+v"(c.pmx[1][1]), "=&s"(so474), "=&s"(so475) : "s"(sso), "v"(svo), "s"(sctx) : "memory", "scc");
  unsigned so476;
  unsigned so477;
  asm volatile("s_waitcnt lgkmcnt(4)\n\tv_mfma_f32_32x32x16_bf16 a[0:15], a[208:211], v[68:71], a[0:15]\n\tds_read_b128 a[232:235], %4 offset:40960\n\tv_max3_f32 %0, %0, v216, v217\n\tv_max3_f32 %1, %1, v218, v219\n\tv_max3_f32 %0, %0, v248, v249\n\tv_max3_f32 %1, %1, v250, v251\n\ts_add_u32 %2, %5, 262144\n\tbuffer_store_dwordx4 v[160:163], %6, %7, %2 offen sc1\n\ts_add_u32 %3, %5, 294912\n\tbuffer_store_dwordx4 v[164:167], %6, %7, %3 offen sc1" : "+v"(c.pmx[1][0]), "+v"(c.pmx[1][1]), "=&s"(so476), "=&s"(so477) : "v"(c.vb_[2]), "s"(sso), "v"(svo), "s"(sctx) : "memory", "scc");
  unsigned so478;
  unsigned so479;
  asm volatile("v_mfma_f32_32x32x16_bf16 a[16:31], a[208:211], v[84:87], a[16:31]\n\tv_max3_f32 %0, %0, v220, v221\n\tv_max3_f32 %1, %1, v222, v223\n\tv_max3_f32 %0, %0, v252, v253\n\tv_max3_f32 %1, %1, v254, v255\n\ts_add_u32 %2, %4, 327680\n\tbuffer_store_dwordx4 v[168:171], %5, %6, %2 offen sc1\n\ts_add_u32 %3, %4, 360448\n\tbuffer_store_dwordx4 v[172:175], %5, %6, %3 offen sc1" : "+v"(c.pmx[1][0]), "+v"(c.pmx[1][1]), "=&s"(so478), "=&s"(so479) : "s"(sso), "v"(svo), "s"(sctx) : "memory", "scc");
  unsigned so480;
  unsigned so481;
  asm volatile("v_mfma_f32_32x32x16_bf16 a[32:47], a[212:215], v[68:71], a[32:47]\n\tds_read_b128 a[236:239], %4 offset:45056\n\tv_max_f32_e32 %0, %0, %1\n\tv_mov_b32_e32 %1, %0\n\ts_nop 1\n\tv_permlane32_swap_b32_e32 %0, %1\n\tv_max_f32_e32 %0, %0, %1\n\ts_add_u32 %2, %5, 393216\n\tbuffer_store_dwordx4 v[176:179], %6, %7, %2 offen sc1\n\ts_add_u32 %3, %5, 425984\n\tbuffer_store_dwordx4 v[180:183], %6, %7, %3 offen sc1" : "+v"(c.pmx[0][0]), "+v"(c.pmx[0][1]), "=&s"(so480), "=&s"(so481) : "v"(c.vb_[2]), "s"(sso), "v"(svo), "s"(sctx) : "memory", "scc");
  unsigned so482;
  unsigned so483;
  asm volatile("v_mfma_f32_32x32x16_bf16 a[48:63], a[212:215], v[84:87], a[48:63]\n\tv_max_f32_e32 %0, %0, %1\n\tv_mov_b32_e32 %1, %0\n\ts_nop 1\n\tv_permlane32_swap_b32_e32 %0, %1\n\tv_max_f32_e32 %0, %0, %1\n\ts_add_u32 %2, %4, 458752\n\tbuffer_store_dwordx4 v[184:187], %5, %6, %2 offen sc1\n\ts_add_u32 %3, %4, 491520\n\tbuffer_store_dwordx4 v[188:191], %5, %6, %3 offen sc1" : "+v"(c.pmx[1][0]), "+v"(c.pmx[1][1]), "=&s"(so482), "=&s"(so483) : "s"(sso), "v"(svo), "s"(sctx) : "memory", "scc");
}
DI void gA0m1(AC& c) {
  asm volatile("s_waitcnt lgkmcnt(4)\n\tv_mfma_f32_32x32x16_bf16 v[128:143], a[192:195], a[128:131], v[96:111]\n\tds_read_b128 a[216:219], %0 offset:0\n\tv_exp_f32_e32 v224, v224\n\tv_exp_f32_e32 v225, v225\n\tv_cvt_pk_bf16_f32 v64, v192, v193" :  : "v"(c.kb_[3]) : "memory", ACL64);
  asm volatile("v_mfma_f32_32x32x16_bf16 v[144:159], a[192:195], a[160:163], v[112:127]\n\tv_exp_f32_e32 v226, v226\n\tv_exp_f32_e32 v227, v227\n\tv_cvt_pk_bf16_f32 v65, v194, v195\n\tv_add_f32_e32 %0, %0, v224" : "+v"(c.rs[0][1]) :  : "memory");
  asm volatile("v_mfma_f32_32x32x16_bf16 v[160:175], a[196:199], a[128:131], v[96:111]\n\tds_read_b128 a[220:223], %1 offset:8192\n\tv_exp_f32_e32 v228, v228\n\tv_cvt_pk_bf16_f32 v66, v196, v197\n\tv_add_f32_e32 %0, %0, v225" : "+v"(c.rs[0][0]) : "v"(c.kb_[3]) : "memory");
  asm volatile("v_mfma_f32_32x32x16_bf16 v[176:191], a[196:199], a[160:163], v[112:127]\n\tv_exp_f32_e32 v229, v229\n\tv_cvt_pk_bf16_f32 v67, v198, v199\n\tv_add_f32_e32 %0, %0, v226" : "+v"(c.rs[0][1]) :  : "memory");
  asm volatile("s_waitcnt lgkmcnt(4)\n\tv_mfma_f32_32x32x16_bf16 v[128:143], a[200:203], a[132:135], v[128:143]\n\tds_read_b128 a[224:227], %1 offset:0\n\tv_exp_f32_e32 v230, v230\n\tv_cvt_pk_bf16_f32 v80, v208, v209\n\tv_add_f32_e32 %0, %0, v227" : "+v"(c.rs[0][0]) : "v"(c.kb_[4]) : "memory");
  asm volatile("v_mfma_f32_32x32x16_bf16 v[144:159], a[200:203], a[164:167], v[144:159]\n\tv_exp_f32_e32 v231, v231\n\tv_cvt_pk_bf16_f32 v81, v210, v211\n\tv_add_f32_e32 %0, %0, v228" : "+v"(c.rs[0][1]) :  : "memory");
  asm volatile("v_mfma_f32_32x32x16_bf16 v[160:175], a[204:207], a[132:135], v[160:175]\n\tds_read_b128 a[228:231], %1 offset:8192\n\tv_exp_f32_e32 v232, v232\n\tv_cvt_pk_bf16_f32 v82, v212, v213\n\tv_add_f32_e32 %0, %0, v229" : "+v"(c.rs[0][0]) : "v"(c.kb_[4]) : "memory");
  asm volatile("v_mfma_f32_32x32x16_bf16 v[176:191], a[204:207], a[164:167], v[176:191]\n\tv_exp_f32_e32 v233, v233\n\tv_cvt_pk_bf16_f32 v83, v214, v215\n\tv_add_f32_e32 %0, %0, v230" : "+v"(c.rs[0][1]) :  : "memory");
  asm volatile("s_waitcnt lgkmcnt(4)\n\tv_mfma_f32_32x32x16_bf16 v[128:143], a[208:211], a[136:139], v[128:143]\n\tds_read_b128 a[232:235], %1 offset:0\n\tv_exp_f32_e32 v234, v234\n\tv_cvt_pk_bf16_f32 v68, v200, v201\n\tv_add_f32_e32 %0, %0, v231" : "+v"(c.rs[0][0]) : "v"(c.kb_[5]) : "memory", ACL64);
  asm volatile("v_mfma_f32_32x32x16_bf16 v[144:159], a[208:211], a[168:171], v[144:159]\n\tv_exp_f32_e32 v235, v235\n\tv_cvt_pk_bf16_f32 v69, v202, v203\n\tv_add_f32_e32 %0, %0, v232" : "+v"(c.rs[0][1]) :  : "memory");
  asm volatile("v_mfma_f32_32x32x16_bf16 v[160:175], a[212:215], a[136:139], v[160:175]\n\tds_read_b128 a[236:239], %1 offset:8192\n\tv_exp_f32_e32 v236, v236\n\tv_cvt_pk_bf16_f32 v70, v204, v205\n\tv_add_f32_e32 %0, %0, v233" : "+v"(c.rs[0][0]) : "v"(c.kb_[5]) : "memory");
  asm volatile("v_mfma_f32_32x32x16_bf16 v[176:191], a[212:215], a[168:171], v[176:191]\n\tv_exp_f32_e32 v237, v237\n\tv_cvt_pk_bf16_f32 v71, v206, v207\n\tv_add_f32_e32 %0, %0, v234" : "+v"(c.rs[0][1]) :  : "memory");
  asm volatile("s_waitcnt lgkmcnt(4)\n\tv_mfma_f32_32x32x16_bf16 v[128:143], a[216:219], a[140:143], v[128:143]\n\tds_read_b128 a[240:243], %1 offset:0\n\tv_exp_f32_e32 v238, v238\n\tv_cvt_pk_bf16_f32 v84, v216, v217\n\tv_add_f32_e32 %0, %0, v235" : "+v"(c.rs[0][0]) : "v"(c.kb_[6]) : "memory");
  asm volatile("v_mfma_f32_32x32x16_bf16 v[144:159], a[216:219], a[172:175], v[144:159]\n\tv_exp_f32_e32 v239, v239\n\tv_cvt_pk_bf16_f32 v85, v218, v219\n\tv_add_f32_e32 %0, %0, v236" : "+v"(c.rs[0][1]) :  : "memory");
  asm volatile("v_mfma_f32_32x32x16_bf16 v[160:175], a[220:223], a[140:143], v[160:175]\n\tds_read_b128 a[244:247], %1 offset:8192\n\tv_exp_f32_e32 v240, v240\n\tv_cvt_pk_bf16_f32 v86, v220, v221\n\tv_add_f32_e32 %0, %0, v237" : "+v"(c.rs[0][0]) : "v"(c.kb_[6]) : "memory");
  asm volatile("v_mfma_f32_32x32x16_bf16 v[176:191], a[220:223], a[172:175], v[176:191]\n\tv_exp_f32_e32 v241, v241\n\tv_cvt_pk_bf16_f32 v87, v222, v223\n\tv_add_f32_e32 %0, %0, v238" : "+v"(c.rs[0][1]) :  : "memory");
  asm volatile("s_waitcnt lgkmcnt(4)\n\tv_mfma_f32_32x32x16_bf16 v[128:143], a[224:227], a[144:147], v[128:143]\n\tds_read_b128 a[248:251], %2 offset:0\n\tv_exp_f32_e32 v242, v242\n\tv_cvt_pk_bf16_f32 v72, v224, v225\n\tv_add_f32_e32 %0, %0, v239\n\tv_add_f32_e32 %1, %1, v208" : "+v"(c.rs[0][0]), "+v"(c.rs[1][2]) : "v"(c.kb_[7]) : "memory", ACL64);
  asm volatile("v_mfma_f32_32x32x16_bf16 v[144:159], a[224:227], a[176:179], v[144:159]\n\tv_exp_f32_e32 v243, v243\n\tv_cvt_pk_bf16_f32 v73, v226, v227\n\tv_add_f32_e32 %0, %0, v240\n\tv_add_f32_e32 %1, %1, v209" : "+v"(c.rs[1][1]), "+v"(c.rs[1][3]) :  : "memory");
  asm volatile("v_mfma_f32_32x32x16_bf16 v[160:175], a[228:231], a[144:147], v[160:175]\n\tds_read_b128 a[252:255], %2 offset:8192\n\tv_exp_f32_e32 v244, v244\n\tv_cvt_pk_bf16_f32 v74, v228, v229\n\tv_add_f32_e32 %0, %0, v241\n\tv_add_f32_e32 %1, %1, v210" : "+v"(c.rs[1][0]), "+v"(c.rs[1][2]) : "v"(c.kb_[7]) : "memory");
  asm volatile("v_mfma_f32_32x32x16_bf16 v[176:191], a[228:231], a[176:179], v[176:191]\n\tv_exp_f32_e32 v245, v245\n\tv_cvt_pk_bf16_f32 v75, v230, v231\n\tv_add_f32_e32 %0, %0, v242\n\tv_add_f32_e32 %1, %1, v211" : "+v"(c.rs[1][1]), "+v"(c.rs[1][3]) :  : "memory");
  asm volatile("s_waitcnt lgkmcnt(4)\n\tv_mfma_f32_32x32x16_bf16 v[128:143], a[232:235], a[148:151], v[128:143]\n\tv_exp_f32_e32 v246, v246\n\tv_cvt_pk_bf16_f32 v76, v232, v233\n\tv_add_f32_e32 %0, %0, v243\n\tv_add_f32_e32 %1, %1, v212" : "+v"(c.rs[1][0]), "+v"(c.rs[1][2]) :  : "memory");
  asm volatile("v_mfma_f32_32x32x16_bf16 v[144:159], a[232:235], a[180:183], v[144:159]\n\tv_exp_f32_e32 v247, v247\n\tv_cvt_pk_bf16_f32 v77, v234, v235\n\tv_add_f32_e32 %0, %0, v244\n\tv_add_f32_e32 %1, %1, v213" : "+v"(c.rs[1][1]), "+v"(c.rs[1][3]) :  : "memory");
  asm volatile("v_mfma_f32_32x32x16_bf16 v[160:175], a[236:239], a[148:151], v[160:175]\n\tv_exp_f32_e32 v248, v248\n\tv_cvt_pk_bf16_f32 v78, v236, v237\n\tv_add_f32_e32 %0, %0, v245\n\tv_add_f32_e32 %1, %1, v214" : "+v"(c.rs[1][0]), "+v"(c.rs[1][2]) :  : "memory");
  asm volatile("v_mfma_f32_32x32x16_bf16 v[176:191], a[236:239], a[180:183], v[176:191]\n\tv_exp_f32_e32 v249, v249\n\tv_cvt_pk_bf16_f32 v79, v238, v239\n\tv_add_f32_e32 %0, %0, v246\n\tv_add_f32_e32 %1, %1, v215" : "+v"(c.rs[1][1]), "+v"(c.rs[1][3]) :  : "memory");
  asm volatile("s_waitcnt vmcnt(0) lgkmcnt(0)\n\ts_barrier\n\ts_waitcnt lgkmcnt(2)\n\tv_mfma_f32_32x32x16_bf16 v[128:143], a[240:243], a[152:155], v[128:143]\n\tds_read_b128 a[192:195], %2 offset:49152\n\tv_exp_f32_e32 v250, v250\n\tv_cvt_pk_bf16_f32 v88, v240, v241\n\tv_add_f32_e32 %0, %0, v247\n\tv_add_f32_e32 %1, %1, v216" : "+v"(c.rs[1][0]), "+v"(c.rs[1][2]) : "v"(c.vb_[0]) : "memory", ACL64);
  asm volatile("v_mfma_f32_32x32x16_bf16 v[144:159], a[240:243], a[184:187], v[144:159]\n\tds_read_b128 a[196:199], %2 offset:53248\n\tv_exp_f32_e32 v251, v251\n\tv_cvt_pk_bf16_f32 v89, v242, v243\n\tv_add_f32_e32 %0, %0, v248\n\tv_add_f32_e32 %1, %1, v217\n\ts_add_u32 m0, %3, 32768\n\ts_nop 0\n\tbuffer_load_dwordx4 %4, %5, %6 offen offset:0 sc1 lds" : "+v"(c.rs[1][1]), "+v"(c.rs[1][3]) : "v"(c.vb_[0]), "s"(c.m0w), "v"(c.vlin), "s"(c.srdV), "s"(c.soVA) : "memory", "m0", "scc");
  asm volatile("v_mfma_f32_32x32x16_bf16 v[160:175], a[244:247], a[152:155], v[160:175]\n\tds_read_b128 a[200:203], %2 offset:57344\n\tv_exp_f32_e32 v252, v252\n\tv_cvt_pk_bf16_f32 v90, v244, v245\n\tv_add_f32_e32 %0, %0, v249\n\tv_add_f32_e32 %1, %1, v218" : "+v"(c.rs[1][0]), "+v"(c.rs[1][2]) : "v"(c.vb_[0]) : "memory");
  asm volatile("v_mfma_f32_32x32x16_bf16 v[176:191], a[244:247], a[184:187], v[176:191]\n\tds_read_b128 a[204:207], %2 offset:61440\n\tv_exp_f32_e32 v253, v253\n\tv_cvt_pk_bf16_f32 v91, v246, v247\n\tv_add_f32_e32 %0, %0, v250\n\tv_add_f32_e32 %1, %1, v219" : "+v"(c.rs[1][1]), "+v"(c.rs[1][3]) : "v"(c.vb_[0]) : "memory");
  asm volatile("s_waitcnt lgkmcnt(4)\n\tv_mfma_f32_32x32x16_bf16 v[128:143], a[248:251], a[156:159], v[128:143]\n\tds_read_b128 a[208:211], %2 offset:49152\n\tv_exp_f32_e32 v254, v254\n\tv_cvt_pk_bf16_f32 v92, v248, v249\n\tv_add_f32_e32 %0, %0, v251\n\tv_add_f32_e32 %1, %1, v220\n\tbuffer_load_dwordx4 %3, %4, %5 offen offset:1024 sc1 lds" : "+v"(c.rs[1][0]), "+v"(c.rs[1][2]) : "v"(c.vb_[1]), "v"(c.vlin), "s"(c.srdV), "s"(c.soVA) : "memory");
  asm volatile("v_mfma_f32_32x32x16_bf16 v[144:159], a[248:251], a[188:191], v[144:159]\n\tv_exp_f32_e32 v255, v255\n\tv_cvt_pk_bf16_f32 v93, v250, v251\n\tv_add_f32_e32 %0, %0, v252\n\tv_add_f32_e32 %1, %1, v221" : "+v"(c.rs[1][1]), "+v"(c.rs[1][3]) :  : "memory");
  asm volatile("v_mfma_f32_32x32x16_bf16 v[160:175], a[252:255], a[156:159], v[160:175]\n\tds_read_b128 a[212:215], %2 offset:53248\n\tv_cvt_pk_bf16_f32 v94, v252, v253\n\tv_add_f32_e32 %0, %0, v253\n\tv_add_f32_e32 %1, %1, v222" : "+v"(c.rs[1][0]), "+v"(c.rs[1][2]) : "v"(c.vb_[1]) : "memory");
  asm volatile("v_mfma_f32_32x32x16_bf16 v[176:191], a[252:255], a[188:191], v[176:191]\n\tv_cvt_pk_bf16_f32 v95, v254, v255\n\tv_add_f32_e32 %0, %0, v254\n\tv_add_f32_e32 %1, %1, v255\n\tv_add_f32_e32 %2, %2, v223\n\tbuffer_load_dwordx4 %3, %4, %5 offen offset:2048 sc1 lds" : "+v"(c.rs[1][1]), "+v"(c.rs[1][0]), "+v"(c.rs[1][3]) : "v"(c.vlin), "s"(c.srdV), "s"(c.soVA) : "memory");
}
DI void gB0am1(AC& c) {
  asm volatile("s_waitcnt lgkmcnt(4)\n\tv_mfma_f32_32x32x16_bf16 a[0:15], a[192:195], v[64:67], a[0:15]\n\tds_read_b128 a[216:219], %0 offset:57344" :  : "v"(c.vb_[1]) : "memory", ACL64);
  asm volatile("v_mfma_f32_32x32x16_bf16 a[16:31], a[192:195], v[80:83], a[16:31]" :  :  : "memory");
  asm volatile("v_mfma_f32_32x32x16_bf16 a[32:47], a[196:199], v[64:67], a[32:47]\n\tds_read_b128 a[220:223], %2 offset:61440\n\tv_max3_f32 %0, v128, v129, v160\n\tv_max3_f32 %1, v130, v131, v161\n\tv_max3_f32 %0, %0, v162, v163\n\tbuffer_load_dwordx4 %3, %4, %5 offen offset:3072 sc1 lds" : "=&v"(c.pmx[0][0]), "=&v"(c.pmx[0][1]) : "v"(c.vb_[1]), "v"(c.vlin), "s"(c.srdV), "s"(c.soVA) : "memory");
  asm volatile("v_mfma_f32_32x32x16_bf16 a[48:63], a[196:199], v[80:83], a[48:63]\n\tv_max3_f32 %0, %0, v132, v133\n\tv_max3_f32 %1, %1, v134, v135\n\tv_max3_f32 %0, %0, v164, v165\n\tv_max3_f32 %1, %1, v166, v167" : "+v"(c.pmx[0][0]), "+v"(c.pmx[0][1]) :  : "memory");
  asm volatile("s_waitcnt lgkmcnt(4)\n\tv_mfma_f32_32x32x16_bf16 a[64:79], a[200:203], v[64:67], a[64:79]\n\tds_read_b128 a[224:227], %2 offset:49152\n\tv_max3_f32 %0, %0, v136, v137\n\tv_max3_f32 %1, %1, v138, v139\n\tv_max3_f32 %0, %0, v168, v169\n\tv_max3_f32 %1, %1, v170, v171" : "+v"(c.pmx[0][0]), "+v"(c.pmx[0][1]) : "v"(c.vb_[2]) : "memory");
  asm volatile("v_mfma_f32_32x32x16_bf16 a[80:95], a[200:203], v[80:83], a[80:95]\n\tv_max3_f32 %0, %0, v140, v141\n\tv_max3_f32 %1, %1, v142, v143\n\tv_max3_f32 %0, %0, v172, v173\n\tv_max3_f32 %1, %1, v174, v175\n\ts_add_u32 m0, %2, 0\n\ts_nop 0\n\tbuffer_load_dwordx4 %3, %4, %5 offen offset:0 sc1 lds" : "+v"(c.pmx[0][0]), "+v"(c.pmx[0][1]) : "s"(c.m0w), "v"(c.vlin), "s"(c.srdK), "s"(c.soKA) : "memory", "m0", "scc");
  asm volatile("v_mfma_f32_32x32x16_bf16 a[96:111], a[204:207], v[64:67], a[96:111]\n\tds_read_b128 a[228:231], %2 offset:53248\n\tv_max3_f32 %0, v144, v145, v176\n\tv_max3_f32 %1, v146, v147, v177\n\tv_max3_f32 %0, %0, v178, v179" : "=&v"(c.pmx[1][0]), "=&v"(c.pmx[1][1]) : "v"(c.vb_[2]) : "memory");
  asm volatile("v_mfma_f32_32x32x16_bf16 a[112:127], a[204:207], v[80:83], a[112:127]\n\tv_max3_f32 %0, %0, v148, v149\n\tv_max3_f32 %1, %1, v150, v151\n\tv_max3_f32 %0, %0, v180, v181\n\tv_max3_f32 %1, %1, v182, v183" : "+v"(c.pmx[1][0]), "+v"(c.pmx[1][1]) :  : "memory");
  asm volatile("s_waitcnt lgkmcnt(4)\n\tv_mfma_f32_32x32x16_bf16 a[0:15], a[208:211], v[68:71], a[0:15]\n\tds_read_b128 a[232:235], %2 offset:57344\n\tv_max3_f32 %0, %0, v152, v153\n\tv_max3_f32 %1, %1, v154, v155\n\tv_max3_f32 %0, %0, v184, v185\n\tv_max3_f32 %1, %1, v186, v187\n\tbuffer_load_dwordx4 %3, %4, %5 offen offset:1024 sc1 lds" : "+v"(c.pmx[1][0]), "+v"(c.pmx[1][1]) : "v"(c.vb_[2]), "v"(c.vlin), "s"(c.srdK), "s"(c.soKA) : "memory", ACL64);
  asm volatile("v_mfma_f32_32x32x16_bf16 a[16:31], a[208:211], v[84:87], a[16:31]\n\tv_max3_f32 %0, %0, v156, v157\n\tv_max3_f32 %1, %1, v158, v159\n\tv_max3_f32 %0, %0, v188, v189\n\tv_max3_f32 %1, %1, v190, v191" : "+v"(c.pmx[1][0]), "+v"(c.pmx[1][1]) :  : "memory");
  asm volatile("v_mfma_f32_32x32x16_bf16 a[32:47], a[212:215], v[68:71], a[32:47]\n\tds_read_b128 a[236:239], %2 offset:61440\n\tv_max_f32_e32 %0, %0, %1\n\tv_mov_b32_e32 %1, %0\n\ts_nop 1\n\tv_permlane32_swap_b32_e32 %0, %1\n\tv_max_f32_e32 %0, %0, %1" : "+v"(c.pmx[0][0]), "+v"(c.pmx[0][1]) : "v"(c.vb_[2]) : "memory");
  asm volatile("v_mfma_f32_32x32x16_bf16 a[48:63], a[212:215], v[84:87], a[48:63]\n\tv_max_f32_e32 %0, %0, %1\n\tv_mov_b32_e32 %1, %0\n\ts_nop 1\n\tv_permlane32_swap_b32_e32 %0, %1\n\tv_max_f32_e32 %0, %0, %1\n\tbuffer_load_dwordx4 %2, %3, %4 offen offset:2048 sc1 lds" : "+v"(c.pmx[1][0]), "+v"(c.pmx[1][1]) : "v"(c.vlin), "s"(c.srdK), "s"(c.soKA) : "memory");
}
DI void gB0bm1(AC& c) {
  asm volatile("s_waitcnt lgkmcnt(4)\n\tv_mfma_f32_32x32x16_bf16 a[64:79], a[216:219], v[68:71], a[64:79]\n\tds_read_b128 a[240:243], %0 offset:49152" :  : "v"(c.vb_[3]) : "memory");
  asm volatile("v_mfma_f32_32x32x16_bf16 a[80:95], a[216:219], v[84:87], a[80:95]\n\tv_exp_f32_e32 v128, v128\n\tv_exp_f32_e32 v129, v129" :  :  : "memory");
  asm volatile("v_mfma_f32_32x32x16_bf16 a[96:111], a[220:223], v[68:71], a[96:111]\n\tds_read_b128 a[244:247], %0 offset:53248\n\tv_exp_f32_e32 v130, v130\n\tv_exp_f32_e32 v131, v131\n\tbuffer_load_dwordx4 %1, %2, %3 offen offset:3072 sc1 lds" :  : "v"(c.vb_[3]), "v"(c.vlin), "s"(c.srdK), "s"(c.soKA) : "memory");
  asm volatile("v_mfma_f32_32x32x16_bf16 a[112:127], a[220:223], v[84:87], a[112:127]\n\tv_exp_f32_e32 v132, v132\n\tv_exp_f32_e32 v133, v133" :  :  : "memory");
  asm volatile("s_waitcnt lgkmcnt(4)\n\tv_mfma_f32_32x32x16_bf16 a[0:15], a[224:227], v[72:75], a[0:15]\n\tds_read_b128 a[248:251], %1 offset:57344\n\tv_exp_f32_e32 v134, v134\n\tv_exp_f32_e32 v135, v135\n\tv_add_f32_e32 %0, %0, v128" : "+v"(c.rs[0][0]) : "v"(c.vb_[3]) : "memory", ACL64);
  asm volatile("v_mfma_f32_32x32x16_bf16 a[16:31], a[224:227], v[88:91], a[16:31]\n\tv_exp_f32_e32 v136, v136\n\tv_exp_f32_e32 v137, v137\n\tv_add_f32_e32 %0, %0, v129" : "+v"(c.rs[0][1]) :  : "memory");
  asm volatile("v_mfma_f32_32x32x16_bf16 a[32:47], a[228:231], v[72:75], a[32:47]\n\tds_read_b128 a[252:255], %1 offset:61440\n\tv_exp_f32_e32 v138, v138\n\tv_exp_f32_e32 v139, v139\n\tv_add_f32_e32 %0, %0, v130" : "+v"(c.rs[0][0]) : "v"(c.vb_[3]) : "memory");
  asm volatile("v_mfma_f32_32x32x16_bf16 a[48:63], a[228:231], v[88:91], a[48:63]\n\tv_exp_f32_e32 v140, v140\n\tv_exp_f32_e32 v141, v141\n\tv_add_f32_e32 %0, %0, v131" : "+v"(c.rs[0][1]) :  : "memory");
  asm volatile("s_waitcnt lgkmcnt(4)\n\tv_mfma_f32_32x32x16_bf16 a[64:79], a[232:235], v[72:75], a[64:79]\n\tds_read_b128 a[192:195], %1 offset:16384\n\tv_exp_f32_e32 v142, v142\n\tv_exp_f32_e32 v143, v143\n\tv_add_f32_e32 %0, %0, v132" : "+v"(c.rs[0][0]) : "v"(c.kb_[0]) : "memory");
  asm volatile("v_mfma_f32_32x32x16_bf16 a[80:95], a[232:235], v[88:91], a[80:95]\n\tv_exp_f32_e32 v144, v144\n\tv_exp_f32_e32 v145, v145\n\tv_add_f32_e32 %0, %0, v133" : "+v"(c.rs[0][1]) :  : "memory");
  asm volatile("v_mfma_f32_32x32x16_bf16 a[96:111], a[236:239], v[72:75], a[96:111]\n\tds_read_b128 a[196:199], %1 offset:24576\n\tv_exp_f32_e32 v146, v146\n\tv_exp_f32_e32 v147, v147\n\tv_add_f32_e32 %0, %0, v134" : "+v"(c.rs[0][0]) : "v"(c.kb_[0]) : "memory");
  asm volatile("v_mfma_f32_32x32x16_bf16 a[112:127], a[236:239], v[88:91], a[112:127]\n\tv_exp_f32_e32 v148, v148\n\tv_exp_f32_e32 v149, v149\n\tv_add_f32_e32 %0, %0, v135" : "+v"(c.rs[0][1]) :  : "memory");
  asm volatile("s_waitcnt lgkmcnt(4)\n\tv_mfma_f32_32x32x16_bf16 a[0:15], a[240:243], v[76:79], a[0:15]\n\tds_read_b128 a[200:203], %1 offset:16384\n\tv_exp_f32_e32 v150, v150\n\tv_exp_f32_e32 v151, v151\n\tv_add_f32_e32 %0, %0, v136" : "+v"(c.rs[0][0]) : "v"(c.kb_[1]) : "memory", ACL64);
  asm volatile("v_mfma_f32_32x32x16_bf16 a[16:31], a[240:243], v[92:95], a[16:31]\n\tv_exp_f32_e32 v152, v152\n\tv_exp_f32_e32 v153, v153\n\tv_add_f32_e32 %0, %0, v137" : "+v"(c.rs[0][1]) :  : "memory");
  asm volatile("v_mfma_f32_32x32x16_bf16 a[32:47], a[244:247], v[76:79], a[32:47]\n\tds_read_b128 a[204:207], %1 offset:24576\n\tv_exp_f32_e32 v154, v154\n\tv_add_f32_e32 %0, %0, v138" : "+v"(c.rs[0][0]) : "v"(c.kb_[1]) : "memory");
  asm volatile("v_mfma_f32_32x32x16_bf16 a[48:63], a[244:247], v[92:95], a[48:63]\n\tv_exp_f32_e32 v155, v155\n\tv_add_f32_e32 %0, %0, v139" : "+v"(c.rs[0][1]) :  : "memory");
  asm volatile("s_waitcnt lgkmcnt(4)\n\tv_mfma_f32_32x32x16_bf16 a[64:79], a[248:251], v[76:79], a[64:79]\n\tds_read_b128 a[208:211], %1 offset:16384\n\tv_exp_f32_e32 v156, v156\n\tv_add_f32_e32 %0, %0, v140" : "+v"(c.rs[0][0]) : "v"(c.kb_[2]) : "memory");
  asm volatile("v_mfma_f32_32x32x16_bf16 a[80:95], a[248:251], v[92:95], a[80:95]\n\tv_exp_f32_e32 v157, v157\n\tv_add_f32_e32 %0, %0, v141" : "+v"(c.rs[0][1]) :  : "memory");
  asm volatile("v_mfma_f32_32x32x16_bf16 a[96:111], a[252:255], v[76:79], a[96:111]\n\tds_read_b128 a[212:215], %1 offset:24576\n\tv_exp_f32_e32 v158, v158\n\tv_add_f32_e32 %0, %0, v142" : "+v"(c.rs[0][0]) : "v"(c.kb_[2]) : "memory");
  asm volatile("v_mfma_f32_32x32x16_bf16 a[112:127], a[252:255], v[92:95], a[112:127]\n\tv_exp_f32_e32 v159, v159\n\tv_add_f32_e32 %0, %0, v143" : "+v"(c.rs[0][1]) :  : "memory");
}
DI void gA1m1(AC& c) {
  asm volatile("s_waitcnt lgkmcnt(4)\n\tv_mfma_f32_32x32x16_bf16 v[192:207], a[192:195], a[128:131], v[96:111]\n\tds_read_b128 a[216:219], %0 offset:16384\n\tv_exp_f32_e32 v160, v160\n\tv_exp_f32_e32 v161, v161\n\tv_cvt_pk_bf16_f32 v64, v128, v129" :  : "v"(c.kb_[3]) : "memory", ACL64);
  asm volatile("v_mfma_f32_32x32x16_bf16 v[208:223], a[192:195], a[160:163], v[112:127]\n\tv_exp_f32_e32 v162, v162\n\tv_exp_f32_e32 v163, v163\n\tv_cvt_pk_bf16_f32 v65, v130, v131\n\tv_add_f32_e32 %0, %0, v160" : "+v"(c.rs[0][1]) :  : "memory");
  asm volatile("v_mfma_f32_32x32x16_bf16 v[224:239], a[196:199], a[128:131], v[96:111]\n\tds_read_b128 a[220:223], %1 offset:24576\n\tv_exp_f32_e32 v164, v164\n\tv_cvt_pk_bf16_f32 v66, v132, v133\n\tv_add_f32_e32 %0, %0, v161" : "+v"(c.rs[0][0]) : "v"(c.kb_[3]) : "memory");
  asm volatile("v_mfma_f32_32x32x16_bf16 v[240:255], a[196:199], a[160:163], v[112:127]\n\tv_exp_f32_e32 v165, v165\n\tv_cvt_pk_bf16_f32 v67, v134, v135\n\tv_add_f32_e32 %0, %0, v162" : "+v"(c.rs[0][1]) :  : "memory");
  asm volatile("s_waitcnt lgkmcnt(4)\n\tv_mfma_f32_32x32x16_bf16 v[192:207], a[200:203], a[132:135], v[192:207]\n\tds_read_b128 a[224:227], %1 offset:16384\n\tv_exp_f32_e32 v166, v166\n\tv_cvt_pk_bf16_f32 v80, v144, v145\n\tv_add_f32_e32 %0, %0, v163" : "+v"(c.rs[0][0]) : "v"(c.kb_[4]) : "memory");
  asm volatile("v_mfma_f32_32x32x16_bf16 v[208:223], a[200:203], a[164:167], v[208:223]\n\tv_exp_f32_e32 v167, v167\n\tv_cvt_pk_bf16_f32 v81, v146, v147\n\tv_add_f32_e32 %0, %0, v164" : "+v"(c.rs[0][1]) :  : "memory");
  asm volatile("v_mfma_f32_32x32x16_bf16 v[224:239], a[204:207], a[132:135], v[224:239]\n\tds_read_b128 a[228:231], %1 offset:24576\n\tv_exp_f32_e32 v168, v168\n\tv_cvt_pk_bf16_f32 v82, v148, v149\n\tv_add_f32_e32 %0, %0, v165" : "+v"(c.rs[0][0]) : "v"(c.kb_[4]) : "memory");
  asm volatile("v_mfma_f32_32x32x16_bf16 v[240:255], a[204:207], a[164:167], v[240:255]\n\tv_exp_f32_e32 v169, v169\n\tv_cvt_pk_bf16_f32 v83, v150, v151\n\tv_add_f32_e32 %0, %0, v166" : "+v"(c.rs[0][1]) :  : "memory");
  asm volatile("s_waitcnt lgkmcnt(4)\n\tv_mfma_f32_32x32x16_bf16 v[192:207], a[208:211], a[136:139], v[192:207]\n\tds_read_b128 a[232:235], %1 offset:16384\n\tv_exp_f32_e32 v170, v170\n\tv_cvt_pk_bf16_f32 v68, v136, v137\n\tv_add_f32_e32 %0, %0, v167" : "+v"(c.rs[0][0]) : "v"(c.kb_[5]) : "memory", ACL64);
  asm volatile("v_mfma_f32_32x32x16_bf16 v[208:223], a[208:211], a[168:171], v[208:223]\n\tv_exp_f32_e32 v171, v171\n\tv_cvt_pk_bf16_f32 v69, v138, v139\n\tv_add_f32_e32 %0, %0, v168" : "+v"(c.rs[0][1]) :  : "memory");
  asm volatile("v_mfma_f32_32x32x16_bf16 v[224:239], a[212:215], a[136:139], v[224:239]\n\tds_read_b128 a[236:239], %1 offset:24576\n\tv_exp_f32_e32 v172, v172\n\tv_cvt_pk_bf16_f32 v70, v140, v141\n\tv_add_f32_e32 %0, %0, v169" : "+v"(c.rs[0][0]) : "v"(c.kb_[5]) : "memory");
  asm volatile("v_mfma_f32_32x32x16_bf16 v[240:255], a[212:215], a[168:171], v[240:255]\n\tv_exp_f32_e32 v173, v173\n\tv_cvt_pk_bf16_f32 v71, v142, v143\n\tv_add_f32_e32 %0, %0, v170" : "+v"(c.rs[0][1]) :  : "memory");
  asm volatile("s_waitcnt lgkmcnt(4)\n\tv_mfma_f32_32x32x16_bf16 v[192:207], a[216:219], a[140:143], v[192:207]\n\tds_read_b128 a[240:243], %1 offset:16384\n\tv_exp_f32_e32 v174, v174\n\tv_cvt_pk_bf16_f32 v84, v152, v153\n\tv_add_f32_e32 %0, %0, v171" : "+v"(c.rs[0][0]) : "v"(c.kb_[6]) : "memory");
  asm volatile("v_mfma_f32_32x32x16_bf16 v[208:223], a[216:219], a[172:175], v[208:223]\n\tv_exp_f32_e32 v175, v175\n\tv_cvt_pk_bf16_f32 v85, v154, v155\n\tv_add_f32_e32 %0, %0, v172" : "+v"(c.rs[0][1]) :  : "memory");
  asm volatile("v_mfma_f32_32x32x16_bf16 v[224:239], a[220:223], a[140:143], v[224:239]\n\tds_read_b128 a[244:247], %1 offset:24576\n\tv_exp_f32_e32 v176, v176\n\tv_cvt_pk_bf16_f32 v86, v156, v157\n\tv_add_f32_e32 %0, %0, v173" : "+v"(c.rs[0][0]) : "v"(c.kb_[6]) : "memory");
  asm volatile("v_mfma_f32_32x32x16_bf16 v[240:255], a[220:223], a[172:175], v[240:255]\n\tv_exp_f32_e32 v177, v177\n\tv_cvt_pk_bf16_f32 v87, v158, v159\n\tv_add_f32_e32 %0, %0, v174" : "+v"(c.rs[0][1]) :  : "memory");
  asm volatile("s_waitcnt lgkmcnt(4)\n\tv_mfma_f32_32x32x16_bf16 v[192:207], a[224:227], a[144:147], v[192:207]\n\tds_read_b128 a[248:251], %2 offset:16384\n\tv_exp_f32_e32 v178, v178\n\tv_cvt_pk_bf16_f32 v72, v160, v161\n\tv_add_f32_e32 %0, %0, v175\n\tv_add_f32_e32 %1, %1, v144" : "+v"(c.rs[0][0]), "+v"(c.rs[1][2]) : "v"(c.kb_[7]) : "memory", ACL64);
  asm volatile("v_mfma_f32_32x32x16_bf16 v[208:223], a[224:227], a[176:179], v[208:223]\n\tv_exp_f32_e32 v179, v179\n\tv_cvt_pk_bf16_f32 v73, v162, v163\n\tv_add_f32_e32 %0, %0, v176\n\tv_add_f32_e32 %1, %1, v145" : "+v"(c.rs[1][1]), "+v"(c.rs[1][3]) :  : "memory");
  asm volatile("v_mfma_f32_32x32x16_bf16 v[224:239], a[228:231], a[144:147], v[224:239]\n\tds_read_b128 a[252:255], %2 offset:24576\n\tv_exp_f32_e32 v180, v180\n\tv_cvt_pk_bf16_f32 v74, v164, v165\n\tv_add_f32_e32 %0, %0, v177\n\tv_add_f32_e32 %1, %1, v146" : "+v"(c.rs[1][0]), "+v"(c.rs[1][2]) : "v"(c.kb_[7]) : "memory");
  asm volatile("v_mfma_f32_32x32x16_bf16 v[240:255], a[228:231], a[176:179], v[240:255]\n\tv_exp_f32_e32 v181, v181\n\tv_cvt_pk_bf16_f32 v75, v166, v167\n\tv_add_f32_e32 %0, %0, v178\n\tv_add_f32_e32 %1, %1, v147" : "+v"(c.rs[1][1]), "+v"(c.rs[1][3]) :  : "memory");
  asm volatile("s_waitcnt lgkmcnt(4)\n\tv_mfma_f32_32x32x16_bf16 v[192:207], a[232:235], a[148:151], v[192:207]\n\tv_exp_f32_e32 v182, v182\n\tv_cvt_pk_bf16_f32 v76, v168, v169\n\tv_add_f32_e32 %0, %0, v179\n\tv_add_f32_e32 %1, %1, v148" : "+v"(c.rs[1][0]), "+v"(c.rs[1][2]) :  : "memory");
  asm volatile("v_mfma_f32_32x32x16_bf16 v[208:223], a[232:235], a[180:183], v[208:223]\n\tv_exp_f32_e32 v183, v183\n\tv_cvt_pk_bf16_f32 v77, v170, v171\n\tv_add_f32_e32 %0, %0, v180\n\tv_add_f32_e32 %1, %1, v149" : "+v"(c.rs[1][1]), "+v"(c.rs[1][3]) :  : "memory");
  asm volatile("v_mfma_f32_32x32x16_bf16 v[224:239], a[236:239], a[148:151], v[224:239]\n\tv_exp_f32_e32 v184, v184\n\tv_cvt_pk_bf16_f32 v78, v172, v173\n\tv_add_f32_e32 %0, %0, v181\n\tv_add_f32_e32 %1, %1, v150" : "+v"(c.rs[1][0]), "+v"(c.rs[1][2]) :  : "memory");
  asm volatile("v_mfma_f32_32x32x16_bf16 v[240:255], a[236:239], a[180:183], v[240:255]\n\tv_exp_f32_e32 v185, v185\n\tv_cvt_pk_bf16_f32 v79, v174, v175\n\tv_add_f32_e32 %0, %0, v182\n\tv_add_f32_e32 %1, %1, v151" : "+v"(c.rs[1][1]), "+v"(c.rs[1][3]) :  : "memory");
  asm volatile("s_waitcnt vmcnt(0) lgkmcnt(0)\n\ts_barrier\n\ts_waitcnt lgkmcnt(2)\n\tv_mfma_f32_32x32x16_bf16 v[192:207], a[240:243], a[152:155], v[192:207]\n\tds_read_b128 a[192:195], %2 offset:32768\n\tv_exp_f32_e32 v186, v186\n\tv_cvt_pk_bf16_f32 v88, v176, v177\n\tv_add_f32_e32 %0, %0, v183\n\tv_add_f32_e32 %1, %1, v152" : "+v"(c.rs[1][0]), "+v"(c.rs[1][2]) : "v"(c.vb_[0]) : "memory", ACL64);
  asm volatile("v_mfma_f32_32x32x16_bf16 v[208:223], a[240:243], a[184:187], v[208:223]\n\tds_read_b128 a[196:199], %2 offset:36864\n\tv_exp_f32_e32 v187, v187\n\tv_cvt_pk_bf16_f32 v89, v178, v179\n\tv_add_f32_e32 %0, %0, v184\n\tv_add_f32_e32 %1, %1, v153\n\ts_add_u32 m0, %3, 49152\n\ts_nop 0\n\tbuffer_load_dwordx4 %4, %5, %6 offen offset:0 sc1 lds" : "+v"(c.rs[1][1]), "+v"(c.rs[1][3]) : "v"(c.vb_[0]), "s"(c.m0w), "v"(c.vlin), "s"(c.srdV), "s"(c.soVA) : "memory", "m0", "scc");
  asm volatile("v_mfma_f32_32x32x16_bf16 v[224:239], a[244:247], a[152:155], v[224:239]\n\tds_read_b128 a[200:203], %2 offset:40960\n\tv_exp_f32_e32 v188, v188\n\tv_cvt_pk_bf16_f32 v90, v180, v181\n\tv_add_f32_e32 %0, %0, v185\n\tv_add_f32_e32 %1, %1, v154" : "+v"(c.rs[1][0]), "+v"(c.rs[1][2]) : "v"(c.vb_[0]) : "memory");
  asm volatile("v_mfma_f32_32x32x16_bf16 v[240:255], a[244:247], a[184:187], v[240:255]\n\tds_read_b128 a[204:207], %2 offset:45056\n\tv_exp_f32_e32 v189, v189\n\tv_cvt_pk_bf16_f32 v91, v182, v183\n\tv_add_f32_e32 %0, %0, v186\n\tv_add_f32_e32 %1, %1, v155" : "+v"(c.rs[1][1]), "+v"(c.rs[1][3]) : "v"(c.vb_[0]) : "memory");
  asm volatile("s_waitcnt lgkmcnt(4)\n\tv_mfma_f32_32x32x16_bf16 v[192:207], a[248:251], a[156:159], v[192:207]\n\tds_read_b128 a[208:211], %2 offset:32768\n\tv_exp_f32_e32 v190, v190\n\tv_cvt_pk_bf16_f32 v92, v184, v185\n\tv_add_f32_e32 %0, %0, v187\n\tv_add_f32_e32 %1, %1, v156\n\tbuffer_load_dwordx4 %3, %4, %5 offen offset:1024 sc1 lds" : "+v"(c.rs[1][0]), "+v"(c.rs[1][2]) : "v"(c.vb_[1]), "v"(c.vlin), "s"(c.srdV), "s"(c.soVA) : "memory");
  asm volatile("v_mfma_f32_32x32x16_bf16 v[208:223], a[248:251], a[188:191], v[208:223]\n\tv_exp_f32_e32 v191, v191\n\tv_cvt_pk_bf16_f32 v93, v186, v187\n\tv_add_f32_e32 %0, %0, v188\n\tv_add_f32_e32 %1, %1, v157" : "+v"(c.rs[1][1]), "+v"(c.rs[1][3]) :  : "memory");
  asm volatile("v_mfma_f32_32x32x16_bf16 v[224:239], a[252:255], a[156:159], v[224:239]\n\tds_read_b128 a[212:215], %2 offset:36864\n\tv_cvt_pk_bf16_f32 v94, v188, v189\n\tv_add_f32_e32 %0, %0, v189\n\tv_add_f32_e32 %1, %1, v158" : "+v"(c.rs[1][0]), "+v"(c.rs[1][2]) : "v"(c.vb_[1]) : "memory");
  asm volatile("v_mfma_f32_32x32x16_bf16 v[240:255], a[252:255], a[188:191], v[240:255]\n\tv_cvt_pk_bf16_f32 v95, v190, v191\n\tv_add_f32_e32 %0, %0, v190\n\tv_add_f32_e32 %1, %1, v191\n\tv_add_f32_e32 %2, %2, v159\n\tbuffer_load_dwordx4 %3, %4, %5 offen offset:2048 sc1 lds" : "+v"(c.rs[1][1]), "+v"(c.rs[1][0]), "+v"(c.rs[1][3]) : "v"(c.vlin), "s"(c.srdV), "s"(c.soVA) : "memory");
}
DI void gB1am1(AC& c) {
  asm volatile("s_waitcnt lgkmcnt(4)\n\tv_mfma_f32_32x32x16_bf16 a[0:15], a[192:195], v[64:67], a[0:15]\n\tds_read_b128 a[216:219], %0 offset:40960" :  : "v"(c.vb_[1]) : "memory", ACL64);
  asm volatile("v_mfma_f32_32x32x16_bf16 a[16:31], a[192:195], v[80:83], a[16:31]" :  :  : "memory");
  asm volatile("v_mfma_f32_32x32x16_bf16 a[32:47], a[196:199], v[64:67], a[32:47]\n\tds_read_b128 a[220:223], %2 offset:45056\n\tv_max3_f32 %0, v192, v193, v224\n\tv_max3_f32 %1, v194, v195, v225\n\tv_max3_f32 %0, %0, v226, v227\n\tbuffer_load_dwordx4 %3, %4, %5 offen offset:3072 sc1 lds" : "=&v"(c.pmx[0][0]), "=&v"(c.pmx[0][1]) : "v"(c.vb_[1]), "v"(c.vlin), "s"(c.srdV), "s"(c.soVA) : "memory");
  asm volatile("v_mfma_f32_32x32x16_bf16 a[48:63], a[196:199], v[80:83], a[48:63]\n\tv_max3_f32 %0, %0, v196, v197\n\tv_max3_f32 %1, %1, v198, v199\n\tv_max3_f32 %0, %0, v228, v229\n\tv_max3_f32 %1, %1, v230, v231" : "+v"(c.pmx[0][0]), "+v"(c.pmx[0][1]) :  : "memory");
  asm volatile("s_waitcnt lgkmcnt(4)\n\tv_mfma_f32_32x32x16_bf16 a[64:79], a[200:203], v[64:67], a[64:79]\n\tds_read_b128 a[224:227], %2 offset:32768\n\tv_max3_f32 %0, %0, v200, v201\n\tv_max3_f32 %1, %1, v202, v203\n\tv_max3_f32 %0, %0, v232, v233\n\tv_max3_f32 %1, %1, v234, v235" : "+v"(c.pmx[0][0]), "+v"(c.pmx[0][1]) : "v"(c.vb_[2]) : "memory");
  asm volatile("v_mfma_f32_32x32x16_bf16 a[80:95], a[200:203], v[80:83], a[80:95]\n\tv_max3_f32 %0, %0, v204, v205\n\tv_max3_f32 %1, %1, v206, v207\n\tv_max3_f32 %0, %0, v236, v237\n\tv_max3_f32 %1, %1, v238, v239\n\ts_add_u32 m0, %2, 16384\n\ts_nop 0\n\tbuffer_load_dwordx4 %3, %4, %5 offen offset:0 sc1 lds" : "+v"(c.pmx[0][0]), "+v"(c.pmx[0][1]) : "s"(c.m0w), "v"(c.vlin), "s"(c.srdK), "s"(c.soKA) : "memory", "m0", "scc");
  asm volatile("v_mfma_f32_32x32x16_bf16 a[96:111], a[204:207], v[64:67], a[96:111]\n\tds_read_b128 a[228:231], %2 offset:36864\n\tv_max3_f32 %0, v208, v209, v240\n\tv_max3_f32 %1, v210, v211, v241\n\tv_max3_f32 %0, %0, v242, v243" : "=&v"(c.pmx[1][0]), "=&v"(c.pmx[1][1]) : "v"(c.vb_[2]) : "memory");
  asm volatile("v_mfma_f32_32x32x16_bf16 a[112:127], a[204:207], v[80:83], a[112:127]\n\tv_max3_f32 %0, %0, v212, v213\n\tv_max3_f32 %1, %1, v214, v215\n\tv_max3_f32 %0, %0, v244, v245\n\tv_max3_f32 %1, %1, v246, v247" : "+v"(c.pmx[1][0]), "+v"(c.pmx[1][1]) :  : "memory");
  asm volatile("s_waitcnt lgkmcnt(4)\n\tv_mfma_f32_32x32x16_bf16 a[0:15], a[208:211], v[68:71], a[0:15]\n\tds_read_b128 a[232:235], %2 offset:40960\n\tv_max3_f32 %0, %0, v216, v217\n\tv_max3_f32 %1, %1, v218, v219\n\tv_max3_f32 %0, %0, v248, v249\n\tv_max3_f32 %1, %1, v250, v251\n\tbuffer_load_dwordx4 %3, %4, %5 offen offset:1024 sc1 lds" : "+v"(c.pmx[1][0]), "+v"(c.pmx[1][1]) : "v"(c.vb_[2]), "v"(c.vlin), "s"(c.srdK), "s"(c.soKA) : "memory", ACL64);
  asm volatile("v_mfma_f32_32x32x16_bf16 a[16:31], a[208:211], v[84:87], a[16:31]\n\tv_max3_f32 %0, %0, v220, v221\n\tv_max3_f32 %1, %1, v222, v223\n\tv_max3_f32 %0, %0, v252, v253\n\tv_max3_f32 %1, %1, v254, v255" : "+v"(c.pmx[1][0]), "+v"(c.pmx[1][1]) :  : "memory");
  asm volatile("v_mfma_f32_32x32x16_bf16 a[32:47], a[212:215], v[68:71], a[32:47]\n\tds_read_b128 a[236:239], %2 offset:45056\n\tv_max_f32_e32 %0, %0, %1\n\tv_mov_b32_e32 %1, %0\n\ts_nop 1\n\tv_permlane32_swap_b32_e32 %0, %1\n\tv_max_f32_e32 %0, %0, %1" : "+v"(c.pmx[0][0]), "+v"(c.pmx[0][1]) : "v"(c.vb_[2]) : "memory");
  asm volatile("v_mfma_f32_32x32x16_bf16 a[48:63], a[212:215], v[84:87], a[48:63]\n\tv_max_f32_e32 %0, %0, %1\n\tv_mov_b32_e32 %1, %0\n\ts_nop 1\n\tv_permlane32_swap_b32_e32 %0, %1\n\tv_max_f32_e32 %0, %0, %1\n\tbuffer_load_dwordx4 %2, %3, %4 offen offset:2048 sc1 lds" : "+v"(c.pmx[1][0]), "+v"(c.pmx[1][1]) : "v"(c.vlin), "s"(c.srdK), "s"(c.soKA) : "memory");
}
DI void gB1bm1(AC& c) {
  asm volatile("s_waitcnt lgkmcnt(4)\n\tv_mfma_f32_32x32x16_bf16 a[64:79], a[216:219], v[68:71], a[64:79]\n\tds_read_b128 a[240:243], %0 offset:32768" :  : "v"(c.vb_[3]) : "memory");
  asm volatile("v_mfma_f32_32x32x16_bf16 a[80:95], a[216:219], v[84:87], a[80:95]\n\tv_exp_f32_e32 v192, v192\n\tv_exp_f32_e32 v193, v193" :  :  : "memory");
  asm volatile("v_mfma_f32_32x32x16_bf16 a[96:111], a[220:223], v[68:71], a[96:111]\n\tds_read_b128 a[244:247], %0 offset:36864\n\tv_exp_f32_e32 v194, v194\n\tv_exp_f32_e32 v195, v195\n\tbuffer_load_dwordx4 %1, %2, %3 offen offset:3072 sc1 lds" :  : "v"(c.vb_[3]), "v"(c.vlin), "s"(c.srdK), "s"(c.soKA) : "memory");
  asm volatile("v_mfma_f32_32x32x16_bf16 a[112:127], a[220:223], v[84:87], a[112:127]\n\tv_exp_f32_e32 v196, v196\n\tv_exp_f32_e32 v197, v197" :  :  : "memory");
  asm volatile("s_waitcnt lgkmcnt(4)\n\tv_mfma_f32_32x32x16_bf16 a[0:15], a[224:227], v[72:75], a[0:15]\n\tds_read_b128 a[248:251], %1 offset:40960\n\tv_exp_f32_e32 v198, v198\n\tv_exp_f32_e32 v199, v199\n\tv_add_f32_e32 %0, %0, v192" : "+v"(c.rs[0][0]) : "v"(c.vb_[3]) : "memory", ACL64);
  asm volatile("v_mfma_f32_32x32x16_bf16 a[16:31], a[224:227], v[88:91], a[16:31]\n\tv_exp_f32_e32 v200, v200\n\tv_exp_f32_e32 v201, v201\n\tv_add_f32_e32 %0, %0, v193" : "+v"(c.rs[0][1]) :  : "memory");
  asm volatile("v_mfma_f32_32x32x16_bf16 a[32:47], a[228:231], v[72:75], a[32:47]\n\tds_read_b128 a[252:255], %1 offset:45056\n\tv_exp_f32_e32 v202, v202\n\tv_exp_f32_e32 v203, v203\n\tv_add_f32_e32 %0, %0, v194" : "+v"(c.rs[0][0]) : "v"(c.vb_[3]) : "memory");
  asm volatile("v_mfma_f32_32x32x16_bf16 a[48:63], a[228:231], v[88:91], a[48:63]\n\tv_exp_f32_e32 v204, v204\n\tv_exp_f32_e32 v205, v205\n\tv_add_f32_e32 %0, %0, v195" : "+v"(c.rs[0][1]) :  : "memory");
  asm volatile("s_waitcnt lgkmcnt(4)\n\tv_mfma_f32_32x32x16_bf16 a[64:79], a[232:235], v[72:75], a[64:79]\n\tds_read_b128 a[192:195], %1 offset:0\n\tv_exp_f32_e32 v206, v206\n\tv_exp_f32_e32 v207, v207\n\tv_add_f32_e32 %0, %0, v196" : "+v"(c.rs[0][0]) : "v"(c.kb_[0]) : "memory");
  asm volatile("v_mfma_f32_32x32x16_bf16 a[80:95], a[232:235], v[88:91], a[80:95]\n\tv_exp_f32_e32 v208, v208\n\tv_exp_f32_e32 v209, v209\n\tv_add_f32_e32 %0, %0, v197" : "+v"(c.rs[0][1]) :  : "memory");
  asm volatile("v_mfma_f32_32x32x16_bf16 a[96:111], a[236:239], v[72:75], a[96:111]\n\tds_read_b128 a[196:199], %1 offset:8192\n\tv_exp_f32_e32 v210, v210\n\tv_exp_f32_e32 v211, v211\n\tv_add_f32_e32 %0, %0, v198" : "+v"(c.rs[0][0]) : "v"(c.kb_[0]) : "memory");
  asm volatile("v_mfma_f32_32x32x16_bf16 a[112:127], a[236:239], v[88:91], a[112:127]\n\tv_exp_f32_e32 v212, v212\n\tv_exp_f32_e32 v213, v213\n\tv_add_f32_e32 %0, %0, v199" : "+v"(c.rs[0][1]) :  : "memory");
  asm volatile("s_waitcnt lgkmcnt(4)\n\tv_mfma_f32_32x32x16_bf16 a[0:15], a[240:243], v[76:79], a[0:15]\n\tds_read_b128 a[200:203], %1 offset:0\n\tv_exp_f32_e32 v214, v214\n\tv_exp_f32_e32 v215, v215\n\tv_add_f32_e32 %0, %0, v200" : "+v"(c.rs[0][0]) : "v"(c.kb_[1]) : "memory", ACL64);
  asm volatile("v_mfma_f32_32x32x16_bf16 a[16:31], a[240:243], v[92:95], a[16:31]\n\tv_exp_f32_e32 v216, v216\n\tv_exp_f32_e32 v217, v217\n\tv_add_f32_e32 %0, %0, v201" : "+v"(c.rs[0][1]) :  : "memory");
  asm volatile("v_mfma_f32_32x32x16_bf16 a[32:47], a[244:247], v[76:79], a[32:47]\n\tds_read_b128 a[204:207], %1 offset:8192\n\tv_exp_f32_e32 v218, v218\n\tv_add_f32_e32 %0, %0, v202" : "+v"(c.rs[0][0]) : "v"(c.kb_[1]) : "memory");
  asm volatile("v_mfma_f32_32x32x16_bf16 a[48:63], a[244:247], v[92:95], a[48:63]\n\tv_exp_f32_e32 v219, v219\n\tv_add_f32_e32 %0, %0, v203" : "+v"(c.rs[0][1]) :  : "memory");
  asm volatile("s_waitcnt lgkmcnt(4)\n\tv_mfma_f32_32x32x16_bf16 a[64:79], a[248:251], v[76:79], a[64:79]\n\tds_read_b128 a[208:211], %1 offset:0\n\tv_exp_f32_e32 v220, v220\n\tv_add_f32_e32 %0, %0, v204" : "+v"(c.rs[0][0]) : "v"(c.kb_[2]) : "memory");
  asm volatile("v_mfma_f32_32x32x16_bf16 a[80:95], a[248:251], v[92:95], a[80:95]\n\tv_exp_f32_e32 v221, v221\n\tv_add_f32_e32 %0, %0, v205" : "+v"(c.rs[0][1]) :  : "memory");
  asm volatile("v_mfma_f32_32x32x16_bf16 a[96:111], a[252:255], v[76:79], a[96:111]\n\tds_read_b128 a[212:215], %1 offset:8192\n\tv_exp_f32_e32 v222, v222\n\tv_add_f32_e32 %0, %0, v206" : "+v"(c.rs[0][0]) : "v"(c.kb_[2]) : "memory");
  asm volatile("v_mfma_f32_32x32x16_bf16 a[112:127], a[252:255], v[92:95], a[112:127]\n\tv_exp_f32_e32 v223, v223\n\tv_add_f32_e32 %0, %0, v207" : "+v"(c.rs[0][1]) :  : "memory");
}
DI void gA0m2(AC& c) {
  asm volatile("s_waitcnt lgkmcnt(4)\n\tv_mfma_f32_32x32x16_bf16 v[128:143], a[192:195], a[128:131], v[96:111]\n\tds_read_b128 a[216:219], %0 offset:0\n\tv_exp_f32_e32 v224, v224\n\tv_exp_f32_e32 v225, v225\n\tv_cvt_pk_bf16_f32 v64, v192, v193" :  : "v"(c.kb_[3]) : "memory", ACL64);
  asm volatile("v_mfma_f32_32x32x16_bf16 v[144:159], a[192:195], a[160:163], v[112:127]\n\tv_exp_f32_e32 v226, v226\n\tv_exp_f32_e32 v227, v227\n\tv_cvt_pk_bf16_f32 v65, v194, v195\n\tv_add_f32_e32 %0, %0, v224" : "+v"(c.rs[0][1]) :  : "memory");
  asm volatile("v_mfma_f32_32x32x16_bf16 v[160:175], a[196:199], a[128:131], v[96:111]\n\tds_read_b128 a[220:223], %1 offset:8192\n\tv_exp_f32_e32 v228, v228\n\tv_cvt_pk_bf16_f32 v66, v196, v197\n\tv_add_f32_e32 %0, %0, v225" : "+v"(c.rs[0][0]) : "v"(c.kb_[3]) : "memory");
  asm volatile("v_mfma_f32_32x32x16_bf16 v[176:191], a[196:199], a[160:163], v[112:127]\n\tv_exp_f32_e32 v229, v229\n\tv_cvt_pk_bf16_f32 v67, v198, v199\n\tv_add_f32_e32 %0, %0, v226" : "+v"(c.rs[0][1]) :  : "memory");
  asm volatile("s_waitcnt lgkmcnt(4)\n\tv_mfma_f32_32x32x16_bf16 v[128:143], a[200:203], a[132:135], v[128:143]\n\tds_read_b128 a[224:227], %1 offset:0\n\tv_exp_f32_e32 v230, v230\n\tv_cvt_pk_bf16_f32 v80, v208, v209\n\tv_add_f32_e32 %0, %0, v227" : "+v"(c.rs[0][0]) : "v"(c.kb_[4]) : "memory");
  asm volatile("v_mfma_f32_32x32x16_bf16 v[144:159], a[200:203], a[164:167], v[144:159]\n\tv_exp_f32_e32 v231, v231\n\tv_cvt_pk_bf16_f32 v81, v210, v211\n\tv_add_f32_e32 %0, %0, v228" : "+v"(c.rs[0][1]) :  : "memory");
  asm volatile("v_mfma_f32_32x32x16_bf16 v[160:175], a[204:207], a[132:135], v[160:175]\n\tds_read_b128 a[228:231], %1 offset:8192\n\tv_exp_f32_e32 v232, v232\n\tv_cvt_pk_bf16_f32 v82, v212, v213\n\tv_add_f32_e32 %0, %0, v229" : "+v"(c.rs[0][0]) : "v"(c.kb_[4]) : "memory");
  asm volatile("v_mfma_f32_32x32x16_bf16 v[176:191], a[204:207], a[164:167], v[176:191]\n\tv_exp_f32_e32 v233, v233\n\tv_cvt_pk_bf16_f32 v83, v214, v215\n\tv_add_f32_e32 %0, %0, v230" : "+v"(c.rs[0][1]) :  : "memory");
  asm volatile("s_waitcnt lgkmcnt(4)\n\tv_mfma_f32_32x32x16_bf16 v[128:143], a[208:211], a[136:139], v[128:143]\n\tds_read_b128 a[232:235], %1 offset:0\n\tv_exp_f32_e32 v234, v234\n\tv_cvt_pk_bf16_f32 v68, v200, v201\n\tv_add_f32_e32 %0, %0, v231" : "+v"(c.rs[0][0]) : "v"(c.kb_[5]) : "memory", ACL64);
  asm volatile("v_mfma_f32_32x32x16_bf16 v[144:159], a[208:211], a[168:171], v[144:159]\n\tv_exp_f32_e32 v235, v235\n\tv_cvt_pk_bf16_f32 v69, v202, v203\n\tv_add_f32_e32 %0, %0, v232" : "+v"(c.rs[0][1]) :  : "memory");
  asm volatile("v_mfma_f32_32x32x16_bf16 v[160:175], a[212:215], a[136:139], v[160:175]\n\tds_read_b128 a[236:239], %1 offset:8192\n\tv_exp_f32_e32 v236, v236\n\tv_cvt_pk_bf16_f32 v70, v204, v205\n\tv_add_f32_e32 %0, %0, v233" : "+v"(c.rs[0][0]) : "v"(c.kb_[5]) : "memory");
  asm volatile("v_mfma_f32_32x32x16_bf16 v[176:191], a[212:215], a[168:171], v[176:191]\n\tv_exp_f32_e32 v237, v237\n\tv_cvt_pk_bf16_f32 v71, v206, v207\n\tv_add_f32_e32 %0, %0, v234" : "+v"(c.rs[0][1]) :  : "memory");
  asm volatile("s_waitcnt lgkmcnt(4)\n\tv_mfma_f32_32x32x16_bf16 v[128:143], a[216:219], a[140:143], v[128:143]\n\tds_read_b128 a[240:243], %1 offset:0\n\tv_exp_f32_e32 v238, v238\n\tv_cvt_pk_bf16_f32 v84, v216, v217\n\tv_add_f32_e32 %0, %0, v235" : "+v"(c.rs[0][0]) : "v"(c.kb_[6]) : "memory");
  asm volatile("v_mfma_f32_32x32x16_bf16 v[144:159], a[216:219], a[172:175], v[144:159]\n\tv_exp_f32_e32 v239, v239\n\tv_cvt_pk_bf16_f32 v85, v218, v219\n\tv_add_f32_e32 %0, %0, v236" : "+v"(c.rs[0][1]) :  : "memory");
  asm volatile("v_mfma_f32_32x32x16_bf16 v[160:175], a[220:223], a[140:143], v[160:175]\n\tds_read_b128 a[244:247], %1 offset:8192\n\tv_exp_f32_e32 v240, v240\n\tv_cvt_pk_bf16_f32 v86, v220, v221\n\tv_add_f32_e32 %0, %0, v237" : "+v"(c.rs[0][0]) : "v"(c.kb_[6]) : "memory");
  asm volatile("v_mfma_f32_32x32x16_bf16 v[176:191], a[220:223], a[172:175], v[176:191]\n\tv_exp_f32_e32 v241, v241\n\tv_cvt_pk_bf16_f32 v87, v222, v223\n\tv_add_f32_e32 %0, %0, v238" : "+v"(c.rs[0][1]) :  : "memory");
  asm volatile("s_waitcnt lgkmcnt(4)\n\tv_mfma_f32_32x32x16_bf16 v[128:143], a[224:227], a[144:147], v[128:143]\n\tds_read_b128 a[248:251], %2 offset:0\n\tv_exp_f32_e32 v242, v242\n\tv_cvt_pk_bf16_f32 v72, v224, v225\n\tv_add_f32_e32 %0, %0, v239\n\tv_add_f32_e32 %1, %1, v208" : "+v"(c.rs[0][0]), "+v"(c.rs[1][2]) : "v"(c.kb_[7]) : "memory", ACL64);
  asm volatile("v_mfma_f32_32x32x16_bf16 v[144:159], a[224:227], a[176:179], v[144:159]\n\tv_exp_f32_e32 v243, v243\n\tv_cvt_pk_bf16_f32 v73, v226, v227\n\tv_add_f32_e32 %0, %0, v240\n\tv_add_f32_e32 %1, %1, v209" : "+v"(c.rs[1][1]), "+v"(c.rs[1][3]) :  : "memory");
  asm volatile("v_mfma_f32_32x32x16_bf16 v[160:175], a[228:231], a[144:147], v[160:175]\n\tds_read_b128 a[252:255], %2 offset:8192\n\tv_exp_f32_e32 v244, v244\n\tv_cvt_pk_bf16_f32 v74, v228, v229\n\tv_add_f32_e32 %0, %0, v241\n\tv_add_f32_e32 %1, %1, v210" : "+v"(c.rs[1][0]), "+v"(c.rs[1][2]) : "v"(c.kb_[7]) : "memory");
  asm volatile("v_mfma_f32_32x32x16_bf16 v[176:191], a[228:231], a[176:179], v[176:191]\n\tv_exp_f32_e32 v245, v245\n\tv_cvt_pk_bf16_f32 v75, v230, v231\n\tv_add_f32_e32 %0, %0, v242\n\tv_add_f32_e32 %1, %1, v211" : "+v"(c.rs[1][1]), "+v"(c.rs[1][3]) :  : "memory");
  asm volatile("s_waitcnt lgkmcnt(4)\n\tv_mfma_f32_32x32x16_bf16 v[128:143], a[232:235], a[148:151], v[128:143]\n\tv_exp_f32_e32 v246, v246\n\tv_cvt_pk_bf16_f32 v76, v232, v233\n\tv_add_f32_e32 %0, %0, v243\n\tv_add_f32_e32 %1, %1, v212" : "+v"(c.rs[1][0]), "+v"(c.rs[1][2]) :  : "memory");
  asm volatile("v_mfma_f32_32x32x16_bf16 v[144:159], a[232:235], a[180:183], v[144:159]\n\tv_exp_f32_e32 v247, v247\n\tv_cvt_pk_bf16_f32 v77, v234, v235\n\tv_add_f32_e32 %0, %0, v244\n\tv_add_f32_e32 %1, %1, v213" : "+v"(c.rs[1][1]), "+v"(c.rs[1][3]) :  : "memory");
  asm volatile("v_mfma_f32_32x32x16_bf16 v[160:175], a[236:239], a[148:151], v[160:175]\n\tv_exp_f32_e32 v248, v248\n\tv_cvt_pk_bf16_f32 v78, v236, v237\n\tv_add_f32_e32 %0, %0, v245\n\tv_add_f32_e32 %1, %1, v214" : "+v"(c.rs[1][0]), "+v"(c.rs[1][2]) :  : "memory");
  asm volatile("v_mfma_f32_32x32x16_bf16 v[176:191], a[236:239], a[180:183], v[176:191]\n\tv_exp_f32_e32 v249, v249\n\tv_cvt_pk_bf16_f32 v79, v238, v239\n\tv_add_f32_e32 %0, %0, v246\n\tv_add_f32_e32 %1, %1, v215" : "+v"(c.rs[1][1]), "+v"(c.rs[1][3]) :  : "memory");
  asm volatile("s_waitcnt vmcnt(0) lgkmcnt(0)\n\ts_barrier\n\ts_waitcnt lgkmcnt(2)\n\tv_mfma_f32_32x32x16_bf16 v[128:143], a[240:243], a[152:155], v[128:143]\n\tds_read_b128 a[192:195], %2 offset:49152\n\tv_exp_f32_e32 v250, v250\n\tv_cvt_pk_bf16_f32 v88, v240, v241\n\tv_add_f32_e32 %0, %0, v247\n\tv_add_f32_e32 %1, %1, v216" : "+v"(c.rs[1][0]), "+v"(c.rs[1][2]) : "v"(c.vb_[0]) : "memory", ACL64);
  asm volatile("v_mfma_f32_32x32x16_bf16 v[144:159], a[240:243], a[184:187], v[144:159]\n\tds_read_b128 a[196:199], %2 offset:53248\n\tv_exp_f32_e32 v251, v251\n\tv_cvt_pk_bf16_f32 v89, v242, v243\n\tv_add_f32_e32 %0, %0, v248\n\tv_add_f32_e32 %1, %1, v217\n\ts_add_u32 m0, %3, 32768\n\ts_nop 0\n\tbuffer_load_dwordx4 %4, %5, %6 offen offset:0 sc1 lds" : "+v"(c.rs[1][1]), "+v"(c.rs[1][3]) : "v"(c.vb_[0]), "s"(c.m0w), "v"(c.vlin), "s"(c.srdV), "s"(c.soVA) : "memory", "m0", "scc");
  asm volatile("v_mfma_f32_32x32x16_bf16 v[160:175], a[244:247], a[152:155], v[160:175]\n\tds_read_b128 a[200:203], %2 offset:57344\n\tv_exp_f32_e32 v252, v252\n\tv_cvt_pk_bf16_f32 v90, v244, v245\n\tv_add_f32_e32 %0, %0, v249\n\tv_add_f32_e32 %1, %1, v218" : "+v"(c.rs[1][0]), "+v"(c.rs[1][2]) : "v"(c.vb_[0]) : "memory");
  asm volatile("v_mfma_f32_32x32x16_bf16 v[176:191], a[244:247], a[184:187], v[176:191]\n\tds_read_b128 a[204:207], %2 offset:61440\n\tv_exp_f32_e32 v253, v253\n\tv_cvt_pk_bf16_f32 v91, v246, v247\n\tv_add_f32_e32 %0, %0, v250\n\tv_add_f32_e32 %1, %1, v219\n\tbuffer_load_dwordx4 %3, %4, %5 offen offset:1024 sc1 lds" : "+v"(c.rs[1][1]), "+v"(c.rs[1][3]) : "v"(c.vb_[0]), "v"(c.vlin), "s"(c.srdV), "s"(c.soVA) : "memory");
  asm volatile("s_waitcnt lgkmcnt(4)\n\tv_mfma_f32_32x32x16_bf16 v[128:143], a[248:251], a[156:159], v[128:143]\n\tds_read_b128 a[208:211], %2 offset:49152\n\tv_exp_f32_e32 v254, v254\n\tv_cvt_pk_bf16_f32 v92, v248, v249\n\tv_add_f32_e32 %0, %0, v251\n\tv_add_f32_e32 %1, %1, v220" : "+v"(c.rs[1][0]), "+v"(c.rs[1][2]) : "v"(c.vb_[1]) : "memory");
  asm volatile("v_mfma_f32_32x32x16_bf16 v[144:159], a[248:251], a[188:191], v[144:159]\n\tv_exp_f32_e32 v255, v255\n\tv_cvt_pk_bf16_f32 v93, v250, v251\n\tv_add_f32_e32 %0, %0, v252\n\tv_add_f32_e32 %1, %1, v221\n\tbuffer_load_dwordx4 %2, %3, %4 offen offset:2048 sc1 lds" : "+v"(c.rs[1][1]), "+v"(c.rs[1][3]) : "v"(c.vlin), "s"(c.srdV), "s"(c.soVA) : "memory");
  asm volatile("v_mfma_f32_32x32x16_bf16 v[160:175], a[252:255], a[156:159], v[160:175]\n\tds_read_b128 a[212:215], %2 offset:53248\n\tv_cvt_pk_bf16_f32 v94, v252, v253\n\tv_add_f32_e32 %0, %0, v253\n\tv_add_f32_e32 %1, %1, v222" : "+v"(c.rs[1][0]), "+v"(c.rs[1][2]) : "v"(c.vb_[1]) : "memory");
  asm volatile("v_mfma_f32_32x32x16_bf16 v[176:191], a[252:255], a[188:191], v[176:191]\n\tv_cvt_pk_bf16_f32 v95, v254, v255\n\tv_add_f32_e32 %0, %0, v254\n\tv_add_f32_e32 %1, %1, v255\n\tv_add_f32_e32 %2, %2, v223\n\tbuffer_load_dwordx4 %3, %4, %5 offen offset:3072 sc1 lds" : "+v"(c.rs[1][1]), "+v"(c.rs[1][0]), "+v"(c.rs[1][3]) : "v"(c.vlin), "s"(c.srdV), "s"(c.soVA) : "memory");
}
DI void gB0am2(AC& c) {
  asm volatile("s_waitcnt lgkmcnt(4)\n\tv_mfma_f32_32x32x16_bf16 a[0:15], a[192:195], v[64:67], a[0:15]\n\tds_read_b128 a[216:219], %0 offset:57344" :  : "v"(c.vb_[1]) : "memory", ACL64);
  asm volatile("v_mfma_f32_32x32x16_bf16 a[16:31], a[192:195], v[80:83], a[16:31]\n\ts_add_u32 m0, %0, 98304\n\ts_nop 0\n\tbuffer_load_dwordx4 %1, %2, %3 offen offset:0 sc1 lds" :  : "s"(c.m0w), "v"(c.vlin), "s"(c.srdV), "s"(c.soVB) : "memory", "m0", "scc");
  asm volatile("v_mfma_f32_32x32x16_bf16 a[32:47], a[196:199], v[64:67], a[32:47]\n\tds_read_b128 a[220:223], %2 offset:61440\n\tv_max3_f32 %0, v128, v129, v160\n\tv_max3_f32 %1, v130, v131, v161\n\tv_max3_f32 %0, %0, v162, v163" : "=&v"(c.pmx[0][0]), "=&v"(c.pmx[0][1]) : "v"(c.vb_[1]) : "memory");
  asm volatile("v_mfma_f32_32x32x16_bf16 a[48:63], a[196:199], v[80:83], a[48:63]\n\tv_max3_f32 %0, %0, v132, v133\n\tv_max3_f32 %1, %1, v134, v135\n\tv_max3_f32 %0, %0, v164, v165\n\tv_max3_f32 %1, %1, v166, v167\n\tbuffer_load_dwordx4 %2, %3, %4 offen offset:1024 sc1 lds" : "+v"(c.pmx[0][0]), "+v"(c.pmx[0][1]) : "v"(c.vlin), "s"(c.srdV), "s"(c.soVB) : "memory");
  asm volatile("s_waitcnt lgkmcnt(4)\n\tv_mfma_f32_32x32x16_bf16 a[64:79], a[200:203], v[64:67], a[64:79]\n\tds_read_b128 a[224:227], %2 offset:49152\n\tv_max3_f32 %0, %0, v136, v137\n\tv_max3_f32 %1, %1, v138, v139\n\tv_max3_f32 %0, %0, v168, v169\n\tv_max3_f32 %1, %1, v170, v171" : "+v"(c.pmx[0][0]), "+v"(c.pmx[0][1]) : "v"(c.vb_[2]) : "memory");
  asm volatile("v_mfma_f32_32x32x16_bf16 a[80:95], a[200:203], v[80:83], a[80:95]\n\tv_max3_f32 %0, %0, v140, v141\n\tv_max3_f32 %1, %1, v142, v143\n\tv_max3_f32 %0, %0, v172, v173\n\tv_max3_f32 %1, %1, v174, v175\n\tbuffer_load_dwordx4 %2, %3, %4 offen offset:2048 sc1 lds" : "+v"(c.pmx[0][0]), "+v"(c.pmx[0][1]) : "v"(c.vlin), "s"(c.srdV), "s"(c.soVB) : "memory");
  asm volatile("v_mfma_f32_32x32x16_bf16 a[96:111], a[204:207], v[64:67], a[96:111]\n\tds_read_b128 a[228:231], %2 offset:53248\n\tv_max3_f32 %0, v144, v145, v176\n\tv_max3_f32 %1, v146, v147, v177\n\tv_max3_f32 %0, %0, v178, v179" : "=&v"(c.pmx[1][0]), "=&v"(c.pmx[1][1]) : "v"(c.vb_[2]) : "memory");
  asm volatile("v_mfma_f32_32x32x16_bf16 a[112:127], a[204:207], v[80:83], a[112:127]\n\tv_max3_f32 %0, %0, v148, v149\n\tv_max3_f32 %1, %1, v150, v151\n\tv_max3_f32 %0, %0, v180, v181\n\tv_max3_f32 %1, %1, v182, v183\n\tbuffer_load_dwordx4 %2, %3, %4 offen offset:3072 sc1 lds" : "+v"(c.pmx[1][0]), "+v"(c.pmx[1][1]) : "v"(c.vlin), "s"(c.srdV), "s"(c.soVB) : "memory");
  asm volatile("s_waitcnt lgkmcnt(4)\n\tv_mfma_f32_32x32x16_bf16 a[0:15], a[208:211], v[68:71], a[0:15]\n\tds_read_b128 a[232:235], %2 offset:57344\n\tv_max3_f32 %0, %0, v152, v153\n\tv_max3_f32 %1, %1, v154, v155\n\tv_max3_f32 %0, %0, v184, v185\n\tv_max3_f32 %1, %1, v186, v187" : "+v"(c.pmx[1][0]), "+v"(c.pmx[1][1]) : "v"(c.vb_[2]) : "memory", ACL64);
  asm volatile("v_mfma_f32_32x32x16_bf16 a[16:31], a[208:211], v[84:87], a[16:31]\n\tv_max3_f32 %0, %0, v156, v157\n\tv_max3_f32 %1, %1, v158, v159\n\tv_max3_f32 %0, %0, v188, v189\n\tv_max3_f32 %1, %1, v190, v191\n\ts_add_u32 m0, %2, 0\n\ts_nop 0\n\tbuffer_load_dwordx4 %3, %4, %5 offen offset:0 sc1 lds" : "+v"(c.pmx[1][0]), "+v"(c.pmx[1][1]) : "s"(c.m0w), "v"(c.vlin), "s"(c.srdK), "s"(c.soKA) : "memory", "m0", "scc");
  asm volatile("v_mfma_f32_32x32x16_bf16 a[32:47], a[212:215], v[68:71], a[32:47]\n\tds_read_b128 a[236:239], %2 offset:61440\n\tv_max_f32_e32 %0, %0, %1\n\tv_mov_b32_e32 %1, %0\n\ts_nop 1\n\tv_permlane32_swap_b32_e32 %0, %1\n\tv_max_f32_e32 %0, %0, %1" : "+v"(c.pmx[0][0]), "+v"(c.pmx[0][1]) : "v"(c.vb_[2]) : "memory");
  asm volatile("v_mfma_f32_32x32x16_bf16 a[48:63], a[212:215], v[84:87], a[48:63]\n\tv_max_f32_e32 %0, %0, %1\n\tv_mov_b32_e32 %1, %0\n\ts_nop 1\n\tv_permlane32_swap_b32_e32 %0, %1\n\tv_max_f32_e32 %0, %0, %1\n\tbuffer_load_dwordx4 %2, %3, %4 offen offset:1024 sc1 lds" : "+v"(c.pmx[1][0]), "+v"(c.pmx[1][1]) : "v"(c.vlin), "s"(c.srdK), "s"(c.soKA) : "memory");
}
DI void gB0bm2(AC& c) {
  asm volatile("s_waitcnt lgkmcnt(4)\n\tv_mfma_f32_32x32x16_bf16 a[64:79], a[216:219], v[68:71], a[64:79]\n\tds_read_b128 a[240:243], %0 offset:49152" :  : "v"(c.vb_[3]) : "memory");
  asm volatile("v_mfma_f32_32x32x16_bf16 a[80:95], a[216:219], v[84:87], a[80:95]\n\tv_exp_f32_e32 v128, v128\n\tv_exp_f32_e32 v129, v129\n\tbuffer_load_dwordx4 %0, %1, %2 offen offset:2048 sc1 lds" :  : "v"(c.vlin), "s"(c.srdK), "s"(c.soKA) : "memory");
  asm volatile("v_mfma_f32_32x32x16_bf16 a[96:111], a[220:223], v[68:71], a[96:111]\n\tds_read_b128 a[244:247], %0 offset:53248\n\tv_exp_f32_e32 v130, v130\n\tv_exp_f32_e32 v131, v131" :  : "v"(c.vb_[3]) : "memory");
  asm volatile("v_mfma_f32_32x32x16_bf16 a[112:127], a[220:223], v[84:87], a[112:127]\n\tv_exp_f32_e32 v132, v132\n\tv_exp_f32_e32 v133, v133\n\tbuffer_load_dwordx4 %0, %1, %2 offen offset:3072 sc1 lds" :  : "v"(c.vlin), "s"(c.srdK), "s"(c.soKA) : "memory");
  asm volatile("s_waitcnt lgkmcnt(4)\n\tv_mfma_f32_32x32x16_bf16 a[0:15], a[224:227], v[72:75], a[0:15]\n\tds_read_b128 a[248:251], %1 offset:57344\n\tv_exp_f32_e32 v134, v134\n\tv_exp_f32_e32 v135, v135\n\tv_add_f32_e32 %0, %0, v128" : "+v"(c.rs[0][0]) : "v"(c.vb_[3]) : "memory", ACL64);
  asm volatile("v_mfma_f32_32x32x16_bf16 a[16:31], a[224:227], v[88:91], a[16:31]\n\tv_exp_f32_e32 v136, v136\n\tv_exp_f32_e32 v137, v137\n\tv_add_f32_e32 %0, %0, v129\n\ts_add_u32 m0, %1, 65536\n\ts_nop 0\n\tbuffer_load_dwordx4 %2, %3, %4 offen offset:0 sc1 lds" : "+v"(c.rs[0][1]) : "s"(c.m0w), "v"(c.vlin), "s"(c.srdK), "s"(c.soKB) : "memory", "m0", "scc");
  asm volatile("v_mfma_f32_32x32x16_bf16 a[32:47], a[228:231], v[72:75], a[32:47]\n\tds_read_b128 a[252:255], %1 offset:61440\n\tv_exp_f32_e32 v138, v138\n\tv_exp_f32_e32 v139, v139\n\tv_add_f32_e32 %0, %0, v130" : "+v"(c.rs[0][0]) : "v"(c.vb_[3]) : "memory");
  asm volatile("v_mfma_f32_32x32x16_bf16 a[48:63], a[228:231], v[88:91], a[48:63]\n\tv_exp_f32_e32 v140, v140\n\tv_exp_f32_e32 v141, v141\n\tv_add_f32_e32 %0, %0, v131\n\tbuffer_load_dwordx4 %1, %2, %3 offen offset:1024 sc1 lds" : "+v"(c.rs[0][1]) : "v"(c.vlin), "s"(c.srdK), "s"(c.soKB) : "memory");
  asm volatile("s_waitcnt lgkmcnt(4)\n\tv_mfma_f32_32x32x16_bf16 a[64:79], a[232:235], v[72:75], a[64:79]\n\tds_read_b128 a[192:195], %1 offset:16384\n\tv_exp_f32_e32 v142, v142\n\tv_exp_f32_e32 v143, v143\n\tv_add_f32_e32 %0, %0, v132" : "+v"(c.rs[0][0]) : "v"(c.kb_[0]) : "memory");
  asm volatile("v_mfma_f32_32x32x16_bf16 a[80:95], a[232:235], v[88:91], a[80:95]\n\tv_exp_f32_e32 v144, v144\n\tv_exp_f32_e32 v145, v145\n\tv_add_f32_e32 %0, %0, v133\n\tbuffer_load_dwordx4 %1, %2, %3 offen offset:2048 sc1 lds" : "+v"(c.rs[0][1]) : "v"(c.vlin), "s"(c.srdK), "s"(c.soKB) : "memory");
  asm volatile("v_mfma_f32_32x32x16_bf16 a[96:111], a[236:239], v[72:75], a[96:111]\n\tds_read_b128 a[196:199], %1 offset:24576\n\tv_exp_f32_e32 v146, v146\n\tv_exp_f32_e32 v147, v147\n\tv_add_f32_e32 %0, %0, v134" : "+v"(c.rs[0][0]) : "v"(c.kb_[0]) : "memory");
  asm volatile("v_mfma_f32_32x32x16_bf16 a[112:127], a[236:239], v[88:91], a[112:127]\n\tv_exp_f32_e32 v148, v148\n\tv_exp_f32_e32 v149, v149\n\tv_add_f32_e32 %0, %0, v135\n\tbuffer_load_dwordx4 %1, %2, %3 offen offset:3072 sc1 lds" : "+v"(c.rs[0][1]) : "v"(c.vlin), "s"(c.srdK), "s"(c.soKB) : "memory");
  asm volatile("s_waitcnt lgkmcnt(4)\n\tv_mfma_f32_32x32x16_bf16 a[0:15], a[240:243], v[76:79], a[0:15]\n\tds_read_b128 a[200:203], %1 offset:16384\n\tv_exp_f32_e32 v150, v150\n\tv_exp_f32_e32 v151, v151\n\tv_add_f32_e32 %0, %0, v136" : "+v"(c.rs[0][0]) : "v"(c.kb_[1]) : "memory", ACL64);
  asm volatile("v_mfma_f32_32x32x16_bf16 a[16:31], a[240:243], v[92:95], a[16:31]\n\tv_exp_f32_e32 v152, v152\n\tv_exp_f32_e32 v153, v153\n\tv_add_f32_e32 %0, %0, v137" : "+v"(c.rs[0][1]) :  : "memory");
  asm volatile("v_mfma_f32_32x32x16_bf16 a[32:47], a[244:247], v[76:79], a[32:47]\n\tds_read_b128 a[204:207], %1 offset:24576\n\tv_exp_f32_e32 v154, v154\n\tv_add_f32_e32 %0, %0, v138" : "+v"(c.rs[0][0]) : "v"(c.kb_[1]) : "memory");
  asm volatile("v_mfma_f32_32x32x16_bf16 a[48:63], a[244:247], v[92:95], a[48:63]\n\tv_exp_f32_e32 v155, v155\n\tv_add_f32_e32 %0, %0, v139" : "+v"(c.rs[0][1]) :  : "memory");
  asm volatile("s_waitcnt lgkmcnt(4)\n\tv_mfma_f32_32x32x16_bf16 a[64:79], a[248:251], v[76:79], a[64:79]\n\tds_read_b128 a[208:211], %1 offset:16384\n\tv_exp_f32_e32 v156, v156\n\tv_add_f32_e32 %0, %0, v140" : "+v"(c.rs[0][0]) : "v"(c.kb_[2]) : "memory");
  asm volatile("v_mfma_f32_32x32x16_bf16 a[80:95], a[248:251], v[92:95], a[80:95]\n\tv_exp_f32_e32 v157, v157\n\tv_add_f32_e32 %0, %0, v141" : "+v"(c.rs[0][1]) :  : "memory");
  asm volatile("v_mfma_f32_32x32x16_bf16 a[96:111], a[252:255], v[76:79], a[96:111]\n\tds_read_b128 a[212:215], %1 offset:24576\n\tv_exp_f32_e32 v158, v158\n\tv_add_f32_e32 %0, %0, v142" : "+v"(c.rs[0][0]) : "v"(c.kb_[2]) : "memory");
  asm volatile("v_mfma_f32_32x32x16_bf16 a[112:127], a[252:255], v[92:95], a[112:127]\n\tv_exp_f32_e32 v159, v159\n\tv_add_f32_e32 %0, %0, v143" : "+v"(c.rs[0][1]) :  : "memory");
}
DI void gA1m2(AC& c) {
  asm volatile("s_waitcnt lgkmcnt(4)\n\tv_mfma_f32_32x32x16_bf16 v[192:207], a[192:195], a[128:131], v[96:111]\n\tds_read_b128 a[216:219], %0 offset:16384\n\tv_exp_f32_e32 v160, v160\n\tv_exp_f32_e32 v161, v161\n\tv_cvt_pk_bf16_f32 v64, v128, v129" :  : "v"(c.kb_[3]) : "memory", ACL64);
  asm volatile("v_mfma_f32_32x32x16_bf16 v[208:223], a[192:195], a[160:163], v[112:127]\n\tv_exp_f32_e32 v162, v162\n\tv_exp_f32_e32 v163, v163\n\tv_cvt_pk_bf16_f32 v65, v130, v131\n\tv_add_f32_e32 %0, %0, v160" : "+v"(c.rs[0][1]) :  : "memory");
  asm volatile("v_mfma_f32_32x32x16_bf16 v[224:239], a[196:199], a[128:131], v[96:111]\n\tds_read_b128 a[220:223], %1 offset:24576\n\tv_exp_f32_e32 v164, v164\n\tv_cvt_pk_bf16_f32 v66, v132, v133\n\tv_add_f32_e32 %0, %0, v161" : "+v"(c.rs[0][0]) : "v"(c.kb_[3]) : "memory");
  asm volatile("v_mfma_f32_32x32x16_bf16 v[240:255], a[196:199], a[160:163], v[112:127]\n\tv_exp_f32_e32 v165, v165\n\tv_cvt_pk_bf16_f32 v67, v134, v135\n\tv_add_f32_e32 %0, %0, v162" : "+v"(c.rs[0][1]) :  : "memory");
  asm volatile("s_waitcnt lgkmcnt(4)\n\tv_mfma_f32_32x32x16_bf16 v[192:207], a[200:203], a[132:135], v[192:207]\n\tds_read_b128 a[224:227], %1 offset:16384\n\tv_exp_f32_e32 v166, v166\n\tv_cvt_pk_bf16_f32 v80, v144, v145\n\tv_add_f32_e32 %0, %0, v163" : "+v"(c.rs[0][0]) : "v"(c.kb_[4]) : "memory");
  asm volatile("v_mfma_f32_32x32x16_bf16 v[208:223], a[200:203], a[164:167], v[208:223]\n\tv_exp_f32_e32 v167, v167\n\tv_cvt_pk_bf16_f32 v81, v146, v147\n\tv_add_f32_e32 %0, %0, v164" : "+v"(c.rs[0][1]) :  : "memory");
  asm volatile("v_mfma_f32_32x32x16_bf16 v[224:239], a[204:207], a[132:135], v[224:239]\n\tds_read_b128 a[228:231], %1 offset:24576\n\tv_exp_f32_e32 v168, v168\n\tv_cvt_pk_bf16_f32 v82, v148, v149\n\tv_add_f32_e32 %0, %0, v165" : "+v"(c.rs[0][0]) : "v"(c.kb_[4]) : "memory");
  asm volatile("v_mfma_f32_32x32x16_bf16 v[240:255], a[204:207], a[164:167], v[240:255]\n\tv_exp_f32_e32 v169, v169\n\tv_cvt_pk_bf16_f32 v83, v150, v151\n\tv_add_f32_e32 %0, %0, v166" : "+v"(c.rs[0][1]) :  : "memory");
  asm volatile("s_waitcnt lgkmcnt(4)\n\tv_mfma_f32_32x32x16_bf16 v[192:207], a[208:211], a[136:139], v[192:207]\n\tds_read_b128 a[232:235], %1 offset:16384\n\tv_exp_f32_e32 v170, v170\n\tv_cvt_pk_bf16_f32 v68, v136, v137\n\tv_add_f32_e32 %0, %0, v167" : "+v"(c.rs[0][0]) : "v"(c.kb_[5]) : "memory", ACL64);
  asm volatile("v_mfma_f32_32x32x16_bf16 v[208:223], a[208:211], a[168:171], v[208:223]\n\tv_exp_f32_e32 v171, v171\n\tv_cvt_pk_bf16_f32 v69, v138, v139\n\tv_add_f32_e32 %0, %0, v168" : "+v"(c.rs[0][1]) :  : "memory");
  asm volatile("v_mfma_f32_32x32x16_bf16 v[224:239], a[212:215], a[136:139], v[224:239]\n\tds_read_b128 a[236:239], %1 offset:24576\n\tv_exp_f32_e32 v172, v172\n\tv_cvt_pk_bf16_f32 v70, v140, v141\n\tv_add_f32_e32 %0, %0, v169" : "+v"(c.rs[0][0]) : "v"(c.kb_[5]) : "memory");
  asm volatile("v_mfma_f32_32x32x16_bf16 v[240:255], a[212:215], a[168:171], v[240:255]\n\tv_exp_f32_e32 v173, v173\n\tv_cvt_pk_bf16_f32 v71, v142, v143\n\tv_add_f32_e32 %0, %0, v170" : "+v"(c.rs[0][1]) :  : "memory");
  asm volatile("s_waitcnt lgkmcnt(4)\n\tv_mfma_f32_32x32x16_bf16 v[192:207], a[216:219], a[140:143], v[192:207]\n\tds_read_b128 a[240:243], %1 offset:16384\n\tv_exp_f32_e32 v174, v174\n\tv_cvt_pk_bf16_f32 v84, v152, v153\n\tv_add_f32_e32 %0, %0, v171" : "+v"(c.rs[0][0]) : "v"(c.kb_[6]) : "memory");
  asm volatile("v_mfma_f32_32x32x16_bf16 v[208:223], a[216:219], a[172:175], v[208:223]\n\tv_exp_f32_e32 v175, v175\n\tv_cvt_pk_bf16_f32 v85, v154, v155\n\tv_add_f32_e32 %0, %0, v172" : "+v"(c.rs[0][1]) :  : "memory");
  asm volatile("v_mfma_f32_32x32x16_bf16 v[224:239], a[220:223], a[140:143], v[224:239]\n\tds_read_b128 a[244:247], %1 offset:24576\n\tv_exp_f32_e32 v176, v176\n\tv_cvt_pk_bf16_f32 v86, v156, v157\n\tv_add_f32_e32 %0, %0, v173" : "+v"(c.rs[0][0]) : "v"(c.kb_[6]) : "memory");
  asm volatile("v_mfma_f32_32x32x16_bf16 v[240:255], a[220:223], a[172:175], v[240:255]\n\tv_exp_f32_e32 v177, v177\n\tv_cvt_pk_bf16_f32 v87, v158, v159\n\tv_add_f32_e32 %0, %0, v174" : "+v"(c.rs[0][1]) :  : "memory");
  asm volatile("s_waitcnt lgkmcnt(4)\n\tv_mfma_f32_32x32x16_bf16 v[192:207], a[224:227], a[144:147], v[192:207]\n\tds_read_b128 a[248:251], %2 offset:16384\n\tv_exp_f32_e32 v178, v178\n\tv_cvt_pk_bf16_f32 v72, v160, v161\n\tv_add_f32_e32 %0, %0, v175\n\tv_add_f32_e32 %1, %1, v144" : "+v"(c.rs[0][0]), "+v"(c.rs[1][2]) : "v"(c.kb_[7]) : "memory", ACL64);
  asm volatile("v_mfma_f32_32x32x16_bf16 v[208:223], a[224:227], a[176:179], v[208:223]\n\tv_exp_f32_e32 v179, v179\n\tv_cvt_pk_bf16_f32 v73, v162, v163\n\tv_add_f32_e32 %0, %0, v176\n\tv_add_f32_e32 %1, %1, v145" : "+v"(c.rs[1][1]), "+v"(c.rs[1][3]) :  : "memory");
  asm volatile("v_mfma_f32_32x32x16_bf16 v[224:239], a[228:231], a[144:147], v[224:239]\n\tds_read_b128 a[252:255], %2 offset:24576\n\tv_exp_f32_e32 v180, v180\n\tv_cvt_pk_bf16_f32 v74, v164, v165\n\tv_add_f32_e32 %0, %0, v177\n\tv_add_f32_e32 %1, %1, v146" : "+v"(c.rs[1][0]), "+v"(c.rs[1][2]) : "v"(c.kb_[7]) : "memory");
  asm volatile("v_mfma_f32_32x32x16_bf16 v[240:255], a[228:231], a[176:179], v[240:255]\n\tv_exp_f32_e32 v181, v181\n\tv_cvt_pk_bf16_f32 v75, v166, v167\n\tv_add_f32_e32 %0, %0, v178\n\tv_add_f32_e32 %1, %1, v147" : "+v"(c.rs[1][1]), "+v"(c.rs[1][3]) :  : "memory");
  asm volatile("s_waitcnt lgkmcnt(4)\n\tv_mfma_f32_32x32x16_bf16 v[192:207], a[232:235], a[148:151], v[192:207]\n\tv_exp_f32_e32 v182, v182\n\tv_cvt_pk_bf16_f32 v76, v168, v169\n\tv_add_f32_e32 %0, %0, v179\n\tv_add_f32_e32 %1, %1, v148" : "+v"(c.rs[1][0]), "+v"(c.rs[1][2]) :  : "memory");
  asm volatile("v_mfma_f32_32x32x16_bf16 v[208:223], a[232:235], a[180:183], v[208:223]\n\tv_exp_f32_e32 v183, v183\n\tv_cvt_pk_bf16_f32 v77, v170, v171\n\tv_add_f32_e32 %0, %0, v180\n\tv_add_f32_e32 %1, %1, v149" : "+v"(c.rs[1][1]), "+v"(c.rs[1][3]) :  : "memory");
  asm volatile("v_mfma_f32_32x32x16_bf16 v[224:239], a[236:239], a[148:151], v[224:239]\n\tv_exp_f32_e32 v184, v184\n\tv_cvt_pk_bf16_f32 v78, v172, v173\n\tv_add_f32_e32 %0, %0, v181\n\tv_add_f32_e32 %1, %1, v150" : "+v"(c.rs[1][0]), "+v"(c.rs[1][2]) :  : "memory");
  asm volatile("v_mfma_f32_32x32x16_bf16 v[240:255], a[236:239], a[180:183], v[240:255]\n\tv_exp_f32_e32 v185, v185\n\tv_cvt_pk_bf16_f32 v79, v174, v175\n\tv_add_f32_e32 %0, %0, v182\n\tv_add_f32_e32 %1, %1, v151" : "+v"(c.rs[1][1]), "+v"(c.rs[1][3]) :  : "memory");
  asm volatile("s_waitcnt vmcnt(0) lgkmcnt(0)\n\ts_barrier\n\ts_waitcnt lgkmcnt(2)\n\tv_mfma_f32_32x32x16_bf16 v[192:207], a[240:243], a[152:155], v[192:207]\n\tds_read_b128 a[192:195], %2 offset:32768\n\tv_exp_f32_e32 v186, v186\n\tv_cvt_pk_bf16_f32 v88, v176, v177\n\tv_add_f32_e32 %0, %0, v183\n\tv_add_f32_e32 %1, %1, v152" : "+v"(c.rs[1][0]), "+v"(c.rs[1][2]) : "v"(c.vb_[0]) : "memory", ACL64);
  asm volatile("v_mfma_f32_32x32x16_bf16 v[208:223], a[240:243], a[184:187], v[208:223]\n\tds_read_b128 a[196:199], %2 offset:36864\n\tv_exp_f32_e32 v187, v187\n\tv_cvt_pk_bf16_f32 v89, v178, v179\n\tv_add_f32_e32 %0, %0, v184\n\tv_add_f32_e32 %1, %1, v153\n\ts_add_u32 m0, %3, 49152\n\ts_nop 0\n\tbuffer_load_dwordx4 %4, %5, %6 offen offset:0 sc1 lds" : "+v"(c.rs[1][1]), "+v"(c.rs[1][3]) : "v"(c.vb_[0]), "s"(c.m0w), "v"(c.vlin), "s"(c.srdV), "s"(c.soVA) : "memory", "m0", "scc");
  asm volatile("v_mfma_f32_32x32x16_bf16 v[224:239], a[244:247], a[152:155], v[224:239]\n\tds_read_b128 a[200:203], %2 offset:40960\n\tv_exp_f32_e32 v188, v188\n\tv_cvt_pk_bf16_f32 v90, v180, v181\n\tv_add_f32_e32 %0, %0, v185\n\tv_add_f32_e32 %1, %1, v154" : "+v"(c.rs[1][0]), "+v"(c.rs[1][2]) : "v"(c.vb_[0]) : "memory");
  asm volatile("v_mfma_f32_32x32x16_bf16 v[240:255], a[244:247], a[184:187], v[240:255]\n\tds_read_b128 a[204:207], %2 offset:45056\n\tv_exp_f32_e32 v189, v189\n\tv_cvt_pk_bf16_f32 v91, v182, v183\n\tv_add_f32_e32 %0, %0, v186\n\tv_add_f32_e32 %1, %1, v155\n\tbuffer_load_dwordx4 %3, %4, %5 offen offset:1024 sc1 lds" : "+v"(c.rs[1][1]), "+v"(c.rs[1][3]) : "v"(c.vb_[0]), "v"(c.vlin), "s"(c.srdV), "s"(c.soVA) : "memory");
  asm volatile("s_waitcnt lgkmcnt(4)\n\tv_mfma_f32_32x32x16_bf16 v[192:207], a[248:251], a[156:159], v[192:207]\n\tds_read_b128 a[208:211], %2 offset:32768\n\tv_exp_f32_e32 v190, v190\n\tv_cvt_pk_bf16_f32 v92, v184, v185\n\tv_add_f32_e32 %0, %0, v187\n\tv_add_f32_e32 %1, %1, v156" : "+v"(c.rs[1][0]), "+v"(c.rs[1][2]) : "v"(c.vb_[1]) : "memory");
  asm volatile("v_mfma_f32_32x32x16_bf16 v[208:223], a[248:251], a[188:191], v[208:223]\n\tv_exp_f32_e32 v191, v191\n\tv_cvt_pk_bf16_f32 v93, v186, v187\n\tv_add_f32_e32 %0, %0, v188\n\tv_add_f32_e32 %1, %1, v157\n\tbuffer_load_dwordx4 %2, %3, %4 offen offset:2048 sc1 lds" : "+v"(c.rs[1][1]), "+v"(c.rs[1][3]) : "v"(c.vlin), "s"(c.srdV), "s"(c.soVA) : "memory");
  asm volatile("v_mfma_f32_32x32x16_bf16 v[224:239], a[252:255], a[156:159], v[224:239]\n\tds_read_b128 a[212:215], %2 offset:36864\n\tv_cvt_pk_bf16_f32 v94, v188, v189\n\tv_add_f32_e32 %0, %0, v189\n\tv_add_f32_e32 %1, %1, v158" : "+v"(c.rs[1][0]), "+v"(c.rs[1][2]) : "v"(c.vb_[1]) : "memory");
  asm volatile("v_mfma_f32_32x32x16_bf16 v[240:255], a[252:255], a[188:191], v[240:255]\n\tv_cvt_pk_bf16_f32 v95, v190, v191\n\tv_add_f32_e32 %0, %0, v190\n\tv_add_f32_e32 %1, %1, v191\n\tv_add_f32_e32 %2, %2, v159\n\tbuffer_load_dwordx4 %3, %4, %5 offen offset:3072 sc1 lds" : "+v"(c.rs[1][1]), "+v"(c.rs[1][0]), "+v"(c.rs[1][3]) : "v"(c.vlin), "s"(c.srdV), "s"(c.soVA) : "memory");
}
DI void gB1am2(AC& c) {
  asm volatile("s_waitcnt lgkmcnt(4)\n\tv_mfma_f32_32x32x16_bf16 a[0:15], a[192:195], v[64:67], a[0:15]\n\tds_read_b128 a[216:219], %0 offset:40960" :  : "v"(c.vb_[1]) : "memory", ACL64);
  asm volatile("v_mfma_f32_32x32x16_bf16 a[16:31], a[192:195], v[80:83], a[16:31]\n\ts_add_u32 m0, %0, 114688\n\ts_nop 0\n\tbuffer_load_dwordx4 %1, %2, %3 offen offset:0 sc1 lds" :  : "s"(c.m0w), "v"(c.vlin), "s"(c.srdV), "s"(c.soVB) : "memory", "m0", "scc");
  asm volatile("v_mfma_f32_32x32x16_bf16 a[32:47], a[196:199], v[64:67], a[32:47]\n\tds_read_b128 a[220:223], %2 offset:45056\n\tv_max3_f32 %0, v192, v193, v224\n\tv_max3_f32 %1, v194, v195, v225\n\tv_max3_f32 %0, %0, v226, v227" : "=&v"(c.pmx[0][0]), "=&v"(c.pmx[0][1]) : "v"(c.vb_[1]) : "memory");
  asm volatile("v_mfma_f32_32x32x16_bf16 a[48:63], a[196:199], v[80:83], a[48:63]\n\tv_max3_f32 %0, %0, v196, v197\n\tv_max3_f32 %1, %1, v198, v199\n\tv_max3_f32 %0, %0, v228, v229\n\tv_max3_f32 %1, %1, v230, v231\n\tbuffer_load_dwordx4 %2, %3, %4 offen offset:1024 sc1 lds" : "+v"(c.pmx[0][0]), "+v"(c.pmx[0][1]) : "v"(c.vlin), "s"(c.srdV), "s"(c.soVB) : "memory");
  asm volatile("s_waitcnt lgkmcnt(4)\n\tv_mfma_f32_32x32x16_bf16 a[64:79], a[200:203], v[64:67], a[64:79]\n\tds_read_b128 a[224:227], %2 offset:32768\n\tv_max3_f32 %0, %0, v200, v201\n\tv_max3_f32 %1, %1, v202, v203\n\tv_max3_f32 %0, %0, v232, v233\n\tv_max3_f32 %1, %1, v234, v235" : "+v"(c.pmx[0][0]), "+v"(c.pmx[0][1]) : "v"(c.vb_[2]) : "memory");
  asm volatile("v_mfma_f32_32x32x16_bf16 a[80:95], a[200:203], v[80:83], a[80:95]\n\tv_max3_f32 %0, %0, v204, v205\n\tv_max3_f32 %1, %1, v206, v207\n\tv_max3_f32 %0, %0, v236, v237\n\tv_max3_f32 %1, %1, v238, v239\n\tbuffer_load_dwordx4 %2, %3, %4 offen offset:2048 sc1 lds" : "+v"(c.pmx[0][0]), "+v"(c.pmx[0][1]) : "v"(c.vlin), "s"(c.srdV), "s"(c.soVB) : "memory");
  asm volatile("v_mfma_f32_32x32x16_bf16 a[96:111], a[204:207], v[64:67], a[96:111]\n\tds_read_b128 a[228:231], %2 offset:36864\n\tv_max3_f32 %0, v208, v209, v240\n\tv_max3_f32 %1, v210, v211, v241\n\tv_max3_f32 %0, %0, v242, v243" : "=&v"(c.pmx[1][0]), "=&v"(c.pmx[1][1]) : "v"(c.vb_[2]) : "memory");
  asm volatile("v_mfma_f32_32x32x16_bf16 a[112:127], a[204:207], v[80:83], a[112:127]\n\tv_max3_f32 %0, %0, v212, v213\n\tv_max3_f32 %1, %1, v214, v215\n\tv_max3_f32 %0, %0, v244, v245\n\tv_max3_f32 %1, %1, v246, v247\n\tbuffer_load_dwordx4 %2, %3, %4 offen offset:3072 sc1 lds" : "+v"(c.pmx[1][0]), "+v"(c.pmx[1][1]) : "v"(c.vlin), "s"(c.srdV), "s"(c.soVB) : "memory");
  asm volatile("s_waitcnt lgkmcnt(4)\n\tv_mfma_f32_32x32x16_bf16 a[0:15], a[208:211], v[68:71], a[0:15]\n\tds_read_b128 a[232:235], %2 offset:40960\n\tv_max3_f32 %0, %0, v216, v217\n\tv_max3_f32 %1, %1, v218, v219\n\tv_max3_f32 %0, %0, v248, v249\n\tv_max3_f32 %1, %1, v250, v251" : "+v"(c.pmx[1][0]), "+v"(c.pmx[1][1]) : "v"(c.vb_[2]) : "memory", ACL64);
  asm volatile("v_mfma_f32_32x32x16_bf16 a[16:31], a[208:211], v[84:87], a[16:31]\n\tv_max3_f32 %0, %0, v220, v221\n\tv_max3_f32 %1, %1, v222, v223\n\tv_max3_f32 %0, %0, v252, v253\n\tv_max3_f32 %1, %1, v254, v255\n\ts_add_u32 m0, %2, 16384\n\ts_nop 0\n\tbuffer_load_dwordx4 %3, %4, %5 offen offset:0 sc1 lds" : "+v"(c.pmx[1][0]), "+v"(c.pmx[1][1]) : "s"(c.m0w), "v"(c.vlin), "s"(c.srdK), "s"(c.soKA) : "memory", "m0", "scc");
  asm volatile("v_mfma_f32_32x32x16_bf16 a[32:47], a[212:215], v[68:71], a[32:47]\n\tds_read_b128 a[236:239], %2 offset:45056\n\tv_max_f32_e32 %0, %0, %1\n\tv_mov_b32_e32 %1, %0\n\ts_nop 1\n\tv_permlane32_swap_b32_e32 %0, %1\n\tv_max_f32_e32 %0, %0, %1" : "+v"(c.pmx[0][0]), "+v"(c.pmx[0][1]) : "v"(c.vb_[2]) : "memory");
  asm volatile("v_mfma_f32_32x32x16_bf16 a[48:63], a[212:215], v[84:87], a[48:63]\n\tv_max_f32_e32 %0, %0, %1\n\tv_mov_b32_e32 %1, %0\n\ts_nop 1\n\tv_permlane32_swap_b32_e32 %0, %1\n\tv_max_f32_e32 %0, %0, %1\n\tbuffer_load_dwordx4 %2, %3, %4 offen offset:1024 sc1 lds" : "+v"(c.pmx[1][0]), "+v"(c.pmx[1][1]) : "v"(c.vlin), "s"(c.srdK), "s"(c.soKA) : "memory");
}
DI void gB1bm2(AC& c) {
  asm volatile("s_waitcnt lgkmcnt(4)\n\tv_mfma_f32_32x32x16_bf16 a[64:79], a[216:219], v[68:71], a[64:79]\n\tds_read_b128 a[240:243], %0 offset:32768" :  : "v"(c.vb_[3]) : "memory");
  asm volatile("v_mfma_f32_32x32x16_bf16 a[80:95], a[216:219], v[84:87], a[80:95]\n\tv_exp_f32_e32 v192, v192\n\tv_exp_f32_e32 v193, v193\n\tbuffer_load_dwordx4 %0, %1, %2 offen offset:2048 sc1 lds" :  : "v"(c.vlin), "s"(c.srdK), "s"(c.soKA) : "memory");
  asm volatile("v_mfma_f32_32x32x16_bf16 a[96:111], a[220:223], v[68:71], a[96:111]\n\tds_read_b128 a[244:247], %0 offset:36864\n\tv_exp_f32_e32 v194, v194\n\tv_exp_f32_e32 v195, v195" :  : "v"(c.vb_[3]) : "memory");
  asm volatile("v_mfma_f32_32x32x16_bf16 a[112:127], a[220:223], v[84:87], a[112:127]\n\tv_exp_f32_e32 v196, v196\n\tv_exp_f32_e32 v197, v197\n\tbuffer_load_dwordx4 %0, %1, %2 offen offset:3072 sc1 lds" :  : "v"(c.vlin), "s"(c.srdK), "s"(c.soKA) : "memory");
  asm volatile("s_waitcnt lgkmcnt(4)\n\tv_mfma_f32_32x32x16_bf16 a[0:15], a[224:227], v[72:75], a[0:15]\n\tds_read_b128 a[248:251], %1 offset:40960\n\tv_exp_f32_e32 v198, v198\n\tv_exp_f32_e32 v199, v199\n\tv_add_f32_e32 %0, %0, v192" : "+v"(c.rs[0][0]) : "v"(c.vb_[3]) : "memory", ACL64);
  asm volatile("v_mfma_f32_32x32x16_bf16 a[16:31], a[224:227], v[88:91], a[16:31]\n\tv_exp_f32_e32 v200, v200\n\tv_exp_f32_e32 v201, v201\n\tv_add_f32_e32 %0, %0, v193\n\ts_add_u32 m0, %1, 81920\n\ts_nop 0\n\tbuffer_load_dwordx4 %2, %3, %4 offen offset:0 sc1 lds" : "+v"(c.rs[0][1]) : "s"(c.m0w), "v"(c.vlin), "s"(c.srdK), "s"(c.soKB) : "memory", "m0", "scc");
  asm volatile("v_mfma_f32_32x32x16_bf16 a[32:47], a[228:231], v[72:75], a[32:47]\n\tds_read_b128 a[252:255], %1 offset:45056\n\tv_exp_f32_e32 v202, v202\n\tv_exp_f32_e32 v203, v203\n\tv_add_f32_e32 %0, %0, v194" : "+v"(c.rs[0][0]) : "v"(c.vb_[3]) : "memory");
  asm volatile("v_mfma_f32_32x32x16_bf16 a[48:63], a[228:231], v[88:91], a[48:63]\n\tv_exp_f32_e32 v204, v204\n\tv_exp_f32_e32 v205, v205\n\tv_add_f32_e32 %0, %0, v195\n\tbuffer_load_dwordx4 %1, %2, %3 offen offset:1024 sc1 lds" : "+v"(c.rs[0][1]) : "v"(c.vlin), "s"(c.srdK), "s"(c.soKB) : "memory");
  asm volatile("s_waitcnt lgkmcnt(4)\n\tv_mfma_f32_32x32x16_bf16 a[64:79], a[232:235], v[72:75], a[64:79]\n\tds_read_b128 a[192:195], %1 offset:0\n\tv_exp_f32_e32 v206, v206\n\tv_exp_f32_e32 v207, v207\n\tv_add_f32_e32 %0, %0, v196" : "+v"(c.rs[0][0]) : "v"(c.kb_[0]) : "memory");
  asm volatile("v_mfma_f32_32x32x16_bf16 a[80:95], a[232:235], v[88:91], a[80:95]\n\tv_exp_f32_e32 v208, v208\n\tv_exp_f32_e32 v209, v209\n\tv_add_f32_e32 %0, %0, v197\n\tbuffer_load_dwordx4 %1, %2, %3 offen offset:2048 sc1 lds" : "+v"(c.rs[0][1]) : "v"(c.vlin), "s"(c.srdK), "s"(c.soKB) : "memory");
  asm volatile("v_mfma_f32_32x32x16_bf16 a[96:111], a[236:239], v[72:75], a[96:111]\n\tds_read_b128 a[196:199], %1 offset:8192\n\tv_exp_f32_e32 v210, v210\n\tv_exp_f32_e32 v211, v211\n\tv_add_f32_e32 %0, %0, v198" : "+v"(c.rs[0][0]) : "v"(c.kb_[0]) : "memory");
  asm volatile("v_mfma_f32_32x32x16_bf16 a[112:127], a[236:239], v[88:91], a[112:127]\n\tv_exp_f32_e32 v212, v212\n\tv_exp_f32_e32 v213, v213\n\tv_add_f32_e32 %0, %0, v199\n\tbuffer_load_dwordx4 %1, %2, %3 offen offset:3072 sc1 lds" : "+v"(c.rs[0][1]) : "v"(c.vlin), "s"(c.srdK), "s"(c.soKB) : "memory");
  asm volatile("s_waitcnt lgkmcnt(4)\n\tv_mfma_f32_32x32x16_bf16 a[0:15], a[240:243], v[76:79], a[0:15]\n\tds_read_b128 a[200:203], %1 offset:0\n\tv_exp_f32_e32 v214, v214\n\tv_exp_f32_e32 v215, v215\n\tv_add_f32_e32 %0, %0, v200" : "+v"(c.rs[0][0]) : "v"(c.kb_[1]) : "memory", ACL64);
  asm volatile("v_mfma_f32_32x32x16_bf16 a[16:31], a[240:243], v[92:95], a[16:31]\n\tv_exp_f32_e32 v216, v216\n\tv_exp_f32_e32 v217, v217\n\tv_add_f32_e32 %0, %0, v201" : "+v"(c.rs[0][1]) :  : "memory");
  asm volatile("v_mfma_f32_32x32x16_bf16 a[32:47], a[244:247], v[76:79], a[32:47]\n\tds_read_b128 a[204:207], %1 offset:8192\n\tv_exp_f32_e32 v218, v218\n\tv_add_f32_e32 %0, %0, v202" : "+v"(c.rs[0][0]) : "v"(c.kb_[1]) : "memory");
  asm volatile("v_mfma_f32_32x32x16_bf16 a[48:63], a[244:247], v[92:95], a[48:63]\n\tv_exp_f32_e32 v219, v219\n\tv_add_f32_e32 %0, %0, v203" : "+v"(c.rs[0][1]) :  : "memory");
  asm volatile("s_waitcnt lgkmcnt(4)\n\tv_mfma_f32_32x32x16_bf16 a[64:79], a[248:251], v[76:79], a[64:79]\n\tds_read_b128 a[208:211], %1 offset:0\n\tv_exp_f32_e32 v220, v220\n\tv_add_f32_e32 %0, %0, v204" : "+v"(c.rs[0][0]) : "v"(c.kb_[2]) : "memory");
  asm volatile("v_mfma_f32_32x32x16_bf16 a[80:95], a[248:251], v[92:95], a[80:95]\n\tv_exp_f32_e32 v221, v221\n\tv_add_f32_e32 %0, %0, v205" : "+v"(c.rs[0][1]) :  : "memory");
  asm volatile("v_mfma_f32_32x32x16_bf16 a[96:111], a[252:255], v[76:79], a[96:111]\n\tds_read_b128 a[212:215], %1 offset:8192\n\tv_exp_f32_e32 v222, v222\n\tv_add_f32_e32 %0, %0, v206" : "+v"(c.rs[0][0]) : "v"(c.kb_[2]) : "memory");
  asm volatile("v_mfma_f32_32x32x16_bf16 a[112:127], a[252:255], v[92:95], a[112:127]\n\tv_exp_f32_e32 v223, v223\n\tv_add_f32_e32 %0, %0, v207" : "+v"(c.rs[0][1]) :  : "memory");
}
DI void g_drain(AC& c) {
  asm volatile("ds_read_b128 a[192:195], %8 offset:32768\n\tds_read_b128 a[196:199], %8 offset:36864\n\tds_read_b128 a[200:203], %8 offset:40960\n\tds_read_b128 a[204:207], %8 offset:45056\n\tds_read_b128 a[208:211], %9 offset:32768\n\tds_read_b128 a[212:215], %9 offset:36864\n\tds_read_b128 a[216:219], %9 offset:40960\n\tds_read_b128 a[220:223], %9 offset:45056\n\tds_read_b128 a[224:227], %10 offset:32768\n\tds_read_b128 a[228:231], %10 offset:36864\n\tds_read_b128 a[232:235], %10 offset:40960\n\tds_read_b128 a[236:239], %10 offset:45056\n\tds_read_b128 a[240:243], %11 offset:32768\n\tds_read_b128 a[244:247], %11 offset:36864\n\tds_read_b128 a[248:251], %11 offset:40960\n\tds_read_b128 a[252:255], %11 offset:45056\n\tv_cvt_pk_bf16_f32 v64, v128, v129\n\tv_cvt_pk_bf16_f32 v65, v130, v131\n\tv_cvt_pk_bf16_f32 v66, v132, v133\n\tv_cvt_pk_bf16_f32 v67, v134, v135\n\tv_cvt_pk_bf16_f32 v80, v144, v145\n\tv_cvt_pk_bf16_f32 v81, v146, v147\n\tv_cvt_pk_bf16_f32 v82, v148, v149\n\tv_cvt_pk_bf16_f32 v83, v150, v151\n\tv_cvt_pk_bf16_f32 v68, v136, v137\n\tv_cvt_pk_bf16_f32 v69, v138, v139\n\tv_cvt_pk_bf16_f32 v70, v140, v141\n\tv_cvt_pk_bf16_f32 v71, v142, v143\n\tv_cvt_pk_bf16_f32 v84, v152, v153\n\tv_cvt_pk_bf16_f32 v85, v154, v155\n\tv_cvt_pk_bf16_f32 v86, v156, v157\n\tv_cvt_pk_bf16_f32 v87, v158, v159\n\ts_waitcnt lgkmcnt(12)\n\tv_mfma_f32_32x32x16_bf16 a[0:15], a[192:195], v[64:67], a[0:15]\n\tv_exp_f32_e32 v160, v160\n\tv_exp_f32_e32 v161, v161\n\tv_exp_f32_e32 v162, v162\n\tv_mfma_f32_32x32x16_bf16 a[16:31], a[192:195], v[80:83], a[16:31]\n\tv_exp_f32_e32 v163, v163\n\tv_exp_f32_e32 v164, v164\n\tv_exp_f32_e32 v165, v165\n\tv_mfma_f32_32x32x16_bf16 a[32:47], a[196:199], v[64:67], a[32:47]\n\tv_exp_f32_e32 v166, v166\n\tv_exp_f32_e32 v167, v167\n\tv_exp_f32_e32 v176, v176\n\tv_mfma_f32_32x32x16_bf16 a[48:63], a[196:199], v[80:83], a[48:63]\n\tv_exp_f32_e32 v177, v177\n\tv_exp_f32_e32 v178, v178\n\tv_exp_f32_e32 v179, v179\n\tv_mfma_f32_32x32x16_bf16 a[64:79], a[200:203], v[64:67], a[64:79]\n\tv_exp_f32_e32 v180, v180\n\tv_exp_f32_e32 v181, v181\n\tv_exp_f32_e32 v182, v182\n\tv_cvt_pk_bf16_f32 v72, v160, v161\n\tv_mfma_f32_32x32x16_bf16 a[80:95], a[200:203], v[80:83], a[80:95]\n\tv_exp_f32_e32 v183, v183\n\tv_exp_f32_e32 v168, v168\n\tv_exp_f32_e32 v169, v169\n\tv_cvt_pk_bf16_f32 v73, v162, v163\n\tv_mfma_f32_32x32x16_bf16 a[96:111], a[204:207], v[64:67], a[96:111]\n\tv_exp_f32_e32 v170, v170\n\tv_exp_f32_e32 v171, v171\n\tv_exp_f32_e32 v172, v172\n\tv_cvt_pk_bf16_f32 v74, v164, v165\n\tv_mfma_f32_32x32x16_bf16 a[112:127], a[204:207], v[80:83], a[112:127]\n\tv_exp_f32_e32 v173, v173\n\tv_exp_f32_e32 v174, v174\n\tv_exp_f32_e32 v175, v175\n\tv_cvt_pk_bf16_f32 v75, v166, v167\n\ts_waitcnt lgkmcnt(8)\n\tv_mfma_f32_32x32x16_bf16 a[0:15], a[208:211], v[68:71], a[0:15]\n\tv_exp_f32_e32 v184, v184\n\tv_exp_f32_e32 v185, v185\n\tv_exp_f32_e32 v186, v186\n\tv_cvt_pk_bf16_f32 v88, v176, v177\n\tv_mfma_f32_32x32x16_bf16 a[16:31], a[208:211], v[84:87], a[16:31]\n\tv_exp_f32_e32 v187, v187\n\tv_exp_f32_e32 v188, v188\n\tv_exp_f32_e32 v189, v189\n\tv_cvt_pk_bf16_f32 v89, v178, v179\n\tv_mfma_f32_32x32x16_bf16 a[32:47], a[212:215], v[68:71], a[32:47]\n\tv_exp_f32_e32 v190, v190\n\tv_exp_f32_e32 v191, v191\n\tv_cvt_pk_bf16_f32 v90, v180, v181\n\tv_mfma_f32_32x32x16_bf16 a[48:63], a[212:215], v[84:87], a[48:63]\n\tv_cvt_pk_bf16_f32 v91, v182, v183\n\tv_mfma_f32_32x32x16_bf16 a[64:79], a[216:219], v[68:71], a[64:79]\n\tv_cvt_pk_bf16_f32 v76, v168, v169\n\tv_add_f32_e32 %0, %0, v160\n\tv_add_f32_e32 %1, %1, v161\n\tv_add_f32_e32 %2, %2, v144\n\tv_mfma_f32_32x32x16_bf16 a[80:95], a[216:219], v[84:87], a[80:95]\n\tv_cvt_pk_bf16_f32 v77, v170, v171\n\tv_add_f32_e32 %3, %3, v162\n\tv_add_f32_e32 %4, %4, v163\n\tv_add_f32_e32 %5, %5, v145\n\tv_mfma_f32_32x32x16_bf16 a[96:111], a[220:223], v[68:71], a[96:111]\n\tv_cvt_pk_bf16_f32 v78, v172, v173\n\tv_add_f32_e32 %1, %1, v164\n\tv_add_f32_e32 %0, %0, v165\n\tv_add_f32_e32 %6, %6, v146\n\tv_mfma_f32_32x32x16_bf16 a[112:127], a[220:223], v[84:87], a[112:127]\n\tv_cvt_pk_bf16_f32 v79, v174, v175\n\tv_add_f32_e32 %4, %4, v166\n\tv_add_f32_e32 %3, %3, v167\n\tv_add_f32_e32 %7, %7, v147\n\ts_waitcnt lgkmcnt(4)\n\ts_nop 1\n\tv_mfma_f32_32x32x16_bf16 a[0:15], a[224:227], v[72:75], a[0:15]\n\tv_cvt_pk_bf16_f32 v92, v184, v185\n\tv_add_f32_e32 %0, %0, v168\n\tv_add_f32_e32 %1, %1, v169\n\tv_add_f32_e32 %2, %2, v148\n\tv_mfma_f32_32x32x16_bf16 a[16:31], a[224:227], v[88:91], a[16:31]\n\tv_cvt_pk_bf16_f32 v93, v186, v187\n\tv_add_f32_e32 %3, %3, v170\n\tv_add_f32_e32 %4, %4, v171\n\tv_add_f32_e32 %5, %5, v149\n\tv_mfma_f32_32x32x16_bf16 a[32:47], a[228:231], v[72:75], a[32:47]\n\tv_cvt_pk_bf16_f32 v94, v188, v189\n\tv_add_f32_e32 %1, %1, v172\n\tv_add_f32_e32 %0, %0, v173\n\tv_add_f32_e32 %6, %6, v150\n\tv_mfma_f32_32x32x16_bf16 a[48:63], a[228:231], v[88:91], a[48:63]\n\tv_cvt_pk_bf16_f32 v95, v190, v191\n\tv_add_f32_e32 %4, %4, v174\n\tv_add_f32_e32 %3, %3, v175\n\tv_add_f32_e32 %7, %7, v151\n\tv_mfma_f32_32x32x16_bf16 a[64:79], a[232:235], v[72:75], a[64:79]\n\tv_add_f32_e32 %7, %7, v176\n\tv_add_f32_e32 %5, %5, v177\n\tv_add_f32_e32 %2, %2, v152\n\tv_mfma_f32_32x32x16_bf16 a[80:95], a[232:235], v[88:91], a[80:95]\n\tv_add_f32_e32 %2, %2, v178\n\tv_add_f32_e32 %6, %6, v179\n\tv_add_f32_e32 %5, %5, v153\n\tv_mfma_f32_32x32x16_bf16 a[96:111], a[236:239], v[72:75], a[96:111]\n\tv_add_f32_e32 %5, %5, v180\n\tv_add_f32_e32 %7, %7, v181\n\tv_add_f32_e32 %6, %6, v154\n\tv_mfma_f32_32x32x16_bf16 a[112:127], a[236:239], v[88:91], a[112:127]\n\tv_add_f32_e32 %6, %6, v182\n\tv_add_f32_e32 %2, %2, v183\n\tv_add_f32_e32 %7, %7, v155\n\ts_waitcnt lgkmcnt(0)\n\tv_mfma_f32_32x32x16_bf16 a[0:15], a[240:243], v[76:79], a[0:15]\n\tv_add_f32_e32 %7, %7, v184\n\tv_add_f32_e32 %5, %5, v185\n\tv_add_f32_e32 %2, %2, v156\n\tv_mfma_f32_32x32x16_bf16 a[16:31], a[240:243], v[92:95], a[16:31]\n\tv_add_f32_e32 %2, %2, v186\n\tv_add_f32_e32 %6, %6, v187\n\tv_add_f32_e32 %5, %5, v157\n\tv_mfma_f32_32x32x16_bf16 a[32:47], a[244:247], v[76:79], a[32:47]\n\tv_add_f32_e32 %5, %5, v188\n\tv_add_f32_e32 %7, %7, v189\n\tv_add_f32_e32 %6, %6, v158\n\tv_mfma_f32_32x32x16_bf16 a[48:63], a[244:247], v[92:95], a[48:63]\n\tv_add_f32_e32 %6, %6, v190\n\tv_add_f32_e32 %2, %2, v191\n\tv_add_f32_e32 %7, %7, v159\n\tv_mfma_f32_32x32x16_bf16 a[64:79], a[248:251], v[76:79], a[64:79]\n\tv_mfma_f32_32x32x16_bf16 a[80:95], a[248:251], v[92:95], a[80:95]\n\tv_mfma_f32_32x32x16_bf16 a[96:111], a[252:255], v[76:79], a[96:111]\n\tv_mfma_f32_32x32x16_bf16 a[112:127], a[252:255], v[92:95], a[112:127]" : "+v"(c.rs[0][0]), "+v"(c.rs[0][2]), "+v"(c.rs[1][1]), "+v"(c.rs[0][1]), "+v"(c.rs[0][3]), "+v"(c.rs[1][2]), "+v"(c.rs[1][3]), "+v"(c.rs[1][0]) : "v"(c.vb_[0]), "v"(c.vb_[1]), "v"(c.vb_[2]), "v"(c.vb_[3]) : "memory", ACL64);
}
DI void g_merge0(const unsigned src, const f32x2 f1, const f32x2 f2, const unsigned eb) {
  asm volatile("ds_read_b128 v[128:131], %0 offset:0\n\tds_read_b128 v[132:135], %0 offset:1024\n\tds_read_b128 v[136:139], %0 offset:2048\n\tds_read_b128 v[140:143], %0 offset:3072\n\tds_read_b128 v[144:147], %0 offset:4096\n\tds_read_b128 v[148:151], %0 offset:5120\n\tds_read_b128 v[152:155], %0 offset:6144\n\tds_read_b128 v[156:159], %0 offset:7168\n\tds_read_b128 v[160:163], %0 offset:8192\n\tds_read_b128 v[164:167], %0 offset:9216\n\tds_read_b128 v[168:171], %0 offset:10240\n\tds_read_b128 v[172:175], %0 offset:11264\n\tds_read_b128 v[176:179], %0 offset:12288\n\tds_read_b128 v[180:183], %0 offset:13312\n\tds_read_b128 v[184:187], %0 offset:14336\n\tds_read_b128 v[188:191], %0 offset:15360\n\tv_accvgpr_read_b32 v192, a0\n\tv_accvgpr_read_b32 v193, a1\n\tv_accvgpr_read_b32 v194, a2\n\tv_accvgpr_read_b32 v195, a3\n\tv_xor_b32_e32 v196, 0, %3\n\tv_pk_mul_f32 v[192:193], v[192:193], %1\n\tv_pk_mul_f32 v[194:195], v[194:195], %1\n\ts_waitcnt lgkmcnt(15)\n\tv_pk_fma_f32 v[192:193], v[128:129], %2, v[192:193]\n\tv_pk_fma_f32 v[194:195], v[130:131], %2, v[194:195]\n\tv_cvt_pk_bf16_f32 v192, v192, v193\n\tv_cvt_pk_bf16_f32 v193, v194, v195\n\tds_write_b64 v196, v[192:193]\n\tv_accvgpr_read_b32 v200, a4\n\tv_accvgpr_read_b32 v201, a5\n\tv_accvgpr_read_b32 v202, a6\n\tv_accvgpr_read_b32 v203, a7\n\tv_xor_b32_e32 v204, 16, %3\n\tv_pk_mul_f32 v[200:201], v[200:201], %1\n\tv_pk_mul_f32 v[202:203], v[202:203], %1\n\ts_waitcnt lgkmcnt(15)\n\tv_pk_fma_f32 v[200:201], v[132:133], %2, v[200:201]\n\tv_pk_fma_f32 v[202:203], v[134:135], %2, v[202:203]\n\tv_cvt_pk_bf16_f32 v200, v200, v201\n\tv_cvt_pk_bf16_f32 v201, v202, v203\n\tds_write_b64 v204, v[200:201]\n\tv_accvgpr_read_b32 v192, a8\n\tv_accvgpr_read_b32 v193, a9\n\tv_accvgpr_read_b32 v194, a10\n\tv_accvgpr_read_b32 v195, a11\n\tv_xor_b32_e32 v196, 32, %3\n\tv_pk_mul_f32 v[192:193], v[192:193], %1\n\tv_pk_mul_f32 v[194:195], v[194:195], %1\n\ts_waitcnt lgkmcnt(15)\n\tv_pk_fma_f32 v[192:193], v[136:137], %2, v[192:193]\n\tv_pk_fma_f32 v[194:195], v[138:139], %2, v[194:195]\n\tv_cvt_pk_bf16_f32 v192, v192, v193\n\tv_cvt_pk_bf16_f32 v193, v194, v195\n\tds_write_b64 v196, v[192:193]\n\tv_accvgpr_read_b32 v200, a12\n\tv_accvgpr_read_b32 v201, a13\n\tv_accvgpr_read_b32 v202, a14\n\tv_accvgpr_read_b32 v203, a15\n\tv_xor_b32_e32 v204, 48, %3\n\tv_pk_mul_f32 v[200:201], v[200:201], %1\n\tv_pk_mul_f32 v[202:203], v[202:203], %1\n\ts_waitcnt lgkmcnt(15)\n\tv_pk_fma_f32 v[200:201], v[140:141], %2, v[200:201]\n\tv_pk_fma_f32 v[202:203], v[142:143], %2, v[202:203]\n\tv_cvt_pk_bf16_f32 v200, v200, v201\n\tv_cvt_pk_bf16_f32 v201, v202, v203\n\tds_write_b64 v204, v[200:201]\n\tv_accvgpr_read_b32 v192, a32\n\tv_accvgpr_read_b32 v193, a33\n\tv_accvgpr_read_b32 v194, a34\n\tv_accvgpr_read_b32 v195, a35\n\tv_xor_b32_e32 v196, 64, %3\n\tv_pk_mul_f32 v[192:193], v[192:193], %1\n\tv_pk_mul_f32 v[194:195], v[194:195], %1\n\ts_waitcnt lgkmcnt(15)\n\tv_pk_fma_f32 v[192:193], v[144:145], %2, v[192:193]\n\tv_pk_fma_f32 v[194:195], v[146:147], %2, v[194:195]\n\tv_cvt_pk_bf16_f32 v192, v192, v193\n\tv_cvt_pk_bf16_f32 v193, v194, v195\n\tds_write_b64 v196, v[192:193]\n\tv_accvgpr_read_b32 v200, a36\n\tv_accvgpr_read_b32 v201, a37\n\tv_accvgpr_read_b32 v202, a38\n\tv_accvgpr_read_b32 v203, a39\n\tv_xor_b32_e32 v204, 80, %3\n\tv_pk_mul_f32 v[200:201], v[200:201], %1\n\tv_pk_mul_f32 v[202:203], v[202:203], %1\n\ts_waitcnt lgkmcnt(15)\n\tv_pk_fma_f32 v[200:201], v[148:149], %2, v[200:201]\n\tv_pk_fma_f32 v[202:203], v[150:151], %2, v[202:203]\n\tv_cvt_pk_bf16_f32 v200, v200, v201\n\tv_cvt_pk_bf16_f32 v201, v202, v203\n\tds_write_b64 v204, v[200:201]\n\tv_accvgpr_read_b32 v192, a40\n\tv_accvgpr_read_b32 v193, a41\n\tv_accvgpr_read_b32 v194, a42\n\tv_accvgpr_read_b32 v195, a43\n\tv_xor_b32_e32 v196, 96, %3\n\tv_pk_mul_f32 v[192:193], v[192:193], %1\n\tv_pk_mul_f32 v[194:195], v[194:195], %1\n\ts_waitcnt lgkmcnt(15)\n\tv_pk_fma_f32 v[192:193], v[152:153], %2, v[192:193]\n\tv_pk_fma_f32 v[194:195], v[154:155], %2, v[194:195]\n\tv_cvt_pk_bf16_f32 v192, v192, v193\n\tv_cvt_pk_bf16_f32 v193, v194, v195\n\tds_write_b64 v196, v[192:193]\n\tv_accvgpr_read_b32 v200, a44\n\tv_accvgpr_read_b32 v201, a45\n\tv_accvgpr_read_b32 v202, a46\n\tv_accvgpr_read_b32 v203, a47\n\tv_xor_b32_e32 v204, 112, %3\n\tv_pk_mul_f32 v[200:201], v[200:201], %1\n\tv_pk_mul_f32 v[202:203], v[202:203], %1\n\ts_waitcnt lgkmcnt(15)\n\tv_pk_fma_f32 v[200:201], v[156:157], %2, v[200:201]\n\tv_pk_fma_f32 v[202:203], v[158:159], %2, v[202:203]\n\tv_cvt_pk_bf16_f32 v200, v200, v201\n\tv_cvt_pk_bf16_f32 v201, v202, v203\n\tds_write_b64 v204, v[200:201]\n\tv_accvgpr_read_b32 v192, a64\n\tv_accvgpr_read_b32 v193, a65\n\tv_accvgpr_read_b32 v194, a66\n\tv_accvgpr_read_b32 v195, a67\n\tv_xor_b32_e32 v196, 128, %3\n\tv_pk_mul_f32 v[192:193], v[192:193], %1\n\tv_pk_mul_f32 v[194:195], v[194:195], %1\n\ts_waitcnt lgkmcnt(15)\n\tv_pk_fma_f32 v[192:193], v[160:161], %2, v[192:193]\n\tv_pk_fma_f32 v[194:195], v[162:163], %2, v[194:195]\n\tv_cvt_pk_bf16_f32 v192, v192, v193\n\tv_cvt_pk_bf16_f32 v193, v194, v195\n\tds_write_b64 v196, v[192:193]\n\tv_accvgpr_read_b32 v200, a68\n\tv_accvgpr_read_b32 v201, a69\n\tv_accvgpr_read_b32 v202, a70\n\tv_accvgpr_read_b32 v203, a71\n\tv_xor_b32_e32 v204, 144, %3\n\tv_pk_mul_f32 v[200:201], v[200:201], %1\n\tv_pk_mul_f32 v[202:203], v[202:203], %1\n\ts_waitcnt lgkmcnt(15)\n\tv_pk_fma_f32 v[200:201], v[164:165], %2, v[200:201]\n\tv_pk_fma_f32 v[202:203], v[166:167], %2, v[202:203]\n\tv_cvt_pk_bf16_f32 v200, v200, v201\n\tv_cvt_pk_bf16_f32 v201, v202, v203\n\tds_write_b64 v204, v[200:201]\n\tv_accvgpr_read_b32 v192, a72\n\tv_accvgpr_read_b32 v193, a73\n\tv_accvgpr_read_b32 v194, a74\n\tv_accvgpr_read_b32 v195, a75\n\tv_xor_b32_e32 v196, 160, %3\n\tv_pk_mul_f32 v[192:193], v[192:193], %1\n\tv_pk_mul_f32 v[194:195], v[194:195], %1\n\ts_waitcnt lgkmcnt(15)\n\tv_pk_fma_f32 v[192:193], v[168:169], %2, v[192:193]\n\tv_pk_fma_f32 v[194:195], v[170:171], %2, v[194:195]\n\tv_cvt_pk_bf16_f32 v192, v192, v193\n\tv_cvt_pk_bf16_f32 v193, v194, v195\n\tds_write_b64 v196, v[192:193]\n\tv_accvgpr_read_b32 v200, a76\n\tv_accvgpr_read_b32 v201, a77\n\tv_accvgpr_read_b32 v202, a78\n\tv_accvgpr_read_b32 v203, a79\n\tv_xor_b32_e32 v204, 176, %3\n\tv_pk_mul_f32 v[200:201], v[200:201], %1\n\tv_pk_mul_f32 v[202:203], v[202:203], %1\n\ts_waitcnt lgkmcnt(15)\n\tv_pk_fma_f32 v[200:201], v[172:173], %2, v[200:201]\n\tv_pk_fma_f32 v[202:203], v[174:175], %2, v[202:203]\n\tv_cvt_pk_bf16_f32 v200, v200, v201\n\tv_cvt_pk_bf16_f32 v201, v202, v203\n\tds_write_b64 v204, v[200:201]\n\tv_accvgpr_read_b32 v192, a96\n\tv_accvgpr_read_b32 v193, a97\n\tv_accvgpr_read_b32 v194, a98\n\tv_accvgpr_read_b32 v195, a99\n\tv_xor_b32_e32 v196, 192, %3\n\tv_pk_mul_f32 v[192:193], v[192:193], %1\n\tv_pk_mul_f32 v[194:195], v[194:195], %1\n\ts_waitcnt lgkmcnt(15)\n\tv_pk_fma_f32 v[192:193], v[176:177], %2, v[192:193]\n\tv_pk_fma_f32 v[194:195], v[178:179], %2, v[194:195]\n\tv_cvt_pk_bf16_f32 v192, v192, v193\n\tv_cvt_pk_bf16_f32 v193, v194, v195\n\tds_write_b64 v196, v[192:193]\n\tv_accvgpr_read_b32 v200, a100\n\tv_accvgpr_read_b32 v201, a101\n\tv_accvgpr_read_b32 v202, a102\n\tv_accvgpr_read_b32 v203, a103\n\tv_xor_b32_e32 v204, 208, %3\n\tv_pk_mul_f32 v[200:201], v[200:201], %1\n\tv_pk_mul_f32 v[202:203], v[202:203], %1\n\ts_waitcnt lgkmcnt(15)\n\tv_pk_fma_f32 v[200:201], v[180:181], %2, v[200:201]\n\tv_pk_fma_f32 v[202:203], v[182:183], %2, v[202:203]\n\tv_cvt_pk_bf16_f32 v200, v200, v201\n\tv_cvt_pk_bf16_f32 v201, v202, v203\n\tds_write_b64 v204, v[200:201]\n\tv_accvgpr_read_b32 v192, a104\n\tv_accvgpr_read_b32 v193, a105\n\tv_accvgpr_read_b32 v194, a106\n\tv_accvgpr_read_b32 v195, a107\n\tv_xor_b32_e32 v196, 224, %3\n\tv_pk_mul_f32 v[192:193], v[192:193], %1\n\tv_pk_mul_f32 v[194:195], v[194:195], %1\n\ts_waitcnt lgkmcnt(15)\n\tv_pk_fma_f32 v[192:193], v[184:185], %2, v[192:193]\n\tv_pk_fma_f32 v[194:195], v[186:187], %2, v[194:195]\n\tv_cvt_pk_bf16_f32 v192, v192, v193\n\tv_cvt_pk_bf16_f32 v193, v194, v195\n\tds_write_b64 v196, v[192:193]\n\tv_accvgpr_read_b32 v200, a108\n\tv_accvgpr_read_b32 v201, a109\n\tv_accvgpr_read_b32 v202, a110\n\tv_accvgpr_read_b32 v203, a111\n\tv_xor_b32_e32 v204, 240, %3\n\tv_pk_mul_f32 v[200:201], v[200:201], %1\n\tv_pk_mul_f32 v[202:203], v[202:203], %1\n\ts_waitcnt lgkmcnt(15)\n\tv_pk_fma_f32 v[200:201], v[188:189], %2, v[200:201]\n\tv_pk_fma_f32 v[202:203], v[190:191], %2, v[202:203]\n\tv_cvt_pk_bf16_f32 v200, v200, v201\n\tv_cvt_pk_bf16_f32 v201, v202, v203\n\tds_write_b64 v204, v[200:201]" :  : "v"(src), "v"(f1), "v"(f2), "v"(eb) : "memory");
}
DI void g_merge1(const unsigned src, const f32x2 f1, const f32x2 f2, const unsigned eb) {
  asm volatile("ds_read_b128 v[128:131], %0 offset:0\n\tds_read_b128 v[132:135], %0 offset:1024\n\tds_read_b128 v[136:139], %0 offset:2048\n\tds_read_b128 v[140:143], %0 offset:3072\n\tds_read_b128 v[144:147], %0 offset:4096\n\tds_read_b128 v[148:151], %0 offset:5120\n\tds_read_b128 v[152:155], %0 offset:6144\n\tds_read_b128 v[156:159], %0 offset:7168\n\tds_read_b128 v[160:163], %0 offset:8192\n\tds_read_b128 v[164:167], %0 offset:9216\n\tds_read_b128 v[168:171], %0 offset:10240\n\tds_read_b128 v[172:175], %0 offset:11264\n\tds_read_b128 v[176:179], %0 offset:12288\n\tds_read_b128 v[180:183], %0 offset:13312\n\tds_read_b128 v[184:187], %0 offset:14336\n\tds_read_b128 v[188:191], %0 offset:15360\n\tv_accvgpr_read_b32 v192, a16\n\tv_accvgpr_read_b32 v193, a17\n\tv_accvgpr_read_b32 v194, a18\n\tv_accvgpr_read_b32 v195, a19\n\tv_xor_b32_e32 v196, 0, %3\n\tv_pk_mul_f32 v[192:193], v[192:193], %1\n\tv_pk_mul_f32 v[194:195], v[194:195], %1\n\ts_waitcnt lgkmcnt(15)\n\tv_pk_fma_f32 v[192:193], v[128:129], %2, v[192:193]\n\tv_pk_fma_f32 v[194:195], v[130:131], %2, v[194:195]\n\tv_cvt_pk_bf16_f32 v192, v192, v193\n\tv_cvt_pk_bf16_f32 v193, v194, v195\n\tds_write_b64 v196, v[192:193]\n\tv_accvgpr_read_b32 v200, a20\n\tv_accvgpr_read_b32 v201, a21\n\tv_accvgpr_read_b32 v202, a22\n\tv_accvgpr_read_b32 v203, a23\n\tv_xor_b32_e32 v204, 16, %3\n\tv_pk_mul_f32 v[200:201], v[200:201], %1\n\tv_pk_mul_f32 v[202:203], v[202:203], %1\n\ts_waitcnt lgkmcnt(15)\n\tv_pk_fma_f32 v[200:201], v[132:133], %2, v[200:201]\n\tv_pk_fma_f32 v[202:203], v[134:135], %2, v[202:203]\n\tv_cvt_pk_bf16_f32 v200, v200, v201\n\tv_cvt_pk_bf16_f32 v201, v202, v203\n\tds_write_b64 v204, v[200:201]\n\tv_accvgpr_read_b32 v192, a24\n\tv_accvgpr_read_b32 v193, a25\n\tv_accvgpr_read_b32 v194, a26\n\tv_accvgpr_read_b32 v195, a27\n\tv_xor_b32_e32 v196, 32, %3\n\tv_pk_mul_f32 v[192:193], v[192:193], %1\n\tv_pk_mul_f32 v[194:195], v[194:195], %1\n\ts_waitcnt lgkmcnt(15)\n\tv_pk_fma_f32 v[192:193], v[136:137], %2, v[192:193]\n\tv_pk_fma_f32 v[194:195], v[138:139], %2, v[194:195]\n\tv_cvt_pk_bf16_f32 v192, v192, v193\n\tv_cvt_pk_bf16_f32 v193, v194, v195\n\tds_write_b64 v196, v[192:193]\n\tv_accvgpr_read_b32 v200, a28\n\tv_accvgpr_read_b32 v201, a29\n\tv_accvgpr_read_b32 v202, a30\n\tv_accvgpr_read_b32 v203, a31\n\tv_xor_b32_e32 v204, 48, %3\n\tv_pk_mul_f32 v[200:201], v[200:201], %1\n\tv_pk_mul_f32 v[202:203], v[202:203], %1\n\ts_waitcnt lgkmcnt(15)\n\tv_pk_fma_f32 v[200:201], v[140:141], %2, v[200:201]\n\tv_pk_fma_f32 v[202:203], v[142:143], %2, v[202:203]\n\tv_cvt_pk_bf16_f32 v200, v200, v201\n\tv_cvt_pk_bf16_f32 v201, v202, v203\n\tds_write_b64 v204, v[200:201]\n\tv_accvgpr_read_b32 v192, a48\n\tv_accvgpr_read_b32 v193, a49\n\tv_accvgpr_read_b32 v194, a50\n\tv_accvgpr_read_b32 v195, a51\n\tv_xor_b32_e32 v196, 64, %3\n\tv_pk_mul_f32 v[192:193], v[192:193], %1\n\tv_pk_mul_f32 v[194:195], v[194:195], %1\n\ts_waitcnt lgkmcnt(15)\n\tv_pk_fma_f32 v[192:193], v[144:145], %2, v[192:193]\n\tv_pk_fma_f32 v[194:195], v[146:147], %2, v[194:195]\n\tv_cvt_pk_bf16_f32 v192, v192, v193\n\tv_cvt_pk_bf16_f32 v193, v194, v195\n\tds_write_b64 v196, v[192:193]\n\tv_accvgpr_read_b32 v200, a52\n\tv_accvgpr_read_b32 v201, a53\n\tv_accvgpr_read_b32 v202, a54\n\tv_accvgpr_read_b32 v203, a55\n\tv_xor_b32_e32 v204, 80, %3\n\tv_pk_mul_f32 v[200:201], v[200:201], %1\n\tv_pk_mul_f32 v[202:203], v[202:203], %1\n\ts_waitcnt lgkmcnt(15)\n\tv_pk_fma_f32 v[200:201], v[148:149], %2, v[200:201]\n\tv_pk_fma_f32 v[202:203], v[150:151], %2, v[202:203]\n\tv_cvt_pk_bf16_f32 v200, v200, v201\n\tv_cvt_pk_bf16_f32 v201, v202, v203\n\tds_write_b64 v204, v[200:201]\n\tv_accvgpr_read_b32 v192, a56\n\tv_accvgpr_read_b32 v193, a57\n\tv_accvgpr_read_b32 v194, a58\n\tv_accvgpr_read_b32 v195, a59\n\tv_xor_b32_e32 v196, 96, %3\n\tv_pk_mul_f32 v[192:193], v[192:193], %1\n\tv_pk_mul_f32 v[194:195], v[194:195], %1\n\ts_waitcnt lgkmcnt(15)\n\tv_pk_fma_f32 v[192:193], v[152:153], %2, v[192:193]\n\tv_pk_fma_f32 v[194:195], v[154:155], %2, v[194:195]\n\tv_cvt_pk_bf16_f32 v192, v192, v193\n\tv_cvt_pk_bf16_f32 v193, v194, v195\n\tds_write_b64 v196, v[192:193]\n\tv_accvgpr_read_b32 v200, a60\n\tv_accvgpr_read_b32 v201, a61\n\tv_accvgpr_read_b32 v202, a62\n\tv_accvgpr_read_b32 v203, a63\n\tv_xor_b32_e32 v204, 112, %3\n\tv_pk_mul_f32 v[200:201], v[200:201], %1\n\tv_pk_mul_f32 v[202:203], v[202:203], %1\n\ts_waitcnt lgkmcnt(15)\n\tv_pk_fma_f32 v[200:201], v[156:157], %2, v[200:201]\n\tv_pk_fma_f32 v[202:203], v[158:159], %2, v[202:203]\n\tv_cvt_pk_bf16_f32 v200, v200, v201\n\tv_cvt_pk_bf16_f32 v201, v202, v203\n\tds_write_b64 v204, v[200:201]\n\tv_accvgpr_read_b32 v192, a80\n\tv_accvgpr_read_b32 v193, a81\n\tv_accvgpr_read_b32 v194, a82\n\tv_accvgpr_read_b32 v195, a83\n\tv_xor_b32_e32 v196, 128, %3\n\tv_pk_mul_f32 v[192:193], v[192:193], %1\n\tv_pk_mul_f32 v[194:195], v[194:195], %1\n\ts_waitcnt lgkmcnt(15)\n\tv_pk_fma_f32 v[192:193], v[160:161], %2, v[192:193]\n\tv_pk_fma_f32 v[194:195], v[162:163], %2, v[194:195]\n\tv_cvt_pk_bf16_f32 v192, v192, v193\n\tv_cvt_pk_bf16_f32 v193, v194, v195\n\tds_write_b64 v196, v[192:193]\n\tv_accvgpr_read_b32 v200, a84\n\tv_accvgpr_read_b32 v201, a85\n\tv_accvgpr_read_b32 v202, a86\n\tv_accvgpr_read_b32 v203, a87\n\tv_xor_b32_e32 v204, 144, %3\n\tv_pk_mul_f32 v[200:201], v[200:201], %1\n\tv_pk_mul_f32 v[202:203], v[202:203], %1\n\ts_waitcnt lgkmcnt(15)\n\tv_pk_fma_f32 v[200:201], v[164:165], %2, v[200:201]\n\tv_pk_fma_f32 v[202:203], v[166:167], %2, v[202:203]\n\tv_cvt_pk_bf16_f32 v200, v200, v201\n\tv_cvt_pk_bf16_f32 v201, v202, v203\n\tds_write_b64 v204, v[200:201]\n\tv_accvgpr_read_b32 v192, a88\n\tv_accvgpr_read_b32 v193, a89\n\tv_accvgpr_read_b32 v194, a90\n\tv_accvgpr_read_b32 v195, a91\n\tv_xor_b32_e32 v196, 160, %3\n\tv_pk_mul_f32 v[192:193], v[192:193], %1\n\tv_pk_mul_f32 v[194:195], v[194:195], %1\n\ts_waitcnt lgkmcnt(15)\n\tv_pk_fma_f32 v[192:193], v[168:169], %2, v[192:193]\n\tv_pk_fma_f32 v[194:195], v[170:171], %2, v[194:195]\n\tv_cvt_pk_bf16_f32 v192, v192, v193\n\tv_cvt_pk_bf16_f32 v193, v194, v195\n\tds_write_b64 v196, v[192:193]\n\tv_accvgpr_read_b32 v200, a92\n\tv_accvgpr_read_b32 v201, a93\n\tv_accvgpr_read_b32 v202, a94\n\tv_accvgpr_read_b32 v203, a95\n\tv_xor_b32_e32 v204, 176, %3\n\tv_pk_mul_f32 v[200:201], v[200:201], %1\n\tv_pk_mul_f32 v[202:203], v[202:203], %1\n\ts_waitcnt lgkmcnt(15)\n\tv_pk_fma_f32 v[200:201], v[172:173], %2, v[200:201]\n\tv_pk_fma_f32 v[202:203], v[174:175], %2, v[202:203]\n\tv_cvt_pk_bf16_f32 v200, v200, v201\n\tv_cvt_pk_bf16_f32 v201, v202, v203\n\tds_write_b64 v204, v[200:201]\n\tv_accvgpr_read_b32 v192, a112\n\tv_accvgpr_read_b32 v193, a113\n\tv_accvgpr_read_b32 v194, a114\n\tv_accvgpr_read_b32 v195, a115\n\tv_xor_b32_e32 v196, 192, %3\n\tv_pk_mul_f32 v[192:193], v[192:193], %1\n\tv_pk_mul_f32 v[194:195], v[194:195], %1\n\ts_waitcnt lgkmcnt(15)\n\tv_pk_fma_f32 v[192:193], v[176:177], %2, v[192:193]\n\tv_pk_fma_f32 v[194:195], v[178:179], %2, v[194:195]\n\tv_cvt_pk_bf16_f32 v192, v192, v193\n\tv_cvt_pk_bf16_f32 v193, v194, v195\n\tds_write_b64 v196, v[192:193]\n\tv_accvgpr_read_b32 v200, a116\n\tv_accvgpr_read_b32 v201, a117\n\tv_accvgpr_read_b32 v202, a118\n\tv_accvgpr_read_b32 v203, a119\n\tv_xor_b32_e32 v204, 208, %3\n\tv_pk_mul_f32 v[200:201], v[200:201], %1\n\tv_pk_mul_f32 v[202:203], v[202:203], %1\n\ts_waitcnt lgkmcnt(15)\n\tv_pk_fma_f32 v[200:201], v[180:181], %2, v[200:201]\n\tv_pk_fma_f32 v[202:203], v[182:183], %2, v[202:203]\n\tv_cvt_pk_bf16_f32 v200, v200, v201\n\tv_cvt_pk_bf16_f32 v201, v202, v203\n\tds_write_b64 v204, v[200:201]\n\tv_accvgpr_read_b32 v192, a120\n\tv_accvgpr_read_b32 v193, a121\n\tv_accvgpr_read_b32 v194, a122\n\tv_accvgpr_read_b32 v195, a123\n\tv_xor_b32_e32 v196, 224, %3\n\tv_pk_mul_f32 v[192:193], v[192:193], %1\n\tv_pk_mul_f32 v[194:195], v[194:195], %1\n\ts_waitcnt lgkmcnt(15)\n\tv_pk_fma_f32 v[192:193], v[184:185], %2, v[192:193]\n\tv_pk_fma_f32 v[194:195], v[186:187], %2, v[194:195]\n\tv_cvt_pk_bf16_f32 v192, v192, v193\n\tv_cvt_pk_bf16_f32 v193, v194, v195\n\tds_write_b64 v196, v[192:193]\n\tv_accvgpr_read_b32 v200, a124\n\tv_accvgpr_read_b32 v201, a125\n\tv_accvgpr_read_b32 v202, a126\n\tv_accvgpr_read_b32 v203, a127\n\tv_xor_b32_e32 v204, 240, %3\n\tv_pk_mul_f32 v[200:201], v[200:201], %1\n\tv_pk_mul_f32 v[202:203], v[202:203], %1\n\ts_waitcnt lgkmcnt(15)\n\tv_pk_fma_f32 v[200:201], v[188:189], %2, v[200:201]\n\tv_pk_fma_f32 v[202:203], v[190:191], %2, v[202:203]\n\tv_cvt_pk_bf16_f32 v200, v200, v201\n\tv_cvt_pk_bf16_f32 v201, v202, v203\n\tds_write_b64 v204, v[200:201]" :  : "v"(src), "v"(f1), "v"(f2), "v"(eb) : "memory");
}
DI void g_ostore(const unsigned (&sr)[4], const unsigned svo, const srd_t sctx, const unsigned sso) {
  unsigned so484;
  unsigned so485;
  unsigned so486;
  unsigned so487;
  unsigned so488;
  unsigned so489;
  unsigned so490;
  unsigned so491;
  asm volatile("s_waitcnt lgkmcnt(0)\n\tds_read_b128 v[128:131], %8 offset:0\n\tds_read_b128 v[132:135], %9 offset:0\n\tds_read_b128 v[136:139], %10 offset:0\n\tds_read_b128 v[140:143], %11 offset:0\n\tds_read_b128 v[144:147], %8 offset:4096\n\tds_read_b128 v[148:151], %9 offset:4096\n\tds_read_b128 v[152:155], %10 offset:4096\n\tds_read_b128 v[156:159], %11 offset:4096\n\ts_add_u32 %0, %12, 0\n\ts_waitcnt lgkmcnt(7)\n\tbuffer_store_dwordx4 v[128:131], %13, %14, %0 offen sc1\n\ts_add_u32 %1, %12, 32768\n\ts_waitcnt lgkmcnt(6)\n\tbuffer_store_dwordx4 v[132:135], %13, %14, %1 offen sc1\n\ts_add_u32 %2, %12, 65536\n\ts_waitcnt lgkmcnt(5)\n\tbuffer_store_dwordx4 v[136:139], %13, %14, %2 offen sc1\n\ts_add_u32 %3, %12, 98304\n\ts_waitcnt lgkmcnt(4)\n\tbuffer_store_dwordx4 v[140:143], %13, %14, %3 offen sc1\n\ts_add_u32 %4, %12, 131072\n\ts_waitcnt lgkmcnt(3)\n\tbuffer_store_dwordx4 v[144:147], %13, %14, %4 offen sc1\n\ts_add_u32 %5, %12, 163840\n\ts_waitcnt lgkmcnt(2)\n\tbuffer_store_dwordx4 v[148:151], %13, %14, %5 offen sc1\n\ts_add_u32 %6, %12, 196608\n\ts_waitcnt lgkmcnt(1)\n\tbuffer_store_dwordx4 v[152:155], %13, %14, %6 offen sc1\n\ts_add_u32 %7, %12, 229376\n\ts_waitcnt lgkmcnt(0)\n\tbuffer_store_dwordx4 v[156:159], %13, %14, %7 offen sc1" : "=&s"(so484), "=&s"(so485), "=&s"(so486), "=&s"(so487), "=&s"(so488), "=&s"(so489), "=&s"(so490), "=&s"(so491) : "v"(sr[0]), "v"(sr[1]), "v"(sr[2]), "v"(sr[3]), "s"(sso), "v"(svo), "s"(sctx) : "memory", "scc");
}
DI void o3_stage0(OC& c, const int s3, const int ns, const unsigned soa, const unsigned sob) {
  asm volatile("s_waitcnt lgkmcnt(3)\n\tv_mfma_f32_32x32x16_bf16 v[128:143], v[72:75], v[64:67], v[128:143]\n\tds_read_b64_tr_b16 v[88:89], %0 offset:20480\n\tds_read_b64_tr_b16 v[90:91], %0 offset:21504\n\ts_cmp_lt_i32 %1, %2\n\ts_cbranch_scc0 .Lo492_%=\n\ts_add_u32 m0, %3, 98304\n\ts_nop 0\n\tbuffer_load_dwordx4 %4, %5, %6 offen offset:0 sc1 lds\n\t.Lo492_%=:" :  : "v"(c.b_rd[0][0]), "s"(s3), "s"(ns), "s"(c.m0w), "v"(c.va[0]), "s"(c.sa), "s"(soa) : "memory", "m0", "scc");
  asm volatile("s_waitcnt lgkmcnt(3)\n\tv_mfma_f32_32x32x16_bf16 v[144:159], v[76:79], v[64:67], v[144:159]\n\tds_read_b128 v[80:83], %0 offset:0\n\tds_read_b64_tr_b16 v[92:93], %1 offset:20480" :  : "v"(c.a_rd[0][1]), "v"(c.b_rd[0][1]) : "memory");
  asm volatile("s_waitcnt lgkmcnt(4)\n\tv_mfma_f32_32x32x16_bf16 v[160:175], v[72:75], v[68:71], v[160:175]\n\tds_read_b64_tr_b16 v[94:95], %0 offset:21504\n\ts_cmp_lt_i32 %1, %2\n\ts_cbranch_scc0 .Lo493_%=\n\ts_add_u32 m0, %3, 99328\n\ts_nop 0\n\tbuffer_load_dwordx4 %4, %5, %6 offen offset:0 sc1 lds\n\t.Lo493_%=:" :  : "v"(c.b_rd[0][1]), "s"(s3), "s"(ns), "s"(c.m0w), "v"(c.va[1]), "s"(c.sa), "s"(soa) : "memory", "m0", "scc");
  asm volatile("v_mfma_f32_32x32x16_bf16 v[176:191], v[76:79], v[68:71], v[176:191]\n\tds_read_b128 v[84:87], %0 offset:4096" :  : "v"(c.a_rd[0][1]) : "memory");
  asm volatile("s_waitcnt lgkmcnt(3)\n\tv_mfma_f32_32x32x16_bf16 v[128:143], v[88:91], v[80:83], v[128:143]\n\tds_read_b64_tr_b16 v[72:73], %0 offset:24576\n\tds_read_b64_tr_b16 v[74:75], %0 offset:25600\n\ts_cmp_lt_i32 %1, %2\n\ts_cbranch_scc0 .Lo494_%=\n\ts_add_u32 m0, %3, 100352\n\ts_nop 0\n\tbuffer_load_dwordx4 %4, %5, %6 offen offset:0 sc1 lds\n\t.Lo494_%=:" :  : "v"(c.b_rd[0][0]), "s"(s3), "s"(ns), "s"(c.m0w), "v"(c.va[2]), "s"(c.sa), "s"(soa) : "memory", "m0", "scc", ACL64);
  asm volatile("s_waitcnt lgkmcnt(3)\n\tv_mfma_f32_32x32x16_bf16 v[144:159], v[92:95], v[80:83], v[144:159]\n\tds_read_b128 v[64:67], %0 offset:0\n\tds_read_b64_tr_b16 v[76:77], %1 offset:24576" :  : "v"(c.a_rd[0][2]), "v"(c.b_rd[0][1]) : "memory");
  asm volatile("s_waitcnt lgkmcnt(4)\n\tv_mfma_f32_32x32x16_bf16 v[160:175], v[88:91], v[84:87], v[160:175]\n\tds_read_b64_tr_b16 v[78:79], %0 offset:25600\n\ts_cmp_lt_i32 %1, %2\n\ts_cbranch_scc0 .Lo495_%=\n\ts_add_u32 m0, %3, 101376\n\ts_nop 0\n\tbuffer_load_dwordx4 %4, %5, %6 offen offset:0 sc1 lds\n\t.Lo495_%=:" :  : "v"(c.b_rd[0][1]), "s"(s3), "s"(ns), "s"(c.m0w), "v"(c.va[3]), "s"(c.sa), "s"(soa) : "memory", "m0", "scc");
  asm volatile("v_mfma_f32_32x32x16_bf16 v[176:191], v[92:95], v[84:87], v[176:191]\n\tds_read_b128 v[68:71], %0 offset:4096" :  : "v"(c.a_rd[0][2]) : "memory");
  asm volatile("s_waitcnt lgkmcnt(3)\n\tv_mfma_f32_32x32x16_bf16 v[128:143], v[72:75], v[64:67], v[128:143]\n\tds_read_b64_tr_b16 v[88:89], %0 offset:28672\n\tds_read_b64_tr_b16 v[90:91], %0 offset:29696\n\ts_cmp_lt_i32 %1, %2\n\ts_cbranch_scc0 .Lo496_%=\n\ts_add_u32 m0, %3, 114688\n\ts_nop 0\n\tbuffer_load_dwordx4 %4, %5, %6 offen offset:0 lds\n\t.Lo496_%=:" :  : "v"(c.b_rd[0][0]), "s"(s3), "s"(ns), "s"(c.m0w), "v"(c.vb), "s"(c.sb), "s"(sob) : "memory", "m0", "scc");
  asm volatile("s_waitcnt lgkmcnt(3)\n\tv_mfma_f32_32x32x16_bf16 v[144:159], v[76:79], v[64:67], v[144:159]\n\tds_read_b128 v[80:83], %0 offset:0\n\tds_read_b64_tr_b16 v[92:93], %1 offset:28672" :  : "v"(c.a_rd[0][3]), "v"(c.b_rd[0][1]) : "memory");
  unsigned so498;
  asm volatile("s_waitcnt lgkmcnt(4)\n\tv_mfma_f32_32x32x16_bf16 v[160:175], v[72:75], v[68:71], v[160:175]\n\tds_read_b64_tr_b16 v[94:95], %1 offset:29696\n\ts_cmp_lt_i32 %2, %3\n\ts_cbranch_scc0 .Lo497_%=\n\ts_add_u32 m0, %4, 115712\n\ts_add_u32 %0, %7, 4096\n\tbuffer_load_dwordx4 %5, %6, %0 offen offset:0 lds\n\t.Lo497_%=:" : "=&s"(so498) : "v"(c.b_rd[0][1]), "s"(s3), "s"(ns), "s"(c.m0w), "v"(c.vb), "s"(c.sb), "s"(sob) : "memory", "m0", "scc");
  asm volatile("v_mfma_f32_32x32x16_bf16 v[176:191], v[76:79], v[68:71], v[176:191]\n\tds_read_b128 v[84:87], %0 offset:4096" :  : "v"(c.a_rd[0][3]) : "memory");
  unsigned so500;
  asm volatile("s_waitcnt lgkmcnt(3)\n\tv_mfma_f32_32x32x16_bf16 v[128:143], v[88:91], v[80:83], v[128:143]\n\tds_read_b64_tr_b16 v[72:73], %1 offset:49152\n\tds_read_b64_tr_b16 v[74:75], %1 offset:50176\n\ts_cmp_lt_i32 %2, %3\n\ts_cbranch_scc0 .Lo499_%=\n\ts_add_u32 m0, %4, 116736\n\ts_add_u32 %0, %7, 8192\n\tbuffer_load_dwordx4 %5, %6, %0 offen offset:0 lds\n\t.Lo499_%=:" : "=&s"(so500) : "v"(c.b_rd[0][0]), "s"(s3), "s"(ns), "s"(c.m0w), "v"(c.vb), "s"(c.sb), "s"(sob) : "memory", "m0", "scc", ACL64);
  asm volatile("s_waitcnt lgkmcnt(3)\n\tv_mfma_f32_32x32x16_bf16 v[144:159], v[92:95], v[80:83], v[144:159]\n\tds_read_b128 v[64:67], %0 offset:32768\n\tds_read_b64_tr_b16 v[76:77], %1 offset:49152" :  : "v"(c.a_rd[0][0]), "v"(c.b_rd[0][1]) : "memory");
  unsigned so502;
  asm volatile("s_waitcnt lgkmcnt(4)\n\tv_mfma_f32_32x32x16_bf16 v[160:175], v[88:91], v[84:87], v[160:175]\n\tds_read_b64_tr_b16 v[78:79], %1 offset:50176\n\ts_cmp_lt_i32 %2, %3\n\ts_cbranch_scc0 .Lo501_%=\n\ts_add_u32 m0, %4, 117760\n\ts_add_u32 %0, %7, 12288\n\tbuffer_load_dwordx4 %5, %6, %0 offen offset:0 lds\n\t.Lo501_%=:" : "=&s"(so502) : "v"(c.b_rd[0][1]), "s"(s3), "s"(ns), "s"(c.m0w), "v"(c.vb), "s"(c.sb), "s"(sob) : "memory", "m0", "scc");
  asm volatile("v_mfma_f32_32x32x16_bf16 v[176:191], v[92:95], v[84:87], v[176:191]\n\tds_read_b128 v[68:71], %0 offset:36864" :  : "v"(c.a_rd[0][0]) : "memory");
}
DI void o3_dmaA0(OC& c, const unsigned soa) {
  asm volatile("s_add_u32 m0, %0, 0\n\ts_nop 0\n\tbuffer_load_dwordx4 %1, %2, %3 offen offset:0 sc1 lds" :  : "s"(c.m0w), "v"(c.va[0]), "s"(c.sa), "s"(soa) : "memory", "m0", "scc");
  asm volatile("s_add_u32 m0, %0, 1024\n\ts_nop 0\n\tbuffer_load_dwordx4 %1, %2, %3 offen offset:0 sc1 lds" :  : "s"(c.m0w), "v"(c.va[1]), "s"(c.sa), "s"(soa) : "memory", "m0", "scc");
  asm volatile("s_add_u32 m0, %0, 2048\n\ts_nop 0\n\tbuffer_load_dwordx4 %1, %2, %3 offen offset:0 sc1 lds" :  : "s"(c.m0w), "v"(c.va[2]), "s"(c.sa), "s"(soa) : "memory", "m0", "scc");
  asm volatile("s_add_u32 m0, %0, 3072\n\ts_nop 0\n\tbuffer_load_dwordx4 %1, %2, %3 offen offset:0 sc1 lds" :  : "s"(c.m0w), "v"(c.va[3]), "s"(c.sa), "s"(soa) : "memory", "m0", "scc");
}
DI void o3_dmaB0(OC& c, const unsigned sob) {
  asm volatile("s_add_u32 m0, %0, 16384\n\ts_nop 0\n\tbuffer_load_dwordx4 %1, %2, %3 offen offset:0 lds" :  : "s"(c.m0w), "v"(c.vb), "s"(c.sb), "s"(sob) : "memory", "m0", "scc", ACL64);
  unsigned so503;
  asm volatile("s_add_u32 m0, %1, 17408\n\ts_add_u32 %0, %4, 4096\n\tbuffer_load_dwordx4 %2, %3, %0 offen offset:0 lds" : "=&s"(so503) : "s"(c.m0w), "v"(c.vb), "s"(c.sb), "s"(sob) : "memory", "m0", "scc");
  unsigned so504;
  asm volatile("s_add_u32 m0, %1, 18432\n\ts_add_u32 %0, %4, 8192\n\tbuffer_load_dwordx4 %2, %3, %0 offen offset:0 lds" : "=&s"(so504) : "s"(c.m0w), "v"(c.vb), "s"(c.sb), "s"(sob) : "memory", "m0", "scc");
  unsigned so505;
  asm volatile("s_add_u32 m0, %1, 19456\n\ts_add_u32 %0, %4, 12288\n\tbuffer_load_dwordx4 %2, %3, %0 offen offset:0 lds" : "=&s"(so505) : "s"(c.m0w), "v"(c.vb), "s"(c.sb), "s"(sob) : "memory", "m0", "scc");
}
DI void o3_stage1(OC& c, const int s3, const int ns, const unsigned soa, const unsigned sob) {
  asm volatile("s_waitcnt lgkmcnt(3)\n\tv_mfma_f32_32x32x16_bf16 v[128:143], v[72:75], v[64:67], v[128:143]\n\tds_read_b64_tr_b16 v[88:89], %0 offset:53248\n\tds_read_b64_tr_b16 v[90:91], %0 offset:54272\n\ts_cmp_lt_i32 %1, %2\n\ts_cbranch_scc0 .Lo506_%=\n\ts_add_u32 m0, %3, 0\n\ts_nop 0\n\tbuffer_load_dwordx4 %4, %5, %6 offen offset:0 sc1 lds\n\t.Lo506_%=:" :  : "v"(c.b_rd[0][0]), "s"(s3), "s"(ns), "s"(c.m0w), "v"(c.va[0]), "s"(c.sa), "s"(soa) : "memory", "m0", "scc");
  asm volatile("s_waitcnt lgkmcnt(3)\n\tv_mfma_f32_32x32x16_bf16 v[144:159], v[76:79], v[64:67], v[144:159]\n\tds_read_b128 v[80:83], %0 offset:32768\n\tds_read_b64_tr_b16 v[92:93], %1 offset:53248" :  : "v"(c.a_rd[0][1]), "v"(c.b_rd[0][1]) : "memory");
  asm volatile("s_waitcnt lgkmcnt(4)\n\tv_mfma_f32_32x32x16_bf16 v[160:175], v[72:75], v[68:71], v[160:175]\n\tds_read_b64_tr_b16 v[94:95], %0 offset:54272\n\ts_cmp_lt_i32 %1, %2\n\ts_cbranch_scc0 .Lo507_%=\n\ts_add_u32 m0, %3, 1024\n\ts_nop 0\n\tbuffer_load_dwordx4 %4, %5, %6 offen offset:0 sc1 lds\n\t.Lo507_%=:" :  : "v"(c.b_rd[0][1]), "s"(s3), "s"(ns), "s"(c.m0w), "v"(c.va[1]), "s"(c.sa), "s"(soa) : "memory", "m0", "scc");
  asm volatile("v_mfma_f32_32x32x16_bf16 v[176:191], v[76:79], v[68:71], v[176:191]\n\tds_read_b128 v[84:87], %0 offset:36864" :  : "v"(c.a_rd[0][1]) : "memory");
  asm volatile("s_waitcnt lgkmcnt(3)\n\tv_mfma_f32_32x32x16_bf16 v[128:143], v[88:91], v[80:83], v[128:143]\n\tds_read_b64_tr_b16 v[72:73], %0 offset:57344\n\tds_read_b64_tr_b16 v[74:75], %0 offset:58368\n\ts_cmp_lt_i32 %1, %2\n\ts_cbranch_scc0 .Lo508_%=\n\ts_add_u32 m0, %3, 2048\n\ts_nop 0\n\tbuffer_load_dwordx4 %4, %5, %6 offen offset:0 sc1 lds\n\t.Lo508_%=:" :  : "v"(c.b_rd[0][0]), "s"(s3), "s"(ns), "s"(c.m0w), "v"(c.va[2]), "s"(c.sa), "s"(soa) : "memory", "m0", "scc", ACL64);
  asm volatile("s_waitcnt lgkmcnt(3)\n\tv_mfma_f32_32x32x16_bf16 v[144:159], v[92:95], v[80:83], v[144:159]\n\tds_read_b128 v[64:67], %0 offset:32768\n\tds_read_b64_tr_b16 v[76:77], %1 offset:57344" :  : "v"(c.a_rd[0][2]), "v"(c.b_rd[0][1]) : "memory");
  asm volatile("s_waitcnt lgkmcnt(4)\n\tv_mfma_f32_32x32x16_bf16 v[160:175], v[88:91], v[84:87], v[160:175]\n\tds_read_b64_tr_b16 v[78:79], %0 offset:58368\n\ts_cmp_lt_i32 %1, %2\n\ts_cbranch_scc0 .Lo509_%=\n\ts_add_u32 m0, %3, 3072\n\ts_nop 0\n\tbuffer_load_dwordx4 %4, %5, %6 offen offset:0 sc1 lds\n\t.Lo509_%=:" :  : "v"(c.b_rd[0][1]), "s"(s3), "s"(ns), "s"(c.m0w), "v"(c.va[3]), "s"(c.sa), "s"(soa) : "memory", "m0", "scc");
  asm volatile("v_mfma_f32_32x32x16_bf16 v[176:191], v[92:95], v[84:87], v[176:191]\n\tds_read_b128 v[68:71], %0 offset:36864" :  : "v"(c.a_rd[0][2]) : "memory");
  asm volatile("s_waitcnt lgkmcnt(3)\n\tv_mfma_f32_32x32x16_bf16 v[128:143], v[72:75], v[64:67], v[128:143]\n\tds_read_b64_tr_b16 v[88:89], %0 offset:61440\n\tds_read_b64_tr_b16 v[90:91], %0 offset:62464\n\ts_cmp_lt_i32 %1, %2\n\ts_cbranch_scc0 .Lo510_%=\n\ts_add_u32 m0, %3, 16384\n\ts_nop 0\n\tbuffer_load_dwordx4 %4, %5, %6 offen offset:0 lds\n\t.Lo510_%=:" :  : "v"(c.b_rd[0][0]), "s"(s3), "s"(ns), "s"(c.m0w), "v"(c.vb), "s"(c.sb), "s"(sob) : "memory", "m0", "scc");
  asm volatile("s_waitcnt lgkmcnt(3)\n\tv_mfma_f32_32x32x16_bf16 v[144:159], v[76:79], v[64:67], v[144:159]\n\tds_read_b128 v[80:83], %0 offset:32768\n\tds_read_b64_tr_b16 v[92:93], %1 offset:61440" :  : "v"(c.a_rd[0][3]), "v"(c.b_rd[0][1]) : "memory");
  unsigned so512;
  asm volatile("s_waitcnt lgkmcnt(4)\n\tv_mfma_f32_32x32x16_bf16 v[160:175], v[72:75], v[68:71], v[160:175]\n\tds_read_b64_tr_b16 v[94:95], %1 offset:62464\n\ts_cmp_lt_i32 %2, %3\n\ts_cbranch_scc0 .Lo511_%=\n\ts_add_u32 m0, %4, 17408\n\ts_add_u32 %0, %7, 4096\n\tbuffer_load_dwordx4 %5, %6, %0 offen offset:0 lds\n\t.Lo511_%=:" : "=&s"(so512) : "v"(c.b_rd[0][1]), "s"(s3), "s"(ns), "s"(c.m0w), "v"(c.vb), "s"(c.sb), "s"(sob) : "memory", "m0", "scc");
  asm volatile("v_mfma_f32_32x32x16_bf16 v[176:191], v[76:79], v[68:71], v[176:191]\n\tds_read_b128 v[84:87], %0 offset:36864" :  : "v"(c.a_rd[0][3]) : "memory");
  unsigned so514;
  asm volatile("s_waitcnt lgkmcnt(3)\n\tv_mfma_f32_32x32x16_bf16 v[128:143], v[88:91], v[80:83], v[128:143]\n\tds_read_b64_tr_b16 v[72:73], %1 offset:16384\n\tds_read_b64_tr_b16 v[74:75], %1 offset:17408\n\ts_cmp_lt_i32 %2, %3\n\ts_cbranch_scc0 .Lo513_%=\n\ts_add_u32 m0, %4, 18432\n\ts_add_u32 %0, %7, 8192\n\tbuffer_load_dwordx4 %5, %6, %0 offen offset:0 lds\n\t.Lo513_%=:" : "=&s"(so514) : "v"(c.b_rd[1][0]), "s"(s3), "s"(ns), "s"(c.m0w), "v"(c.vb), "s"(c.sb), "s"(sob) : "memory", "m0", "scc", ACL64);
  asm volatile("s_waitcnt lgkmcnt(3)\n\tv_mfma_f32_32x32x16_bf16 v[144:159], v[92:95], v[80:83], v[144:159]\n\tds_read_b128 v[64:67], %0 offset:0\n\tds_read_b64_tr_b16 v[76:77], %1 offset:16384" :  : "v"(c.a_rd[1][0]), "v"(c.b_rd[1][1]) : "memory");
  unsigned so516;
  asm volatile("s_waitcnt lgkmcnt(4)\n\tv_mfma_f32_32x32x16_bf16 v[160:175], v[88:91], v[84:87], v[160:175]\n\tds_read_b64_tr_b16 v[78:79], %1 offset:17408\n\ts_cmp_lt_i32 %2, %3\n\ts_cbranch_scc0 .Lo515_%=\n\ts_add_u32 m0, %4, 19456\n\ts_add_u32 %0, %7, 12288\n\tbuffer_load_dwordx4 %5, %6, %0 offen offset:0 lds\n\t.Lo515_%=:" : "=&s"(so516) : "v"(c.b_rd[1][1]), "s"(s3), "s"(ns), "s"(c.m0w), "v"(c.vb), "s"(c.sb), "s"(sob) : "memory", "m0", "scc");
  asm volatile("v_mfma_f32_32x32x16_bf16 v[176:191], v[92:95], v[84:87], v[176:191]\n\tds_read_b128 v[68:71], %0 offset:4096" :  : "v"(c.a_rd[1][0]) : "memory");
}
DI void o3_dmaA1(OC& c, const unsigned soa) {
  asm volatile("s_add_u32 m0, %0, 32768\n\ts_nop 0\n\tbuffer_load_dwordx4 %1, %2, %3 offen offset:0 sc1 lds" :  : "s"(c.m0w), "v"(c.va[0]), "s"(c.sa), "s"(soa) : "memory", "m0", "scc");
  asm volatile("s_add_u32 m0, %0, 33792\n\ts_nop 0\n\tbuffer_load_dwordx4 %1, %2, %3 offen offset:0 sc1 lds" :  : "s"(c.m0w), "v"(c.va[1]), "s"(c.sa), "s"(soa) : "memory", "m0", "scc");
  asm volatile("s_add_u32 m0, %0, 34816\n\ts_nop 0\n\tbuffer_load_dwordx4 %1, %2, %3 offen offset:0 sc1 lds" :  : "s"(c.m0w), "v"(c.va[2]), "s"(c.sa), "s"(soa) : "memory", "m0", "scc");
  asm volatile("s_add_u32 m0, %0, 35840\n\ts_nop 0\n\tbuffer_load_dwordx4 %1, %2, %3 offen offset:0 sc1 lds" :  : "s"(c.m0w), "v"(c.va[3]), "s"(c.sa), "s"(soa) : "memory", "m0", "scc");
}
DI void o3_dmaB1(OC& c, const unsigned sob) {
  asm volatile("s_add_u32 m0, %0, 49152\n\ts_nop 0\n\tbuffer_load_dwordx4 %1, %2, %3 offen offset:0 lds" :  : "s"(c.m0w), "v"(c.vb), "s"(c.sb), "s"(sob) : "memory", "m0", "scc", ACL64);
  unsigned so517;
  asm volatile("s_add_u32 m0, %1, 50176\n\ts_add_u32 %0, %4, 4096\n\tbuffer_load_dwordx4 %2, %3, %0 offen offset:0 lds" : "=&s"(so517) : "s"(c.m0w), "v"(c.vb), "s"(c.sb), "s"(sob) : "memory", "m0", "scc");
  unsigned so518;
  asm volatile("s_add_u32 m0, %1, 51200\n\ts_add_u32 %0, %4, 8192\n\tbuffer_load_dwordx4 %2, %3, %0 offen offset:0 lds" : "=&s"(so518) : "s"(c.m0w), "v"(c.vb), "s"(c.sb), "s"(sob) : "memory", "m0", "scc");
  unsigned so519;
  asm volatile("s_add_u32 m0, %1, 52224\n\ts_add_u32 %0, %4, 12288\n\tbuffer_load_dwordx4 %2, %3, %0 offen offset:0 lds" : "=&s"(so519) : "s"(c.m0w), "v"(c.vb), "s"(c.sb), "s"(sob) : "memory", "m0", "scc");
}
DI void o3_stage2(OC& c, const int s3, const int ns, const unsigned soa, const unsigned sob) {
  asm volatile("s_waitcnt lgkmcnt(3)\n\tv_mfma_f32_32x32x16_bf16 v[128:143], v[72:75], v[64:67], v[128:143]\n\tds_read_b64_tr_b16 v[88:89], %0 offset:20480\n\tds_read_b64_tr_b16 v[90:91], %0 offset:21504\n\ts_cmp_lt_i32 %1, %2\n\ts_cbranch_scc0 .Lo520_%=\n\ts_add_u32 m0, %3, 32768\n\ts_nop 0\n\tbuffer_load_dwordx4 %4, %5, %6 offen offset:0 sc1 lds\n\t.Lo520_%=:" :  : "v"(c.b_rd[1][0]), "s"(s3), "s"(ns), "s"(c.m0w), "v"(c.va[0]), "s"(c.sa), "s"(soa) : "memory", "m0", "scc");
  asm volatile("s_waitcnt lgkmcnt(3)\n\tv_mfma_f32_32x32x16_bf16 v[144:159], v[76:79], v[64:67], v[144:159]\n\tds_read_b128 v[80:83], %0 offset:0\n\tds_read_b64_tr_b16 v[92:93], %1 offset:20480" :  : "v"(c.a_rd[1][1]), "v"(c.b_rd[1][1]) : "memory");
  asm volatile("s_waitcnt lgkmcnt(4)\n\tv_mfma_f32_32x32x16_bf16 v[160:175], v[72:75], v[68:71], v[160:175]\n\tds_read_b64_tr_b16 v[94:95], %0 offset:21504\n\ts_cmp_lt_i32 %1, %2\n\ts_cbranch_scc0 .Lo521_%=\n\ts_add_u32 m0, %3, 33792\n\ts_nop 0\n\tbuffer_load_dwordx4 %4, %5, %6 offen offset:0 sc1 lds\n\t.Lo521_%=:" :  : "v"(c.b_rd[1][1]), "s"(s3), "s"(ns), "s"(c.m0w), "v"(c.va[1]), "s"(c.sa), "s"(soa) : "memory", "m0", "scc");
  asm volatile("v_mfma_f32_32x32x16_bf16 v[176:191], v[76:79], v[68:71], v[176:191]\n\tds_read_b128 v[84:87], %0 offset:4096" :  : "v"(c.a_rd[1][1]) : "memory");
  asm volatile("s_waitcnt lgkmcnt(3)\n\tv_mfma_f32_32x32x16_bf16 v[128:143], v[88:91], v[80:83], v[128:143]\n\tds_read_b64_tr_b16 v[72:73], %0 offset:24576\n\tds_read_b64_tr_b16 v[74:75], %0 offset:25600\n\ts_cmp_lt_i32 %1, %2\n\ts_cbranch_scc0 .Lo522_%=\n\ts_add_u32 m0, %3, 34816\n\ts_nop 0\n\tbuffer_load_dwordx4 %4, %5, %6 offen offset:0 sc1 lds\n\t.Lo522_%=:" :  : "v"(c.b_rd[1][0]), "s"(s3), "s"(ns), "s"(c.m0w), "v"(c.va[2]), "s"(c.sa), "s"(soa) : "memory", "m0", "scc", ACL64);
  asm volatile("s_waitcnt lgkmcnt(3)\n\tv_mfma_f32_32x32x16_bf16 v[144:159], v[92:95], v[80:83], v[144:159]\n\tds_read_b128 v[64:67], %0 offset:0\n\tds_read_b64_tr_b16 v[76:77], %1 offset:24576" :  : "v"(c.a_rd[1][2]), "v"(c.b_rd[1][1]) : "memory");
  asm volatile("s_waitcnt lgkmcnt(4)\n\tv_mfma_f32_32x32x16_bf16 v[160:175], v[88:91], v[84:87], v[160:175]\n\tds_read_b64_tr_b16 v[78:79], %0 offset:25600\n\ts_cmp_lt_i32 %1, %2\n\ts_cbranch_scc0 .Lo523_%=\n\ts_add_u32 m0, %3, 35840\n\ts_nop 0\n\tbuffer_load_dwordx4 %4, %5, %6 offen offset:0 sc1 lds\n\t.Lo523_%=:" :  : "v"(c.b_rd[1][1]), "s"(s3), "s"(ns), "s"(c.m0w), "v"(c.va[3]), "s"(c.sa), "s"(soa) : "memory", "m0", "scc");
  asm volatile("v_mfma_f32_32x32x16_bf16 v[176:191], v[92:95], v[84:87], v[176:191]\n\tds_read_b128 v[68:71], %0 offset:4096" :  : "v"(c.a_rd[1][2]) : "memory");
  asm volatile("s_waitcnt lgkmcnt(3)\n\tv_mfma_f32_32x32x16_bf16 v[128:143], v[72:75], v[64:67], v[128:143]\n\tds_read_b64_tr_b16 v[88:89], %0 offset:28672\n\tds_read_b64_tr_b16 v[90:91], %0 offset:29696\n\ts_cmp_lt_i32 %1, %2\n\ts_cbranch_scc0 .Lo524_%=\n\ts_add_u32 m0, %3, 49152\n\ts_nop 0\n\tbuffer_load_dwordx4 %4, %5, %6 offen offset:0 lds\n\t.Lo524_%=:" :  : "v"(c.b_rd[1][0]), "s"(s3), "s"(ns), "s"(c.m0w), "v"(c.vb), "s"(c.sb), "s"(sob) : "memory", "m0", "scc");
  asm volatile("s_waitcnt lgkmcnt(3)\n\tv_mfma_f32_32x32x16_bf16 v[144:159], v[76:79], v[64:67], v[144:159]\n\tds_read_b128 v[80:83], %0 offset:0\n\tds_read_b64_tr_b16 v[92:93], %1 offset:28672" :  : "v"(c.a_rd[1][3]), "v"(c.b_rd[1][1]) : "memory");
  unsigned so526;
  asm volatile("s_waitcnt lgkmcnt(4)\n\tv_mfma_f32_32x32x16_bf16 v[160:175], v[72:75], v[68:71], v[160:175]\n\tds_read_b64_tr_b16 v[94:95], %1 offset:29696\n\ts_cmp_lt_i32 %2, %3\n\ts_cbranch_scc0 .Lo525_%=\n\ts_add_u32 m0, %4, 50176\n\ts_add_u32 %0, %7, 4096\n\tbuffer_load_dwordx4 %5, %6, %0 offen offset:0 lds\n\t.Lo525_%=:" : "=&s"(so526) : "v"(c.b_rd[1][1]), "s"(s3), "s"(ns), "s"(c.m0w), "v"(c.vb), "s"(c.sb), "s"(sob) : "memory", "m0", "scc");
  asm volatile("v_mfma_f32_32x32x16_bf16 v[176:191], v[76:79], v[68:71], v[176:191]\n\tds_read_b128 v[84:87], %0 offset:4096" :  : "v"(c.a_rd[1][3]) : "memory");
  unsigned so528;
  asm volatile("s_waitcnt lgkmcnt(3)\n\tv_mfma_f32_32x32x16_bf16 v[128:143], v[88:91], v[80:83], v[128:143]\n\tds_read_b64_tr_b16 v[72:73], %1 offset:49152\n\tds_read_b64_tr_b16 v[74:75], %1 offset:50176\n\ts_cmp_lt_i32 %2, %3\n\ts_cbranch_scc0 .Lo527_%=\n\ts_add_u32 m0, %4, 51200\n\ts_add_u32 %0, %7, 8192\n\tbuffer_load_dwordx4 %5, %6, %0 offen offset:0 lds\n\t.Lo527_%=:" : "=&s"(so528) : "v"(c.b_rd[1][0]), "s"(s3), "s"(ns), "s"(c.m0w), "v"(c.vb), "s"(c.sb), "s"(sob) : "memory", "m0", "scc", ACL64);
  asm volatile("s_waitcnt lgkmcnt(3)\n\tv_mfma_f32_32x32x16_bf16 v[144:159], v[92:95], v[80:83], v[144:159]\n\tds_read_b128 v[64:67], %0 offset:32768\n\tds_read_b64_tr_b16 v[76:77], %1 offset:49152" :  : "v"(c.a_rd[1][0]), "v"(c.b_rd[1][1]) : "memory");
  unsigned so530;
  asm volatile("s_waitcnt lgkmcnt(4)\n\tv_mfma_f32_32x32x16_bf16 v[160:175], v[88:91], v[84:87], v[160:175]\n\tds_read_b64_tr_b16 v[78:79], %1 offset:50176\n\ts_cmp_lt_i32 %2, %3\n\ts_cbranch_scc0 .Lo529_%=\n\ts_add_u32 m0, %4, 52224\n\ts_add_u32 %0, %7, 12288\n\tbuffer_load_dwordx4 %5, %6, %0 offen offset:0 lds\n\t.Lo529_%=:" : "=&s"(so530) : "v"(c.b_rd[1][1]), "s"(s3), "s"(ns), "s"(c.m0w), "v"(c.vb), "s"(c.sb), "s"(sob) : "memory", "m0", "scc");
  asm volatile("v_mfma_f32_32x32x16_bf16 v[176:191], v[92:95], v[84:87], v[176:191]\n\tds_read_b128 v[68:71], %0 offset:36864" :  : "v"(c.a_rd[1][0]) : "memory");
}
DI void o3_dmaA2(OC& c, const unsigned soa) {
  asm volatile("s_add_u32 m0, %0, 65536\n\ts_nop 0\n\tbuffer_load_dwordx4 %1, %2, %3 offen offset:0 sc1 lds" :  : "s"(c.m0w), "v"(c.va[0]), "s"(c.sa), "s"(soa) : "memory", "m0", "scc");
  asm volatile("s_add_u32 m0, %0, 66560\n\ts_nop 0\n\tbuffer_load_dwordx4 %1, %2, %3 offen offset:0 sc1 lds" :  : "s"(c.m0w), "v"(c.va[1]), "s"(c.sa), "s"(soa) : "memory", "m0", "scc");
  asm volatile("s_add_u32 m0, %0, 67584\n\ts_nop 0\n\tbuffer_load_dwordx4 %1, %2, %3 offen offset:0 sc1 lds" :  : "s"(c.m0w), "v"(c.va[2]), "s"(c.sa), "s"(soa) : "memory", "m0", "scc");
  asm volatile("s_add_u32 m0, %0, 68608\n\ts_nop 0\n\tbuffer_load_dwordx4 %1, %2, %3 offen offset:0 sc1 lds" :  : "s"(c.m0w), "v"(c.va[3]), "s"(c.sa), "s"(soa) : "memory", "m0", "scc");
}
DI void o3_dmaB2(OC& c, const unsigned sob) {
  asm volatile("s_add_u32 m0, %0, 81920\n\ts_nop 0\n\tbuffer_load_dwordx4 %1, %2, %3 offen offset:0 lds" :  : "s"(c.m0w), "v"(c.vb), "s"(c.sb), "s"(sob) : "memory", "m0", "scc", ACL64);
  unsigned so531;
  asm volatile("s_add_u32 m0, %1, 82944\n\ts_add_u32 %0, %4, 4096\n\tbuffer_load_dwordx4 %2, %3, %0 offen offset:0 lds" : "=&s"(so531) : "s"(c.m0w), "v"(c.vb), "s"(c.sb), "s"(sob) : "memory", "m0", "scc");
  unsigned so532;
  asm volatile("s_add_u32 m0, %1, 83968\n\ts_add_u32 %0, %4, 8192\n\tbuffer_load_dwordx4 %2, %3, %0 offen offset:0 lds" : "=&s"(so532) : "s"(c.m0w), "v"(c.vb), "s"(c.sb), "s"(sob) : "memory", "m0", "scc");
  unsigned so533;
  asm volatile("s_add_u32 m0, %1, 84992\n\ts_add_u32 %0, %4, 12288\n\tbuffer_load_dwordx4 %2, %3, %0 offen offset:0 lds" : "=&s"(so533) : "s"(c.m0w), "v"(c.vb), "s"(c.sb), "s"(sob) : "memory", "m0", "scc");
}
DI void o3_stage3(OC& c, const int s3, const int ns, const unsigned soa, const unsigned sob) {
  asm volatile("s_waitcnt lgkmcnt(3)\n\tv_mfma_f32_32x32x16_bf16 v[128:143], v[72:75], v[64:67], v[128:143]\n\tds_read_b64_tr_b16 v[88:89], %0 offset:53248\n\tds_read_b64_tr_b16 v[90:91], %0 offset:54272\n\ts_cmp_lt_i32 %1, %2\n\ts_cbranch_scc0 .Lo534_%=\n\ts_add_u32 m0, %3, 65536\n\ts_nop 0\n\tbuffer_load_dwordx4 %4, %5, %6 offen offset:0 sc1 lds\n\t.Lo534_%=:" :  : "v"(c.b_rd[1][0]), "s"(s3), "s"(ns), "s"(c.m0w), "v"(c.va[0]), "s"(c.sa), "s"(soa) : "memory", "m0", "scc");
  asm volatile("s_waitcnt lgkmcnt(3)\n\tv_mfma_f32_32x32x16_bf16 v[144:159], v[76:79], v[64:67], v[144:159]\n\tds_read_b128 v[80:83], %0 offset:32768\n\tds_read_b64_tr_b16 v[92:93], %1 offset:53248" :  : "v"(c.a_rd[1][1]), "v"(c.b_rd[1][1]) : "memory");
  asm volatile("s_waitcnt lgkmcnt(4)\n\tv_mfma_f32_32x32x16_bf16 v[160:175], v[72:75], v[68:71], v[160:175]\n\tds_read_b64_tr_b16 v[94:95], %0 offset:54272\n\ts_cmp_lt_i32 %1, %2\n\ts_cbranch_scc0 .Lo535_%=\n\ts_add_u32 m0, %3, 66560\n\ts_nop 0\n\tbuffer_load_dwordx4 %4, %5, %6 offen offset:0 sc1 lds\n\t.Lo535_%=:" :  : "v"(c.b_rd[1][1]), "s"(s3), "s"(ns), "s"(c.m0w), "v"(c.va[1]), "s"(c.sa), "s"(soa) : "memory", "m0", "scc");
  asm volatile("v_mfma_f32_32x32x16_bf16 v[176:191], v[76:79], v[68:71], v[176:191]\n\tds_read_b128 v[84:87], %0 offset:36864" :  : "v"(c.a_rd[1][1]) : "memory");
  asm volatile("s_waitcnt lgkmcnt(3)\n\tv_mfma_f32_32x32x16_bf16 v[128:143], v[88:91], v[80:83], v[128:143]\n\tds_read_b64_tr_b16 v[72:73], %0 offset:57344\n\tds_read_b64_tr_b16 v[74:75], %0 offset:58368\n\ts_cmp_lt_i32 %1, %2\n\ts_cbranch_scc0 .Lo536_%=\n\ts_add_u32 m0, %3, 67584\n\ts_nop 0\n\tbuffer_load_dwordx4 %4, %5, %6 offen offset:0 sc1 lds\n\t.Lo536_%=:" :  : "v"(c.b_rd[1][0]), "s"(s3), "s"(ns), "s"(c.m0w), "v"(c.va[2]), "s"(c.sa), "s"(soa) : "memory", "m0", "scc", ACL64);
  asm volatile("s_waitcnt lgkmcnt(3)\n\tv_mfma_f32_32x32x16_bf16 v[144:159], v[92:95], v[80:83], v[144:159]\n\tds_read_b128 v[64:67], %0 offset:32768\n\tds_read_b64_tr_b16 v[76:77], %1 offset:57344" :  : "v"(c.a_rd[1][2]), "v"(c.b_rd[1][1]) : "memory");
  asm volatile("s_waitcnt lgkmcnt(4)\n\tv_mfma_f32_32x32x16_bf16 v[160:175], v[88:91], v[84:87], v[160:175]\n\tds_read_b64_tr_b16 v[78:79], %0 offset:58368\n\ts_cmp_lt_i32 %1, %2\n\ts_cbranch_scc0 .Lo537_%=\n\ts_add_u32 m0, %3, 68608\n\ts_nop 0\n\tbuffer_load_dwordx4 %4, %5, %6 offen offset:0 sc1 lds\n\t.Lo537_%=:" :  : "v"(c.b_rd[1][1]), "s"(s3), "s"(ns), "s"(c.m0w), "v"(c.va[3]), "s"(c.sa), "s"(soa) : "memory", "m0", "scc");
  asm volatile("v_mfma_f32_32x32x16_bf16 v[176:191], v[92:95], v[84:87], v[176:191]\n\tds_read_b128 v[68:71], %0 offset:36864" :  : "v"(c.a_rd[1][2]) : "memory");
  asm volatile("s_waitcnt lgkmcnt(3)\n\tv_mfma_f32_32x32x16_bf16 v[128:143], v[72:75], v[64:67], v[128:143]\n\tds_read_b64_tr_b16 v[88:89], %0 offset:61440\n\tds_read_b64_tr_b16 v[90:91], %0 offset:62464\n\ts_cmp_lt_i32 %1, %2\n\ts_cbranch_scc0 .Lo538_%=\n\ts_add_u32 m0, %3, 81920\n\ts_nop 0\n\tbuffer_load_dwordx4 %4, %5, %6 offen offset:0 lds\n\t.Lo538_%=:" :  : "v"(c.b_rd[1][0]), "s"(s3), "s"(ns), "s"(c.m0w), "v"(c.vb), "s"(c.sb), "s"(sob) : "memory", "m0", "scc");
  asm volatile("s_waitcnt lgkmcnt(3)\n\tv_mfma_f32_32x32x16_bf16 v[144:159], v[76:79], v[64:67], v[144:159]\n\tds_read_b128 v[80:83], %0 offset:32768\n\tds_read_b64_tr_b16 v[92:93], %1 offset:61440" :  : "v"(c.a_rd[1][3]), "v"(c.b_rd[1][1]) : "memory");
  unsigned so540;
  asm volatile("s_waitcnt lgkmcnt(4)\n\tv_mfma_f32_32x32x16_bf16 v[160:175], v[72:75], v[68:71], v[160:175]\n\tds_read_b64_tr_b16 v[94:95], %1 offset:62464\n\ts_cmp_lt_i32 %2, %3\n\ts_cbranch_scc0 .Lo539_%=\n\ts_add_u32 m0, %4, 82944\n\ts_add_u32 %0, %7, 4096\n\tbuffer_load_dwordx4 %5, %6, %0 offen offset:0 lds\n\t.Lo539_%=:" : "=&s"(so540) : "v"(c.b_rd[1][1]), "s"(s3), "s"(ns), "s"(c.m0w), "v"(c.vb), "s"(c.sb), "s"(sob) : "memory", "m0", "scc");
  asm volatile("v_mfma_f32_32x32x16_bf16 v[176:191], v[76:79], v[68:71], v[176:191]\n\tds_read_b128 v[84:87], %0 offset:36864" :  : "v"(c.a_rd[1][3]) : "memory");
  unsigned so542;
  asm volatile("s_waitcnt lgkmcnt(3)\n\tv_mfma_f32_32x32x16_bf16 v[128:143], v[88:91], v[80:83], v[128:143]\n\tds_read_b64_tr_b16 v[72:73], %1 offset:16384\n\tds_read_b64_tr_b16 v[74:75], %1 offset:17408\n\ts_cmp_lt_i32 %2, %3\n\ts_cbranch_scc0 .Lo541_%=\n\ts_add_u32 m0, %4, 83968\n\ts_add_u32 %0, %7, 8192\n\tbuffer_load_dwordx4 %5, %6, %0 offen offset:0 lds\n\t.Lo541_%=:" : "=&s"(so542) : "v"(c.b_rd[0][0]), "s"(s3), "s"(ns), "s"(c.m0w), "v"(c.vb), "s"(c.sb), "s"(sob) : "memory", "m0", "scc", ACL64);
  asm volatile("s_waitcnt lgkmcnt(3)\n\tv_mfma_f32_32x32x16_bf16 v[144:159], v[92:95], v[80:83], v[144:159]\n\tds_read_b128 v[64:67], %0 offset:0\n\tds_read_b64_tr_b16 v[76:77], %1 offset:16384" :  : "v"(c.a_rd[0][0]), "v"(c.b_rd[0][1]) : "memory");
  unsigned so544;
  asm volatile("s_waitcnt lgkmcnt(4)\n\tv_mfma_f32_32x32x16_bf16 v[160:175], v[88:91], v[84:87], v[160:175]\n\tds_read_b64_tr_b16 v[78:79], %1 offset:17408\n\ts_cmp_lt_i32 %2, %3\n\ts_cbranch_scc0 .Lo543_%=\n\ts_add_u32 m0, %4, 84992\n\ts_add_u32 %0, %7, 12288\n\tbuffer_load_dwordx4 %5, %6, %0 offen offset:0 lds\n\t.Lo543_%=:" : "=&s"(so544) : "v"(c.b_rd[0][1]), "s"(s3), "s"(ns), "s"(c.m0w), "v"(c.vb), "s"(c.sb), "s"(sob) : "memory", "m0", "scc");
  asm volatile("v_mfma_f32_32x32x16_bf16 v[176:191], v[92:95], v[84:87], v[176:191]\n\tds_read_b128 v[68:71], %0 offset:4096" :  : "v"(c.a_rd[0][0]) : "memory");
}
DI void o3_first(OC& c) {
  asm volatile("ds_read_b64_tr_b16 v[72:73], %0 offset:16384\n\tds_read_b64_tr_b16 v[74:75], %0 offset:17408\n\tds_read_b128 v[64:67], %1 offset:0\n\tds_read_b64_tr_b16 v[76:77], %2 offset:16384\n\tds_read_b64_tr_b16 v[78:79], %2 offset:17408\n\tds_read_b128 v[68:71], %1 offset:4096\n\tv_mov_b32 v128, 0\n\tv_mov_b32 v129, 0\n\tv_mov_b32 v130, 0\n\tv_mov_b32 v131, 0\n\tv_mov_b32 v132, 0\n\tv_mov_b32 v133, 0\n\tv_mov_b32 v134, 0\n\tv_mov_b32 v135, 0\n\tv_mov_b32 v136, 0\n\tv_mov_b32 v137, 0\n\tv_mov_b32 v138, 0\n\tv_mov_b32 v139, 0\n\tv_mov_b32 v140, 0\n\tv_mov_b32 v141, 0\n\tv_mov_b32 v142, 0\n\tv_mov_b32 v143, 0\n\tv_mov_b32 v144, 0\n\tv_mov_b32 v145, 0\n\tv_mov_b32 v146, 0\n\tv_mov_b32 v147, 0\n\tv_mov_b32 v148, 0\n\tv_mov_b32 v149, 0\n\tv_mov_b32 v150, 0\n\tv_mov_b32 v151, 0\n\tv_mov_b32 v152, 0\n\tv_mov_b32 v153, 0\n\tv_mov_b32 v154, 0\n\tv_mov_b32 v155, 0\n\tv_mov_b32 v156, 0\n\tv_mov_b32 v157, 0\n\tv_mov_b32 v158, 0\n\tv_mov_b32 v159, 0\n\tv_mov_b32 v160, 0\n\tv_mov_b32 v161, 0\n\tv_mov_b32 v162, 0\n\tv_mov_b32 v163, 0\n\tv_mov_b32 v164, 0\n\tv_mov_b32 v165, 0\n\tv_mov_b32 v166, 0\n\tv_mov_b32 v167, 0\n\tv_mov_b32 v168, 0\n\tv_mov_b32 v169, 0\n\tv_mov_b32 v170, 0\n\tv_mov_b32 v171, 0\n\tv_mov_b32 v172, 0\n\tv_mov_b32 v173, 0\n\tv_mov_b32 v174, 0\n\tv_mov_b32 v175, 0\n\tv_mov_b32 v176, 0\n\tv_mov_b32 v177, 0\n\tv_mov_b32 v178, 0\n\tv_mov_b32 v179, 0\n\tv_mov_b32 v180, 0\n\tv_mov_b32 v181, 0\n\tv_mov_b32 v182, 0\n\tv_mov_b32 v183, 0\n\tv_mov_b32 v184, 0\n\tv_mov_b32 v185, 0\n\tv_mov_b32 v186, 0\n\tv_mov_b32 v187, 0\n\tv_mov_b32 v188, 0\n\tv_mov_b32 v189, 0\n\tv_mov_b32 v190, 0\n\tv_mov_b32 v191, 0" :  : "v"(c.b_rd[0][0]), "v"(c.a_rd[0][0]), "v"(c.b_rd[0][1]) : "memory");
}
DI void x_load(const unsigned vo, const srd_t sx, const unsigned (&src)[3]) {
  asm volatile("buffer_load_dwordx4 v[128:131], %0, %1, %2 offen offset:0 sc1\n\tbuffer_load_dwordx4 v[132:135], %0, %1, %2 offen offset:1024 sc1\n\tbuffer_load_dwordx4 v[136:139], %0, %1, %2 offen offset:2048 sc1\n\tbuffer_load_dwordx4 v[140:143], %0, %1, %2 offen offset:3072 sc1\n\tbuffer_load_dwordx4 v[144:147], %0, %1, %3 offen offset:0 sc1\n\tbuffer_load_dwordx4 v[148:151], %0, %1, %3 offen offset:1024 sc1\n\tbuffer_load_dwordx4 v[152:155], %0, %1, %3 offen offset:2048 sc1\n\tbuffer_load_dwordx4 v[156:159], %0, %1, %3 offen offset:3072 sc1\n\tbuffer_load_dwordx4 v[160:163], %0, %1, %4 offen offset:0 sc1\n\tbuffer_load_dwordx4 v[164:167], %0, %1, %4 offen offset:1024 sc1\n\tbuffer_load_dwordx4 v[168:171], %0, %1, %4 offen offset:2048 sc1\n\tbuffer_load_dwordx4 v[172:175], %0, %1, %4 offen offset:3072 sc1" :  : "v"(vo), "s"(sx), "s"(src[0]), "s"(src[1]), "s"(src[2]) : "memory");
}
DI void x_add01(float (&a)[16]) {
  asm volatile("s_waitcnt vmcnt(8)\n\tv_add_f32_e32 %0, %0, v128\n\tv_add_f32_e32 %1, %1, v129\n\tv_add_f32_e32 %2, %2, v130\n\tv_add_f32_e32 %3, %3, v131\n\tv_add_f32_e32 %4, %4, v132\n\tv_add_f32_e32 %5, %5, v133\n\tv_add_f32_e32 %6, %6, v134\n\tv_add_f32_e32 %7, %7, v135\n\tv_add_f32_e32 %8, %8, v136\n\tv_add_f32_e32 %9, %9, v137\n\tv_add_f32_e32 %10, %10, v138\n\tv_add_f32_e32 %11, %11, v139\n\tv_add_f32_e32 %12, %12, v140\n\tv_add_f32_e32 %13, %13, v141\n\tv_add_f32_e32 %14, %14, v142\n\tv_add_f32_e32 %15, %15, v143\n\ts_waitcnt vmcnt(4)\n\tv_add_f32_e32 %0, %0, v144\n\tv_add_f32_e32 %1, %1, v145\n\tv_add_f32_e32 %2, %2, v146\n\tv_add_f32_e32 %3, %3, v147\n\tv_add_f32_e32 %4, %4, v148\n\tv_add_f32_e32 %5, %5, v149\n\tv_add_f32_e32 %6, %6, v150\n\tv_add_f32_e32 %7, %7, v151\n\tv_add_f32_e32 %8, %8, v152\n\tv_add_f32_e32 %9, %9, v153\n\tv_add_f32_e32 %10, %10, v154\n\tv_add_f32_e32 %11, %11, v155\n\tv_add_f32_e32 %12, %12, v156\n\tv_add_f32_e32 %13, %13, v157\n\tv_add_f32_e32 %14, %14, v158\n\tv_add_f32_e32 %15, %15, v159" : "+v"(a[0]), "+v"(a[1]), "+v"(a[2]), "+v"(a[3]), "+v"(a[4]), "+v"(a[5]), "+v"(a[6]), "+v"(a[7]), "+v"(a[8]), "+v"(a[9]), "+v"(a[10]), "+v"(a[11]), "+v"(a[12]), "+v"(a[13]), "+v"(a[14]), "+v"(a[15]) :  : "memory");
}
DI void x_mov2(float (&a)[16]) {
  asm volatile("s_waitcnt vmcnt(0)\n\tv_mov_b32_e32 %0, v160\n\tv_mov_b32_e32 %1, v161\n\tv_mov_b32_e32 %2, v162\n\tv_mov_b32_e32 %3, v163\n\tv_mov_b32_e32 %4, v164\n\tv_mov_b32_e32 %5, v165\n\tv_mov_b32_e32 %6, v166\n\tv_mov_b32_e32 %7, v167\n\tv_mov_b32_e32 %8, v168\n\tv_mov_b32_e32 %9, v169\n\tv_mov_b32_e32 %10, v170\n\tv_mov_b32_e32 %11, v171\n\tv_mov_b32_e32 %12, v172\n\tv_mov_b32_e32 %13, v173\n\tv_mov_b32_e32 %14, v174\n\tv_mov_b32_e32 %15, v175" : "=v"(a[0]), "=v"(a[1]), "=v"(a[2]), "=v"(a[3]), "=v"(a[4]), "=v"(a[5]), "=v"(a[6]), "=v"(a[7]), "=v"(a[8]), "=v"(a[9]), "=v"(a[10]), "=v"(a[11]), "=v"(a[12]), "=v"(a[13]), "=v"(a[14]), "=v"(a[15]) :  : "memory");
}
DI void x_sum012(float (&a)[16]) {
  asm volatile("s_waitcnt vmcnt(8)\n\tv_mov_b32_e32 %0, v128\n\tv_mov_b32_e32 %1, v129\n\tv_mov_b32_e32 %2, v130\n\tv_mov_b32_e32 %3, v131\n\tv_mov_b32_e32 %4, v132\n\tv_mov_b32_e32 %5, v133\n\tv_mov_b32_e32 %6, v134\n\tv_mov_b32_e32 %7, v135\n\tv_mov_b32_e32 %8, v136\n\tv_mov_b32_e32 %9, v137\n\tv_mov_b32_e32 %10, v138\n\tv_mov_b32_e32 %11, v139\n\tv_mov_b32_e32 %12, v140\n\tv_mov_b32_e32 %13, v141\n\tv_mov_b32_e32 %14, v142\n\tv_mov_b32_e32 %15, v143\n\ts_waitcnt vmcnt(4)\n\tv_add_f32_e32 %0, %0, v144\n\tv_add_f32_e32 %1, %1, v145\n\tv_add_f32_e32 %2, %2, v146\n\tv_add_f32_e32 %3, %3, v147\n\tv_add_f32_e32 %4, %4, v148\n\tv_add_f32_e32 %5, %5, v149\n\tv_add_f32_e32 %6, %6, v150\n\tv_add_f32_e32 %7, %7, v151\n\tv_add_f32_e32 %8, %8, v152\n\tv_add_f32_e32 %9, %9, v153\n\tv_add_f32_e32 %10, %10, v154\n\tv_add_f32_e32 %11, %11, v155\n\tv_add_f32_e32 %12, %12, v156\n\tv_add_f32_e32 %13, %13, v157\n\tv_add_f32_e32 %14, %14, v158\n\tv_add_f32_e32 %15, %15, v159\n\ts_waitcnt vmcnt(0)\n\tv_add_f32_e32 %0, %0, v160\n\tv_add_f32_e32 %1, %1, v161\n\tv_add_f32_e32 %2, %2, v162\n\tv_add_f32_e32 %3, %3, v163\n\tv_add_f32_e32 %4, %4, v164\n\tv_add_f32_e32 %5, %5, v165\n\tv_add_f32_e32 %6, %6, v166\n\tv_add_f32_e32 %7, %7, v167\n\tv_add_f32_e32 %8, %8, v168\n\tv_add_f32_e32 %9, %9, v169\n\tv_add_f32_e32 %10, %10, v170\n\tv_add_f32_e32 %11, %11, v171\n\tv_add_f32_e32 %12, %12, v172\n\tv_add_f32_e32 %13, %13, v173\n\tv_add_f32_e32 %14, %14, v174\n\tv_add_f32_e32 %15, %15, v175" : "=v"(a[0]), "=v"(a[1]), "=v"(a[2]), "=v"(a[3]), "=v"(a[4]), "=v"(a[5]), "=v"(a[6]), "=v"(a[7]), "=v"(a[8]), "=v"(a[9]), "=v"(a[10]), "=v"(a[11]), "=v"(a[12]), "=v"(a[13]), "=v"(a[14]), "=v"(a[15]) :  : "memory", ACL64);
}


template <int CTRL> DI float dpp_add(float v) { return v + __int_as_float(__builtin_amdgcn_update_dpp(0, __float_as_int(v), CTRL, 0xf, 0xf, true)); }
DI float wave_sum(float v) {
  v = dpp_add<0xB1>(v); v = dpp_add<0x4E>(v); v = dpp_add<0x141>(v); v = dpp_add<0x140>(v);
  v = v + __int_as_float(__builtin_amdgcn_update_dpp(0, __float_as_int(v), 0x142, 0xa, 0xf, false));
  v = v + __int_as_float(__builtin_amdgcn_update_dpp(0, __float_as_int(v), 0x143, 0xc, 0xf, false));
  return __int_as_float(__builtin_amdgcn_readlane(__float_as_int(v), 63));
}
DI void ln_load(const bf16_t* x, int row0, u32x4 (&v)[2]) {
  const unsigned tid = threadIdx.x;
  asm volatile("global_load_dwordx4 %0, %2, off\n\tglobal_load_dwordx4 %1, %2, off offset:1024" : "=&v"(v[0]), "=&v"(v[1]) : "v"(x + (size_t)(row0 + (tid >> 6) * 2) * DM + (tid & 63) * 8) : "memory");
}
DI void ln_rows(const u32x4 (&v)[2], bf16_t* xn, int row0) {
  const unsigned tid = threadIdx.x, lane = tid & 63, wid = __builtin_amdgcn_readfirstlane(tid >> 6);
#pragma unroll
  for (int rr = 0; rr < 2; ++rr) {
    float f[8];
#pragma unroll
    for (int e = 0; e < 4; ++e) { f[2 * e] = __uint_as_float(v[rr][e] << 16); f[2 * e + 1] = __uint_as_float(v[rr][e] & 0xffff0000u); }
    const float mean = wave_sum(((f[0] + f[1]) + (f[2] + f[3])) + ((f[4] + f[5]) + (f[6] + f[7]))) * (1.f / DM);
    float q = 0.f;
#pragma unroll
    for (int e = 0; e < 8; ++e) { const float d = f[e] - mean; q += d * d; }
    const float var = wave_sum(q) * (1.f / DM);
    const float mb = rbf(mean), sd = rbf(__builtin_amdgcn_sqrtf(rbf(rbf(var) + 1e-5f))), rs = __builtin_amdgcn_rcpf(sd);
    u32x4 o;
#pragma unroll
    for (int e = 0; e < 4; ++e) o[e] = cvtpk(rbf(f[2 * e] - mb) * rs, rbf(f[2 * e + 1] - mb) * rs);
    __builtin_amdgcn_raw_buffer_store_b128(o, make_srd(xn, SEQ * DM * 2), lane * 16, (row0 + wid * 2 + rr) * 1024, 16);
  }
}

DI void kv_setup(KVC& c, unsigned ldsb, const bf16_t* ckv, const bf16_t* W, int kb, int cb) {
  const unsigned tid = threadIdx.x, lane = tid & 63, l32 = lane & 31, hi = lane >> 5;
  const unsigned wid = __builtin_amdgcn_readfirstlane(tid >> 6), wr = wid >> 1, wc = wid & 1;
  c.m0w = ldsb + wid * 4096;
  c.sa = make_srd(ckv, SEQ * DM * 2); c.sb = make_srd(W, DM * NH * HD * 2);
  c.va = (64 * wid + (lane >> 2)) * 1024 + 16 * ((lane & 3) ^ ((lane >> 4) & 3)); c.soa = kb * 256 * 1024;
  c.vb[0] = (8 * wid + hi) * 8192 + 16 * ((lane & 31) ^ (hi << 2));
  c.vb[1] = (8 * wid + hi + 2) * 8192 + 16 * ((lane & 31) ^ ((2 + hi) << 2));
  c.sob = cb * 512;
  const unsigned blk = (lane >> 4) & 1, q = (lane & 15) >> 2, p = lane & 3;
#pragma unroll
  for (int s = 0; s < 2; ++s) {
#pragma unroll
    for (int ks = 0; ks < 2; ++ks) c.a_rd[s][ks] = ldsb + 65536 * s + 64 * (128 * wr + l32) + 16 * ((2 * ks + hi) ^ ((l32 >> 2) & 3));
#pragma unroll
    for (int j = 0; j < 4; ++j) c.b_rd[s][j] = ldsb + 65536 * s + 512 * (8 * hi + q) + 256 * wc + ((j ^ q) << 6) + 32 * blk + 8 * p;
  }
}
typedef unsigned long long u64;
extern "C" __device__ u64 kf_dispatch_id() __asm("llvm.amdgcn.dispatch.id");
constexpr unsigned F_XN = 0, F_KV = 256, F_CTX = 512, F_HH = 1024, F_XCH = 1536;
DI u64 launch_tag() {
  const u64 id = kf_dispatch_id(), pk = (u64)__builtin_amdgcn_dispatch_ptr();
  return ((u64)(0x5A000000u | ((unsigned)id & 0xFFFFFFu)) << 32) | ((((unsigned)(pk >> 6) & 0xFFFFFFu) << 8) | 0xC3u);
}
DI u64* flag(char* ws, unsigned i) { return (u64*)(ws + WS_FLAG) + i; }
DI void flag_set(u64* f, u64 tag) { __hip_atomic_store(f, tag, __ATOMIC_RELAXED, __HIP_MEMORY_SCOPE_AGENT); }
DI void flag_wait(u64* f, bool on, u64 tag) {
  unsigned spins = 0;
  while (!__all(!on || __hip_atomic_load(f, __ATOMIC_RELAXED, __HIP_MEMORY_SCOPE_AGENT) == tag) && ++spins < 200000u) __builtin_amdgcn_s_sleep(1);
}
DI void flag_peek(u64* f, u64& v) { asm volatile("global_load_dwordx2 %0, %1, off sc1" : "=&v"(v) : "v"(f) : "memory"); }
DI void flag_settle(u64* f, bool on, u64& v, u64 tag) { asm volatile("" : "+v"(v)); if (!__all(!on || v == tag)) flag_wait(f, on, tag); }
constexpr int AO_(int db, int qb) { return 16 * (2 * db + qb); }
constexpr float THR = 8.0f;
constexpr int FWV = 6;
constexpr unsigned L_KA = 0, L_VA = 32768, L_SB = 65536, L_X = 131072, L_G = 49152, GST = 28672;
template <int Q> DI void negm_bcast(float neg_mhat) {
  const float aone = lane_id() < 32u ? 1.f : 0.f;
  asm volatile("s_nop 1\n\tv_mfma_f32_32x32x2_f32 v[%c2:%c3], %0, %1, 0" ::"v"(aone), "v"(neg_mhat), "i"(96 + 16 * Q), "i"(111 + 16 * Q));
}
template <int A> DI float acc_rd() { float r; asm volatile("v_accvgpr_read_b32 %0, a[%c1]" : "=v"(r) : "i"(A) : "memory"); return r; }
template <int A> DI void acc_scale(float f) { float t; asm volatile("v_accvgpr_read_b32 %0, a[%c2]\n\tv_mul_f32 %0, %1, %0\n\tv_accvgpr_write_b32 a[%c2], %0" : "=&v"(t) : "v"(f), "i"(A)); }
template <int... As> DI void acc_scale_all(float f0, float f1, iseq<As...>) { (acc_scale<As>(((As >> 4) & 1) ? f1 : f0), ...); }
DI float lane_halves_sum(float v) {
  auto pr = __builtin_amdgcn_permlane32_swap(__float_as_uint(v), __float_as_uint(v), false, false);
  return __uint_as_float(pr[0]) + __uint_as_float(pr[1]);
}
DI float l_total(const AC& c, int q) { return lane_halves_sum((c.rs[q][0] + c.rs[q][1]) + (c.rs[q][2] + c.rs[q][3])); }
DI void step_setup(AC& c, const int t) {
  c.soVA = c.sofw + (unsigned)t * 16384u; c.soKA = c.soVA + 32768u; c.soVB = c.soVA + (unsigned)c.n2 * 16384u; c.soKB = c.soVB + 32768u;
  c.t = t; c.u3 = (unsigned)(t + 2 - c.n1); c.lim3 = (unsigned)(17 - c.n1);
}
template <int X> DI void mask_hook(AC& c, const int t) {
  const int kt = (t >= c.n1 && !c.helper) ? t + c.n2 : t;
  if (__builtin_expect(64 * kt + 63 > c.row0, 0)) {
    const float neg = -0x1p100f;
    if (64 * kt > c.row0) { if (X) s_mask1_full(neg); else s_mask0_full(neg); }
    else { if (X) s_mask1_tri(neg); else s_mask0_tri(neg); }
  }
}
template <int X, bool FIRST> DI void decide(AC& c) {
  bool nopend = FIRST;
  if (!FIRST && __builtin_expect(c.first, 0)) {
#pragma unroll
    for (int q = 0; q < 2; ++q) { c.l_light[q] = l_total(c, q); c.rs[q][0] = c.rs[q][1] = c.rs[q][2] = c.rs[q][3] = 0.f; }
    c.first = false; nopend = true;
  }
  const bool grew = fmaxf(c.pmx[0][0], c.pmx[1][0]) > THR;
  if (__builtin_expect(FIRST || __any((int)grew), 0)) {
    float dl[2];
#pragma unroll
    for (int q = 0; q < 2; ++q) {
      dl[q] = FIRST ? c.pmx[q][0] : fmaxf(c.pmx[q][0], 0.f);
      c.mhat[q] += dl[q]; c.fct[q] = __builtin_amdgcn_exp2f(-dl[q]);
      if (!FIRST) { c.rs[q][0] *= c.fct[q]; c.rs[q][1] *= c.fct[q]; c.rs[q][2] *= c.fct[q]; c.rs[q][3] *= c.fct[q]; }
    }
    if (X) s_sub1(dl); else s_sub0(dl);
    negm_bcast<0>(-c.mhat[0]); negm_bcast<1>(-c.mhat[1]);
    c.pend = !nopend;
  }
}
DI void rescale_hook(AC& c) {
  if (__builtin_expect(c.pend, 0)) {
    asm volatile("s_nop 7\n\ts_nop 7" ::: "memory");
    acc_scale_all(c.fct[0], c.fct[1], make_iseq<128>{});
    asm volatile("s_nop 1" ::: "memory");
    c.pend = false;
  }
}
template <int N> DI void k_rd0(AC& c) { asm volatile("ds_read_b128 a[%c1:%c2], %0 offset:%c3" ::"v"(c.kb_[N >> 1]), "i"(192 + 4 * N), "i"(195 + 4 * N), "i"(8192 * (N & 1)) : "memory"); }
template <int... Ns> DI void k_reads0(AC& c, iseq<Ns...>) { (k_rd0<Ns>(c), ...); }
template <int QB, int DB, int G> DI void o_stage1(LAS char* stg, unsigned l32, unsigned hi, float sc) {
  constexpr int a = AO_(DB, QB) + 4 * G;
  const float o0 = acc_rd<a>(), o1 = acc_rd<a + 1>(), o2 = acc_rd<a + 2>(), o3 = acc_rd<a + 3>();
  u32x2 w = {cvtpk(o0 * sc, o1 * sc), cvtpk(o2 * sc, o3 * sc)};
  *(LAS u32x2*)(stg + 256 * l32 + 16 * ((4 * DB + G) ^ (l32 & 15u)) + 8 * hi) = w;
}
template <int QB, int... Is> DI void o_stage(LAS char* stg, unsigned l32, unsigned hi, float sc, iseq<Is...>) { (o_stage1<QB, (Is >> 2), (Is & 3)>(stg, l32, hi, sc), ...); }
#define CTX_AUX 16
DI void o_store32(LAS const char* stg, srd_t sctx, unsigned so  , unsigned lane) {
  const unsigned rr = lane >> 4, ch = lane & 15;
#pragma unroll
  for (int j = 0; j < 8; ++j) { const unsigned r = 4 * j + rr; u32x4 v = *(LAS const u32x4*)(stg + 256 * r + 16 * (ch ^ (r & 15u)));
    __builtin_amdgcn_raw_buffer_store_b128(v, sctx, rr * 8192 + 16 * ch, so + j * 32768, CTX_AUX); }
}
template <int QB, int I> DI void o_xw(unsigned addr) { constexpr int a = AO_(I >> 2, QB) + 4 * (I & 3); asm volatile("ds_write_b128 %0, a[%c1:%c2] offset:%c3" ::"v"(addr), "i"(a), "i"(a + 3), "i"(I * 1024) : "memory"); }
template <int QB, int... Is> DI void o_xw_all(unsigned addr, iseq<Is...>) { (o_xw<QB, Is>(addr), ...); }
template <int QB, int I> DI void o_merge1(LAS const char* src, LAS char* stg, unsigned l32, unsigned hi, float f_own, float f_oth) {
  constexpr int DB = I >> 2, G = I & 3, a = AO_(DB, QB) + 4 * G;
  const f32x4 p = *(LAS const f32x4*)(src + I * 1024);
  const float o0 = acc_rd<a>(), o1 = acc_rd<a + 1>(), o2 = acc_rd<a + 2>(), o3 = acc_rd<a + 3>();
  u32x2 w = {cvtpk(o0 * f_own + p[0] * f_oth, o1 * f_own + p[1] * f_oth), cvtpk(o2 * f_own + p[2] * f_oth, o3 * f_own + p[3] * f_oth)};
  *(LAS u32x2*)(stg + 256 * l32 + 16 * ((4 * DB + G) ^ (l32 & 15u)) + 8 * hi) = w;
}
template <int QB, int... Is> DI void o_merge(LAS const char* src, LAS char* stg, unsigned l32, unsigned hi, float f_own, float f_oth, iseq<Is...>) { (o_merge1<QB, Is>(src, stg, l32, hi, f_own, f_oth), ...); }
template <int I> DI void q_ld(unsigned addr) { asm volatile("ds_read_b128 a[%c1:%c2], %0 offset:%c3" ::"v"(addr), "i"(128 + 4 * I), "i"(128 + 4 * I + 3), "i"(I * 1024) : "memory"); }
template <int... Is> DI void q_ld_all(unsigned addr, iseq<Is...>) { (q_ld<Is>(addr), ...); }
template <int I> DI void q_st(unsigned addr) { asm volatile("ds_write_b128 %0, a[%c1:%c2] offset:%c3" ::"v"(addr), "i"(128 + 4 * I), "i"(128 + 4 * I + 3), "i"(I * 1024) : "memory"); }
template <int... Is> DI void q_st_all(unsigned addr, iseq<Is...>) { (q_st<Is>(addr), ...); }
template <int B> DI void o_zero1(u32x4 zq) { asm volatile("v_mfma_f32_32x32x16_bf16 a[%c1:%c2], %0, %0, 0" ::"v"(zq), "i"(16 * B), "i"(16 * B + 15)); }
template <int... Bs> DI void o_zero(u32x4 zq, iseq<Bs...>) { (o_zero1<Bs>(zq), ...); }

DI void pj_setup(PC& p, unsigned ldsb, const char* xn, const bf16_t* wq, const bf16_t* w1, int h, int row0) {
  const unsigned tid = threadIdx.x, lane = tid & 63u, l32 = lane & 31u, hi = lane >> 5;
  const unsigned wid = __builtin_amdgcn_readfirstlane(tid >> 6);
  p.sx = make_srd(xn, SEQ * DM * 2); p.sq = make_srd(wq, DM * NH * HD * 2); p.s1 = make_srd(w1, DM * DFF * 2);
  p.vx = (lane >> 2) * 1024 + 16 * ((lane & 3) ^ ((lane >> 4) & 3)); p.sox = (unsigned)row0 * 1024u; p.m0x = ldsb + 4096 * wid;
  p.vq = (8 * wid + (lane >> 4)) * 8192 + 16 * ((lane & 15) ^ ((lane >> 4) << 2)); p.soq = h * 256; p.m0q = ldsb + 16384 + 2048 * wid;
  const int fb1 = h < 22 ? 32 + (h >> 1) : h, qsel = h & 1;
  p.v1 = (16 * (wid & 1) + (lane >> 2)) * 2752 + 16 * (lane & 3); p.so1 = ((wid >> 1) ? fb1 : h) * 64; p.m01 = ldsb + 24576 + 1024 * wid;
  const unsigned blk = (lane >> 4) & 1, q4 = (lane & 15) >> 2, pp = lane & 3;
#pragma unroll
  for (int s = 0; s < 2; ++s) {
    const unsigned b = ldsb + L_G + 2 * GST * s;
#pragma unroll
    for (int ks = 0; ks < 2; ++ks) { p.x_rd[s][ks] = b + 64 * (64 * wid + l32) + 16 * ((2 * ks + hi) ^ ((l32 >> 2) & 3)); p.xs_rd[s][ks] = p.x_rd[s][ks] + 2048 * qsel; }
#pragma unroll
    for (int db = 0; db < 4; ++db) p.q_rd[s][db] = b + 16384 + 256 * (8 * hi + q4) + ((db ^ q4) << 6) + 32 * blk + 8 * pp;
    p.f_rd[s] = b + 24576 + 64 * (8 * hi + q4) + 32 * blk + 8 * pp;
  }
}

DI void attn_phase(AC& c, LAS char* lds, char* ws, const int h, const int pi, const srd_t wsrd, const unsigned wso, const u64 tag, const bf16_t* xblk, u32x2 (&xv)[4]) {
  const unsigned ldsb = (unsigned)(size_t)lds;
  const unsigned wid = __builtin_amdgcn_readfirstlane(threadIdx.x >> 6);
  const int rowL = 128 * pi, rowH = 128 * (15 - pi);
  srd_t sctx = make_srd(ws + WS_CTX, SEQ * 4096 * 2);
  c.mhat[0] = c.mhat[1] = 0.f; c.fct[0] = c.fct[1] = 1.f; c.pend = false; c.first = false; c.l_light[0] = c.l_light[1] = 1.f;
#pragma unroll
  for (int q = 0; q < 2; ++q) c.rs[q][0] = c.rs[q][1] = c.rs[q][2] = c.rs[q][3] = 0.f;
  step_setup(c, 0);
  gA0f(c);
  mask_hook<0>(c, 0); gB0fa(c); decide<0, true>(c); gB0fb(c);
  constexpr int TOUCH_T = 15;
  bool epi = false; unsigned junk = 0; f32x2 mseed = {0.f, 0.f};
  for (int t = 1; t < 17; t += 2) {
#define PAIR(M) do { step_setup(c, t); gA1m##M(c); gB1am##M(c); decide<1, false>(c); gB1bm##M(c); rescale_hook(c); \
      step_setup(c, t + 1); gA0m##M(c); gB0am##M(c); decide<0, false>(c); gB0bm##M(c); rescale_hook(c); } while (0)
    for (; t + 3 < c.n1; t += 2) PAIR(1);
    if (t > c.n1 + 1) for (; t + 3 <= 16; t += 2) PAIR(2);
    const bool sw = (t + 1 == c.n1);
    step_setup(c, t);
    if (t == TOUCH_T) {
      const unsigned ln = lane_id();
      asm volatile("buffer_load_dword %0, %1, %2, %3 offen\n\tbuffer_load_dword %0, %1, %2, %3 offen offset:3072" : "+v"(junk) : "v"(ln * 128), "s"(wsrd), "s"(wso) : "memory");
    }
    if (__builtin_expect(epi, 0)) {
      const unsigned ln = lane_id(), ln32 = ln & 31u;
      const float il[2] = {__builtin_amdgcn_rcpf(c.l_light[0]), __builtin_amdgcn_rcpf(c.l_light[1])};
      const unsigned eb = ldsb + L_X + 16384 * wid + 256 * ln32 + 16 * (ln32 & 15u) + 8 * (ln >> 5);
      const unsigned ebase[2] = {eb, eb + 8192};
      asm volatile("s_nop 1" ::: "memory");
      gA1e(c, il, ebase);
      const unsigned rr = ln >> 4, ch = ln & 15u;
      unsigned sr[4];
#pragma unroll
      for (int j = 0; j < 4; ++j) sr[j] = ldsb + L_X + 16384 * wid + 256 * (4 * j + rr) + 16 * (ch ^ (4 * j + rr));
      mask_hook<1>(c, t); gB1az(c, sr, rr * 8192 + 16 * ch, sctx, (unsigned)(rowL + 64 * wid) * 8192u + h * 256);
      epi = false;
    } else {
      if (sw && !c.helper) *(LAS f32x2*)(lds + L_SB + L_VA + 16384 + 512 * (wid & 1) + lane_id() * 8) = f32x2{c.mhat[0], c.mhat[1]};
      gA1(c);
      if (sw) {
        if (c.helper) { q_ld_all(ldsb + L_X + 16384 * wid + lane_id() * 16, make_iseq<16>{});
          asm volatile("ds_read_b64 %0, %1" : "=v"(mseed) : "v"(ldsb + L_SB + L_VA + 16384 + 512 * wid + lane_id() * 8) : "memory"); }
        else {
#pragma unroll
          for (int d0 = 0; d0 < 8; ++d0) c.kb_[d0] += L_SB;
        }
      }
      mask_hook<1>(c, t); gB1a(c);
    }
    decide<1, false>(c); gB1b(c); rescale_hook(c);
    if (sw && c.helper) {
      asm volatile("s_waitcnt lgkmcnt(0)" : "+v"(mseed)::"memory");
      c.mhat[0] = mseed[0]; c.mhat[1] = mseed[1]; negm_bcast<0>(-c.mhat[0]); negm_bcast<1>(-c.mhat[1]);
      c.first = true; c.row0 = rowH + 64 * (int)wid;
    }
    step_setup(c, t + 1);
    gA0(c);
    mask_hook<0>(c, t + 1); gB0a(c); decide<0, false>(c); gB0b(c); rescale_hook(c);
    if (sw) {
      if (c.helper) epi = true;
      else {
#pragma unroll
        for (int ks = 0; ks < 4; ++ks) c.vb_[ks] += L_SB;
      }
    }
  }
  const unsigned lane = lane_id(), l32 = lane & 31u, hi = lane >> 5;
  if (epi) {
    asm volatile("s_nop 7\n\ts_nop 7" ::: "memory");
    LAS char* stg = lds + L_X + 16384 * wid;
    o_stage<0>(stg, l32, hi, __builtin_amdgcn_rcpf(c.l_light[0]), make_iseq<16>{});
    o_stage<1>(stg + 8192, l32, hi, __builtin_amdgcn_rcpf(c.l_light[1]), make_iseq<16>{});
    { u32x4 zq = {0u, 0u, 0u, 0u}; asm volatile("s_nop 1" : "+v"(zq)); o_zero(zq, make_iseq<8>{}); }
    asm volatile("s_waitcnt lgkmcnt(0)" ::: "memory");
    const unsigned so = (unsigned)(rowL + 64 * wid) * 8192u + h * 256;
    o_store32(stg, sctx, so, lane); o_store32(stg + 8192, sctx, so + 32 * 8192, lane);
    asm volatile("s_waitcnt lgkmcnt(0)" ::: "memory");
  }
  if (epi) asm volatile("s_waitcnt vmcnt(16) lgkmcnt(0)\n\ts_barrier" ::: "memory"); else asm volatile("s_waitcnt vmcnt(0) lgkmcnt(0)\n\ts_barrier" ::: "memory");
  asm volatile("" ::"v"(junk));
  if (threadIdx.x == 0) { flag_set(flag(ws, F_HH + 32 * pi + h), tag); flag_set(flag(ws, F_HH + 32 * (15 - pi) + h), tag); if (pi != 7) flag_set(flag(ws, F_CTX + 32 * pi + h), tag); }
  asm volatile("global_load_dwordx2 %0, %4, off\n\tglobal_load_dwordx2 %1, %4, off offset:16\n\tglobal_load_dwordx2 %2, %4, off offset:32\n\tglobal_load_dwordx2 %3, %4, off offset:48"
               : "=&v"(xv[0]), "=&v"(xv[1]), "=&v"(xv[2]), "=&v"(xv[3]) : "v"(xblk + l32 * DM + 4 * hi) : "memory");
  g_drain(c);
  const float lsum0 = l_total(c, 0), lsum1 = l_total(c, 1);
  asm volatile("s_nop 7\n\ts_nop 7\n\ts_barrier" ::: "memory");
  const unsigned xw = 20480 * wid, xr = 20480 * (wid ^ 2);
  float mh0 = c.mhat[0], mh1 = c.mhat[1]; asm volatile("" : "+v"(mh0), "+v"(mh1));
  if (c.helper) o_xw_all<1>(ldsb + xw + lane * 16, make_iseq<16>{}); else o_xw_all<0>(ldsb + xw + lane * 16, make_iseq<16>{});
  *(LAS f32x2*)(lds + xw + 16384 + lane * 8) = f32x2{c.helper ? mh1 : mh0, c.helper ? lsum1 : lsum0};
  raw_barrier();
  const f32x2 ml = *(LAS const f32x2*)(lds + xr + 16384 + lane * 8);
  const float m1 = c.helper ? mh0 : mh1, l1 = c.helper ? lsum0 : lsum1;
  const float m = fmaxf(m1, ml[0]), e1 = __builtin_amdgcn_exp2f(m1 - m), e2 = __builtin_amdgcn_exp2f(ml[0] - m);
  const float il = __builtin_amdgcn_rcpf(l1 * e1 + ml[1] * e2);
  { const float a = e1 * il, b = e2 * il; const unsigned sb = ldsb + L_X + 8192 * wid, eb = sb + 256 * l32 + 16 * (l32 & 15u) + 8 * hi;
    if (c.helper) g_merge0(ldsb + xr + lane * 16, f32x2{a, a}, f32x2{b, b}, eb); else g_merge1(ldsb + xr + lane * 16, f32x2{a, a}, f32x2{b, b}, eb);
    const unsigned rr = lane >> 4, ch = lane & 15u; unsigned sr[4];
#pragma unroll
    for (int j = 0; j < 4; ++j) sr[j] = sb + 256 * (4 * j + rr) + 16 * (ch ^ (4 * j + rr));
    g_ostore(sr, rr * 8192 + 16 * ch, sctx, (unsigned)(rowH + 64 * (wid & 1) + 32 * (wid >> 1)) * 8192u + h * 256); }
}

template <int B, int E> DI void x_st(unsigned voff, srd_t s, unsigned so) { asm volatile("buffer_store_dwordx4 v[%c3:%c4], %0, %1, %2 offen offset:%c5 sc1" ::"v"(voff), "s"(s), "s"(so), "i"(128 + 16 * B + 4 * E), "i"(131 + 16 * B + 4 * E), "i"(E * 1024) : "memory"); }
template <int B> DI void x_st4(unsigned voff, srd_t s, unsigned so) { x_st<B, 0>(voff, s, so); x_st<B, 1>(voff, s, so); x_st<B, 2>(voff, s, so); x_st<B, 3>(voff, s, so); }
template <int R> DI float v_rd() { float r; asm volatile("v_mov_b32 %0, v%c1" : "=v"(r) : "i"(R)); return r; }
template <int B, int... Ks> DI void own_rd(float (&o)[16], iseq<Ks...>) { ((o[Ks] = v_rd<128 + 16 * B + Ks>()), ...); }

__global__ KATTR void kf(const bf16_t* __restrict__ x, const bf16_t* __restrict__ ckv, const bf16_t* __restrict__ wq, const bf16_t* __restrict__ wuk, const bf16_t* __restrict__ wuv,
                         const bf16_t* __restrict__ wo, const bf16_t* __restrict__ w1, const bf16_t* __restrict__ w2, bf16_t* __restrict__ out, char* __restrict__ ws) {
  extern __shared__ __attribute__((aligned(16))) char smem[];
  LAS char* lds = (LAS char*)smem;
  OWN_REGS();
  const unsigned ldsb = (unsigned)(size_t)lds;
  const unsigned tid = threadIdx.x;
  const unsigned wid = __builtin_amdgcn_readfirstlane(tid >> 6), wr = wid >> 1, wc = wid & 1;
  const int id = blockIdx.x, xc = id & 7, jx = id >> 3;
  const int h = xc + 8 * (jx & 3), pi = id >> 5;
  { unsigned long long pc; unsigned sz;
    asm volatile(".Lkf_begin:\n\ts_getpc_b64 %0\n\ts_mov_b32 %1, .Lkf_end-.Lkf_begin" : "=s"(pc), "=s"(sz));
    srd_t cs = make_srd((const void*)pc, sz);
    const unsigned per = ((sz + 32 * 256 * 128 - 1) / (32 * 256 * 128)) * 256 * 128;
    for (unsigned o = 0; o < per; o += 256 * 128)
      asm volatile("s_mov_b32 m0, %0\n\ts_nop 0\n\tbuffer_load_dword %1, %2, %3 offen lds" ::"s"(ldsb + 155648 + 1024 * wid), "v"((((tid & 63u) * 4 + wid) * 32 + jx) * 128), "s"(cs), "s"(o * 32) : "m0", "memory");
  }
  const int cb = 4 * xc + (jx & 3), kb = jx >> 2;
  const bool isk = cb < 16;
  AC c;
  c.n1 = 2 * (pi + 1); c.n2 = 15 - 2 * pi; c.helper = wid < 2;
  c.row0 = (wid < 2 ? 128 * pi : 128 * (15 - pi)) + 64 * (int)(wid & 1);
  PC p; u64 pk = 0; const u64 tag = launch_tag();
  {
    u32x4 xr[2]; ln_load(x, id * 8, xr);
    KVC k; kv_setup(k, ldsb, ckv, isk ? wuk : wuv, kb, cb & 15);
    kv_pro(k);
    pj_setup(p, ldsb, ws + WS_XN, wq, w1, h, c.row0);
    { const unsigned ln = tid & 63u; const int fb1 = h < 22 ? 32 + (h >> 1) : h; const unsigned m0t = ldsb + 159744 + 1024 * wid;
      const unsigned t0 = (64 * pi + 16 * wid + ((ln >> 1) & 15)) * 8192 + (ln & 1) * 128, t1 = (64 * pi + 16 * wid + (ln & 15)) * 2752 + ((ln & 16) ? fb1 : h) * 64;
      asm volatile("s_mov_b32 m0, %0\n\ts_nop 0\n\tbuffer_load_dword %1, %3, %5 offen lds\n\tbuffer_load_dword %2, %4, 0 offen lds" ::"s"(m0t), "v"(t0), "v"(t1), "s"(p.sq), "s"(p.s1), "s"(h * 256) : "m0", "memory"); }
    wait_vm<18>(); raw_barrier();
#define KV_HOOK(S) do { \
      if (S == 1) { asm volatile("" : "+v"(xr[0]), "+v"(xr[1])); ln_rows(xr, (bf16_t*)(ws + WS_XN), id * 8); } \
      if (S == 4) { if (tid == 0) flag_set(flag(ws, F_XN + id), tag); } \
      if (S == 8) { if (wid == 0) flag_peek(flag(ws, F_XN + 16 * ((tid & 16) ? 15 - pi : pi) + (tid & 15)), pk); } \
      if (S == 11) { if (wid == 0) flag_settle(flag(ws, F_XN + 16 * ((tid & 16) ? 15 - pi : pi) + (tid & 15)), true, pk, tag); } \
    } while (0)
    if (isk) KV_BODY_K(k, p); else KV_BODY_V(k, p);
  }
  c.srdK = make_srd(ws + WS_K, 16u << 20); c.srdV = make_srd(ws + WS_V, 16u << 20);
  c.m0w = ldsb + 4096 * wid; c.sofw = (unsigned)h * 32u * 16384u + 4096 * wid;
  {
    const unsigned lane = lane_id(), l32 = lane & 31u, hi = lane >> 5;
    c.vlin = lane * 16;
    asm volatile("s_barrier" ::: "memory");
    pj_pro(p);
    p.tsrd = isk ? c.srdK : c.srdV; p.tvo = lane * 16; p.tl = ldsb + 8192 * wid + lane * 16;
    p.tso = ((unsigned)(2 * (cb & 15) + wc) * 32 + kb * 4 + wr * 2) * 16384;
#pragma unroll
    for (int k = 0; k < 8; ++k)
      p.te[k] = ldsb + 8192 * wid + (isk ? 256 * l32 + 16 * ((2 * k + hi) ^ (l32 & 15u)) : 4096 * (k >> 2) + 128 * l32 + 16 * ((2 * (k & 3) + hi) ^ ((l32 >> 1) & 7u)));
    asm volatile("s_nop 7\n\ts_nop 7" ::: "memory");
    kv_pack_lo();
    wait_vm<14>(); raw_barrier();
#define SIG_STEP 11
#define PJ_HOOK(S) do { \
      if (S == SIG_STEP) { if (tid == 0) flag_set(flag(ws, F_KV + 8 * cb + kb), tag); } \
      if (S == 13 && SIG_STEP < 13) { if (wid == 0) flag_peek(flag(ws, F_KV + 8 * ((h >> 1) + (tid & 8) * 2) + (tid & 7)), pk); } \
      if (S == 14 && SIG_STEP < 13) { if (wid == 0) flag_settle(flag(ws, F_KV + 8 * ((h >> 1) + (tid & 8) * 2) + (tid & 7)), true, pk, tag); } \
      if (S == 15 && SIG_STEP < 13) { _Pragma("unroll") for (int q = 0; q < 8; ++q) dma16(c.m0w + L_KA + 16384 * (q >> 2) + 1024 * (q & 3), c.vlin, c.srdK, c.sofw + 16384 * (q >> 2) + 1024 * (q & 3)); } \
    } while (0)
    PJ_BODY(p);
    asm volatile("s_nop 7\n\ts_nop 7" ::: "memory");
    srd_t hs = make_srd(ws + WS_H, SEQ * DFF * 2); const unsigned hoff = (unsigned)(c.row0 + l32) * 2752u + 16 * hi;
    h_store0(hoff, hs, 64 * h); h_store1(hoff, hs, 32 * 2752 + 64 * h);
    if (h < 22) h_store2(hoff, hs, (h & 1) * 32 * 2752 + 64 * (32 + (h >> 1)));
#pragma unroll
    for (int d0 = 0; d0 < 8; ++d0) c.kb_[d0] = ldsb + 256 * l32 + 16 * ((2 * d0 + hi) ^ (l32 & 15u));
#pragma unroll
    for (int ks = 0; ks < 4; ++ks) c.vb_[ks] = ldsb + 128 * l32 + 16 * ((2 * ks + hi) ^ ((l32 >> 1) & 7u));
    q_pack(f32x2{0.12751743082f, 0.12751743082f});
    if (h < 22) wait_vm<6>(); else wait_vm<4>();
    raw_barrier();
    k_reads0(c, make_iseq<FWV>{});
    if (wid >= 2) q_st_all(ldsb + L_X + 16384 * (wid & 1) + lane * 16, make_iseq<16>{});
  }
  u32x2 xv[4];
  { const int part = xc & 3;
    attn_phase(c, lds, ws, h, pi, part == 3 ? make_srd(w2, DFF * DM * 2) : make_srd(wo, 4096 * DM * 2), (unsigned)((part == 3 ? 0 : 1408 * part) + 44 * jx + 11 * (int)wid) * 1024u, tag,
               x + (size_t)(((xc >> 2) * 8 + (jx >> 2)) * 128 + 64 * wr + 32 * (part >> 1)) * DM + (jx & 3) * 128 + 64 * wc + 32 * (part & 1), xv); }
  {
  const unsigned lane = lane_id(), l32 = lane & 31u, hi = lane >> 5;
  const int part = xc & 3, tn = jx & 3, tm = (xc >> 2) * 8 + (jx >> 2);
  OC o; o.m0w = ldsb + 4096 * wid;
  const bool isg = part == 3;
  const unsigned apitch = isg ? 2752u : 8192u;
  const int k0 = isg ? 0 : 22 * part, ns = part == 2 ? 20 : 22;
  o.sa = isg ? make_srd(ws + WS_H, SEQ * DFF * 2) : make_srd(ws + WS_CTX, SEQ * 4096 * 2);
  o.sb = isg ? make_srd(w2, DFF * DM * 2) : make_srd(wo, 4096 * DM * 2);
#pragma unroll
  for (int q = 0; q < 4; ++q) o.va[q] = (32 * wid + 8 * q + (lane >> 3)) * apitch + 16 * ((lane & 7) ^ ((4 * (q & 1) + (lane >> 4)) & 7));
  const unsigned soa = (unsigned)(tm * 128) * apitch + (unsigned)k0 * 128u;
  o.vb = (16 * wid + (lane >> 4)) * 1024 + 16 * ((lane & 15) ^ ((lane >> 4) << 2));
  const unsigned sob = tn * 256 + (unsigned)k0 * 65536u;
  { const unsigned blk = (lane >> 4) & 1, q4 = (lane & 15) >> 2, pp = lane & 3;
#pragma unroll
    for (int s = 0; s < 2; ++s) {
#pragma unroll
      for (int ks = 0; ks < 4; ++ks) o.a_rd[s][ks] = ldsb + 65536 * s + 128 * (64 * wr + l32) + 16 * ((2 * ks + hi) ^ ((l32 >> 1) & 7u));
#pragma unroll
      for (int j = 0; j < 2; ++j) o.b_rd[s][j] = ldsb + 65536 * s + 256 * (8 * hi + q4) + (((2 * wc + j) ^ q4) << 6) + 32 * blk + 8 * pp;
    } }
  raw_barrier();
  o3_dmaB0(o, sob); o3_dmaB1(o, sob + 65536); o3_dmaB2(o, sob + 131072);
  wait_vm<12>();
  asm volatile("s_barrier" ::: "memory");
  if (tid == 0) { if (pi == 7) flag_set(flag(ws, F_CTX + 32 * pi + h), tag); flag_set(flag(ws, F_CTX + 32 * (15 - pi) + h), tag); }
#pragma unroll
  for (int e = 0; e < 4; ++e) asm volatile("" : "+v"(xv[e]));
  const size_t off = (size_t)(tm * 128 + 64 * wr + 32 * (part >> 1) + l32) * DM + tn * 128 + 64 * wc + 32 * (part & 1) + 4 * hi;
  if (wid == 0) flag_wait(flag(ws, (isg ? F_HH : F_CTX + 11 * part) + 32 * tm + (lane & 31u)), lane < (isg ? 32u : (part == 2 ? 10u : 11u)), tag);
  asm volatile("s_barrier" ::: "memory");
  o3_dmaA0(o, soa); o3_dmaA1(o, soa + 128); o3_dmaA2(o, soa + 256);
  wait_vm<4>(); raw_barrier();
  o3_first(o);
#define O3_STEP(SL, s) do { if ((s) + 2 < ns) wait_vm<8>(); else wait_vm<0>(); asm volatile("s_barrier" ::: "memory"); \
    o3_stage##SL(o, (s) + 3, ns, soa + (unsigned)((s) + 3) * 128u, sob + (unsigned)((s) + 3) * 65536u); } while (0)
  for (int s = 0; s < 20; s += 4) { O3_STEP(0, s); O3_STEP(1, s + 1); O3_STEP(2, s + 2); O3_STEP(3, s + 3); }
  if (ns == 22) { O3_STEP(0, 20); O3_STEP(1, 21); }
  asm volatile("s_waitcnt lgkmcnt(0)\n\ts_nop 7\n\ts_nop 7" ::: "memory");
  const int tile = tm * 4 + tn;
  srd_t sx = make_srd(ws + WS_PART, 16u << 20);
  const unsigned xb = (unsigned)tile * 262144u + wid * 16384u;
  if (part != 0) x_st4<0>(lane * 16, sx, xb + part * 65536u);
  if (part != 1) x_st4<1>(lane * 16, sx, xb + part * 65536u + 4096);
  if (part != 2) x_st4<2>(lane * 16, sx, xb + part * 65536u + 8192);
  if (part != 3) x_st4<3>(lane * 16, sx, xb + part * 65536u + 12288);
  float own[16];
  if (part == 0) own_rd<0>(own, make_iseq<16>{}); else if (part == 1) own_rd<1>(own, make_iseq<16>{}); else if (part == 2) own_rd<2>(own, make_iseq<16>{}); else own_rd<3>(own, make_iseq<16>{});
  asm volatile("s_waitcnt vmcnt(0)" ::: "memory");
  if (lane == 0) flag_set(flag(ws, F_XCH + (tile * 4 + wid) * 4 + part), tag);
  flag_wait(flag(ws, F_XCH + (tile * 4 + wid) * 4 + (lane & 3u)), lane < 4u && (int)lane != part, tag);
  asm volatile("" ::: "memory");
  float s[16], g[16];
  {
    const unsigned src[3] = {xb + (part == 0 ? 1 : 0) * 65536u + part * 4096u, xb + (part <= 1 ? 2 : 1) * 65536u + part * 4096u, xb + (isg ? 2 : 3) * 65536u + part * 4096u};
    x_load(lane * 16, sx, src);
    if (isg) { x_sum012(s);
#pragma unroll
      for (int k = 0; k < 16; ++k) g[k] = own[k]; }
    else {
#pragma unroll
      for (int k = 0; k < 16; ++k) s[k] = own[k];
      x_add01(s); x_mov2(g); } }
  asm volatile("" : "+v"(s[0]), "+v"(s[15]), "+v"(g[0]), "+v"(g[15]));
#pragma unroll
  for (int e = 0; e < 4; ++e) {
    float o[4];
#pragma unroll
    for (int k = 0; k < 4; ++k) {
      const float xf = __uint_as_float((k & 1) ? (xv[e][k >> 1] & 0xffff0000u) : (xv[e][k >> 1] << 16));
      const float sb = rbf(s[4 * e + k]), gb = rbf(g[4 * e + k]);
      const float sg = rbf(gb * __builtin_amdgcn_rcpf(1.f + __expf(-gb)));
      o[k] = xf + rbf(sb * sg);
    }
    *(u32x2*)(out + off + 8 * e) = u32x2{cvtpk(o[0], o[1]), cvtpk(o[2], o[3])};
  }
  }
  asm volatile(".Lkf_end:");
}


extern "C" void kernel_launch(void* const* d_in, const int* in_sizes, int n_in, void* d_out, int out_size, void* d_ws, size_t ws_size, hipStream_t stream) {
  const bf16_t* x = (const bf16_t*)d_in[0]; const bf16_t* ckv = (const bf16_t*)d_in[1]; const bf16_t* wq = (const bf16_t*)d_in[2];
  const bf16_t* wuk = (const bf16_t*)d_in[3]; const bf16_t* wuv = (const bf16_t*)d_in[4]; const bf16_t* wo = (const bf16_t*)d_in[5];
  const bf16_t* w1 = (const bf16_t*)d_in[7]; const bf16_t* w2 = (const bf16_t*)d_in[8];
  (void)hipFuncSetAttribute((const void*)kf, hipFuncAttributeMaxDynamicSharedMemorySize, 163840);
  kf<<<256, 256, 163840, stream>>>(x, ckv, wq, wuk, wuv, wo, w1, w2, (bf16_t*)d_out, (char*)d_ws);
}
```
